```python
import torch

def _softmax(x, axis=-1):
    return torch.softmax(x, dim=axis)
def _gelu(x):
    return torch.nn.functional.gelu(x, approximate="tanh")
def _silu(x):
    return torch.nn.functional.silu(x)
def _sigmoid(x):
    return torch.sigmoid(x)
def _layernorm(x, axis=-1, eps=1e-5):
    m = x.mean(dim=axis, keepdim=True)
    v = x.var(dim=axis, keepdim=True, unbiased=False)
    return (x - m) / torch.sqrt(v + eps)
def _scatter_add(tgt, idx, src, axis=0):
    return tgt.clone().index_add_(axis, idx, src)
def _segment_sum(src, seg, n):
    out = torch.zeros((n,) + src.shape[1:], dtype=src.dtype)
    return out.index_add_(0, seg, src)
def _pad(x, pw):
    flat = tuple(v for pair in reversed(pw) for v in pair)
    return torch.nn.functional.pad(x, flat)

kernel_name = 'gated_mix__linear_attn_block__ssm_conv_s4d__prompt'

def setup_inputs():

    _w = (lambda a: (lambda t: t.to(torch.bfloat16) if t.is_floating_point() else t)(torch.as_tensor(a)))
    def _A_setup():
        B, S, D = 1, 2048, 576
        return {
            "x": _w((lambda *s: torch.randn(*s, dtype=torch.bfloat16) * 0.05)(B, S, D)),
            "wqkv": _w((lambda *s: torch.randn(*s, dtype=torch.bfloat16) * 0.8)(D, 3 * D)),
            "wo": _w((lambda *s: torch.randn(*s, dtype=torch.bfloat16) * 0.05)(D, D)),
        }
    _A_in = _A_setup()

    def _B_setup():
        Bsz, L, D, N = 1, 2048, 576, 128
        return {
            "u": _w((lambda *s: torch.randn(*s, dtype=torch.bfloat16) * 0.05)(Bsz, L, D)),
            "log_A_real": _w((lambda *s: torch.randn(*s, dtype=torch.bfloat16) * 6.4)(D, N) * 0.5),
            "A_imag": _w((lambda *s: torch.randn(*s, dtype=torch.bfloat16) * 0.05)(D, N) * 3.14),
            "C_real": _w((lambda *s: torch.randn(*s, dtype=torch.bfloat16) * 0.2)(D, N) / N ** 0.5),
            "C_imag": _w((lambda *s: torch.randn(*s, dtype=torch.bfloat16) * 0.2)(D, N) / N ** 0.5),
            "log_dt": _w((lambda *s: torch.randn(*s, dtype=torch.bfloat16) * 0.05)(D) * 0.5 - 4.0),
            "D_skip": _w((lambda *s: torch.randn(*s, dtype=torch.bfloat16) * 0.05)(D)),
        }
    _B_in = _B_setup()

    return {
        "A_x": _A_in["x"],
        "A_wqkv": _A_in["wqkv"],
        "A_wo": _A_in["wo"],
        "B_u": _B_in["u"],
        "B_log_A_real": _B_in["log_A_real"],
        "B_A_imag": _B_in["A_imag"],
        "B_C_real": _B_in["C_real"],
        "B_C_imag": _B_in["C_imag"],
        "B_log_dt": _B_in["log_dt"],
        "B_D_skip": _B_in["D_skip"],
        "gate_w": _w((lambda *s: torch.randn(*s, dtype=torch.bfloat16) * 0.05)(576) * 16.0),
    }

def reference(A_x, A_wqkv, A_wo, B_u, B_log_A_real, B_A_imag, B_C_real, B_C_imag, B_log_dt, B_D_skip, gate_w):
    def _A_ref(x, wqkv, wo):
        B, S, D = x.shape
        H = 32; Dh = D // H
        qkv = (lambda x, *d: x.permute(*d))((x @ wqkv).reshape(B, S, 3, H, Dh), 2, 0, 3, 1, 4)
        q, k, v = qkv[0], qkv[1], qkv[2]
        phi = lambda t: torch.where(t > 0, t + 1, torch.exp(t)) + 1e-6
        pq, pk = phi(q), phi(k)
        kv = torch.einsum("bhsd,bhse->bhde", pk, v)
        z = torch.einsum("bhsd->bhd", pk)
        num = torch.einsum("bhqd,bhde->bhqe", pq, kv)
        den = torch.einsum("bhqd,bhd->bhq", pq, z)[..., None] + 1e-6
        o = num / den
        return (lambda x, *d: x.permute(*d))(o, 0, 2, 1, 3).reshape(B, S, D) @ wo

    def _B_ref(u, log_A_real, A_imag, C_real, C_imag, log_dt, D_skip):
        L = u.shape[1]
        dt = torch.exp(log_dt)
        A = -torch.exp(log_A_real) + 1j * A_imag
        C = (C_real + 1j * C_imag) * dt[:, None]
        pos = (lambda x, dt: x.to(dt))(torch.arange(L), log_dt.dtype)
        P = torch.exp((A * dt[:, None])[:, :, None] * pos[None, None, :])
        K = 2.0 * torch.einsum("dn,dnl->dl", C, P).real
        L2 = 2 * L
        u_t = (lambda x, *d: x.permute(*d))(u, 0, 2, 1)
        Kf = (lambda x, *a, **k: torch.fft.rfft(x.float(), *a, **k))(K, L2)
        Uf = (lambda x, *a, **k: torch.fft.rfft(x.float(), *a, **k))(u_t, L2)
        y = (lambda x, *a, **k: torch.fft.irfft(x, *a, **k).to(torch.bfloat16))(Uf * Kf[None, :, :], L2)[:, :, :L]
        y = (lambda x, *d: x.permute(*d))(y, 0, 2, 1)
        return y + u * D_skip[None, None, :]

    x = A_x
    g = _sigmoid((x * gate_w).sum(-1, keepdims=True))
    a = _A_ref(x, A_wqkv, A_wo)
    b = _B_ref(x, B_log_A_real, B_A_imag, B_C_real, B_C_imag, B_log_dt, B_D_skip)
    return g * a + (1 - g) * b
```

```cpp
#include <hip/hip_runtime.h>
#include <hip/hip_bf16.h>
#include <stdint.h>
typedef __hip_bfloat16 bf16;
#define ROUND_POS 0
constexpr int S = 2048, D = 576, H = 32, DH = 18, N = 128;
__device__ __forceinline__ float b2f(bf16 v) { return __bfloat162float(v); }
__device__ __forceinline__ float rb(float v) { return __bfloat162float(__float2bfloat16(v)); }

__global__ void k_qkv(const bf16* x, const bf16* w, float* qkv) {
    int n = blockIdx.x * blockDim.x + threadIdx.x; int s = blockIdx.y;
    if (n >= 3 * D) return;
    float acc = 0.f;
    for (int k = 0; k < D; ++k) acc += b2f(x[s * D + k]) * b2f(w[k * 3 * D + n]);
    float t = rb(acc);
    if (n < 2 * D) { t = t > 0 ? rb(t + 1.f) : rb(expf(t)); t = rb(t + 1e-6f); }
    qkv[s * 3 * D + n] = t;
}
__global__ void k_kv(const float* qkv, float* kv, float* z) {
    int h = blockIdx.x; int t = threadIdx.x;
    if (t < DH * DH) {
        int d = t / DH, e = t % DH; float acc = 0.f;
        for (int s = 0; s < S; ++s) acc += qkv[s * 3 * D + D + h * DH + d] * qkv[s * 3 * D + 2 * D + h * DH + e];
        kv[h * DH * DH + t] = rb(acc);
    } else if (t < DH * DH + DH) {
        int d = t - DH * DH; float acc = 0.f;
        for (int s = 0; s < S; ++s) acc += qkv[s * 3 * D + D + h * DH + d];
        z[h * DH + d] = rb(acc);
    }
}
__global__ void k_o(const float* qkv, const float* kv, const float* z, float* o) {
    int s = blockIdx.x; int c = threadIdx.x; if (c >= D) return;
    int h = c / DH, e = c % DH; float num = 0.f, den = 0.f;
    for (int d = 0; d < DH; ++d) { float p = qkv[s * 3 * D + h * DH + d]; num += p * kv[h * DH * DH + d * DH + e]; den += p * z[h * DH + d]; }
    num = rb(num); den = rb(rb(den) + 1e-6f);
    o[s * D + c] = rb(num / den);
}
__global__ void k_kgen(const bf16* logA, const bf16* Aim, const bf16* Cr, const bf16* Ci, const bf16* logdt, float* K) {
    int d = blockIdx.y; int l = blockIdx.x * blockDim.x + threadIdx.x;
    float dt = rb(expf(b2f(logdt[d])));
    float pos = ROUND_POS ? rb((float)l) : (float)l;
    float acc = 0.f;
    for (int n = 0; n < N; ++n) {
        float ar = -rb(expf(b2f(logA[d * N + n]))) * dt, ai = b2f(Aim[d * N + n]) * dt;
        float cr = b2f(Cr[d * N + n]) * dt, ci = b2f(Ci[d * N + n]) * dt;
        float m = expf(ar * pos); float sn, cs; sincosf(ai * pos, &sn, &cs);
        acc += cr * m * cs - ci * m * sn;
    }
    K[d * S + l] = 2.f * acc;
}
__global__ void k_conv(const bf16* x, const float* K, const bf16* Dsk, float* b) {
    int t = blockIdx.x; int d = threadIdx.x; if (d >= D) return;
    float acc = 0.f;
    for (int s = 0; s <= t; ++s) acc += K[d * S + t - s] * b2f(x[s * D + d]);
    float y = rb(acc);
    b[t * D + d] = rb(y + rb(b2f(x[t * D + d]) * b2f(Dsk[d])));
}
__global__ void k_out(const bf16* x, const bf16* gw, const float* o, const bf16* wo, const float* b, bf16* out) {
    int s = blockIdx.x; int c = threadIdx.x; if (c >= D) return;
    float gs = 0.f;
    for (int k = 0; k < D; ++k) gs += rb(b2f(x[s * D + k]) * b2f(gw[k]));
    gs = rb(gs);
    float g = rb(1.f / (1.f + expf(-gs)));
    float acc = 0.f;
    for (int k = 0; k < D; ++k) acc += o[s * D + k] * b2f(wo[k * D + c]);
    float a = rb(acc);
    out[s * D + c] = __float2bfloat16(rb(g * a) + rb(rb(1.f - g) * b[s * D + c]));
}
extern "C" void kernel_launch(void* const* d_in, const int* in_sizes, int n_in, void* d_out, int out_size, void* d_ws, size_t ws_size, hipStream_t stream) {
    const bf16* x = (const bf16*)d_in[0]; const bf16* wqkv = (const bf16*)d_in[1]; const bf16* wo = (const bf16*)d_in[2];
    const bf16* logA = (const bf16*)d_in[4]; const bf16* Aim = (const bf16*)d_in[5]; const bf16* Cr = (const bf16*)d_in[6];
    const bf16* Ci = (const bf16*)d_in[7]; const bf16* logdt = (const bf16*)d_in[8]; const bf16* Dsk = (const bf16*)d_in[9]; const bf16* gw = (const bf16*)d_in[10];
    float* ws = (float*)d_ws;
    float* qkv = ws; float* kv = qkv + S * 3 * D; float* z = kv + H * DH * DH; float* o = z + 1024; float* K = o + S * D; float* b = K + S * D;
    k_qkv<<<dim3((3 * D + 255) / 256, S), 256, 0, stream>>>(x, wqkv, qkv);
    k_kv<<<H, 384, 0, stream>>>(qkv, kv, z);
    k_o<<<S, 576, 0, stream>>>(qkv, kv, z, o);
    k_kgen<<<dim3(S / 256, D), 256, 0, stream>>>(logA, Aim, Cr, Ci, logdt, K);
    k_conv<<<S, 576, 0, stream>>>(x, K, Dsk, b);
    k_out<<<S, 576, 0, stream>>>(x, gw, o, wo, b, (bf16*)d_out);
}
```

```cpp
#include <hip/hip_runtime.h>
#include <stdint.h>

#define GAS __attribute__((address_space(1)))
#define LAS __attribute__((address_space(3)))
#define DEVI __device__ __forceinline__
typedef unsigned short u16;
typedef unsigned long long u64;
typedef __attribute__((ext_vector_type(2))) unsigned u32x2;
typedef __attribute__((ext_vector_type(4))) unsigned u32x4;
typedef __attribute__((ext_vector_type(4))) short s16x4;
typedef __attribute__((ext_vector_type(2))) float f32x2;
typedef __attribute__((ext_vector_type(4))) float f32x4;
typedef __attribute__((ext_vector_type(16))) float f32x16;
typedef __attribute__((ext_vector_type(2))) __bf16 bf16x2;
typedef __attribute__((ext_vector_type(8))) __bf16 bf16x8;
typedef __attribute__((ext_vector_type(2))) _Float16 f16x2;
typedef __attribute__((ext_vector_type(8))) _Float16 f16x8;
typedef LAS char* lptr;

constexpr int SEQ = 2048, DM = 576;

constexpr size_t WS_CTL = 0;
constexpr size_t WS_ARR = 256;
constexpr size_t WS_GATE = 8192;
constexpr size_t WS_L2G = 16384;
constexpr size_t WS_L1G = 131072;
constexpr size_t WS_O = WS_L1G + 8 * 16 * 16 * 768;
constexpr size_t WS_B = WS_O + (size_t)SEQ * DM * 2;
constexpr size_t WS_END = WS_B + (size_t)SEQ * DM * 2;

struct Params {
    const char* x; const char* wqkv; const char* wo; const char* logA; const char* aim; const char* cre; const char* cim;
    const char* logdt; const char* dskip; const char* gw; char* out; char* ws;
};

constexpr int LDS_BYTES = 152576;

DEVI float bf2f(unsigned h) { return __uint_as_float(h << 16); }
DEVI unsigned pk_bf16(float a, float b) { return __builtin_bit_cast(unsigned, __builtin_convertvector(f32x2{a, b}, bf16x2)); }
DEVI unsigned pk_f16(f32x2 v) { return __builtin_bit_cast(unsigned, __builtin_convertvector(v, f16x2)); }
DEVI float rbf(float a) { return bf2f(pk_bf16(a, a) & 0xffffu); }
DEVI f32x4 mfma16(u32x4 a, u32x4 b, f32x4 c) {
    return __builtin_amdgcn_mfma_f32_16x16x32_bf16(__builtin_bit_cast(bf16x8, a), __builtin_bit_cast(bf16x8, b), c, 0, 0, 0);
}
DEVI u32x4 lds_r128(lptr p) { return *(LAS u32x4*)p; }
DEVI u32x2 lds_r64(lptr p) { return *(LAS u32x2*)p; }
DEVI void lds_w128(lptr p, u32x4 v) { *(LAS u32x4*)p = v; }
DEVI void lds_w64(lptr p, u32x2 v) { *(LAS u32x2*)p = v; }
DEVI void lds_w32(lptr p, unsigned v) { *(LAS unsigned*)p = v; }
DEVI void lds_w16(lptr p, unsigned v) { *(LAS u16*)p = (u16)v; }
DEVI u32x2 lds_tr(lptr p) { return __builtin_bit_cast(u32x2, __builtin_amdgcn_ds_read_tr16_b64_v4i16((LAS s16x4*)p)); }
DEVI u32x4 lds_trfrag(lptr p, int off2) { u32x2 a = lds_tr(p), b = lds_tr(p + off2); return u32x4{a.x, a.y, b.x, b.y}; }
DEVI void glds16(const char* sbase, unsigned voff, lptr l) {
    asm volatile("s_mov_b32 m0, %2\n\ts_nop 0\n\tglobal_load_lds_dwordx4 %0, %1" :: "v"(voff), "s"(sbase), "s"((unsigned)(size_t)l) : "memory");
}
#define CFENCE() asm volatile("" ::: "memory")
#define WAIT_VM(n) asm volatile("s_waitcnt vmcnt(" #n ")" ::: "memory")
#define WAIT_LGKM0() asm volatile("s_waitcnt lgkmcnt(0)" ::: "memory")
#define BARRIER() do { WAIT_LGKM0(); __builtin_amdgcn_s_barrier(); CFENCE(); } while (0)
#define RLX_AGENT __ATOMIC_RELAXED, __HIP_MEMORY_SCOPE_AGENT
constexpr unsigned SPIN_CAP = 1u << 16;

DEVI u32x4 pack8(f32x4 a, f32x4 b) { return u32x4{pk_bf16(a[0], a[1]), pk_bf16(a[2], a[3]), pk_bf16(b[0], b[1]), pk_bf16(b[2], b[3])}; }
DEVI float phi(float t) {
    t = rbf(t);
    float e = __builtin_amdgcn_exp2f(t * 1.44269504089f);
    return rbf(t > 0.f ? t + 1.f : e) + 1e-6f;
}
DEVI f32x4 phi4(f32x4 v) { return f32x4{phi(v[0]), phi(v[1]), phi(v[2]), phi(v[3])}; }

DEVI u32x2 gran_make(float v, unsigned tlo, unsigned thi) { return u32x2{tlo, ((__float_as_uint(v) + 0x80u) & 0xffffff00u) | thi}; }

constexpr int XT_BYTES = 4608, CH_BYTES = 19072, XPAD = 256;
constexpr int TC = 1344, PA_OFF = 5376, PC_OFF = 7936;

DEVI f32x2 cmul(f32x2 a, f32x2 b, f32x2 bs) {
    f32x2 t = f32x2{a.x, a.x} * b;
    return __builtin_elementwise_fma(f32x2{a.y, a.y}, bs, t);
}
DEVI f32x2 swp(f32x2 b) { return f32x2{-b.y, b.x}; }
DEVI f32x2 cexp_p(float a2, float f, float p) {
    float m = __builtin_amdgcn_exp2f(a2 * p);
    float r = __builtin_amdgcn_fractf(f * p);
    return f32x2{m * __builtin_amdgcn_cosf(r), m * __builtin_amdgcn_sinf(r)};
}
DEVI u32x4 mask_frag(unsigned bits, int g, unsigned val) {
    unsigned by = bits >> (8 * g);
    u32x4 r;
#pragma unroll
    for (int i = 0; i < 4; ++i) { unsigned t = (by >> (2 * i)) & 3u; r[i] = ((t | (t << 15)) & 0x10001u) * val; }
    return r;
}
DEVI void t_write(lptr tb, int wb, float k0, float k1, float k2, float k3) {
    unsigned d0 = pk_bf16(k3, k2), d1 = pk_bf16(k1, k0), dm = (d0 >> 16) | (d1 << 16);
    lptr p = tb + wb;
    lds_w64(p, u32x2{d0, d1});
    lds_w16(p + TC - 2, d0); lds_w32(p + TC, dm); lds_w16(p + TC + 4, d1 >> 16);
    lds_w32(p + 2 * TC - 4, d0); lds_w32(p + 2 * TC, d1);
    lds_w16(p + 3 * TC - 6, d0); lds_w32(p + 3 * TC - 4, dm); lds_w16(p + 3 * TC, d1 >> 16);
}

DEVI void ssm_role(const Params& P, lptr smem, int cg, int rot) {
    const int tid = threadIdx.x, lane = tid & 63, w = __builtin_amdgcn_readfirstlane(tid >> 6);
    const int d = cg * 8 + w;
    const int lo16 = lane & 15, g = lane >> 4;
    const unsigned po = (unsigned)(d * 256 + lane * 4);
    const unsigned r_la = *(const GAS unsigned*)(P.logA + po), r_ai = *(const GAS unsigned*)(P.aim + po);
    const unsigned r_cr = *(const GAS unsigned*)(P.cre + po), r_ci = *(const GAS unsigned*)(P.cim + po);
    const unsigned r_dt = *(const GAS u16*)(P.logdt + d * 2), r_ds = *(const GAS u16*)(P.dskip + d * 2);
    const int tt = (tid + rot * 64) & 511;
    u32x4 xr[4];
#pragma unroll
    for (int i = 0; i < 4; ++i) xr[i] = *(const GAS u32x4*)(P.x + (unsigned)((tt * 4 + i) * 1152 + cg * 16));
    const u32x4 F21 = mask_frag(7u << (lo16 + 11), g, 0x3f80u), F22 = mask_frag(31u << (lo16 + 6), g, 0x3f80u);
    const u32x4 F23 = mask_frag(7u << (lo16 + 3), g, 0x3f80u), F31 = mask_frag(127u << (lo16 + 5), g, 0x3f80u);
    const u32x4 F32 = mask_frag(511u << (lo16 + 4), g, 0x3f80u);

    lptr xt = smem + w * CH_BYTES, stg = xt + XT_BYTES;
    const float dt = rbf(__expf(bf2f(r_dt)));
    float cm = fmaxf(fmaxf(fabsf(bf2f(r_cr & 0xffffu)), fabsf(bf2f(r_cr >> 16))), fmaxf(fabsf(bf2f(r_ci & 0xffffu)), fabsf(bf2f(r_ci >> 16))));
#pragma unroll
    for (int m = 32; m >= 1; m >>= 1) cm = fmaxf(cm, __shfl_xor(cm, m));
    unsigned ce = __float_as_uint(cm) >> 23; ce = ce < 16u ? 16u : (ce > 240u ? 240u : ce);
    const float cs = __uint_as_float((263u - ce) << 23), ics = __uint_as_float((ce - 9u) << 23);
    f32x16 kacc;
#pragma unroll
    for (int i = 0; i < 16; ++i) kacc[i] = 0.f;
    const int j32 = lane & 31, h = lane >> 5;
    int arow;
    {
        const u64 map0 = 0x0706050403020100ull, map1 = 0x0c0b0a0a0a090808ull, map2 = 0x141311 * 0x10000000000ull + 0x0e12100f0dull;
        u64 mm = j32 < 8 ? map0 : (j32 < 16 ? map1 : map2);
        arow = 32 + (int)((mm >> (8 * (j32 & 7))) & 0xff);
    }
#pragma unroll 1
    for (int pass = 0; pass < 2; ++pass) {
        const unsigned sh = pass * 16;
        const float la = bf2f((r_la >> sh) & 0xffffu), ai = bf2f((r_ai >> sh) & 0xffffu);
        const float cr = bf2f((r_cr >> sh) & 0xffffu) * cs, ci = bf2f((r_ci >> sh) & 0xffffu) * cs;
        const float a2 = (-rbf(__expf(la)) * dt) * 1.44269504089f, f = (ai * dt) * 0.15915494309f;
        lptr sp = stg + lane * 4;
#define PUT(row, v) lds_w32(sp + (row) * 272, pk_f16(v))
        f32x2 u[8], v[4];
        u[1] = cexp_p(a2, f, 8.f); v[1] = cexp_p(a2, f, 64.f);
        { f32x2 s1 = swp(u[1]); u[2] = cmul(u[1], u[1], s1); u[3] = cmul(u[2], u[1], s1); u[4] = cmul(u[3], u[1], s1);
          f32x2 s4 = swp(u[4]); u[5] = cmul(u[1], u[4], s4); u[6] = cmul(u[2], u[4], s4); u[7] = cmul(u[3], u[4], s4); }
        f32x2 vs = swp(v[1]);
        v[2] = cmul(v[1], v[1], vs); v[3] = cmul(v[2], v[1], vs);
        PUT(0, (f32x2{1.f, 0.f}));
#pragma unroll
        for (int b = 1; b < 8; ++b) PUT(b, u[b]);
#pragma unroll
        for (int a = 1; a < 4; ++a) {
            vs = swp(v[a]);
            PUT(8 * a, v[a]);
#pragma unroll
            for (int b = 1; b < 8; ++b) PUT(8 * a + b, cmul(u[b], v[a], vs));
        }
        const f32x2 w1 = cexp_p(a2, -f, 1.f), w256 = cexp_p(a2, -f, 256.f);
        const f32x2 w1s = swp(w1), w2s = swp(w256);
        f32x2 c[9];
        c[0] = f32x2{cr, -ci};
#pragma unroll
        for (int b = 1; b < 9; ++b) c[b] = cmul(c[b - 1], w1, w1s);
#pragma unroll
        for (int b = 0; b < 8; ++b) PUT(32 + b, c[b]);
        PUT(40, cmul(c[0], w256, w2s)); PUT(41, cmul(c[2], w256, w2s)); PUT(43, cmul(c[6], w256, w2s));
        f32x2 e4 = cmul(c[4], w256, w2s), e8 = cmul(c[8], w256, w2s);
        PUT(42, e4); PUT(44, e8);
        e4 = cmul(e4, w256, w2s); e8 = cmul(e8, w256, w2s); PUT(45, e4); PUT(46, e8);
        e4 = cmul(e4, w256, w2s); e8 = cmul(e8, w256, w2s); PUT(47, e4); PUT(49, e8);
        e8 = cmul(e8, w256, w2s); PUT(48, e8);
        e8 = cmul(e8, w256, w2s); PUT(50, e8);
        e8 = cmul(e8, w256, w2s); PUT(51, e8);
        e8 = cmul(e8, w256, w2s); PUT(52, e8);
#undef PUT
        CFENCE();
        lptr ap = stg + arow * 272 + h * 16, bp = stg + j32 * 272 + h * 16;
#pragma unroll
        for (int s = 0; s < 8; ++s) {
            u32x4 a = lds_r128(ap + 32 * s), b = lds_r128(bp + 32 * s);
            kacc = __builtin_amdgcn_mfma_f32_32x32x16_f16(__builtin_bit_cast(f16x8, a), __builtin_bit_cast(f16x8, b), kacc, 0, 0, 0);
        }
        CFENCE();
    }
    {
        lptr xb = smem + (XPAD + tt * 4) * 2;
#pragma unroll
        for (int c = 0; c < 4; ++c) {
            u32x2 lo = {__builtin_amdgcn_perm(xr[1][c], xr[0][c], 0x05040100u), __builtin_amdgcn_perm(xr[3][c], xr[2][c], 0x05040100u)};
            u32x2 hi = {__builtin_amdgcn_perm(xr[1][c], xr[0][c], 0x07060302u), __builtin_amdgcn_perm(xr[3][c], xr[2][c], 0x07060302u)};
            lds_w64(xb + (2 * c) * CH_BYTES, lo); lds_w64(xb + (2 * c + 1) * CH_BYTES, hi);
        }
        lds_w64(xt + lane * 8, u32x2{0u, 0u});
    }
#pragma unroll
    for (int i = 0; i < 10; ++i) lds_w128(stg + i * 1024 + lane * 16, u32x4{0u, 0u, 0u, 0u});
    CFENCE();
    const float ks = 2.f * dt * ics;
#pragma unroll
    for (int i = 0; i < 12; ++i) kacc[i] *= ks;
    const float k512 = __int_as_float(__builtin_amdgcn_readlane(__float_as_int(kacc[7]), 63)), k1024 = __int_as_float(__builtin_amdgcn_readlane(__float_as_int(kacc[9]), 63));
    {
        const int m = 2 * j32 + h;
        t_write(stg, 2 * (544 - 4 * m), kacc[0], kacc[1], kacc[2], kacc[3]);
        t_write(stg, 2 * (544 - 4 * (m + 64)), kacc[4], kacc[5], kacc[6], kacc[7]);
        t_write(stg, 2 * (544 - 4 * 128), k512, k512, k512, 0.f);
        const unsigned p89 = pk_bf16(kacc[8], kacc[9]), pab = pk_bf16(kacc[10], kacc[11]);
        const int u2 = 79 - (32 + (j32 >> 1)), u3 = 127 - (64 + 32 * h + (j32 >> 1));
#pragma unroll
        for (int c = 0; c < 4; ++c) {
            const int v2 = u2 - c + 4, v3 = u3 - c + 4;
            lptr q2 = stg + PA_OFF + (j32 & 1) * 1280 + c * 320 + (v2 >> 2) * 16 + h * 8 + (v2 & 3) * 2 - 64;
            lptr q3 = stg + PC_OFF + c * 576 + (v3 >> 2) * 16 + (j32 & 1) * 8 + (v3 & 3) * 2 - 64;
            lds_w16(q2 + 64, p89); lds_w16(q2, p89 >> 16);
            lds_w16(q3 + 64, pab); lds_w16(q3, pab >> 16);
        }
    }
    const u32x4 FX = mask_frag(3u << (lo16 + 12), g, pk_bf16(k1024, k1024) & 0xffffu);
    BARRIER();
    f32x4 acc[8];
#pragma unroll
    for (int i = 0; i < 8; ++i) acc[i] = f32x4{0.f, 0.f, 0.f, 0.f};
    const int cpy = (3 - lo16) & 3;
    lptr xf = xt + (XPAD - 16 + 16 * lo16 + 8 * g) * 2;
    lptr tf = stg + cpy * TC + 2 * (527 - lo16 + 8 * g - cpy + 4) - 64 * 16;
    auto TF = [&](int st) { u32x2 a = lds_r64(tf + 64 * (16 - st)), b = lds_r64(tf + 64 * (16 - st) + 8); return u32x4{a.x, a.y, b.x, b.y}; };
    {
        u32x4 X[8];
#pragma unroll
        for (int i = 0; i < 8; ++i) X[i] = lds_r128(xf + 512 * i);
        const u32x4 k0 = TF(0), k8 = TF(8), k16 = TF(16);
#pragma unroll
        for (int i = 0; i < 8; ++i) acc[i] = mfma16(k0, X[i], acc[i]);
#pragma unroll
        for (int i = 1; i < 8; ++i) acc[i] = mfma16(k8, X[i - 1], acc[i]);
#pragma unroll
        for (int i = 2; i < 8; ++i) acc[i] = mfma16(k16, X[i - 2], acc[i]);
#pragma unroll
        for (int i = 4; i < 8; ++i) acc[i] = mfma16(FX, X[i - 4], acc[i]);
        lptr pf2 = stg + PA_OFF + cpy * 320 + ((79 - 64 - lo16 + 4 * g - cpy + 4) >> 2) * 16;
        lptr pf3 = stg + PC_OFF + cpy * 576 + ((127 - 112 - lo16 + 4 * g - cpy + 4) >> 2) * 16;
        u32x4 TA[3], TB[3], TCc[4];
#pragma unroll
        for (int i = 0; i < 3; ++i) { TA[i] = lds_r128(pf2 + 64 * (2 - i)); TB[i] = lds_r128(pf2 + 1280 + 64 * (2 - i)); }
#pragma unroll
        for (int i = 0; i < 4; ++i) TCc[i] = lds_r128(pf3 + 64 * (3 - i));
        const f32x4 z4 = {0.f, 0.f, 0.f, 0.f};
#pragma unroll
        for (int st = 0; st < 6; ++st) {
            const u32x4 Xc = lds_r128(xf + 512 * st - 16);
            const u32x4 pa = pack8(mfma16(X[st], F21, z4), mfma16(X[st], F22, z4));
            const u32x4 pb = pack8(mfma16(X[st], F23, z4), mfma16(Xc, F22, z4));
#pragma unroll
            for (int dl = 2; dl <= 4; ++dl) if (st + dl < 8) {
                acc[st + dl] = mfma16(pa, TA[dl - 2], acc[st + dl]);
                acc[st + dl] = mfma16(pb, TB[dl - 2], acc[st + dl]);
            }
            if (st < 4) {
                const u32x4 pc = pack8(mfma16(X[st], F31, z4), mfma16(Xc, F32, z4));
#pragma unroll
                for (int dl = 4; dl <= 7; ++dl) if (st + dl < 8) acc[st + dl] = mfma16(pc, TCc[dl - 4], acc[st + dl]);
            }
        }
    }
#pragma unroll 1
    for (int c = 1; c < 8; ++c) {
        u32x4 X[8];
#pragma unroll
        for (int i = 0; i < 8; ++i) X[i] = lds_r128(xf - 64 * c + 512 * i);
        u32x2 a0 = lds_r64(tf - 64 * c + 1024), a1 = lds_r64(tf - 64 * c + 1032), b0 = lds_r64(tf - 64 * c + 512), b1 = lds_r64(tf - 64 * c + 520);
        const u32x4 k0 = {a0.x, a0.y, a1.x, a1.y}, k8 = {b0.x, b0.y, b1.x, b1.y};
#pragma unroll
        for (int i = 0; i < 8; ++i) acc[i] = mfma16(k0, X[i], acc[i]);
#pragma unroll
        for (int i = 1; i < 8; ++i) acc[i] = mfma16(k8, X[i - 1], acc[i]);
    }
    CFENCE();
    const float dsk = bf2f(r_ds);
    lptr ep = xt + (XPAD + 16 * lo16 + 4 * g) * 2;
#pragma unroll
    for (int i = 0; i < 8; ++i) {
        const u32x2 xv = lds_r64(ep + 512 * i);
        const float b0 = rbf(acc[i][0]) + rbf(bf2f(xv.x & 0xffffu) * dsk), b1 = rbf(acc[i][1]) + rbf(bf2f(xv.x >> 16) * dsk);
        const float b2 = rbf(acc[i][2]) + rbf(bf2f(xv.y & 0xffffu) * dsk), b3 = rbf(acc[i][3]) + rbf(bf2f(xv.y >> 16) * dsk);
        lds_w64(ep + 512 * i, u32x2{pk_bf16(b0, b1), pk_bf16(b2, b3)});
    }
    CFENCE();
#pragma unroll
    for (int i = 0; i < 4; ++i) {
        const u32x4 v = lds_r128(xt + XPAD * 2 + i * 1024 + lane * 16);
        *(GAS u32x4*)(P.ws + WS_B + (unsigned)(d * 4096 + i * 1024 + lane * 16)) = v;
    }
}

constexpr int SLOT = 34816, XCH = 16384, GW_OFF = 4 * SLOT;
constexpr int RED_OFF = 0, KVT_OFF = 4 * SLOT + 1280;

DEVI bool gran_ok(u32x4 a, unsigned tlo, unsigned thi) { return a.x == tlo && a.z == tlo && ((a.y & 0xffu) == thi) && ((a.w & 0xffu) == thi); }

DEVI void gemm_role(const Params& P, lptr smem, int rb, int hg, int rot, u64 epoch, int arr) {
    const int tid = threadIdx.x, lane = tid & 63, w = __builtin_amdgcn_readfirstlane(tid >> 6);
    const int lo16 = lane & 15, g = lane >> 4;
    const int rg = w >> 1, p = w & 1;
    unsigned xo[2], wo_[3], qo[2];
#pragma unroll
    for (int i = 0; i < 2; ++i) { const int row = 8 * (2 * w + i) + (lane >> 3); xo[i] = (unsigned)((rb * 128 + row) * 1152 + (((lane & 7) ^ ((row >> 1) & 7)) << 4)); }
#pragma unroll
    for (int i = 0; i < 3; ++i) {
        const int q = 64 * (i < 2 ? w + 8 * i : 16 + (w & 1)) + lane, rho = q / 18, pc = q - rho * 18;
        const int k = (rho & 32) + 8 * ((rho >> 2) & 3) + 4 * ((rho >> 4) & 1) + (rho & 3);
        wo_[i] = (unsigned)(k * 3456 + 1152 + hg * 144 + (pc < 9 ? pc * 16 : 1152 + (pc - 9) * 16));
    }
#pragma unroll
    for (int i = 0; i < 2; ++i) {
        const int q = 64 * (i == 0 ? w : 8) + lane, rho = q / 9, pc = q - rho * 9;
        const int k = (rho & 32) + 8 * ((rho >> 3) & 3) + 4 * (rho & 1) + ((rho >> 1) & 3);
        qo[i] = (unsigned)(k * 3456 + hg * 144 + pc * 16);
    }
    auto chunk = [&](int s) { int c = rot + s; return c >= 9 ? c - 9 : c; };
    auto issue = [&](int s) {
        lptr sl = smem + (s & 3) * SLOT;
        if (s < 9) {
            const int c = chunk(s);
            const char* xs = P.x + c * 128; const char* ws = P.wqkv + c * (64 * 3456);
            glds16(xs, xo[0], sl + (2 * w) * 1024); glds16(xs, xo[1], sl + (2 * w + 1) * 1024);
            glds16(ws, wo_[0], sl + XCH + w * 1024); glds16(ws, wo_[1], sl + XCH + (w + 8) * 1024);
            if (w < 2) glds16(ws, wo_[2], sl + XCH + (16 + w) * 1024);
        } else {
#pragma unroll
            for (int i = 0; i < 3; ++i) glds16(P.wqkv + chunk(3 * (s - 9) + i) * (64 * 3456), qo[0], sl + i * 9216 + w * 1024);
            if (w < 3) glds16(P.wqkv + chunk(3 * (s - 9) + w) * (64 * 3456), qo[1], sl + w * 9216 + 8 * 1024);
        }
    };
    if (w == 7) {
#pragma unroll
        for (int i = 0; i < 2; ++i) { int q = lane + 64 * i; q = q < 72 ? q : 71; glds16(P.gw, (unsigned)(chunk(q >> 3) * 128 + (q & 7) * 16), smem + GW_OFF + i * 1024); }
    }
    issue(0); issue(1); issue(2);
    const int row0 = 16 * (2 * rg + p) + lo16, row1 = 16 * (2 * rg + 1 - p) + lo16;
    const int xa0 = row0 * 128 + ((g ^ ((row0 >> 1) & 7)) << 4), xa1 = row1 * 128 + ((g ^ ((row1 >> 1) & 7)) << 4);
    const int wrow = (4 * g + (lo16 >> 2)) * 288;
    const int wa = XCH + wrow + (9 * p + (lane & 3)) * 8, wa2 = XCH + wrow + (9 * p + ((lane & 3) == 3 ? 18 : 8)) * 8;
    f32x4 acc[2][5];
#pragma unroll
    for (int i = 0; i < 2; ++i)
#pragma unroll
        for (int j = 0; j < 5; ++j) acc[i][j] = f32x4{0.f, 0.f, 0.f, 0.f};
    u32x4 xk[18];
#pragma unroll
    for (int s = 0; s < 9; ++s) {
        if (s < 7) WAIT_VM(8); else if (s == 7) WAIT_VM(7); else WAIT_VM(6);
        BARRIER();
        issue(s + 3);
        lptr sl = smem + (s & 3) * SLOT;
#pragma unroll
        for (int ks = 0; ks < 2; ++ks) {
            const u32x4 x0 = lds_r128(sl + (xa0 ^ (64 * ks))), x1 = lds_r128(sl + (xa1 ^ (64 * ks)));
            xk[2 * s + ks] = x0;
            u32x4 wf[5];
            wf[0] = lds_trfrag(sl + wa + ks * 9216, 4608); wf[1] = lds_trfrag(sl + wa + ks * 9216 + 32, 4608);
            wf[2] = lds_trfrag(sl + wa2 + ks * 9216, 4608);
            wf[3] = lds_trfrag(sl + wa + ks * 9216 + 152, 4608); wf[4] = lds_trfrag(sl + wa + ks * 9216 + 184, 4608);
#pragma unroll
            for (int j = 0; j < 5; ++j) { acc[0][j] = mfma16(x0, wf[j], acc[0][j]); acc[1][j] = mfma16(x1, wf[j], acc[1][j]); }
        }
    }
    const f32x4 z4 = {0.f, 0.f, 0.f, 0.f};
    {
        u32x4 pk[3], vb[3];
#pragma unroll
        for (int j = 0; j < 3; ++j) pk[j] = pack8(phi4(acc[0][j]), phi4(acc[1][j]));
        vb[0] = pack8(acc[0][2], acc[1][2]);
        if (lo16 < 12) vb[0] = u32x4{0x3f803f80u, 0x3f803f80u, 0x3f803f80u, 0x3f803f80u};
        vb[1] = pack8(acc[0][3], acc[1][3]); vb[2] = pack8(acc[0][4], acc[1][4]);
        BARRIER();
        lptr rp = smem + RED_OFF + rg * 6144 + p * 3072;
#pragma unroll
        for (int dt = 0; dt < 3; ++dt) {
            const f32x4 r = mfma16(pk[dt], vb[0], z4);
            const int dq = 4 * dt + g;
            if (lo16 >= 12 && dq < 5) lds_w128(rp + ((lo16 - 12) * 5 + dq) * 16, __builtin_bit_cast(u32x4, r));
            if (lo16 == 0 && dq < 12) lds_w128(rp + (180 + dq) * 16, __builtin_bit_cast(u32x4, r));
        }
#pragma unroll
        for (int dt = 0; dt < 3; ++dt) {
            const f32x4 r = mfma16(pk[dt], vb[1], z4);
            const int e = 4 + lo16, qd = 4 * dt + g - (e >= 18 ? 4 : 0);
            if (qd >= 0 && qd < 5) lds_w128(rp + (e * 5 + qd) * 16, __builtin_bit_cast(u32x4, r));
        }
#pragma unroll
        for (int dt = 1; dt < 3; ++dt) {
            const f32x4 r = mfma16(pk[dt], vb[2], z4);
            const int e = 20 + lo16, qd = 4 * dt + g - 4;
            if (qd < 5) lds_w128(rp + (e * 5 + qd) * 16, __builtin_bit_cast(u32x4, r));
        }
        if (tid < 384) { lds_w128(smem + KVT_OFF + tid * 32, u32x4{0u, 0u, 0u, 0u}); lds_w128(smem + KVT_OFF + tid * 32 + 16, u32x4{0u, 0u, 0u, 0u}); }
    }
    WAIT_VM(0);
    BARRIER();
    const u64 tag = epoch + 1ull;
    const unsigned tlo = (unsigned)tag, thi = (unsigned)(tag >> 32) & 0xffu;
    if (tid == 511) __hip_atomic_store((GAS u64*)(P.ws + WS_ARR) + arr, tag, RLX_AGENT);
    const __amdgpu_buffer_rsrc_t wsr = __builtin_amdgcn_make_buffer_rsrc(P.ws, 0, (int)WS_END, 0x00020000);
    if (tid < 384) {
        f32x4 s = __builtin_bit_cast(f32x4, lds_r128(smem + RED_OFF + tid * 16));
#pragma unroll
        for (int i = 1; i < 4; ++i) s += __builtin_bit_cast(f32x4, lds_r128(smem + RED_OFF + i * 6144 + tid * 16));
        const int sl = tid / 24, qi = tid - sl * 24;
        const unsigned off = (unsigned)WS_L1G + (unsigned)(((hg * 16 + sl) * 16 + rb) * 768 + qi * 32);
        const u32x2 a = gran_make(s[0], tlo, thi), b = gran_make(s[1], tlo, thi), c = gran_make(s[2], tlo, thi), e = gran_make(s[3], tlo, thi);
        __builtin_amdgcn_raw_buffer_store_b128(u32x4{a.x, a.y, b.x, b.y}, wsr, off, 0, 16);
        __builtin_amdgcn_raw_buffer_store_b128(u32x4{c.x, c.y, e.x, e.y}, wsr, off + 16, 0, 16);
    }
    f32x4 qa[5];
#pragma unroll
    for (int j = 0; j < 5; ++j) qa[j] = z4;
    const int qrow = (2 * (4 * g + (lo16 >> 2))) * 144;
    const int qw = qrow + (lane & 3) * 8, qw4 = qrow + (16 + (lane & 1)) * 8;
    auto qsteps = [&](int k0, int k1) {
#pragma unroll
        for (int ks = k0; ks < k1; ++ks) {
            lptr sl = smem + ((9 + ks / 6) & 3) * SLOT + (ks % 6) * 4608;
#pragma unroll
            for (int j = 0; j < 4; ++j) qa[j] = mfma16(lds_trfrag(sl + qw + 32 * j, 144), xk[ks], qa[j]);
            u32x4 w4 = lds_trfrag(sl + qw4, 144);
            const u32x4 gv = lds_r128(smem + GW_OFF + ks * 64 + g * 16);
            if (lo16 >= 8) w4 = gv;
            qa[4] = mfma16(w4, xk[ks], qa[4]);
        }
    };
    qsteps(0, 9);
    {
        BARRIER();
        if (tid < 384) {
            const unsigned off = (unsigned)WS_L1G + (unsigned)((hg * 16 + rb) * 12288 + tid * 32);
            u32x4 a, b;
#pragma unroll 1
            for (unsigned spins = 0; spins < SPIN_CAP; ++spins) {
                a = __builtin_amdgcn_raw_buffer_load_b128(wsr, off, 0, 16);
                b = __builtin_amdgcn_raw_buffer_load_b128(wsr, off + 16, 0, 16);
                if (__all(gran_ok(a, tlo, thi) && gran_ok(b, tlo, thi))) break;
                CFENCE();
                __builtin_amdgcn_s_sleep(1);
            }
            lds_w128(smem + RED_OFF + tid * 16, u32x4{a.y & 0xffffff00u, a.w & 0xffffff00u, b.y & 0xffffff00u, b.w & 0xffffff00u});
        }
        BARRIER();
        if (tid < 48) {
            f32x2 s = {0.f, 0.f};
#pragma unroll
            for (int i = 0; i < 16; ++i) s += __builtin_bit_cast(f32x2, lds_r64(smem + RED_OFF + i * 384 + tid * 8));
            const u32x2 a = gran_make(s.x, tlo, thi), b = gran_make(s.y, tlo, thi);
            __builtin_amdgcn_raw_buffer_store_b128(u32x4{a.x, a.y, b.x, b.y}, wsr, (unsigned)WS_L2G + (unsigned)(hg * 12288 + rb * 768 + tid * 16), 0, 16);
        }
    }
    qsteps(9, 18);
    if (tid < 384) {
        const unsigned off = (unsigned)WS_L2G + (unsigned)(hg * 12288 + tid * 32);
        u32x4 a, b;
#pragma unroll 1
        for (unsigned spins = 0; spins < SPIN_CAP; ++spins) {
            a = __builtin_amdgcn_raw_buffer_load_b128(wsr, off, 0, 16);
            b = __builtin_amdgcn_raw_buffer_load_b128(wsr, off + 16, 0, 16);
            if (__all(gran_ok(a, tlo, thi) && gran_ok(b, tlo, thi))) break;
            CFENCE();
            __builtin_amdgcn_s_sleep(1);
        }
        unsigned v01 = pk_bf16(__uint_as_float(a.y & 0xffffff00u), __uint_as_float(a.w & 0xffffff00u));
        unsigned v23 = pk_bf16(__uint_as_float(b.y & 0xffffff00u), __uint_as_float(b.w & 0xffffff00u));
        const int pp = tid >= 192 ? 1 : 0, qi = tid - 192 * pp;
        if (qi < 180) {
            const int el = qi / 5, qd = qi - el * 5, hi = el >= 18 ? 1 : 0;
            if (!hi && qd == 4) v23 = 0u;
            if (hi && qd == 0) v01 = 0u;
            const int e = 36 * pp + el, dq = 9 * pp + qd + 4 * hi;
            const int et = e >> 4, ks = dq >> 3;
            const int fr = et == 0 ? 0 : (et < 3 ? 2 * et - 1 + ks : 2 * et - 2 + ks);
            lds_w64(smem + KVT_OFF + fr * 1024 + ((dq & 3) * 16 + (e & 15)) * 16 + ((dq >> 2) & 1) * 8, u32x2{v01, v23});
        } else if (qi < 189) {
            const int qz = qi - 180, dq = 9 * pp + qz, ks = dq >> 3;
            lptr zp = smem + KVT_OFF + (9 + ks) * 1024 + (dq & 3) * 256 + ((dq >> 2) & 1) * 8;
            const int h01 = 2 * pp + (qz > 4 ? 1 : 0), h23 = 2 * pp + (qz >= 4 ? 1 : 0);
#pragma unroll
            for (int r = 0; r < 4; ++r) { lds_w32(zp + (4 * r + h01) * 16, v01); lds_w32(zp + (4 * r + h23) * 16 + 4, v23); }
        }
    }
    BARRIER();
    {
        const float gs = rbf(__shfl(qa[4][0], lo16 + 32));
        const float gate = rbf(1.f / (1.f + __expf(-gs)));
        if (hg == 0 && g == 0) *(GAS float*)(P.ws + WS_GATE + (unsigned)((rb * 128 + row0) * 4)) = gate;
        f32x4 q4 = phi4(qa[4]);
        if (g >= 2) q4 = z4;
        const u32x4 b0 = pack8(phi4(qa[0]), phi4(qa[1])), b1 = pack8(phi4(qa[2]), phi4(qa[3])), b2 = pack8(q4, z4);
        lptr kf = smem + KVT_OFF + lane * 16;
        f32x4 den = mfma16(lds_r128(kf + 9 * 1024), b0, z4); den = mfma16(lds_r128(kf + 10 * 1024), b1, den); den = mfma16(lds_r128(kf + 11 * 1024), b2, den);
        f32x4 o[5];
        o[0] = mfma16(lds_r128(kf), b0, z4);
        o[1] = mfma16(lds_r128(kf + 2 * 1024), b1, mfma16(lds_r128(kf + 1 * 1024), b0, z4));
        o[2] = mfma16(lds_r128(kf + 4 * 1024), b1, mfma16(lds_r128(kf + 3 * 1024), b0, z4));
        o[3] = mfma16(lds_r128(kf + 6 * 1024), b2, mfma16(lds_r128(kf + 5 * 1024), b1, z4));
        o[4] = mfma16(lds_r128(kf + 8 * 1024), b2, mfma16(lds_r128(kf + 7 * 1024), b1, z4));
        f32x4 r;
#pragma unroll
        for (int i = 0; i < 4; ++i) r[i] = __builtin_amdgcn_rcpf(den[i] + 1e-6f);
        const float r1a = g == 0 ? r[0] : r[1], r2 = g == 0 ? r[1] : r[2], r3a = g <= 1 ? r[2] : r[3], r3b = g == 0 ? r[2] : r[3];
        u32x2 ob[5];
        ob[0] = u32x2{pk_bf16(o[0][0] * r[0], o[0][1] * r[0]), pk_bf16(o[0][2] * r[0], o[0][3] * r[0])};
        ob[1] = u32x2{pk_bf16(o[1][0] * r1a, o[1][1] * r1a), pk_bf16(o[1][2] * r[1], o[1][3] * r[1])};
        ob[2] = u32x2{pk_bf16(o[2][0] * r2, o[2][1] * r2), pk_bf16(o[2][2] * r2, o[2][3] * r2)};
        ob[3] = u32x2{pk_bf16(o[3][0] * r3a, o[3][1] * r3a), pk_bf16(o[3][2] * r3b, o[3][3] * r3b)};
        ob[4] = u32x2{pk_bf16(o[4][0] * r[3], o[4][1] * r[3]), pk_bf16(o[4][2] * r[3], o[4][3] * r[3])};
        const unsigned ob0 = (unsigned)WS_O + (unsigned)(((rb * 8 + 2 * rg + p) * 72 + hg * 9) * 256 + lo16 * 16);
#pragma unroll
        for (int t = 0; t < 5; ++t)
            if (t < 4 || g < 2) *(GAS u32x2*)(P.ws + ob0 + (unsigned)((2 * t + (g >> 1)) * 256 + (g & 1) * 8)) = ob[t];
    }
}

__global__ __launch_bounds__(512, 2) void k_phase1(Params P) {
    __shared__ __attribute__((aligned(16))) char smem_[LDS_BYTES + 1024];
    lptr smem = (lptr)smem_;
    const int b = blockIdx.x, xcd = b & 7, idx = b >> 3;
    GAS u64* ctl = (GAS u64*)(P.ws + WS_CTL);
    if (idx < 16) {
        const u64 epoch = __hip_atomic_load(ctl, RLX_AGENT);
        const int rbl = idx & 3, hgl = idx >> 2;
        gemm_role(P, smem, (xcd >> 1) * 4 + rbl, (xcd & 1) * 4 + hgl, ((rbl + hgl) & 3) * 2, epoch, xcd * 16 + idx);
    } else if (idx < 25) {
        ssm_role(P, smem, xcd * 9 + idx - 16, (idx - 16) & 7);
    } else if (b == 255 && threadIdx.x < 64) {
        const u64 tag = __hip_atomic_load(ctl, RLX_AGENT) + 1ull;
#pragma unroll 1
        for (unsigned spins = 0; spins < SPIN_CAP; ++spins) {
            const u64 a = __hip_atomic_load((GAS u64*)(P.ws + WS_ARR) + threadIdx.x, RLX_AGENT);
            const u64 c = __hip_atomic_load((GAS u64*)(P.ws + WS_ARR) + 64 + threadIdx.x, RLX_AGENT);
            if (__all(a == tag && c == tag)) break;
            __builtin_amdgcn_s_sleep(4);
        }
        if (threadIdx.x == 0) __hip_atomic_store(ctl, tag, RLX_AGENT);
    }
}

constexpr int P2_W = 0, P2_ACC = 82944, P2_B = P2_ACC + 2 * 64 * 336;
__global__ __launch_bounds__(512, 2) void k_phase2(Params P) {
    __shared__ __attribute__((aligned(16))) char smem_[P2_B + 64 * 160];
    lptr smem = (lptr)smem_;
    const int tid = threadIdx.x, lane = tid & 63, w = __builtin_amdgcn_readfirstlane(tid >> 6);
    const int lo16 = lane & 15, g = lane >> 4;
    const int b = blockIdx.x, xcd = b & 7, idx = b >> 3;
    const int rt64 = xcd * 4 + (idx & 3), cg = idx >> 2;
    const int rt = w >> 1, kh = w & 1;
    unsigned qo[2];
#pragma unroll
    for (int i = 0; i < 2; ++i) {
        const int q = 64 * (i == 0 ? w : 8) + lane, rho = q / 9, pc = q - rho * 9;
        const int k = (rho & 32) + 8 * ((rho >> 3) & 3) + 4 * (rho & 1) + ((rho >> 1) & 3);
        qo[i] = (unsigned)(k * 1152 + cg * 144 + pc * 16);
    }
#pragma unroll
    for (int c = 0; c < 9; ++c) glds16(P.wo + c * (64 * 1152), qo[0], smem + P2_W + c * 9216 + w * 1024);
    glds16(P.wo + w * (64 * 1152), qo[1], smem + P2_W + w * 9216 + 8192);
    if (w == 0) glds16(P.wo + 8 * (64 * 1152), qo[1], smem + P2_W + 8 * 9216 + 8192);
    u32x4 of[9];
#pragma unroll
    for (int i = 0; i < 9; ++i) of[i] = *(const GAS u32x4*)(P.ws + WS_O + (unsigned)(((rt64 * 4 + rt) * 18 + kh * 9 + i) * 1024 + lane * 16));
    u32x4 bv0 = *(const GAS u32x4*)(P.ws + WS_B + (unsigned)((cg * 72 + (tid >> 3)) * 4096 + rt64 * 128 + (tid & 7) * 16));
    u32x4 bv1 = *(const GAS u32x4*)(P.ws + WS_B + (unsigned)((cg * 72 + 64 + (lane >> 3)) * 4096 + rt64 * 128 + (tid & 7) * 16));
    const int r0 = tid / 9, c0 = tid - r0 * 9, r1 = (512 + lane) / 9, c1 = 512 + lane - r1 * 9;
    const float g0 = *(const GAS float*)(P.ws + WS_GATE + (unsigned)((rt64 * 64 + r0) * 4));
    const float g1 = *(const GAS float*)(P.ws + WS_GATE + (unsigned)((rt64 * 64 + r1) * 4));
    {
        lptr bp = smem + P2_B + (tid & 7) * 8 * 160 + (tid >> 3) * 2;
#pragma unroll
        for (int i = 0; i < 4; ++i) { lds_w16(bp + (2 * i) * 160, bv0[i]); lds_w16(bp + (2 * i + 1) * 160, bv0[i] >> 16); }
        if (w == 0) {
#pragma unroll
            for (int i = 0; i < 4; ++i) { lds_w16(bp + 128 + (2 * i) * 160, bv1[i]); lds_w16(bp + 128 + (2 * i + 1) * 160, bv1[i] >> 16); }
        }
    }
    WAIT_VM(0);
    BARRIER();
    f32x4 acc[5];
#pragma unroll
    for (int j = 0; j < 5; ++j) acc[j] = f32x4{0.f, 0.f, 0.f, 0.f};
    const int qrow = (2 * (4 * g + (lo16 >> 2))) * 144 + kh * 9 * 4608;
    const int qw = qrow + (lane & 3) * 8, qw4 = qrow + (16 + (lane & 1)) * 8;
#pragma unroll
    for (int i = 0; i < 9; ++i) {
#pragma unroll
        for (int j = 0; j < 4; ++j) acc[j] = mfma16(lds_trfrag(smem + P2_W + qw + i * 4608 + 32 * j, 144), of[i], acc[j]);
        acc[4] = mfma16(lds_trfrag(smem + P2_W + qw4 + i * 4608, 144), of[i], acc[4]);
    }
    lptr ap = smem + P2_ACC + kh * (64 * 336) + (rt * 16 + lo16) * 336 + g * 16;
#pragma unroll
    for (int j = 0; j < 5; ++j) lds_w128(ap + j * 64, __builtin_bit_cast(u32x4, acc[j]));
    BARRIER();
    auto fin = [&](int r, int c, float gt) {
        lptr a = smem + P2_ACC + r * 336 + c * 32;
        const f32x4 s0 = __builtin_bit_cast(f32x4, lds_r128(a)) + __builtin_bit_cast(f32x4, lds_r128(a + 64 * 336));
        const f32x4 s1 = __builtin_bit_cast(f32x4, lds_r128(a + 16)) + __builtin_bit_cast(f32x4, lds_r128(a + 64 * 336 + 16));
        const u32x4 bb = lds_r128(smem + P2_B + r * 160 + c * 16);
        const float og = rbf(1.f - gt);
        u32x4 o;
#pragma unroll
        for (int i = 0; i < 4; ++i) {
            const float a0 = i < 2 ? s0[2 * i] : s1[2 * i - 4], a1 = i < 2 ? s0[2 * i + 1] : s1[2 * i - 3];
            o[i] = pk_bf16(rbf(gt * rbf(a0)) + rbf(og * bf2f(bb[i] & 0xffffu)), rbf(gt * rbf(a1)) + rbf(og * bf2f(bb[i] >> 16)));
        }
        *(GAS u32x4*)(P.out + (unsigned)((rt64 * 64 + r) * 1152 + cg * 144 + c * 16)) = o;
    };
    fin(r0, c0, g0);
    if (w == 0) fin(r1, c1, g1);
}

extern "C" void kernel_launch(void* const* d_in, const int* in_sizes, int n_in, void* d_out, int out_size, void* d_ws, size_t ws_size,
                              hipStream_t stream) {
    Params P;
    P.x = (const char*)d_in[0]; P.wqkv = (const char*)d_in[1]; P.wo = (const char*)d_in[2];
    P.logA = (const char*)d_in[4]; P.aim = (const char*)d_in[5]; P.cre = (const char*)d_in[6]; P.cim = (const char*)d_in[7];
    P.logdt = (const char*)d_in[8]; P.dskip = (const char*)d_in[9]; P.gw = (const char*)d_in[10];
    P.out = (char*)d_out; P.ws = (char*)d_ws;
    k_phase1<<<256, 512, 0, stream>>>(P);
    k_phase2<<<256, 512, 0, stream>>>(P);
}
```

```cpp
#include <hip/hip_runtime.h>
#include <stdint.h>

#define GAS __attribute__((address_space(1)))
#define LAS __attribute__((address_space(3)))
#define DEVI __device__ __forceinline__
typedef unsigned short u16;
typedef unsigned long long u64;
typedef __attribute__((ext_vector_type(2))) unsigned u32x2;
typedef __attribute__((ext_vector_type(3))) unsigned u32x3;
typedef __attribute__((ext_vector_type(4))) unsigned u32x4;
typedef __attribute__((ext_vector_type(4))) short s16x4;
typedef __attribute__((ext_vector_type(2))) float f32x2;
typedef __attribute__((ext_vector_type(4))) float f32x4;
typedef __attribute__((ext_vector_type(16))) float f32x16;
typedef __attribute__((ext_vector_type(2))) __bf16 bf16x2;
typedef __attribute__((ext_vector_type(8))) __bf16 bf16x8;
typedef __attribute__((ext_vector_type(2))) _Float16 f16x2;
typedef __attribute__((ext_vector_type(8))) _Float16 f16x8;
typedef LAS char* lptr;

constexpr int SEQ = 2048, DM = 576;

constexpr unsigned WS_CTL = 0;
constexpr unsigned WS_ARR = 256;
constexpr unsigned WS_CFL = 4096;
constexpr unsigned WS_PART = 16384;
constexpr unsigned WS_GATE = WS_PART + 8 * 16 * 3072;
constexpr unsigned WS_O = WS_GATE + 8192;
constexpr unsigned WS_B = WS_O + SEQ * DM * 2;
constexpr unsigned WS_END = WS_B + SEQ * DM * 2;
constexpr unsigned EMPTY = 0xffffffffu;

constexpr size_t WS_STAMP = 16u << 20;
#define STAMP_DECL
#define STAMP(i)
#define CSTAMP(i)
#define STAMP_OUT(role, slot)

struct Params {
    const char* x; const char* wqkv; const char* wo; const char* logA; const char* aim; const char* cre; const char* cim;
    const char* logdt; const char* dskip; const char* gw; char* out; char* ws;
};

constexpr int LDS_BYTES = 163840, LDS_TAG = LDS_BYTES - 16;

DEVI float bf2f(unsigned h) { return __uint_as_float(h << 16); }
DEVI float bfhi(unsigned h) { return __uint_as_float(h & 0xffff0000u); }
DEVI unsigned pk_bf16(float a, float b) { return __builtin_bit_cast(unsigned, __builtin_convertvector(f32x2{a, b}, bf16x2)); }
DEVI unsigned pk_f16(f32x2 v) { return __builtin_bit_cast(unsigned, __builtin_convertvector(v, f16x2)); }
DEVI float rbf(float a) { return bf2f(pk_bf16(a, a)); }
DEVI f32x4 mfma16(u32x4 a, u32x4 b, f32x4 c) {
    return __builtin_amdgcn_mfma_f32_16x16x32_bf16(__builtin_bit_cast(bf16x8, a), __builtin_bit_cast(bf16x8, b), c, 0, 0, 0);
}
DEVI u32x4 lds_r128(lptr p) { return *(LAS u32x4*)p; }
DEVI u32x2 lds_r64(lptr p) { return *(LAS u32x2*)p; }
DEVI float lds_rf(lptr p) { return *(LAS float*)p; }
DEVI void lds_w128(lptr p, u32x4 v) { *(LAS u32x4*)p = v; }
DEVI void lds_w64(lptr p, u32x2 v) { *(LAS u32x2*)p = v; }
DEVI void lds_w32(lptr p, unsigned v) { *(LAS unsigned*)p = v; }
DEVI void lds_w16(lptr p, unsigned v) { *(LAS u16*)p = (u16)v; }
DEVI u32x2 lds_tr(lptr p) { return __builtin_bit_cast(u32x2, __builtin_amdgcn_ds_read_tr16_b64_v4i16((LAS s16x4*)p)); }
DEVI u32x4 lds_trfrag(lptr p, int off2) { u32x2 a = lds_tr(p), b = lds_tr(p + off2); return u32x4{a.x, a.y, b.x, b.y}; }
DEVI void glds16(const char* sbase, unsigned voff, unsigned l) {
    asm volatile("s_mov_b32 m0, %2\n\ts_nop 0\n\tglobal_load_lds_dwordx4 %0, %1" :: "v"(voff), "s"(sbase), "s"(l) : "memory");
}
#define SET_M0(v) asm volatile("s_mov_b32 m0, %0\n\ts_nop 0" :: "s"(v) : "memory")
#define W_ADDTID(v, off) asm volatile("ds_write_addtid_b32 %0 offset:%1" :: "v"(v), "i"(off) : "memory")
#define CFENCE() asm volatile("" ::: "memory")
#define SCHED() __builtin_amdgcn_sched_barrier(0)
#define WAIT_VM(n) asm volatile("s_waitcnt vmcnt(" #n ")" ::: "memory")
#define WAIT_LGKM0() asm volatile("s_waitcnt lgkmcnt(0)" ::: "memory")
#define BARRIER() do { SCHED(); asm volatile("s_waitcnt lgkmcnt(0)\n\ts_barrier" ::: "memory"); SCHED(); } while (0)
#define RLX_AGENT __ATOMIC_RELAXED, __HIP_MEMORY_SCOPE_AGENT
constexpr unsigned SPIN_CAP = 1u << 16;
typedef __amdgpu_buffer_rsrc_t rsrc_t;
DEVI u32x4 ld_sc1(rsrc_t r, unsigned off) { return __builtin_amdgcn_raw_buffer_load_b128(r, off, 0, 16); }
DEVI void st_sc1(rsrc_t r, unsigned off, u32x4 v) { __builtin_amdgcn_raw_buffer_store_b128(v, r, off, 0, 16); }

DEVI u32x4 pack8(f32x4 a, f32x4 b) { return u32x4{pk_bf16(a[0], a[1]), pk_bf16(a[2], a[3]), pk_bf16(b[0], b[1]), pk_bf16(b[2], b[3])}; }
DEVI float phi(float t) {
    const float e = __builtin_amdgcn_fmed3f(__builtin_amdgcn_exp2f(t * 1.44269504089f), 0.f, 1.f);
    return __int_as_float(max(__float_as_int(t), 0x358637bd)) + e;
}
DEVI f32x4 phi4(f32x4 v) { return f32x4{phi(v[0]), phi(v[1]), phi(v[2]), phi(v[3])}; }
DEVI unsigned umax4(u32x4 a) { return max(max(a.x, a.y), max(a.z, a.w)); }
DEVI u32x4 not_empty(u32x4 a) { return u32x4{min(a.x, EMPTY - 1), min(a.y, EMPTY - 1), min(a.z, EMPTY - 1), min(a.w, EMPTY - 1)}; }
DEVI void tag_post(const Params& P, lptr smem, u64 epoch, int lane) {
    const u64 tag = epoch + 1ull;
    if (lane == 0) { lds_w64(smem + LDS_TAG, u32x2{(unsigned)tag, (unsigned)(tag >> 32)}); __hip_atomic_store((GAS u64*)(P.ws + WS_ARR) + blockIdx.x, tag, RLX_AGENT); }
}
DEVI u64 tag_get(lptr smem) { const u32x2 t = lds_r64(smem + LDS_TAG); return (u64)t.x | ((u64)t.y << 32); }
DEVI void poll_flags(const Params& P, unsigned off, int n, u64 tag, int lane) {
    const GAS u64* f = (const GAS u64*)(P.ws + off) + (lane < n ? lane : 0);
#pragma unroll 1
    for (unsigned spins = 0; spins < SPIN_CAP; ++spins) {
        if (__all(__hip_atomic_load(f, RLX_AGENT) == tag)) break;
        __builtin_amdgcn_s_sleep(1);
    }
}
template <unsigned CH>
DEVI void wo_issue(const Params& P, int cg, int first, int stride, int lane) {
#pragma unroll 1
    for (int m = first; m < 9; m += stride) {
        const int q = 64 * m + lane, rho = q / 9, pc = q - rho * 9;
        const int k = (rho & 32) + 8 * ((rho >> 3) & 3) + 4 * (rho & 1) + ((rho >> 1) & 3);
        const unsigned qo = (unsigned)(k * 1152 + cg * 144 + pc * 16);
#pragma unroll
        for (int c = 0; c < 9; ++c) if ((CH >> c) & 1) glds16(P.wo + c * (64 * 1152), qo, (unsigned)(c * 9216 + m * 1024));
    }
}

constexpr int SP = 3088;
constexpr int XT_OFF = 101376, XT_BYTES = 4608, XPAD = 256, XCHG_OFF = 129024;
constexpr int TB_BYTES = 8704, TC = 1216, PA_OFF = 4864, PC_OFF = 7424, ACX_OFF = 52224;

DEVI f32x2 cmul(f32x2 a, f32x2 b, f32x2 bs) {
    f32x2 t = f32x2{a.x, a.x} * b;
    return __builtin_elementwise_fma(f32x2{a.y, a.y}, bs, t);
}
DEVI f32x2 swp(f32x2 b) { return f32x2{-b.y, b.x}; }
DEVI f32x2 cexp_p(float a2, float f, float p) {
    float m = __builtin_amdgcn_exp2f(a2 * p);
    float r = __builtin_amdgcn_fractf(f * p);
    return f32x2{m * __builtin_amdgcn_cosf(r), m * __builtin_amdgcn_sinf(r)};
}
DEVI u32x4 mask_frag(unsigned bits, int g, unsigned val) {
    unsigned by = bits >> (8 * g);
    u32x4 r;
#pragma unroll
    for (int i = 0; i < 4; ++i) { unsigned t = (by >> (2 * i)) & 3u; r[i] = ((t | (t << 15)) & 0x10001u) * val; }
    return r;
}
DEVI float wave_max(float v) {
    v = fmaxf(v, __int_as_float(__builtin_amdgcn_update_dpp(0, __float_as_int(v), 0xB1, 0xf, 0xf, true)));
    v = fmaxf(v, __int_as_float(__builtin_amdgcn_update_dpp(0, __float_as_int(v), 0x4E, 0xf, 0xf, true)));
    v = fmaxf(v, __int_as_float(__builtin_amdgcn_update_dpp(0, __float_as_int(v), 0x124, 0xf, 0xf, true)));
    v = fmaxf(v, __int_as_float(__builtin_amdgcn_update_dpp(0, __float_as_int(v), 0x128, 0xf, 0xf, true)));
    const int i = __float_as_int(v);
    const int a = max(__builtin_amdgcn_readlane(i, 0), __builtin_amdgcn_readlane(i, 16)), b = max(__builtin_amdgcn_readlane(i, 32), __builtin_amdgcn_readlane(i, 48));
    return __int_as_float(max(a, b));
}

DEVI void ssm_role(const Params& P, lptr smem, int cg6, int rot, rsrc_t wsr) {
    const int tid = threadIdx.x, lane = tid & 63, w = __builtin_amdgcn_readfirstlane(tid >> 6);
    const int ch = w >> 1, half = (w ^ (w >> 2)) & 1, d = cg6 * 6 + ch;
    const int lo16 = lane & 15, g = lane >> 4;
    STAMP_DECL; STAMP(1);
    const unsigned po = (unsigned)(d * 256 + lane * 4);
    const unsigned r_la = *(const GAS unsigned*)(P.logA + po), r_ai = *(const GAS unsigned*)(P.aim + po);
    const unsigned r_cr = *(const GAS unsigned*)(P.cre + po), r_ci = *(const GAS unsigned*)(P.cim + po);
    const unsigned r_dt = *(const GAS u16*)(P.logdt + d * 2), r_ds = *(const GAS u16*)(P.dskip + d * 2);
    u64 epoch = 0;
    if (w == 11) epoch = __hip_atomic_load((GAS u64*)(P.ws + WS_CTL), RLX_AGENT);
    asm volatile("s_barrier" ::: "memory");
    const int tt = (tid + rot * 32) & 255;
    u32x3 xr[8];
    if (w >= 8) {
#pragma unroll
        for (int i = 0; i < 8; ++i) xr[i] = *(const GAS u32x3*)(P.x + (unsigned)(tt * 9216 + cg6 * 12 + i * 1152));
    }
    SCHED();
    u32x4 F21, F22, F23, F31, F32, FX;
    if (half) {
        F21 = mask_frag(7u << (lo16 + 11), g, 0x3f80u); F22 = mask_frag(31u << (lo16 + 6), g, 0x3f80u);
        F23 = mask_frag(7u << (lo16 + 3), g, 0x3f80u); F31 = mask_frag(127u << (lo16 + 5), g, 0x3f80u);
        F32 = mask_frag(511u << (lo16 + 4), g, 0x3f80u); FX = mask_frag(3u << (lo16 + 12), g, 0xffffu);
        asm volatile("" : "+v"(F21), "+v"(F22), "+v"(F23), "+v"(F31), "+v"(F32), "+v"(FX));
    }
    lptr xt = smem + XT_OFF + ch * XT_BYTES, tb = smem + ch * TB_BYTES;
    const int j32 = lane & 31, h = lane >> 5;
    int arow;
    {
        const u64 map0 = 0x0a09080803020100ull, map1 = 0x0c0b0a0a07060504ull, map2 = 0x1413110e12100f0dull;
        const u64 mm = j32 < 8 ? map0 : (j32 < 16 ? map1 : map2);
        arow = (int)((mm >> (8 * (j32 & 7))) & 0xff);
    }
    const float dt = rbf(__expf(bf2f(r_dt)));
    const float cm = wave_max(fmaxf(fmaxf(fabsf(bf2f(r_cr)), fabsf(bfhi(r_cr))), fmaxf(fabsf(bf2f(r_ci)), fabsf(bfhi(r_ci)))));
    unsigned ce = __float_as_uint(cm) >> 23; ce = ce < 16u ? 16u : (ce > 240u ? 240u : ce);
    const float cs = __uint_as_float((263u - ce) << 23), ics = __uint_as_float((ce - 9u) << 23);
    f32x16 kacc;
#pragma unroll
    for (int i = 0; i < 16; ++i) kacc[i] = 0.f;
    STAMP(2);
    {
        const unsigned sh = half * 16;
        const float la = bf2f(r_la >> sh), ai = bf2f(r_ai >> sh), cr = bf2f(r_cr >> sh) * cs, ci = bf2f(r_ci >> sh) * cs;
        const float a2 = (-rbf(__expf(la)) * dt) * 1.44269504089f, f = (ai * dt) * 0.15915494309f;
#define PUT(row, v) W_ADDTID(pk_f16(v), ((row) & 15) * SP)
        const f32x2 w1 = cexp_p(a2, -f, 1.f), w256 = cexp_p(a2, -f, 256.f);
        f32x2 u[8], v[4];
        u[1] = cexp_p(a2, f, 8.f); v[1] = cexp_p(a2, f, 64.f);
        const f32x2 w1s = swp(w1), w2s = swp(w256);
        f32x2 c[9];
        c[0] = f32x2{cr, -ci};
#pragma unroll
        for (int b = 1; b < 9; ++b) c[b] = cmul(c[b - 1], w1, w1s);
        SET_M0(w * 256);
#pragma unroll
        for (int b = 0; b < 8; ++b) PUT(b, c[b]);
        PUT(8, cmul(c[0], w256, w2s)); PUT(9, cmul(c[2], w256, w2s)); PUT(11, cmul(c[6], w256, w2s));
        f32x2 e4 = cmul(c[4], w256, w2s), e8 = cmul(c[8], w256, w2s);
        PUT(10, e4); PUT(12, e8);
        e4 = cmul(e4, w256, w2s); e8 = cmul(e8, w256, w2s); PUT(13, e4); PUT(14, e8);
        e4 = cmul(e4, w256, w2s); e8 = cmul(e8, w256, w2s); PUT(15, e4);
        SET_M0(w * 256 + 16 * SP);
        PUT(17, e8);
        e8 = cmul(e8, w256, w2s); PUT(16, e8);
        e8 = cmul(e8, w256, w2s); PUT(18, e8);
        e8 = cmul(e8, w256, w2s); PUT(19, e8);
        e8 = cmul(e8, w256, w2s); PUT(20, e8);
        u32x4 af[8];
        lptr ap = smem + arow * SP + w * 256 + h * 16, bp = smem + j32 * SP + w * 256 + h * 16;
#pragma unroll
        for (int s = 0; s < 8; ++s) af[s] = lds_r128(ap + 32 * s);
        { f32x2 s1 = swp(u[1]); u[2] = cmul(u[1], u[1], s1); u[3] = cmul(u[2], u[1], s1); u[4] = cmul(u[3], u[1], s1);
          f32x2 s4 = swp(u[4]); u[5] = cmul(u[1], u[4], s4); u[6] = cmul(u[2], u[4], s4); u[7] = cmul(u[3], u[4], s4); }
        f32x2 vs = swp(v[1]);
        v[2] = cmul(v[1], v[1], vs); v[3] = cmul(v[2], v[1], vs);
#pragma unroll
        for (int a = 3; a >= 1; --a) {
            if (a == 1) SET_M0(w * 256);
            vs = swp(v[a]);
            PUT(8 * a, v[a]);
#pragma unroll
            for (int b = 1; b < 8; ++b) PUT(8 * a + b, cmul(u[b], v[a], vs));
        }
        PUT(0, (f32x2{1.f, 0.f}));
#pragma unroll
        for (int b = 1; b < 8; ++b) PUT(b, u[b]);
#undef PUT
        u32x4 bfr[8];
#pragma unroll
        for (int s = 0; s < 8; ++s) bfr[s] = lds_r128(bp + 32 * s);
        SCHED();
#pragma unroll
        for (int s = 0; s < 8; ++s)
            kacc = __builtin_amdgcn_mfma_f32_32x32x16_f16(__builtin_bit_cast(f16x8, af[s]), __builtin_bit_cast(f16x8, bfr[s]), kacc, 0, 0, 0);
    }
    STAMP(3);
    if (w >= 8) {
        lptr xb = smem + XT_OFF + (XPAD + tt * 8) * 2;
#pragma unroll
        for (int c = 0; c < 3; ++c) {
            u32x4 lo, hi;
#pragma unroll
            for (int i = 0; i < 4; ++i) { lo[i] = __builtin_amdgcn_perm(xr[2 * i + 1][c], xr[2 * i][c], 0x05040100u); hi[i] = __builtin_amdgcn_perm(xr[2 * i + 1][c], xr[2 * i][c], 0x07060302u); }
            lds_w128(xb + (2 * c) * XT_BYTES, lo); lds_w128(xb + (2 * c + 1) * XT_BYTES, hi);
        }
    }
    if (w == 11) tag_post(P, smem, epoch, lane);
    lptr xc = smem + XCHG_OFF + ch * 3072 + lane * 16;
    if (half) {
        lds_w128(xc, __builtin_bit_cast(u32x4, f32x4{kacc[0], kacc[1], kacc[2], kacc[3]}));
        lds_w128(xc + 1024, __builtin_bit_cast(u32x4, f32x4{kacc[4], kacc[5], kacc[6], kacc[7]}));
    } else {
        lds_w128(xc + 2048, __builtin_bit_cast(u32x4, f32x4{kacc[8], kacc[9], kacc[10], kacc[11]}));
        lds_w64(xt + lane * 8, u32x2{0u, 0u});
    }
    STAMP(4);
    BARRIER();
    STAMP(5);
    const float ks = 2.f * dt * ics;
    const int cpy = (3 - lo16) & 3;
    lptr xf = xt + (XPAD - 16 + 16 * lo16 + 8 * g) * 2;
    lptr tf = tb + cpy * TC + 2 * (527 - lo16 + 8 * g - cpy) - 64 * 16;
    lptr xf7 = xf - 64 * 7;
    asm volatile("" : "+v"(xf7), "+v"(tf));
    auto TF = [&](int off) { u32x2 a = lds_r64(tf + off), b = lds_r64(tf + off + 8); return u32x4{a.x, a.y, b.x, b.y}; };
    f32x4 acc[8];
#pragma unroll
    for (int i = 0; i < 8; ++i) acc[i] = f32x4{0.f, 0.f, 0.f, 0.f};
    lptr axw, axr;
    if (!half) {
        const f32x4 p0 = __builtin_bit_cast(f32x4, lds_r128(xc)), p1 = __builtin_bit_cast(f32x4, lds_r128(xc + 1024));
        unsigned dd[6];
        dd[0] = pk_bf16((kacc[7] + p1[3]) * ks, (kacc[6] + p1[2]) * ks); dd[1] = pk_bf16((kacc[5] + p1[1]) * ks, (kacc[4] + p1[0]) * ks);
        dd[2] = pk_bf16((kacc[3] + p0[3]) * ks, (kacc[2] + p0[2]) * ks); dd[3] = pk_bf16((kacc[1] + p0[1]) * ks, (kacc[0] + p0[0]) * ks + (lane == 0 ? bf2f(r_ds) : 0.f));
        dd[4] = __builtin_amdgcn_update_dpp(0, dd[0], 0x138, 0xf, 0xf, true);
        dd[5] = __builtin_amdgcn_update_dpp(0, dd[1], 0x138, 0xf, 0xf, true);
        unsigned mm[5];
#pragma unroll
        for (int i = 0; i < 5; ++i) mm[i] = __builtin_amdgcn_alignbit(dd[i + 1], dd[i], 16);
        lptr tw = tb + 2 * (536 - 8 * lane);
        lds_w128(tw, u32x4{dd[0], dd[1], dd[2], dd[3]}); lds_w128(tw + TC, u32x4{mm[0], mm[1], mm[2], mm[3]});
        lds_w128(tw + 2 * TC, u32x4{dd[1], dd[2], dd[3], dd[4]}); lds_w128(tw + 3 * TC, u32x4{mm[1], mm[2], mm[3], mm[4]});
        {
            const unsigned n0 = __builtin_amdgcn_readlane(dd[0], 63), n1 = __builtin_amdgcn_readlane(dd[1], 63);
            const unsigned kk = (n0 & 0xffffu) * 0x10001u, kh = n0 << 16, m3 = (n0 << 16) | (n0 & 0xffffu), m4 = (n0 >> 16) | (n1 << 16);
            const int c = lane & 3, bl = lane >> 2;
            u32x4 e = c == 0 ? u32x4{0u, 0u, kh, kk} : (c == 1 ? u32x4{0u, 0u, kk, m3} : (c == 2 ? u32x4{0u, kh, kk, n0} : u32x4{0u, kk, m3, m4}));
            if (bl != 3) e = u32x4{0u, 0u, 0u, 0u};
            if (bl < 6) lds_w128(tb + c * TC + (bl < 4 ? bl * 16 : 1024 + bl * 16), e);
        }
        BARRIER();
        STAMP(6);
        u32x4 X[8];
#pragma unroll
        for (int i = 0; i < 8; ++i) X[i] = lds_r128(xf7 + 64 * 6 + 512 * i);
        u32x4 k0 = TF(1024 - 64), k8 = TF(512 - 64);
#pragma unroll
        for (int c = 1; c < 8; ++c) {
            u32x4 k0n, k8n;
            if (c < 7) { k0n = TF(1024 - 64 * (c + 1)); k8n = TF(512 - 64 * (c + 1)); }
#pragma unroll
            for (int i = 0; i < 8; ++i) {
                SCHED();
                acc[i] = mfma16(k0, X[i], acc[i]);
                if (i < 7) acc[i + 1] = mfma16(k8, X[i], acc[i + 1]);
                SCHED();
                if (c < 7) X[i] = lds_r128(xf7 + 64 * (6 - c) + 512 * i);
            }
            k0 = k0n; k8 = k8n;
        }
        SCHED();
        axw = smem + ACX_OFF + ch * 8192 + lane * 16; axr = axw + 4096;
#pragma unroll
        for (int i = 0; i < 4; ++i) lds_w128(axw + i * 1024, __builtin_bit_cast(u32x4, acc[4 + i]));
    } else {
#pragma unroll
        for (int i = 0; i < 4; ++i) if (i < 3 || lane < 48) lds_w128(tb + PA_OFF + i * 1024 + lane * 16, u32x4{0u, 0u, 0u, 0u});
        const f32x4 p2 = __builtin_bit_cast(f32x4, lds_r128(xc + 2048));
        const unsigned p89 = pk_bf16((kacc[8] + p2[0]) * ks, (kacc[9] + p2[1]) * ks), pab = pk_bf16((kacc[10] + p2[2]) * ks, (kacc[11] + p2[3]) * ks);
        CFENCE();
        const int u2 = 79 - (32 + (j32 >> 1)), u3 = 127 - (64 + 32 * h + (j32 >> 1));
#pragma unroll
        for (int c = 0; c < 4; ++c) {
            const int v2 = u2 - c + 4, v3 = u3 - c + 4;
            lptr q2 = tb + PA_OFF + (j32 & 1) * 1280 + c * 320 + (v2 >> 2) * 16 + h * 8 + (v2 & 3) * 2 - 64;
            lptr q3 = tb + PC_OFF + c * 320 + (v3 >> 2) * 16 + (j32 & 1) * 8 + (v3 & 3) * 2 - 64;
            lds_w16(q2 + 64, p89); lds_w16(q2, p89 >> 16);
            lds_w16(q3 + 64, pab); lds_w16(q3, pab >> 16);
        }
        FX &= ((unsigned)__builtin_amdgcn_readlane(p89, 63) >> 16) * 0x10001u;
        u32x4 X[8];
#pragma unroll
        for (int i = 0; i < 8; ++i) X[i] = lds_r128(xf + 512 * i);
        lptr pf2 = tb + PA_OFF + cpy * 320 + ((79 - 64 - lo16 + 4 * g - cpy + 4) >> 2) * 16;
        lptr pf3 = tb + PC_OFF + cpy * 320 + ((127 - 112 - lo16 + 4 * g - cpy + 4) >> 2) * 16;
        u32x4 TA[3], TB[3], TCc[4];
#pragma unroll
        for (int i = 0; i < 3; ++i) { TA[i] = lds_r128(pf2 + 64 * (2 - i)); TB[i] = lds_r128(pf2 + 1280 + 64 * (2 - i)); }
#pragma unroll
        for (int i = 0; i < 4; ++i) TCc[i] = lds_r128(pf3 + 64 * (3 - i));
        BARRIER();
        STAMP(6);
        const f32x4 z4 = {0.f, 0.f, 0.f, 0.f};
#pragma unroll
        for (int st = 0; st < 6; ++st) {
            const u32x4 Xc = lds_r128(xf + 512 * st - 16);
            const u32x4 pa = pack8(mfma16(X[st], F21, z4), mfma16(X[st], F22, z4));
            const u32x4 pb = pack8(mfma16(X[st], F23, z4), mfma16(Xc, F22, z4));
#pragma unroll
            for (int dl = 2; dl <= 4; ++dl) if (st + dl < 8) {
                acc[st + dl] = mfma16(pa, TA[dl - 2], acc[st + dl]);
                acc[st + dl] = mfma16(pb, TB[dl - 2], acc[st + dl]);
            }
            if (st < 4) {
                const u32x4 pc = pack8(mfma16(X[st], F31, z4), mfma16(Xc, F32, z4));
#pragma unroll
                for (int dl = 4; dl <= 7; ++dl) if (st + dl < 8) acc[st + dl] = mfma16(pc, TCc[dl - 4], acc[st + dl]);
            }
        }
        const u32x4 k0 = TF(1024), k8 = TF(512), k16 = TF(0);
#pragma unroll
        for (int i = 4; i < 8; ++i) acc[i] = mfma16(FX, X[i - 4], acc[i]);
#pragma unroll
        for (int i = 0; i < 8; ++i) acc[i] = mfma16(k0, X[i], acc[i]);
#pragma unroll
        for (int i = 1; i < 8; ++i) acc[i] = mfma16(k8, X[i - 1], acc[i]);
#pragma unroll
        for (int i = 2; i < 8; ++i) acc[i] = mfma16(k16, X[i - 2], acc[i]);
        axr = smem + ACX_OFF + ch * 8192 + lane * 16; axw = axr + 4096;
#pragma unroll
        for (int i = 0; i < 4; ++i) { lds_w128(axw + i * 1024, __builtin_bit_cast(u32x4, acc[i])); acc[i] = acc[4 + i]; }
    }
    STAMP(7);
    if (w == 11) poll_flags(P, WS_CFL, 32, epoch + 1ull, lane);
    BARRIER();
    STAMP(8);
#pragma unroll
    for (int i = 0; i < 2; ++i) {
        const f32x4 ya = acc[2 * i] + __builtin_bit_cast(f32x4, lds_r128(axr + (2 * i) * 1024));
        const f32x4 yb = acc[2 * i + 1] + __builtin_bit_cast(f32x4, lds_r128(axr + (2 * i + 1) * 1024));
        const auto sx = __builtin_amdgcn_permlane16_swap(pk_bf16(ya[0], ya[1]), pk_bf16(yb[0], yb[1]), false, false);
        const auto sy = __builtin_amdgcn_permlane16_swap(pk_bf16(ya[2], ya[3]), pk_bf16(yb[2], yb[3]), false, false);
        st_sc1(wsr, WS_B + (unsigned)(d * 4096 + half * 2048 + i * 1024 + lane * 16), not_empty(u32x4{sx[0], sy[0], sx[1], sy[1]}));
    }
    BARRIER();
    STAMP(9);
    if (w < 2) STAMP_OUT(2 + 2 * w, blockIdx.x + 256 * w);
}

constexpr int SLOT = 34816, XCH = 16384, GW_OFF = 4 * SLOT, KVT_OFF = GW_OFF + 1280, KVP_OFF = 27648;
DEVI constexpr int slot_off(int s) { return (s & 3) == 0 ? SLOT : ((s & 3) == 1 ? 0 : (s & 3) * SLOT); }
DEVI constexpr int wq_off(int t) { return t == 0 ? 0 : (t + 1) * SLOT + 7168; }

DEVI void gemm_compute(const Params& P, lptr smem, int rb, int hg, int w, rsrc_t wsr) {
    const int lane = threadIdx.x & 63, lo16 = lane & 15, g = lane >> 4;
    const int rg = w >> 1, p = w & 1;
    STAMP_DECL; STAMP(1);
    const int row0 = 16 * (2 * rg + p) + lo16, row1 = 16 * (2 * rg + 1 - p) + lo16;
    const int sw = (g ^ ((lo16 >> 1) & 7)) << 4;
    lptr xa0[2] = {smem + row0 * 128 + sw, smem + row0 * 128 + (sw ^ 64)}, xa1[2] = {smem + row1 * 128 + sw, smem + row1 * 128 + (sw ^ 64)};
    const int wrow = (4 * g + (lo16 >> 2)) * 288;
    lptr wa = smem + XCH + wrow + (9 * p + (lane & 3)) * 8, wa2 = smem + XCH + wrow + (9 * p + ((lane & 3) == 3 ? 18 : 8)) * 8;
    const f32x4 z4 = {0.f, 0.f, 0.f, 0.f};
    f32x4 acc[2][5];
#pragma unroll
    for (int i = 0; i < 2; ++i)
#pragma unroll
        for (int j = 0; j < 5; ++j) { const float e = j < 2 ? 1e-6f : (j == 2 && lo16 < 12 ? 1e-6f : 0.f); acc[i][j] = f32x4{e, e, e, e}; }
    u32x4 xk[18], x1[2], wf[5];
    auto rel = [&](int s) { return (s & 2) ? slot_off(s) - 2 * SLOT : slot_off(s); };
    auto rebase = [&](int dlt) {
        xa0[0] += dlt; xa0[1] += dlt; xa1[0] += dlt; xa1[1] += dlt; wa += dlt; wa2 += dlt;
        asm volatile("" : "+v"(xa0[0]), "+v"(xa0[1]), "+v"(xa1[0]), "+v"(xa1[1]), "+v"(wa), "+v"(wa2));
    };
    auto rdw = [&](int n, int j) {
        const int o = rel(n >> 1) + (n & 1) * 9216;
        return j == 2 ? lds_trfrag(wa2 + o, 4608) : lds_trfrag(wa + o + (j == 0 ? 0 : (j == 1 ? 32 : (j == 3 ? 152 : 184))), 4608);
    };
    BARRIER();
    STAMP(2);
    xk[0] = lds_r128(xa0[0] + rel(0)); x1[0] = lds_r128(xa1[0] + rel(0));
#pragma unroll
    for (int j = 0; j < 5; ++j) wf[j] = rdw(0, j);
#pragma unroll
    for (int n = 0; n < 18; ++n) {
        const int nx = n + 1;
        SCHED();
        if (!(n & 1)) { xk[nx] = lds_r128(xa0[1] + rel(n >> 1)); x1[1] = lds_r128(xa1[1] + rel(n >> 1)); }
        if ((n & 3) == 3) rebase((n & 4) ? -2 * SLOT : 2 * SLOT);
#pragma unroll
        for (int j = 0; j < 5; ++j) {
            SCHED();
            acc[0][j] = mfma16(xk[n], wf[j], acc[0][j]); acc[1][j] = mfma16(x1[n & 1], wf[j], acc[1][j]);
            SCHED();
            if (n & 1) {
                if (j == 1) {
                    BARRIER();
                    if (n == 1) STAMP(3);
                    if (n == 9) STAMP(4);
                    if (nx < 18) { xk[nx] = lds_r128(xa0[0] + rel(nx >> 1)); x1[0] = lds_r128(xa1[0] + rel(nx >> 1)); wf[0] = rdw(nx, 0); wf[1] = rdw(nx, 1); }
                } else if (j >= 2 && nx < 18) wf[j] = rdw(nx, j);
            } else wf[j] = rdw(nx, j);
        }
    }
    SCHED();
    STAMP(5);
    const int qrow = (2 * (4 * g + (lo16 >> 2))) * 144;
    lptr qw = smem + qrow + (lane & 3) * 8, qw4 = smem + qrow + (16 + (lane & 1)) * 8, gwp = smem + GW_OFF + g * 16;
    lptr qwh = qw + wq_off(1), qw4h = qw4 + wq_off(1);
    asm volatile("" : "+v"(qwh), "+v"(qw4h), "+v"(gwp));
    auto rdq = [&](int ks, int j) {
        const int o = (ks < 6 ? 0 : wq_off(ks / 6) - wq_off(1)) + (ks % 6) * 4608;
        return j < 4 ? lds_trfrag((ks < 6 ? qw : qwh) + o + 32 * j, 144) : lds_trfrag((ks < 6 ? qw4 : qw4h) + o, 144);
    };
    {
        u32x4 pk[3], vb[3];
#pragma unroll
        for (int j = 0; j < 3; ++j) pk[j] = pack8(phi4(acc[0][j]), phi4(acc[1][j]));
        vb[0] = pack8(acc[0][2], acc[1][2]);
        if (lo16 < 12) vb[0] = u32x4{0x3f803f80u, 0x3f803f80u, 0x3f803f80u, 0x3f803f80u};
        vb[1] = pack8(acc[0][3], acc[1][3]); vb[2] = pack8(acc[0][4], acc[1][4]);
        f32x4 r[6];
        const f32x4 t0 = mfma16(pk[2], vb[0], z4), t1 = mfma16(pk[2], vb[1], z4), t2 = mfma16(pk[2], vb[2], z4);
        r[0] = mfma16(pk[0], vb[0], z4); r[1] = mfma16(pk[1], vb[0], z4); r[2] = mfma16(pk[0], vb[1], z4);
        r[3] = mfma16(pk[1], vb[1], z4); r[4] = mfma16(pk[1], vb[2], z4);
        r[5] = g == 0 ? t0 : (g == 1 ? t1 : t2);
        SCHED();
        asm volatile("s_nop 7" ::: "memory");
        SET_M0(KVP_OFF - 8192 + w * 6144);
#pragma unroll
        for (int t = 5; t >= 0; --t) {
            W_ADDTID(r[t][0], 8192 + t * 1024); W_ADDTID(r[t][1], 8192 + t * 1024 + 256);
            W_ADDTID(r[t][2], 8192 + t * 1024 + 512); W_ADDTID(r[t][3], 8192 + t * 1024 + 768);
        }
    }
    BARRIER();
    STAMP(6);
    f32x4 qa[5];
#pragma unroll
    for (int j = 0; j < 5; ++j) { const float e = (j < 4 || g < 2) ? 1e-6f : 0.f; qa[j] = f32x4{e, e, e, e}; }
    u32x4 wq[2][5], gv[2];
#pragma unroll
    for (int i = 0; i < 2; ++i) {
#pragma unroll
        for (int j = 0; j < 5; ++j) wq[i][j] = rdq(i, j);
        gv[i] = lds_r128(gwp + i * 64);
    }
#pragma unroll
    for (int ks = 0; ks < 18; ++ks) {
#pragma unroll
        for (int j = 0; j < 5; ++j) {
            SCHED();
            u32x4 a = wq[ks & 1][j];
            if (j == 4 && lo16 >= 8) a = gv[ks & 1];
            qa[j] = mfma16(a, xk[ks], qa[j]);
            SCHED();
            if (ks + 2 < 18) { wq[ks & 1][j] = rdq(ks + 2, j); if (j == 4) gv[ks & 1] = lds_r128(gwp + (ks + 2) * 64); }
        }
    }
    SCHED();
    const float gs = rbf(__shfl(qa[4][0], lo16 + 32));
    const float gate = rbf(__builtin_amdgcn_rcpf(1.f + __expf(-gs)));
    if (hg == 0 && g == 0) __hip_atomic_store((GAS float*)(P.ws + WS_GATE) + (rb * 128 + row0), gate, RLX_AGENT);
    f32x4 q4 = phi4(qa[4]);
    if (g >= 2) q4 = z4;
    u32x4 b0 = pack8(phi4(qa[0]), phi4(qa[1])), b1 = pack8(phi4(qa[2]), phi4(qa[3])), b2 = pack8(q4, z4);
    lptr kfp = smem + KVT_OFF + lane * 16;
    unsigned ob0 = WS_O + (unsigned)(((rb * 8 + 2 * rg + p) * 72 + hg * 9 + 2 * (g & 1) + (g >> 1)) * 256 + lo16 * 16);
    asm volatile("" : "+v"(b0), "+v"(b1), "+v"(b2), "+v"(kfp), "+v"(ob0));
    STAMP(7);
    BARRIER();
    STAMP(8);
    {
        u32x4 kf[12];
#pragma unroll
        for (int i = 0; i < 12; ++i) kf[i] = lds_r128(kfp + (i < 3 ? 9 + i : i - 3) * 1024);
        SCHED();
        const f32x4 den = mfma16(kf[2], b2, mfma16(kf[1], b1, mfma16(kf[0], b0, f32x4{1e-6f, 1e-6f, 1e-6f, 1e-6f})));
        f32x4 o[5];
        o[0] = mfma16(kf[3], b0, z4);
        o[1] = mfma16(kf[5], b1, mfma16(kf[4], b0, z4));
        o[2] = mfma16(kf[7], b1, mfma16(kf[6], b0, z4));
        o[3] = mfma16(kf[9], b2, mfma16(kf[8], b1, z4));
        o[4] = mfma16(kf[11], b2, mfma16(kf[10], b1, z4));
        f32x4 r;
#pragma unroll
        for (int i = 0; i < 4; ++i) r[i] = __builtin_amdgcn_rcpf(den[i]);
        const float r1a = g == 0 ? r[0] : r[1], r2 = g == 0 ? r[1] : r[2], r3a = g <= 1 ? r[2] : r[3], r3b = g == 0 ? r[2] : r[3];
        u32x2 ob[5];
        ob[0] = u32x2{pk_bf16(o[0][0] * r[0], o[0][1] * r[0]), pk_bf16(o[0][2] * r[0], o[0][3] * r[0])};
        ob[1] = u32x2{pk_bf16(o[1][0] * r1a, o[1][1] * r1a), pk_bf16(o[1][2] * r[1], o[1][3] * r[1])};
        ob[2] = u32x2{pk_bf16(o[2][0] * r2, o[2][1] * r2), pk_bf16(o[2][2] * r2, o[2][3] * r2)};
        ob[3] = u32x2{pk_bf16(o[3][0] * r3a, o[3][1] * r3a), pk_bf16(o[3][2] * r3b, o[3][3] * r3b)};
        ob[4] = u32x2{pk_bf16(o[4][0] * r[3], o[4][1] * r[3]), pk_bf16(o[4][2] * r[3], o[4][3] * r[3])};
#pragma unroll
        for (int t = 0; t < 3; ++t) {
            const u32x2 a = ob[2 * t], b = ob[t < 2 ? 2 * t + 1 : 4];
            const auto sx = __builtin_amdgcn_permlane16_swap(a.x, b.x, false, false), sy = __builtin_amdgcn_permlane16_swap(a.y, b.y, false, false);
            if (t < 2 || g == 0) st_sc1(wsr, ob0 + 1024 * t, not_empty(u32x4{sx[0], sy[0], sx[1], sy[1]}));
        }
    }
    STAMP(9);
    if (w == 0) STAMP_OUT(1, blockIdx.x);
}

DEVI void gemm_loader(const Params& P, lptr smem, int rb, int hg, int rot, int lw, int cg2, rsrc_t wsr) {
    const int lane = threadIdx.x & 63;
    STAMP_DECL; STAMP(1);
    __builtin_amdgcn_s_setprio(3);
    unsigned xo[2], wo_[5], qo[3];
    xo[0] = (unsigned)((rb * 128 + 32 * lw + (lane >> 3)) * 1152 + (((lane & 7) ^ (lane >> 4)) << 4));
    xo[1] = (xo[0] ^ 64u) + 9216u;
    auto chunk = [&](int s) { int c = rot + s; return c >= 9 ? c - 9 : c; };
    auto issue_x = [&](int s) {
        const int sl = (s & 3) == 0 ? SLOT : ((s & 3) == 1 ? 0 : (s & 3) * SLOT);
        const char* xs = P.x + chunk(s) * 128;
#pragma unroll
        for (int i = 0; i < 4; ++i) glds16(xs + (i >> 1) * 18432, xo[i & 1], (unsigned)(sl + (4 * lw + i) * 1024));
    };
    auto issue_w = [&](int s) {
        const int sl = (s & 3) == 0 ? SLOT : ((s & 3) == 1 ? 0 : (s & 3) * SLOT);
        const char* ws = P.wqkv + chunk(s) * (64 * 3456);
#pragma unroll
        for (int i = 0; i < 4; ++i) glds16(ws, wo_[i], (unsigned)(sl + XCH + (4 * lw + i) * 1024));
        if (lw < 2) glds16(ws, wo_[4], (unsigned)(sl + XCH + (16 + lw) * 1024));
    };
    auto issue_kv = [&](int s) { issue_x(s); issue_w(s); };
    issue_x(0);
    SCHED();
#pragma unroll
    for (int i = 0; i < 5; ++i) {
        const unsigned q = 64 * (i < 4 ? 4 * lw + i : 16 + (lw & 1)) + lane, rho = (q * 3641u) >> 16, pc = q - rho * 18;
        const unsigned k = (rho & 35u) + 8 * ((rho >> 2) & 3) + 4 * ((rho >> 4) & 1);
        wo_[i] = k * 3456 + (1152 + hg * 144) + pc * 16 + (pc < 9 ? 0u : 1008u);
    }
    issue_w(0); issue_kv(1);
    u64 epoch = 0;
    if (lw == 3) {
        glds16(P.gw, (unsigned)(chunk(lane >> 3) * 128 + (lane & 7) * 16), (unsigned)GW_OFF);
        if (lane < 8) glds16(P.gw, (unsigned)(chunk(8) * 128 + lane * 16), (unsigned)(GW_OFF + 1024));
        epoch = __hip_atomic_load((GAS u64*)(P.ws + WS_CTL), RLX_AGENT);
    }
    WAIT_VM(8);
    BARRIER();
    issue_kv(2);
#pragma unroll
    for (int i = 0; i < 3; ++i) {
        const int q = 64 * (i < 2 ? 2 * lw + i : 8) + lane, rho = q / 9, pc = q - rho * 9;
        const int k = (rho & 32) + 8 * ((rho >> 3) & 3) + 4 * (rho & 1) + ((rho >> 1) & 3);
        qo[i] = (unsigned)(k * 3456 + hg * 144 + pc * 16);
    }
    asm volatile("" : "+v"(qo[0]), "+v"(qo[1]), "+v"(qo[2]));
    auto issue_q = [&](int t) {
        const int sl = t == 0 ? 0 : (t + 1) * SLOT + 7168;
#pragma unroll
        for (int i = 0; i < 3; ++i) {
            const char* ws = P.wqkv + chunk(3 * t + i) * (64 * 3456);
            glds16(ws, qo[0], (unsigned)(sl + i * 9216 + (2 * lw) * 1024)); glds16(ws, qo[1], (unsigned)(sl + i * 9216 + (2 * lw + 1) * 1024));
        }
        if (lw < 3) glds16(P.wqkv + chunk(3 * t + lw) * (64 * 3456), qo[2], (unsigned)(sl + lw * 9216 + 8192));
    };
    const int ct = lw * 64 + lane, lo16 = lane & 15, g = lane >> 4;
    auto quad_src = [&](int quad) {
        const int pp = quad >= 192 ? 1 : 0, qi = quad - 192 * pp;
        const bool isz = qi >= 180;
        const int el = isz ? 0 : qi / 5, qd = qi - el * 5, hi = el >= 18 ? 1 : 0;
        const int dql = isz ? (qi < 189 ? qi - 180 : 0) : qd + 4 * hi;
        const int et = isz ? 0 : (el < 4 ? 0 : (el < 20 ? 1 : 2)), col = isz ? 0 : (el < 4 ? 12 + el : (el < 20 ? el - 4 : el - 20));
        const int tile = dql == 8 ? 5 : (et == 0 ? (dql >> 2) : (et == 1 ? 2 + (dql >> 2) : 4));
        return KVP_OFF + pp * 6144 + tile * 1024 + ((dql == 8 ? et : (dql & 3)) * 16 + col) * 4;
    };
    auto quad_dst = [&](int quad) {
        const int pp = quad >= 192 ? 1 : 0, qi = quad - 192 * pp;
        const bool isz = qi >= 180;
        const int el = isz ? 0 : qi / 5, qd = qi - el * 5, hi = el >= 18 ? 1 : 0;
        const int dql = isz ? (qi < 189 ? qi - 180 : 0) : qd + 4 * hi;
        const int dq = 9 * pp + dql, ks = dq >> 3, e = 36 * pp + el, ee = e >> 4;
        const int fr = ee == 0 ? 0 : (ee < 3 ? 2 * ee - 1 + ks : 2 * ee - 2 + ks);
        const int a = qi >= 189 ? LDS_TAG - 512 : KVT_OFF + (isz ? (9 + ks) * 1024 + (dq & 3) * 256 + (2 * pp + (dql > 4 ? 1 : 0)) * 16 : fr * 1024 + ((dq & 3) * 16 + (e & 15)) * 16) + ((dq >> 2) & 1) * 8;
        return a | (isz ? (dql == 4 ? 0x300003 : 0x100003) : ((!hi && qd == 4) ? 1 : ((hi && qd == 0) ? 2 : 3)));
    };
    int src[2], dst[2];
    u32x4 sel[2];
    WAIT_VM(8); BARRIER(); issue_kv(3);
    src[0] = quad_src(2 * (ct < 192 ? ct : 0));
    asm volatile("" : "+v"(src[0]));
    WAIT_VM(8); BARRIER(); issue_kv(4);
    src[1] = quad_src(2 * (ct < 192 ? ct : 0) + 1);
    asm volatile("" : "+v"(src[1]));
    WAIT_VM(8); BARRIER(); issue_kv(5);
    dst[0] = quad_dst(2 * ((lw < 3 ? lw : 0) * 64 + (g >> 1) * 16 + lo16) + (g & 1));
    asm volatile("" : "+v"(dst[0]));
    WAIT_VM(8); BARRIER(); issue_kv(6);
    dst[1] = quad_dst(2 * ((lw < 3 ? lw : 0) * 64 + 32 + (g >> 1) * 16 + lo16) + (g & 1));
    asm volatile("" : "+v"(dst[1]));
    WAIT_VM(8); BARRIER(); issue_kv(7);
    sel[0] = mask_frag(lo16 < 8 ? 1u << lo16 : 0u, 0, 0x3f80u); sel[1] = mask_frag(lo16 >= 8 ? 1u << (lo16 - 8) : 0u, 0, 0x3f80u);
    asm volatile("" : "+v"(sel[0]), "+v"(sel[1]));
    WAIT_VM(8); BARRIER(); issue_kv(8);
#pragma unroll
    for (int i = 0; i < 3; ++i) lds_w128(smem + KVT_OFF + i * 4096 + ct * 16, u32x4{0u, 0u, 0u, 0u});
    if (lw == 3 && lane == 0) lds_w32(smem + LDS_TAG - 16, 0u);
    WAIT_VM(8); BARRIER(); issue_q(0);
    WAIT_VM(6); BARRIER(); issue_q(1);
    BARRIER();
    issue_q(2);
    if (lw == 3) { tag_post(P, smem, epoch, lane); poll_flags(P, WS_CFL, 32, epoch + 1ull, lane); }
    WAIT_VM(0);
    BARRIER();
    STAMP(2);
    if (lw < 3) {
        u32x4 pv;
#pragma unroll
        for (int i = 0; i < 2; ++i) {
            f32x4 a = {0.f, 0.f, 0.f, 0.f};
#pragma unroll
            for (int r = 0; r < 4; ++r)
#pragma unroll
                for (int k = 0; k < 4; ++k) a[k] += lds_rf(smem + src[i] + r * 12288 + k * 256);
            pv[2 * i] = pk_bf16(a[0], a[1]); pv[2 * i + 1] = pk_bf16(a[2], a[3]);
        }
        st_sc1(wsr, WS_PART + (unsigned)((hg * 16 + rb) * 3072 + ct * 16), not_empty(pv));
        if (lane == 0) __hip_atomic_fetch_add((LAS unsigned*)(smem + LDS_TAG - 16), 1u, __ATOMIC_RELAXED, __HIP_MEMORY_SCOPE_WORKGROUP);
        STAMP(3);
        {
            const GAS unsigned* pp = (const GAS unsigned*)(P.ws + WS_PART + (hg * 16 + lo16) * 3072 + lw * 1024);
#pragma unroll 1
            for (unsigned spins = 0; spins < SPIN_CAP; ++spins) {
                if (__all(__hip_atomic_load(pp, RLX_AGENT) != EMPTY)) break;
                __builtin_amdgcn_s_sleep(1);
            }
        }
        STAMP(4);
        const unsigned off = WS_PART + (unsigned)((hg * 16 + g) * 3072 + lw * 1024 + lo16 * 16);
        f32x4 sum[2];
#pragma unroll 1
        for (unsigned spins = 0; spins < SPIN_CAP; ++spins) {
            u32x4 a[4][4];
#pragma unroll
            for (int t = 0; t < 4; ++t)
#pragma unroll
                for (int i = 0; i < 4; ++i) a[t][i] = ld_sc1(wsr, off + (unsigned)(i * 12288 + t * 256));
            sum[0] = sum[1] = f32x4{0.f, 0.f, 0.f, 0.f};
#pragma unroll
            for (int t = 0; t < 4; ++t)
#pragma unroll
                for (int i = 0; i < 4; ++i) sum[t >> 1] = mfma16(sel[t & 1], a[t][i], sum[t >> 1]);
            if (__all(sum[0][0] == sum[0][0] && sum[1][0] == sum[1][0])) break;
            unsigned m = 0;
#pragma unroll
            for (int t = 0; t < 4; ++t)
#pragma unroll
                for (int i = 0; i < 4; ++i) m = max(m, umax4(a[t][i]));
            if (__all(m != EMPTY)) break;
            CFENCE();
        }
#pragma unroll
        for (int i = 0; i < 2; ++i) {
            const unsigned lo = (dst[i] & 1) ? pk_bf16(sum[i][0], sum[i][1]) : 0u, hi = (dst[i] & 2) ? pk_bf16(sum[i][2], sum[i][3]) : 0u;
            lptr dp = smem + (dst[i] & 0xffffc);
            if (dst[i] & 0x100000) {
                const int hs = (dst[i] & 0x200000) ? 20 : 4;
#pragma unroll
                for (int r = 0; r < 4; ++r) { lds_w32(dp + 64 * r, lo); lds_w32(dp + 64 * r + hs, hi); }
            } else lds_w64(dp, u32x2{lo, hi});
        }
        STAMP(5);
    } else {
#pragma unroll 1
        for (unsigned spins = 0; spins < SPIN_CAP; ++spins) {
            if (__hip_atomic_load((LAS unsigned*)(smem + LDS_TAG - 16), __ATOMIC_RELAXED, __HIP_MEMORY_SCOPE_WORKGROUP) == 3u) break;
            __builtin_amdgcn_s_sleep(1);
        }
        wo_issue<0x0f8>(P, cg2, 0, 1, lane);
    }
    BARRIER();
    wo_issue<0x107>(P, cg2, lw, 4, lane);
    __builtin_amdgcn_s_setprio(0);
    STAMP(6);
    if (lw == 0) STAMP_OUT(5, blockIdx.x + 256);
}

constexpr int P2_ACC = 82944, P2_AP = 336, P2_B = P2_ACC + 3 * 64 * P2_AP;
DEVI void phase2(const Params& P, lptr smem, int rt64, int cg, bool dma, rsrc_t wsr) {
    const int tid = threadIdx.x, lane = tid & 63, w = __builtin_amdgcn_readfirstlane(tid >> 6);
    const int lo16 = lane & 15, g = lane >> 4;
    const int rt = w & 3, kt = w >> 2;
    STAMP_DECL; STAMP(1);
    if (dma) WAIT_VM(0);
    BARRIER();
    STAMP(2);
    const unsigned boff = WS_B + (unsigned)((cg * 72 + (tid >> 3)) * 4096 + (rt64 >> 3) * 1024 + (((tid >> 2) & 1) * 32 + ((rt64 >> 2) & 1) * 16 + (rt64 & 3) * 4 + (tid & 3)) * 16);
    u32x4 bv;
    if (w < 9) bv = ld_sc1(wsr, boff);
    const int qrow = (2 * (4 * g + (lo16 >> 2))) * 144 + kt * 6 * 4608;
    lptr qw = smem + qrow + (lane & 3) * 8, qw4 = smem + qrow + (16 + (lane & 1)) * 8;
    u32x4 wf[6][5];
#pragma unroll
    for (int i = 0; i < 6; ++i) {
#pragma unroll
        for (int j = 0; j < 4; ++j) wf[i][j] = lds_trfrag(qw + i * 4608 + 32 * j, 144);
        wf[i][4] = lds_trfrag(qw4 + i * 4608, 144);
    }
    if (w < 9) {
#pragma unroll 1
        for (unsigned spins = 0; spins < SPIN_CAP; ++spins) {
            if (__all(umax4(bv) != EMPTY)) break;
            __builtin_amdgcn_s_sleep(8);
            CFENCE();
            bv = ld_sc1(wsr, boff);
        }
        lptr bp = smem + P2_B + ((tid & 3) * 16 + ((tid >> 2) & 1) * 8) * 160 + (tid >> 3) * 2;
#pragma unroll
        for (int i = 0; i < 4; ++i) { lds_w16(bp + (2 * i) * 160, bv[i]); lds_w16(bp + (2 * i + 1) * 160, bv[i] >> 16); }
    }
    STAMP(3);
    const unsigned ooff = WS_O + (unsigned)(((rt64 * 4 + rt) * 18 + kt * 6) * 1024);
    {
        const GAS unsigned* pp = (const GAS unsigned*)(P.ws + ooff + ((lane & 3) == 3 ? 23 : 8 * (lane & 3)) * 256);
#pragma unroll 1
        for (unsigned spins = 0; spins < SPIN_CAP; ++spins) {
            if (__all(__hip_atomic_load(pp, RLX_AGENT) != EMPTY)) break;
            __builtin_amdgcn_s_sleep(2);
        }
    }
    STAMP(4);
    const int r0 = tid < 576 ? tid / 9 : 0, c0 = tid - r0 * 9;
    f32x4 acc[5];
    unsigned gm;
    {
        u32x4 of[6];
#pragma unroll
        for (int i = 0; i < 6; ++i) of[i] = ld_sc1(wsr, ooff + (unsigned)(i * 1024 + lane * 16));
        gm = __hip_atomic_load((GAS unsigned*)(P.ws + WS_GATE) + (rt64 * 64 + r0), RLX_AGENT);
#pragma unroll
        for (int j = 0; j < 5; ++j) acc[j] = f32x4{0.f, 0.f, 0.f, 0.f};
#pragma unroll
        for (int i = 0; i < 6; ++i)
#pragma unroll
            for (int j = 0; j < 5; ++j) acc[j] = mfma16(wf[i][j], of[i], acc[j]);
    }
    if (!__all(acc[0][0] == acc[0][0] && gm != EMPTY)) {
#pragma unroll
        for (int j = 0; j < 5; ++j) acc[j] = f32x4{0.f, 0.f, 0.f, 0.f};
        u32x4 of[6];
#pragma unroll 1
        for (unsigned spins = 0; spins < SPIN_CAP; ++spins) {
            CFENCE();
#pragma unroll
            for (int i = 0; i < 6; ++i) of[i] = ld_sc1(wsr, ooff + (unsigned)(i * 1024 + lane * 16));
            unsigned m = gm = __hip_atomic_load((GAS unsigned*)(P.ws + WS_GATE) + (rt64 * 64 + r0), RLX_AGENT);
#pragma unroll
            for (int i = 0; i < 6; ++i) m = max(m, umax4(of[i]));
            if (__all(m != EMPTY)) break;
        }
#pragma unroll
        for (int i = 0; i < 6; ++i) {
#pragma unroll
            for (int j = 0; j < 4; ++j) acc[j] = mfma16(lds_trfrag(qw + i * 4608 + 32 * j, 144), of[i], acc[j]);
            acc[4] = mfma16(lds_trfrag(qw4 + i * 4608, 144), of[i], acc[4]);
        }
    }
    const float gt = __uint_as_float(gm);
    lptr ap = smem + P2_ACC + kt * (64 * P2_AP) + (rt * 16 + lo16) * P2_AP + g * 16;
#pragma unroll
    for (int j = 0; j < 5; ++j) if (j < 4 || g < 2) lds_w128(ap + j * 64, __builtin_bit_cast(u32x4, acc[j]));
    STAMP(5);
    BARRIER();
    STAMP(6);
    if (w < 9) {
        lptr a = smem + P2_ACC + r0 * P2_AP + c0 * 32;
        const f32x4 s0 = __builtin_bit_cast(f32x4, lds_r128(a)) + __builtin_bit_cast(f32x4, lds_r128(a + 64 * P2_AP)) + __builtin_bit_cast(f32x4, lds_r128(a + 128 * P2_AP));
        const f32x4 s1 = __builtin_bit_cast(f32x4, lds_r128(a + 16)) + __builtin_bit_cast(f32x4, lds_r128(a + 64 * P2_AP + 16)) + __builtin_bit_cast(f32x4, lds_r128(a + 128 * P2_AP + 16));
        const u32x4 bb = lds_r128(smem + P2_B + r0 * 160 + c0 * 16);
        const float og = rbf(1.f - gt);
        u32x4 o;
#pragma unroll
        for (int i = 0; i < 4; ++i) {
            const float a0 = i < 2 ? s0[2 * i] : s1[2 * i - 4], a1 = i < 2 ? s0[2 * i + 1] : s1[2 * i - 3];
            o[i] = pk_bf16(gt * a0 + og * bf2f(bb[i]), gt * a1 + og * bfhi(bb[i]));
        }
        *(GAS u32x4*)(P.out + (unsigned)((rt64 * 64 + r0) * 1152 + cg * 144 + c0 * 16)) = o;
    }
    STAMP(7);
    if (w == 4) STAMP_OUT(blockIdx.x < 128 ? 3 : 6, blockIdx.x + 512);
}

__global__ __launch_bounds__(768, 3) void k_fused(Params P) {
    __shared__ __attribute__((aligned(16))) char smem_[LDS_BYTES];
    lptr smem = (lptr)smem_;
    const int b = blockIdx.x, xcd = b & 7, idx = b >> 3;
    const int lane = threadIdx.x & 63, w = __builtin_amdgcn_readfirstlane(threadIdx.x >> 6);
    const rsrc_t wsr = __builtin_amdgcn_make_buffer_rsrc(P.ws, 0, (int)WS_END, 0x00020000);
    const int rt64 = xcd * 4 + (idx & 3), cg2 = idx >> 2;
    if (idx < 16) {
        const int rbl = idx & 3, hgl = idx >> 2, rb = (xcd >> 1) * 4 + rbl, hg = (xcd & 1) * 4 + hgl;
        if (w >= 4) gemm_compute(P, smem, rb, hg, w - 4, wsr);
        else gemm_loader(P, smem, rb, hg, ((rbl + hgl) & 3) * 2, w, cg2, wsr);
    } else {
        if (idx < 28) ssm_role(P, smem, xcd * 12 + idx - 16, (idx - 16) & 7, wsr);
        else {
            u64 epoch = 0;
            if (w == 11) epoch = __hip_atomic_load((GAS u64*)(P.ws + WS_CTL), RLX_AGENT);
            const int cid = (idx - 28) * 8 + xcd;
#pragma unroll
            for (int i = 0; i < 14; ++i)
                st_sc1(wsr, WS_PART + (unsigned)((i * 24576 + cid * 768 + (int)threadIdx.x) * 16), u32x4{EMPTY, EMPTY, EMPTY, EMPTY});
            WAIT_VM(0);
            BARRIER();
            if (w == 11) {
                const u64 t = epoch + 1ull;
                if (lane == 0) __hip_atomic_store((GAS u64*)(P.ws + WS_CFL) + cid, t, RLX_AGENT);
                tag_post(P, smem, epoch, lane);
                if (b == 255) {
                    const GAS u64* f = (const GAS u64*)(P.ws + WS_ARR) + lane;
#pragma unroll 1
                    for (unsigned spins = 0; spins < SPIN_CAP; ++spins) {
                        const bool ok = __hip_atomic_load(f, RLX_AGENT) == t && __hip_atomic_load(f + 64, RLX_AGENT) == t &&
                                        __hip_atomic_load(f + 128, RLX_AGENT) == t && __hip_atomic_load(f + 192, RLX_AGENT) == t;
                        if (__all(ok)) break;
                        __builtin_amdgcn_s_sleep(8);
                    }
                    if (lane == 0) __hip_atomic_store((GAS u64*)(P.ws + WS_CTL), t, RLX_AGENT);
                }
                poll_flags(P, WS_CFL, 32, t, lane);
            }
        }
        if (w < 9) wo_issue<0x1ff>(P, cg2, w, 9, lane);
    }
    phase2(P, smem, rt64, cg2, idx >= 16 || w < 4, wsr);
}

extern "C" void kernel_launch(void* const* d_in, const int* in_sizes, int n_in, void* d_out, int out_size, void* d_ws, size_t ws_size,
                              hipStream_t stream) {
    Params P;
    P.x = (const char*)d_in[0]; P.wqkv = (const char*)d_in[1]; P.wo = (const char*)d_in[2];
    P.logA = (const char*)d_in[4]; P.aim = (const char*)d_in[5]; P.cre = (const char*)d_in[6]; P.cim = (const char*)d_in[7];
    P.logdt = (const char*)d_in[8]; P.dskip = (const char*)d_in[9]; P.gw = (const char*)d_in[10];
    P.out = (char*)d_out; P.ws = (char*)d_ws;
    k_fused<<<256, 768, 0, stream>>>(P);
}
```

```cpp
#include <hip/hip_runtime.h>
#include <stdint.h>

#define GAS __attribute__((address_space(1)))
#define LAS __attribute__((address_space(3)))
#define DEVI __device__ __forceinline__
typedef unsigned short u16;
typedef unsigned long long u64;
typedef __attribute__((ext_vector_type(2))) unsigned u32x2;
typedef __attribute__((ext_vector_type(3))) unsigned u32x3;
typedef __attribute__((ext_vector_type(4))) unsigned u32x4;
typedef __attribute__((ext_vector_type(4))) short s16x4;
typedef __attribute__((ext_vector_type(2))) float f32x2;
typedef __attribute__((ext_vector_type(4))) float f32x4;
typedef __attribute__((ext_vector_type(16))) float f32x16;
typedef __attribute__((ext_vector_type(2))) __bf16 bf16x2;
typedef __attribute__((ext_vector_type(8))) __bf16 bf16x8;
typedef __attribute__((ext_vector_type(2))) _Float16 f16x2;
typedef __attribute__((ext_vector_type(8))) _Float16 f16x8;
typedef LAS char* lptr;

constexpr int SEQ = 2048, DM = 576;

constexpr unsigned WS_CTL = 0;
constexpr unsigned WS_ARR = 256;
constexpr unsigned WS_CFL = 4096;
constexpr unsigned WS_PART = 16384;
constexpr unsigned WS_GATE = WS_PART + 8 * 16 * 3072;
constexpr unsigned WS_O = WS_GATE + 8192;
constexpr unsigned WS_B = WS_O + SEQ * DM * 2;
constexpr unsigned WS_END = WS_B + SEQ * DM * 2;
constexpr unsigned EMPTY = 0xffffffffu;

constexpr size_t WS_STAMP = 16u << 20;
#define STAMP_DECL
#define STAMP(i)
#define CSTAMP(i)
#define STAMP_OUT(role, slot)

struct Params {
    const char* x; const char* wqkv; const char* wo; const char* logA; const char* aim; const char* cre; const char* cim;
    const char* logdt; const char* dskip; const char* gw; char* out; char* ws;
};

constexpr int LDS_BYTES = 163840, LDS_TAG = LDS_BYTES - 16;

DEVI float bf2f(unsigned h) { return __uint_as_float(h << 16); }
DEVI float bfhi(unsigned h) { return __uint_as_float(h & 0xffff0000u); }
DEVI unsigned pk_bf16(float a, float b) { return __builtin_bit_cast(unsigned, __builtin_convertvector(f32x2{a, b}, bf16x2)); }
DEVI unsigned pk_f16(f32x2 v) { return __builtin_bit_cast(unsigned, __builtin_convertvector(v, f16x2)); }
DEVI float rbf(float a) { return bf2f(pk_bf16(a, a)); }
DEVI f32x4 mfma16(u32x4 a, u32x4 b, f32x4 c) {
    return __builtin_amdgcn_mfma_f32_16x16x32_bf16(__builtin_bit_cast(bf16x8, a), __builtin_bit_cast(bf16x8, b), c, 0, 0, 0);
}
DEVI u32x4 lds_r128(lptr p) { return *(LAS u32x4*)p; }
DEVI u32x2 lds_r64(lptr p) { return *(LAS u32x2*)p; }
DEVI float lds_rf(lptr p) { return *(LAS float*)p; }
DEVI void lds_w128(lptr p, u32x4 v) { *(LAS u32x4*)p = v; }
DEVI void lds_w64(lptr p, u32x2 v) { *(LAS u32x2*)p = v; }
DEVI void lds_w32(lptr p, unsigned v) { *(LAS unsigned*)p = v; }
DEVI void lds_w16(lptr p, unsigned v) { *(LAS u16*)p = (u16)v; }
DEVI u32x2 lds_tr(lptr p) { return __builtin_bit_cast(u32x2, __builtin_amdgcn_ds_read_tr16_b64_v4i16((LAS s16x4*)p)); }
DEVI u32x4 lds_trfrag(lptr p, int off2) { u32x2 a = lds_tr(p), b = lds_tr(p + off2); return u32x4{a.x, a.y, b.x, b.y}; }
DEVI void glds16(const char* sbase, unsigned voff, unsigned l) {
    asm volatile("s_mov_b32 m0, %2\n\ts_nop 0\n\tglobal_load_lds_dwordx4 %0, %1" :: "v"(voff), "s"(sbase), "s"(l) : "memory");
}
#define SET_M0(v) asm volatile("s_mov_b32 m0, %0\n\ts_nop 0" :: "s"(v) : "memory")
#define W_ADDTID(v, off) asm volatile("ds_write_addtid_b32 %0 offset:%1" :: "v"(v), "i"(off) : "memory")
#define CFENCE() asm volatile("" ::: "memory")
#define SCHED() __builtin_amdgcn_sched_barrier(0)
#define WAIT_VM(n) asm volatile("s_waitcnt vmcnt(" #n ")" ::: "memory")
#define WAIT_LGKM0() asm volatile("s_waitcnt lgkmcnt(0)" ::: "memory")
#define BARRIER() do { SCHED(); asm volatile("s_waitcnt lgkmcnt(0)\n\ts_barrier" ::: "memory"); SCHED(); } while (0)
#define RLX_AGENT __ATOMIC_RELAXED, __HIP_MEMORY_SCOPE_AGENT
constexpr unsigned SPIN_CAP = 1u << 16;
typedef __amdgpu_buffer_rsrc_t rsrc_t;
DEVI u32x4 ld_sc1(rsrc_t r, unsigned off) { return __builtin_amdgcn_raw_buffer_load_b128(r, off, 0, 16); }
DEVI void st_sc1(rsrc_t r, unsigned off, u32x4 v) { __builtin_amdgcn_raw_buffer_store_b128(v, r, off, 0, 16); }

DEVI u32x4 pack8(f32x4 a, f32x4 b) { return u32x4{pk_bf16(a[0], a[1]), pk_bf16(a[2], a[3]), pk_bf16(b[0], b[1]), pk_bf16(b[2], b[3])}; }
DEVI float phi(float t) {
    const float e = __builtin_amdgcn_fmed3f(__builtin_amdgcn_exp2f(t * 1.44269504089f), 0.f, 1.f);
    return __int_as_float(max(__float_as_int(t), 0x358637bd)) + e;
}
DEVI f32x4 phi4(f32x4 v) { return f32x4{phi(v[0]), phi(v[1]), phi(v[2]), phi(v[3])}; }
DEVI unsigned umax4(u32x4 a) { return max(max(a.x, a.y), max(a.z, a.w)); }
DEVI u32x4 not_empty(u32x4 a) { return u32x4{min(a.x, EMPTY - 1), min(a.y, EMPTY - 1), min(a.z, EMPTY - 1), min(a.w, EMPTY - 1)}; }
DEVI void tag_post(const Params& P, lptr smem, u64 epoch, int lane) {
    const u64 tag = epoch + 1ull;
    if (lane == 0) { lds_w64(smem + LDS_TAG, u32x2{(unsigned)tag, (unsigned)(tag >> 32)}); __hip_atomic_store((GAS u64*)(P.ws + WS_ARR) + blockIdx.x, tag, RLX_AGENT); }
}
DEVI u64 tag_get(lptr smem) { const u32x2 t = lds_r64(smem + LDS_TAG); return (u64)t.x | ((u64)t.y << 32); }
DEVI void poll_flags(const Params& P, unsigned off, int n, u64 tag, int lane) {
    const GAS u64* f = (const GAS u64*)(P.ws + off) + (lane < n ? lane : 0);
#pragma unroll 1
    for (unsigned spins = 0; spins < SPIN_CAP; ++spins) {
        if (__all(__hip_atomic_load(f, RLX_AGENT) == tag)) break;
        __builtin_amdgcn_s_sleep(1);
    }
}
template <unsigned CH>
DEVI void wo_issue(const Params& P, int cg, int first, int stride, int lane) {
#pragma unroll 1
    for (int m = first; m < 9; m += stride) {
        const int q = 64 * m + lane, rho = q / 9, pc = q - rho * 9;
        const int k = (rho & 32) + 8 * ((rho >> 3) & 3) + 4 * (rho & 1) + ((rho >> 1) & 3);
        const unsigned qo = (unsigned)(k * 1152 + cg * 144 + pc * 16);
#pragma unroll
        for (int c = 0; c < 9; ++c) if ((CH >> c) & 1) glds16(P.wo + c * (64 * 1152), qo, (unsigned)(c * 9216 + m * 1024));
    }
}

constexpr int SP = 3088;
constexpr int XT_OFF = 101376, XT_BYTES = 4608, XPAD = 256, XCHG_OFF = 129024;
constexpr int TB_BYTES = 8704, TC = 1216, PA_OFF = 4864, PC_OFF = 7424, ACX_OFF = 52224;

DEVI f32x2 cmul(f32x2 a, f32x2 b, f32x2 bs) {
    f32x2 t = f32x2{a.x, a.x} * b;
    return __builtin_elementwise_fma(f32x2{a.y, a.y}, bs, t);
}
DEVI f32x2 swp(f32x2 b) { return f32x2{-b.y, b.x}; }
DEVI f32x2 cexp_p(float a2, float f, float p) {
    float m = __builtin_amdgcn_exp2f(a2 * p);
    float r = __builtin_amdgcn_fractf(f * p);
    return f32x2{m * __builtin_amdgcn_cosf(r), m * __builtin_amdgcn_sinf(r)};
}
DEVI u32x4 mask_frag(unsigned bits, int g, unsigned val) {
    unsigned by = bits >> (8 * g);
    u32x4 r;
#pragma unroll
    for (int i = 0; i < 4; ++i) { unsigned t = (by >> (2 * i)) & 3u; r[i] = ((t | (t << 15)) & 0x10001u) * val; }
    return r;
}
DEVI float wave_max(float v) {
    v = fmaxf(v, __int_as_float(__builtin_amdgcn_update_dpp(0, __float_as_int(v), 0xB1, 0xf, 0xf, true)));
    v = fmaxf(v, __int_as_float(__builtin_amdgcn_update_dpp(0, __float_as_int(v), 0x4E, 0xf, 0xf, true)));
    v = fmaxf(v, __int_as_float(__builtin_amdgcn_update_dpp(0, __float_as_int(v), 0x124, 0xf, 0xf, true)));
    v = fmaxf(v, __int_as_float(__builtin_amdgcn_update_dpp(0, __float_as_int(v), 0x128, 0xf, 0xf, true)));
    const int i = __float_as_int(v);
    const int a = max(__builtin_amdgcn_readlane(i, 0), __builtin_amdgcn_readlane(i, 16)), b = max(__builtin_amdgcn_readlane(i, 32), __builtin_amdgcn_readlane(i, 48));
    return __int_as_float(max(a, b));
}

DEVI void ssm_role(const Params& P, lptr smem, int cg6, int rot, rsrc_t wsr) {
    const int tid = threadIdx.x, lane = tid & 63, w = __builtin_amdgcn_readfirstlane(tid >> 6);
    const int ch = w >> 1, half = (w ^ (w >> 2)) & 1, d = cg6 * 6 + ch;
    const int lo16 = lane & 15, g = lane >> 4;
    STAMP_DECL; STAMP(1);
    const unsigned po = (unsigned)(d * 256 + lane * 4);
    const unsigned r_la = *(const GAS unsigned*)(P.logA + po), r_ai = *(const GAS unsigned*)(P.aim + po);
    const unsigned r_cr = *(const GAS unsigned*)(P.cre + po), r_ci = *(const GAS unsigned*)(P.cim + po);
    const unsigned r_dt = *(const GAS u16*)(P.logdt + d * 2), r_ds = *(const GAS u16*)(P.dskip + d * 2);
    u64 epoch = 0;
    if (w == 11) epoch = __hip_atomic_load((GAS u64*)(P.ws + WS_CTL), RLX_AGENT);
    asm volatile("s_barrier" ::: "memory");
    const int tt = (tid + rot * 32) & 255;
    u32x3 xr[8];
    if (w >= 8) {
#pragma unroll
        for (int i = 0; i < 8; ++i) xr[i] = *(const GAS u32x3*)(P.x + (unsigned)(tt * 9216 + cg6 * 12 + i * 1152));
    }
    SCHED();
    u32x4 F21, F22, F23, F31, F32, FX;
    if (half) {
        F21 = mask_frag(7u << (lo16 + 11), g, 0x3f80u); F22 = mask_frag(31u << (lo16 + 6), g, 0x3f80u);
        F23 = mask_frag(7u << (lo16 + 3), g, 0x3f80u); F31 = mask_frag(127u << (lo16 + 5), g, 0x3f80u);
        F32 = mask_frag(511u << (lo16 + 4), g, 0x3f80u); FX = mask_frag(3u << (lo16 + 12), g, 0xffffu);
        asm volatile("" : "+v"(F21), "+v"(F22), "+v"(F23), "+v"(F31), "+v"(F32), "+v"(FX));
    }
    lptr xt = smem + XT_OFF + ch * XT_BYTES, tb = smem + ch * TB_BYTES;
    const int j32 = lane & 31, h = lane >> 5;
    int arow;
    {
        const u64 map0 = 0x0a09080803020100ull, map1 = 0x0c0b0a0a07060504ull, map2 = 0x1413110e12100f0dull;
        const u64 mm = j32 < 8 ? map0 : (j32 < 16 ? map1 : map2);
        arow = (int)((mm >> (8 * (j32 & 7))) & 0xff);
    }
    const float dt = rbf(__expf(bf2f(r_dt)));
    const float cm = wave_max(fmaxf(fmaxf(fabsf(bf2f(r_cr)), fabsf(bfhi(r_cr))), fmaxf(fabsf(bf2f(r_ci)), fabsf(bfhi(r_ci)))));
    unsigned ce = __float_as_uint(cm) >> 23; ce = ce < 16u ? 16u : (ce > 240u ? 240u : ce);
    const float cs = __uint_as_float((263u - ce) << 23), ics = __uint_as_float((ce - 9u) << 23);
    f32x16 kacc;
#pragma unroll
    for (int i = 0; i < 16; ++i) kacc[i] = 0.f;
    STAMP(2);
    {
        const unsigned sh = half * 16;
        const float la = bf2f(r_la >> sh), ai = bf2f(r_ai >> sh), cr = bf2f(r_cr >> sh) * cs, ci = bf2f(r_ci >> sh) * cs;
        const float a2 = (-rbf(__expf(la)) * dt) * 1.44269504089f, f = (ai * dt) * 0.15915494309f;
#define PUT(row, v) W_ADDTID(pk_f16(v), ((row) & 15) * SP)
        const f32x2 w1 = cexp_p(a2, -f, 1.f), w256 = cexp_p(a2, -f, 256.f);
        f32x2 u[8], v[4];
        u[1] = cexp_p(a2, f, 8.f); v[1] = cexp_p(a2, f, 64.f);
        const f32x2 w1s = swp(w1), w2s = swp(w256);
        f32x2 c[9];
        c[0] = f32x2{cr, -ci};
#pragma unroll
        for (int b = 1; b < 9; ++b) c[b] = cmul(c[b - 1], w1, w1s);
        SET_M0(w * 256);
#pragma unroll
        for (int b = 0; b < 8; ++b) PUT(b, c[b]);
        PUT(8, cmul(c[0], w256, w2s)); PUT(9, cmul(c[2], w256, w2s)); PUT(11, cmul(c[6], w256, w2s));
        f32x2 e4 = cmul(c[4], w256, w2s), e8 = cmul(c[8], w256, w2s);
        PUT(10, e4); PUT(12, e8);
        e4 = cmul(e4, w256, w2s); e8 = cmul(e8, w256, w2s); PUT(13, e4); PUT(14, e8);
        e4 = cmul(e4, w256, w2s); e8 = cmul(e8, w256, w2s); PUT(15, e4);
        SET_M0(w * 256 + 16 * SP);
        PUT(17, e8);
        e8 = cmul(e8, w256, w2s); PUT(16, e8);
        e8 = cmul(e8, w256, w2s); PUT(18, e8);
        e8 = cmul(e8, w256, w2s); PUT(19, e8);
        e8 = cmul(e8, w256, w2s); PUT(20, e8);
        u32x4 af[8];
        lptr ap = smem + arow * SP + w * 256 + h * 16, bp = smem + j32 * SP + w * 256 + h * 16;
#pragma unroll
        for (int s = 0; s < 8; ++s) af[s] = lds_r128(ap + 32 * s);
        { f32x2 s1 = swp(u[1]); u[2] = cmul(u[1], u[1], s1); u[3] = cmul(u[2], u[1], s1); u[4] = cmul(u[3], u[1], s1);
          f32x2 s4 = swp(u[4]); u[5] = cmul(u[1], u[4], s4); u[6] = cmul(u[2], u[4], s4); u[7] = cmul(u[3], u[4], s4); }
        f32x2 vs = swp(v[1]);
        v[2] = cmul(v[1], v[1], vs); v[3] = cmul(v[2], v[1], vs);
#pragma unroll
        for (int a = 3; a >= 1; --a) {
            if (a == 1) SET_M0(w * 256);
            vs = swp(v[a]);
            PUT(8 * a, v[a]);
#pragma unroll
            for (int b = 1; b < 8; ++b) PUT(8 * a + b, cmul(u[b], v[a], vs));
        }
        PUT(0, (f32x2{1.f, 0.f}));
#pragma unroll
        for (int b = 1; b < 8; ++b) PUT(b, u[b]);
#undef PUT
        u32x4 bfr[8];
#pragma unroll
        for (int s = 0; s < 8; ++s) bfr[s] = lds_r128(bp + 32 * s);
        SCHED();
#pragma unroll
        for (int s = 0; s < 8; ++s)
            kacc = __builtin_amdgcn_mfma_f32_32x32x16_f16(__builtin_bit_cast(f16x8, af[s]), __builtin_bit_cast(f16x8, bfr[s]), kacc, 0, 0, 0);
    }
    STAMP(3);
    if (w >= 8) {
        lptr xb = smem + XT_OFF + (XPAD + tt * 8) * 2;
#pragma unroll
        for (int c = 0; c < 3; ++c) {
            u32x4 lo, hi;
#pragma unroll
            for (int i = 0; i < 4; ++i) { lo[i] = __builtin_amdgcn_perm(xr[2 * i + 1][c], xr[2 * i][c], 0x05040100u); hi[i] = __builtin_amdgcn_perm(xr[2 * i + 1][c], xr[2 * i][c], 0x07060302u); }
            lds_w128(xb + (2 * c) * XT_BYTES, lo); lds_w128(xb + (2 * c + 1) * XT_BYTES, hi);
        }
    }
    if (w == 11) tag_post(P, smem, epoch, lane);
    lptr xc = smem + XCHG_OFF + ch * 3072 + lane * 16;
    if (half) {
        lds_w128(xc, __builtin_bit_cast(u32x4, f32x4{kacc[0], kacc[1], kacc[2], kacc[3]}));
        lds_w128(xc + 1024, __builtin_bit_cast(u32x4, f32x4{kacc[4], kacc[5], kacc[6], kacc[7]}));
    } else {
        lds_w128(xc + 2048, __builtin_bit_cast(u32x4, f32x4{kacc[8], kacc[9], kacc[10], kacc[11]}));
        lds_w64(xt + lane * 8, u32x2{0u, 0u});
    }
    STAMP(4);
    BARRIER();
    STAMP(5);
    const float ks = 2.f * dt * ics;
    const int cpy = (3 - lo16) & 3;
    lptr xf = xt + (XPAD - 16 + 16 * lo16 + 8 * g) * 2;
    lptr tf = tb + cpy * TC + 2 * (527 - lo16 + 8 * g - cpy) - 64 * 16;
    lptr xf7 = xf - 64 * 7;
    asm volatile("" : "+v"(xf7), "+v"(tf));
    auto TF = [&](int off) { u32x2 a = lds_r64(tf + off), b = lds_r64(tf + off + 8); return u32x4{a.x, a.y, b.x, b.y}; };
    f32x4 acc[8];
#pragma unroll
    for (int i = 0; i < 8; ++i) acc[i] = f32x4{0.f, 0.f, 0.f, 0.f};
    lptr axw, axr;
    if (!half) {
        const f32x4 p0 = __builtin_bit_cast(f32x4, lds_r128(xc)), p1 = __builtin_bit_cast(f32x4, lds_r128(xc + 1024));
        unsigned dd[6];
        dd[0] = pk_bf16((kacc[7] + p1[3]) * ks, (kacc[6] + p1[2]) * ks); dd[1] = pk_bf16((kacc[5] + p1[1]) * ks, (kacc[4] + p1[0]) * ks);
        dd[2] = pk_bf16((kacc[3] + p0[3]) * ks, (kacc[2] + p0[2]) * ks); dd[3] = pk_bf16((kacc[1] + p0[1]) * ks, (kacc[0] + p0[0]) * ks + (lane == 0 ? bf2f(r_ds) : 0.f));
        dd[4] = __builtin_amdgcn_update_dpp(0, dd[0], 0x138, 0xf, 0xf, true);
        dd[5] = __builtin_amdgcn_update_dpp(0, dd[1], 0x138, 0xf, 0xf, true);
        unsigned mm[5];
#pragma unroll
        for (int i = 0; i < 5; ++i) mm[i] = __builtin_amdgcn_alignbit(dd[i + 1], dd[i], 16);
        lptr tw = tb + 2 * (536 - 8 * lane);
        lds_w128(tw, u32x4{dd[0], dd[1], dd[2], dd[3]}); lds_w128(tw + TC, u32x4{mm[0], mm[1], mm[2], mm[3]});
        lds_w128(tw + 2 * TC, u32x4{dd[1], dd[2], dd[3], dd[4]}); lds_w128(tw + 3 * TC, u32x4{mm[1], mm[2], mm[3], mm[4]});
        {
            const unsigned n0 = __builtin_amdgcn_readlane(dd[0], 63), n1 = __builtin_amdgcn_readlane(dd[1], 63);
            const unsigned kk = (n0 & 0xffffu) * 0x10001u, kh = n0 << 16, m3 = (n0 << 16) | (n0 & 0xffffu), m4 = (n0 >> 16) | (n1 << 16);
            const int c = lane & 3, bl = lane >> 2;
            u32x4 e = c == 0 ? u32x4{0u, 0u, kh, kk} : (c == 1 ? u32x4{0u, 0u, kk, m3} : (c == 2 ? u32x4{0u, kh, kk, n0} : u32x4{0u, kk, m3, m4}));
            if (bl != 3) e = u32x4{0u, 0u, 0u, 0u};
            if (bl < 6) lds_w128(tb + c * TC + (bl < 4 ? bl * 16 : 1024 + bl * 16), e);
        }
        BARRIER();
        STAMP(6);
        u32x4 X[8];
#pragma unroll
        for (int i = 0; i < 8; ++i) X[i] = lds_r128(xf7 + 64 * 6 + 512 * i);
        u32x4 k0 = TF(1024 - 64), k8 = TF(512 - 64);
#pragma unroll
        for (int c = 1; c < 8; ++c) {
            u32x4 k0n, k8n;
            if (c < 7) { k0n = TF(1024 - 64 * (c + 1)); k8n = TF(512 - 64 * (c + 1)); }
#pragma unroll
            for (int i = 0; i < 8; ++i) {
                SCHED();
                acc[i] = mfma16(k0, X[i], acc[i]);
                if (i < 7) acc[i + 1] = mfma16(k8, X[i], acc[i + 1]);
                SCHED();
                if (c < 7) X[i] = lds_r128(xf7 + 64 * (6 - c) + 512 * i);
            }
            k0 = k0n; k8 = k8n;
        }
        SCHED();
        axw = smem + ACX_OFF + ch * 8192 + lane * 16; axr = axw + 4096;
#pragma unroll
        for (int i = 0; i < 4; ++i) lds_w128(axw + i * 1024, __builtin_bit_cast(u32x4, acc[4 + i]));
    } else {
#pragma unroll
        for (int i = 0; i < 4; ++i) if (i < 3 || lane < 48) lds_w128(tb + PA_OFF + i * 1024 + lane * 16, u32x4{0u, 0u, 0u, 0u});
        const f32x4 p2 = __builtin_bit_cast(f32x4, lds_r128(xc + 2048));
        const unsigned p89 = pk_bf16((kacc[8] + p2[0]) * ks, (kacc[9] + p2[1]) * ks), pab = pk_bf16((kacc[10] + p2[2]) * ks, (kacc[11] + p2[3]) * ks);
        CFENCE();
        const int u2 = 79 - (32 + (j32 >> 1)), u3 = 127 - (64 + 32 * h + (j32 >> 1));
#pragma unroll
        for (int c = 0; c < 4; ++c) {
            const int v2 = u2 - c + 4, v3 = u3 - c + 4;
            lptr q2 = tb + PA_OFF + (j32 & 1) * 1280 + c * 320 + (v2 >> 2) * 16 + h * 8 + (v2 & 3) * 2 - 64;
            lptr q3 = tb + PC_OFF + c * 320 + (v3 >> 2) * 16 + (j32 & 1) * 8 + (v3 & 3) * 2 - 64;
            lds_w16(q2 + 64, p89); lds_w16(q2, p89 >> 16);
            lds_w16(q3 + 64, pab); lds_w16(q3, pab >> 16);
        }
        FX &= ((unsigned)__builtin_amdgcn_readlane(p89, 63) >> 16) * 0x10001u;
        u32x4 X[8];
#pragma unroll
        for (int i = 0; i < 8; ++i) X[i] = lds_r128(xf + 512 * i);
        lptr pf2 = tb + PA_OFF + cpy * 320 + ((79 - 64 - lo16 + 4 * g - cpy + 4) >> 2) * 16;
        lptr pf3 = tb + PC_OFF + cpy * 320 + ((127 - 112 - lo16 + 4 * g - cpy + 4) >> 2) * 16;
        u32x4 TA[3], TB[3], TCc[4];
#pragma unroll
        for (int i = 0; i < 3; ++i) { TA[i] = lds_r128(pf2 + 64 * (2 - i)); TB[i] = lds_r128(pf2 + 1280 + 64 * (2 - i)); }
#pragma unroll
        for (int i = 0; i < 4; ++i) TCc[i] = lds_r128(pf3 + 64 * (3 - i));
        BARRIER();
        STAMP(6);
        const f32x4 z4 = {0.f, 0.f, 0.f, 0.f};
#pragma unroll
        for (int st = 0; st < 6; ++st) {
            const u32x4 Xc = lds_r128(xf + 512 * st - 16);
            const u32x4 pa = pack8(mfma16(X[st], F21, z4), mfma16(X[st], F22, z4));
            const u32x4 pb = pack8(mfma16(X[st], F23, z4), mfma16(Xc, F22, z4));
#pragma unroll
            for (int dl = 2; dl <= 4; ++dl) if (st + dl < 8) {
                acc[st + dl] = mfma16(pa, TA[dl - 2], acc[st + dl]);
                acc[st + dl] = mfma16(pb, TB[dl - 2], acc[st + dl]);
            }
            if (st < 4) {
                const u32x4 pc = pack8(mfma16(X[st], F31, z4), mfma16(Xc, F32, z4));
#pragma unroll
                for (int dl = 4; dl <= 7; ++dl) if (st + dl < 8) acc[st + dl] = mfma16(pc, TCc[dl - 4], acc[st + dl]);
            }
        }
        const u32x4 k0 = TF(1024), k8 = TF(512), k16 = TF(0);
#pragma unroll
        for (int i = 4; i < 8; ++i) acc[i] = mfma16(FX, X[i - 4], acc[i]);
#pragma unroll
        for (int i = 0; i < 8; ++i) acc[i] = mfma16(k0, X[i], acc[i]);
#pragma unroll
        for (int i = 1; i < 8; ++i) acc[i] = mfma16(k8, X[i - 1], acc[i]);
#pragma unroll
        for (int i = 2; i < 8; ++i) acc[i] = mfma16(k16, X[i - 2], acc[i]);
        axr = smem + ACX_OFF + ch * 8192 + lane * 16; axw = axr + 4096;
#pragma unroll
        for (int i = 0; i < 4; ++i) { lds_w128(axw + i * 1024, __builtin_bit_cast(u32x4, acc[i])); acc[i] = acc[4 + i]; }
    }
    STAMP(7);
    if (w == 11) poll_flags(P, WS_CFL, 32, epoch + 1ull, lane);
    BARRIER();
    STAMP(8);
#pragma unroll
    for (int i = 0; i < 2; ++i) {
        const f32x4 ya = acc[2 * i] + __builtin_bit_cast(f32x4, lds_r128(axr + (2 * i) * 1024));
        const f32x4 yb = acc[2 * i + 1] + __builtin_bit_cast(f32x4, lds_r128(axr + (2 * i + 1) * 1024));
        const auto sx = __builtin_amdgcn_permlane16_swap(pk_bf16(ya[0], ya[1]), pk_bf16(yb[0], yb[1]), false, false);
        const auto sy = __builtin_amdgcn_permlane16_swap(pk_bf16(ya[2], ya[3]), pk_bf16(yb[2], yb[3]), false, false);
        st_sc1(wsr, WS_B + (unsigned)(d * 4096 + half * 2048 + i * 1024 + lane * 16), not_empty(u32x4{sx[0], sy[0], sx[1], sy[1]}));
    }
    BARRIER();
    STAMP(9);
    if (w < 2) STAMP_OUT(2 + 2 * w, blockIdx.x + 256 * w);
}

constexpr int SLOT = 34816, XCH = 16384, GW_OFF = 4 * SLOT, KVT_OFF = GW_OFF + 1280, KVP_OFF = 27648;
DEVI constexpr int slot_off(int s) { return (s & 3) == 0 ? SLOT : ((s & 3) == 1 ? 0 : (s & 3) * SLOT); }
DEVI constexpr int wq_off(int t) { return t == 0 ? 0 : (t + 1) * SLOT + 7168; }

DEVI void gemm_compute(const Params& P, lptr smem, int rb, int hg, int w, rsrc_t wsr) {
    const int lane = threadIdx.x & 63, lo16 = lane & 15, g = lane >> 4;
    const int rg = w >> 1, p = w & 1;
    STAMP_DECL; STAMP(1);
    const int row0 = 16 * (2 * rg + p) + lo16, row1 = 16 * (2 * rg + 1 - p) + lo16;
    const int sw = (g ^ ((lo16 >> 1) & 7)) << 4;
    lptr xa0[2] = {smem + row0 * 128 + sw, smem + row0 * 128 + (sw ^ 64)}, xa1[2] = {smem + row1 * 128 + sw, smem + row1 * 128 + (sw ^ 64)};
    const int wrow = (4 * g + (lo16 >> 2)) * 288;
    lptr wa = smem + XCH + wrow + (9 * p + (lane & 3)) * 8, wa2 = smem + XCH + wrow + (9 * p + ((lane & 3) == 3 ? 18 : 8)) * 8;
    const f32x4 z4 = {0.f, 0.f, 0.f, 0.f};
    f32x4 acc[2][5];
#pragma unroll
    for (int i = 0; i < 2; ++i)
#pragma unroll
        for (int j = 0; j < 5; ++j) { const float e = j < 2 ? 1e-6f : (j == 2 && lo16 < 12 ? 1e-6f : 0.f); acc[i][j] = f32x4{e, e, e, e}; }
    u32x4 xk[18], x1[2], wf[5];
    auto rel = [&](int s) { return (s & 2) ? slot_off(s) - 2 * SLOT : slot_off(s); };
    auto rebase = [&](int dlt) {
        xa0[0] += dlt; xa0[1] += dlt; xa1[0] += dlt; xa1[1] += dlt; wa += dlt; wa2 += dlt;
        asm volatile("" : "+v"(xa0[0]), "+v"(xa0[1]), "+v"(xa1[0]), "+v"(xa1[1]), "+v"(wa), "+v"(wa2));
    };
    auto rdw = [&](int n, int j) {
        const int o = rel(n >> 1) + (n & 1) * 9216;
        return j == 2 ? lds_trfrag(wa2 + o, 4608) : lds_trfrag(wa + o + (j == 0 ? 0 : (j == 1 ? 32 : (j == 3 ? 152 : 184))), 4608);
    };
    BARRIER();
    STAMP(2);
    xk[0] = lds_r128(xa0[0] + rel(0)); x1[0] = lds_r128(xa1[0] + rel(0));
#pragma unroll
    for (int j = 0; j < 5; ++j) wf[j] = rdw(0, j);
#pragma unroll
    for (int n = 0; n < 18; ++n) {
        const int nx = n + 1;
        SCHED();
        if (!(n & 1)) { xk[nx] = lds_r128(xa0[1] + rel(n >> 1)); x1[1] = lds_r128(xa1[1] + rel(n >> 1)); }
        if ((n & 3) == 3) rebase((n & 4) ? -2 * SLOT : 2 * SLOT);
#pragma unroll
        for (int j = 0; j < 5; ++j) {
            SCHED();
            acc[0][j] = mfma16(xk[n], wf[j], acc[0][j]); acc[1][j] = mfma16(x1[n & 1], wf[j], acc[1][j]);
            SCHED();
            if (n & 1) {
                if (j == 1) {
                    BARRIER();
                    if (n == 1) STAMP(3);
                    if (n == 9) STAMP(4);
                    if (nx < 18) { xk[nx] = lds_r128(xa0[0] + rel(nx >> 1)); x1[0] = lds_r128(xa1[0] + rel(nx >> 1)); wf[0] = rdw(nx, 0); wf[1] = rdw(nx, 1); }
                } else if (j >= 2 && nx < 18) wf[j] = rdw(nx, j);
            } else wf[j] = rdw(nx, j);
        }
    }
    SCHED();
    STAMP(5);
    const int qrow = (2 * (4 * g + (lo16 >> 2))) * 144;
    lptr qw = smem + qrow + (lane & 3) * 8, qw4 = smem + qrow + (16 + (lane & 1)) * 8, gwp = smem + GW_OFF + g * 16;
    lptr qwh = qw + wq_off(1), qw4h = qw4 + wq_off(1);
    asm volatile("" : "+v"(qwh), "+v"(qw4h), "+v"(gwp));
    auto rdq = [&](int ks, int j) {
        const int o = (ks < 6 ? 0 : wq_off(ks / 6) - wq_off(1)) + (ks % 6) * 4608;
        return j < 4 ? lds_trfrag((ks < 6 ? qw : qwh) + o + 32 * j, 144) : lds_trfrag((ks < 6 ? qw4 : qw4h) + o, 144);
    };
    {
        u32x4 pk[3], vb[3];
#pragma unroll
        for (int j = 0; j < 3; ++j) pk[j] = pack8(phi4(acc[0][j]), phi4(acc[1][j]));
        vb[0] = pack8(acc[0][2], acc[1][2]);
        if (lo16 < 12) vb[0] = u32x4{0x3f803f80u, 0x3f803f80u, 0x3f803f80u, 0x3f803f80u};
        vb[1] = pack8(acc[0][3], acc[1][3]); vb[2] = pack8(acc[0][4], acc[1][4]);
        f32x4 r[6];
        const f32x4 t0 = mfma16(pk[2], vb[0], z4), t1 = mfma16(pk[2], vb[1], z4), t2 = mfma16(pk[2], vb[2], z4);
        r[0] = mfma16(pk[0], vb[0], z4); r[1] = mfma16(pk[1], vb[0], z4); r[2] = mfma16(pk[0], vb[1], z4);
        r[3] = mfma16(pk[1], vb[1], z4); r[4] = mfma16(pk[1], vb[2], z4);
        r[5] = g == 0 ? t0 : (g == 1 ? t1 : t2);
        SCHED();
        asm volatile("s_nop 7" ::: "memory");
        SET_M0(KVP_OFF - 8192 + w * 6144);
#pragma unroll
        for (int t = 5; t >= 0; --t) {
            W_ADDTID(r[t][0], 8192 + t * 1024); W_ADDTID(r[t][1], 8192 + t * 1024 + 256);
            W_ADDTID(r[t][2], 8192 + t * 1024 + 512); W_ADDTID(r[t][3], 8192 + t * 1024 + 768);
        }
    }
    BARRIER();
    STAMP(6);
    f32x4 qa[5];
#pragma unroll
    for (int j = 0; j < 5; ++j) { const float e = (j < 4 || g < 2) ? 1e-6f : 0.f; qa[j] = f32x4{e, e, e, e}; }
    u32x4 wq[2][5], gv[2];
#pragma unroll
    for (int i = 0; i < 2; ++i) {
#pragma unroll
        for (int j = 0; j < 5; ++j) wq[i][j] = rdq(i, j);
        gv[i] = lds_r128(gwp + i * 64);
    }
#pragma unroll
    for (int ks = 0; ks < 18; ++ks) {
#pragma unroll
        for (int j = 0; j < 5; ++j) {
            SCHED();
            u32x4 a = wq[ks & 1][j];
            if (j == 4 && lo16 >= 8) a = gv[ks & 1];
            qa[j] = mfma16(a, xk[ks], qa[j]);
            SCHED();
            if (ks + 2 < 18) { wq[ks & 1][j] = rdq(ks + 2, j); if (j == 4) gv[ks & 1] = lds_r128(gwp + (ks + 2) * 64); }
        }
    }
    SCHED();
    const float gs = rbf(__shfl(qa[4][0], lo16 + 32));
    const float gate = rbf(__builtin_amdgcn_rcpf(1.f + __expf(-gs)));
    if (hg == 0 && g == 0) __hip_atomic_store((GAS float*)(P.ws + WS_GATE) + (rb * 128 + row0), gate, RLX_AGENT);
    f32x4 q4 = phi4(qa[4]);
    if (g >= 2) q4 = z4;
    u32x4 b0 = pack8(phi4(qa[0]), phi4(qa[1])), b1 = pack8(phi4(qa[2]), phi4(qa[3])), b2 = pack8(q4, z4);
    lptr kfp = smem + KVT_OFF + lane * 16;
    unsigned ob0 = WS_O + (unsigned)(((rb * 8 + 2 * rg + p) * 72 + hg * 9 + 2 * (g & 1) + (g >> 1)) * 256 + lo16 * 16);
    asm volatile("" : "+v"(b0), "+v"(b1), "+v"(b2), "+v"(kfp), "+v"(ob0));
    STAMP(7);
    BARRIER();
    STAMP(8);
    {
        u32x4 kf[12];
#pragma unroll
        for (int i = 0; i < 12; ++i) kf[i] = lds_r128(kfp + (i < 3 ? 9 + i : i - 3) * 1024);
        SCHED();
        const f32x4 den = mfma16(kf[2], b2, mfma16(kf[1], b1, mfma16(kf[0], b0, f32x4{1e-6f, 1e-6f, 1e-6f, 1e-6f})));
        f32x4 o[5];
        o[0] = mfma16(kf[3], b0, z4);
        o[1] = mfma16(kf[5], b1, mfma16(kf[4], b0, z4));
        o[2] = mfma16(kf[7], b1, mfma16(kf[6], b0, z4));
        o[3] = mfma16(kf[9], b2, mfma16(kf[8], b1, z4));
        o[4] = mfma16(kf[11], b2, mfma16(kf[10], b1, z4));
        f32x4 r;
#pragma unroll
        for (int i = 0; i < 4; ++i) r[i] = __builtin_amdgcn_rcpf(den[i]);
        const float r1a = g == 0 ? r[0] : r[1], r2 = g == 0 ? r[1] : r[2], r3a = g <= 1 ? r[2] : r[3], r3b = g == 0 ? r[2] : r[3];
        u32x2 ob[5];
        ob[0] = u32x2{pk_bf16(o[0][0] * r[0], o[0][1] * r[0]), pk_bf16(o[0][2] * r[0], o[0][3] * r[0])};
        ob[1] = u32x2{pk_bf16(o[1][0] * r1a, o[1][1] * r1a), pk_bf16(o[1][2] * r[1], o[1][3] * r[1])};
        ob[2] = u32x2{pk_bf16(o[2][0] * r2, o[2][1] * r2), pk_bf16(o[2][2] * r2, o[2][3] * r2)};
        ob[3] = u32x2{pk_bf16(o[3][0] * r3a, o[3][1] * r3a), pk_bf16(o[3][2] * r3b, o[3][3] * r3b)};
        ob[4] = u32x2{pk_bf16(o[4][0] * r[3], o[4][1] * r[3]), pk_bf16(o[4][2] * r[3], o[4][3] * r[3])};
#pragma unroll
        for (int t = 0; t < 3; ++t) {
            const u32x2 a = ob[2 * t], b = ob[t < 2 ? 2 * t + 1 : 4];
            const auto sx = __builtin_amdgcn_permlane16_swap(a.x, b.x, false, false), sy = __builtin_amdgcn_permlane16_swap(a.y, b.y, false, false);
            if (t < 2 || g == 0) st_sc1(wsr, ob0 + 1024 * t, not_empty(u32x4{sx[0], sy[0], sx[1], sy[1]}));
        }
    }
    STAMP(9);
    if (w == 0) STAMP_OUT(1, blockIdx.x);
}

DEVI void gemm_loader(const Params& P, lptr smem, int rb, int hg, int rot, int lw, int cg2, rsrc_t wsr) {
    const int lane = threadIdx.x & 63;
    STAMP_DECL; STAMP(1);
    __builtin_amdgcn_s_setprio(3);
    unsigned xo[2], wo_[5], qo[3];
    xo[0] = (unsigned)((rb * 128 + 32 * lw + (lane >> 3)) * 1152 + (((lane & 7) ^ (lane >> 4)) << 4));
    xo[1] = (xo[0] ^ 64u) + 9216u;
    auto chunk = [&](int s) { int c = rot + s; return c >= 9 ? c - 9 : c; };
    auto issue_x = [&](int s) {
        const int sl = (s & 3) == 0 ? SLOT : ((s & 3) == 1 ? 0 : (s & 3) * SLOT);
        const char* xs = P.x + chunk(s) * 128;
#pragma unroll
        for (int i = 0; i < 4; ++i) glds16(xs + (i >> 1) * 18432, xo[i & 1], (unsigned)(sl + (4 * lw + i) * 1024));
    };
    auto issue_w = [&](int s) {
        const int sl = (s & 3) == 0 ? SLOT : ((s & 3) == 1 ? 0 : (s & 3) * SLOT);
        const char* ws = P.wqkv + chunk(s) * (64 * 3456);
#pragma unroll
        for (int i = 0; i < 4; ++i) glds16(ws, wo_[i], (unsigned)(sl + XCH + (4 * lw + i) * 1024));
        if (lw < 2) glds16(ws, wo_[4], (unsigned)(sl + XCH + (16 + lw) * 1024));
    };
    auto issue_kv = [&](int s) { issue_x(s); issue_w(s); };
    issue_x(0);
    SCHED();
#pragma unroll
    for (int i = 0; i < 5; ++i) {
        const unsigned q = 64 * (i < 4 ? 4 * lw + i : 16 + (lw & 1)) + lane, rho = (q * 3641u) >> 16, pc = q - rho * 18;
        const unsigned k = (rho & 35u) + 8 * ((rho >> 2) & 3) + 4 * ((rho >> 4) & 1);
        wo_[i] = k * 3456 + (1152 + hg * 144) + pc * 16 + (pc < 9 ? 0u : 1008u);
    }
    issue_w(0); issue_kv(1);
    u64 epoch = 0;
    if (lw == 3) {
        glds16(P.gw, (unsigned)(chunk(lane >> 3) * 128 + (lane & 7) * 16), (unsigned)GW_OFF);
        if (lane < 8) glds16(P.gw, (unsigned)(chunk(8) * 128 + lane * 16), (unsigned)(GW_OFF + 1024));
        epoch = __hip_atomic_load((GAS u64*)(P.ws + WS_CTL), RLX_AGENT);
    }
    WAIT_VM(8);
    BARRIER();
    issue_kv(2);
#pragma unroll
    for (int i = 0; i < 3; ++i) {
        const int q = 64 * (i < 2 ? 2 * lw + i : 8) + lane, rho = q / 9, pc = q - rho * 9;
        const int k = (rho & 32) + 8 * ((rho >> 3) & 3) + 4 * (rho & 1) + ((rho >> 1) & 3);
        qo[i] = (unsigned)(k * 3456 + hg * 144 + pc * 16);
    }
    asm volatile("" : "+v"(qo[0]), "+v"(qo[1]), "+v"(qo[2]));
    auto issue_q = [&](int t) {
        const int sl = t == 0 ? 0 : (t + 1) * SLOT + 7168;
#pragma unroll
        for (int i = 0; i < 3; ++i) {
            const char* ws = P.wqkv + chunk(3 * t + i) * (64 * 3456);
            glds16(ws, qo[0], (unsigned)(sl + i * 9216 + (2 * lw) * 1024)); glds16(ws, qo[1], (unsigned)(sl + i * 9216 + (2 * lw + 1) * 1024));
        }
        if (lw < 3) glds16(P.wqkv + chunk(3 * t + lw) * (64 * 3456), qo[2], (unsigned)(sl + lw * 9216 + 8192));
    };
    const int ct = lw * 64 + lane, lo16 = lane & 15, g = lane >> 4;
    auto quad_src = [&](int quad) {
        const int pp = quad >= 192 ? 1 : 0, qi = quad - 192 * pp;
        const bool isz = qi >= 180;
        const int el = isz ? 0 : qi / 5, qd = qi - el * 5, hi = el >= 18 ? 1 : 0;
        const int dql = isz ? (qi < 189 ? qi - 180 : 0) : qd + 4 * hi;
        const int et = isz ? 0 : (el < 4 ? 0 : (el < 20 ? 1 : 2)), col = isz ? 0 : (el < 4 ? 12 + el : (el < 20 ? el - 4 : el - 20));
        const int tile = dql == 8 ? 5 : (et == 0 ? (dql >> 2) : (et == 1 ? 2 + (dql >> 2) : 4));
        return KVP_OFF + pp * 6144 + tile * 1024 + ((dql == 8 ? et : (dql & 3)) * 16 + col) * 4;
    };
    auto quad_dst = [&](int quad) {
        const int pp = quad >= 192 ? 1 : 0, qi = quad - 192 * pp;
        const bool isz = qi >= 180;
        const int el = isz ? 0 : qi / 5, qd = qi - el * 5, hi = el >= 18 ? 1 : 0;
        const int dql = isz ? (qi < 189 ? qi - 180 : 0) : qd + 4 * hi;
        const int dq = 9 * pp + dql, ks = dq >> 3, e = 36 * pp + el, ee = e >> 4;
        const int fr = ee == 0 ? 0 : (ee < 3 ? 2 * ee - 1 + ks : 2 * ee - 2 + ks);
        const int a = qi >= 189 ? LDS_TAG - 512 : KVT_OFF + (isz ? (9 + ks) * 1024 + (dq & 3) * 256 + (2 * pp + (dql > 4 ? 1 : 0)) * 16 : fr * 1024 + ((dq & 3) * 16 + (e & 15)) * 16) + ((dq >> 2) & 1) * 8;
        return a | (isz ? (dql == 4 ? 0x300003 : 0x100003) : ((!hi && qd == 4) ? 1 : ((hi && qd == 0) ? 2 : 3)));
    };
    int src[2], dst[2];
    u32x4 sel[2];
    WAIT_VM(8); BARRIER(); issue_kv(3);
    src[0] = quad_src(2 * (ct < 192 ? ct : 0));
    asm volatile("" : "+v"(src[0]));
    WAIT_VM(8); BARRIER(); issue_kv(4);
    src[1] = quad_src(2 * (ct < 192 ? ct : 0) + 1);
    asm volatile("" : "+v"(src[1]));
    WAIT_VM(8); BARRIER(); issue_kv(5);
    dst[0] = quad_dst(2 * ((lw < 3 ? lw : 0) * 64 + (g >> 1) * 16 + lo16) + (g & 1));
    asm volatile("" : "+v"(dst[0]));
    WAIT_VM(8); BARRIER(); issue_kv(6);
    dst[1] = quad_dst(2 * ((lw < 3 ? lw : 0) * 64 + 32 + (g >> 1) * 16 + lo16) + (g & 1));
    asm volatile("" : "+v"(dst[1]));
    WAIT_VM(8); BARRIER(); issue_kv(7);
    sel[0] = mask_frag(lo16 < 8 ? 1u << lo16 : 0u, 0, 0x3f80u); sel[1] = mask_frag(lo16 >= 8 ? 1u << (lo16 - 8) : 0u, 0, 0x3f80u);
    asm volatile("" : "+v"(sel[0]), "+v"(sel[1]));
    WAIT_VM(8); BARRIER(); issue_kv(8);
#pragma unroll
    for (int i = 0; i < 3; ++i) lds_w128(smem + KVT_OFF + i * 4096 + ct * 16, u32x4{0u, 0u, 0u, 0u});
    if (lw == 3 && lane == 0) lds_w32(smem + LDS_TAG - 16, 0u);
    WAIT_VM(8); BARRIER(); issue_q(0);
    WAIT_VM(6); BARRIER(); issue_q(1);
    BARRIER();
    issue_q(2);
    if (lw == 3) { tag_post(P, smem, epoch, lane); poll_flags(P, WS_CFL, 32, epoch + 1ull, lane); }
    WAIT_VM(0);
    BARRIER();
    STAMP(2);
    if (lw < 3) {
        u32x4 pv;
#pragma unroll
        for (int i = 0; i < 2; ++i) {
            f32x4 a = {0.f, 0.f, 0.f, 0.f};
#pragma unroll
            for (int r = 0; r < 4; ++r)
#pragma unroll
                for (int k = 0; k < 4; ++k) a[k] += lds_rf(smem + src[i] + r * 12288 + k * 256);
            pv[2 * i] = pk_bf16(a[0], a[1]); pv[2 * i + 1] = pk_bf16(a[2], a[3]);
        }
        st_sc1(wsr, WS_PART + (unsigned)((hg * 16 + rb) * 3072 + ct * 16), not_empty(pv));
        if (lane == 0) __hip_atomic_fetch_add((LAS unsigned*)(smem + LDS_TAG - 16), 1u, __ATOMIC_RELAXED, __HIP_MEMORY_SCOPE_WORKGROUP);
        STAMP(3);
        {
            const GAS unsigned* pp = (const GAS unsigned*)(P.ws + WS_PART + (hg * 16 + lo16) * 3072 + lw * 1024);
#pragma unroll 1
            for (unsigned spins = 0; spins < SPIN_CAP; ++spins) {
                if (__all(__hip_atomic_load(pp, RLX_AGENT) != EMPTY)) break;
                __builtin_amdgcn_s_sleep(1);
            }
        }
        STAMP(4);
        const unsigned off = WS_PART + (unsigned)((hg * 16 + g) * 3072 + lw * 1024 + lo16 * 16);
        f32x4 sum[2];
#pragma unroll 1
        for (unsigned spins = 0; spins < SPIN_CAP; ++spins) {
            u32x4 a[4][4];
#pragma unroll
            for (int t = 0; t < 4; ++t)
#pragma unroll
                for (int i = 0; i < 4; ++i) a[t][i] = ld_sc1(wsr, off + (unsigned)(i * 12288 + t * 256));
            sum[0] = sum[1] = f32x4{0.f, 0.f, 0.f, 0.f};
#pragma unroll
            for (int t = 0; t < 4; ++t)
#pragma unroll
                for (int i = 0; i < 4; ++i) sum[t >> 1] = mfma16(sel[t & 1], a[t][i], sum[t >> 1]);
            if (__all(sum[0][0] == sum[0][0] && sum[1][0] == sum[1][0])) break;
            unsigned m = 0;
#pragma unroll
            for (int t = 0; t < 4; ++t)
#pragma unroll
                for (int i = 0; i < 4; ++i) m = max(m, umax4(a[t][i]));
            if (__all(m != EMPTY)) break;
            CFENCE();
        }
#pragma unroll
        for (int i = 0; i < 2; ++i) {
            const unsigned lo = (dst[i] & 1) ? pk_bf16(sum[i][0], sum[i][1]) : 0u, hi = (dst[i] & 2) ? pk_bf16(sum[i][2], sum[i][3]) : 0u;
            lptr dp = smem + (dst[i] & 0xffffc);
            if (dst[i] & 0x100000) {
                const int hs = (dst[i] & 0x200000) ? 20 : 4;
#pragma unroll
                for (int r = 0; r < 4; ++r) { lds_w32(dp + 64 * r, lo); lds_w32(dp + 64 * r + hs, hi); }
            } else lds_w64(dp, u32x2{lo, hi});
        }
        STAMP(5);
    } else {
#pragma unroll 1
        for (unsigned spins = 0; spins < SPIN_CAP; ++spins) {
            if (__hip_atomic_load((LAS unsigned*)(smem + LDS_TAG - 16), __ATOMIC_RELAXED, __HIP_MEMORY_SCOPE_WORKGROUP) == 3u) break;
            __builtin_amdgcn_s_sleep(1);
        }
        wo_issue<0x0f8>(P, cg2, 0, 1, lane);
    }
    BARRIER();
    wo_issue<0x107>(P, cg2, lw, 4, lane);
    __builtin_amdgcn_s_setprio(0);
    STAMP(6);
    if (lw == 0) STAMP_OUT(5, blockIdx.x + 256);
}

constexpr int P2_ACC = 82944, P2_AP = 336, P2_B = P2_ACC + 3 * 64 * P2_AP;
DEVI void phase2(const Params& P, lptr smem, int rt64, int cg, bool dma, rsrc_t wsr) {
    const int tid = threadIdx.x, lane = tid & 63, w = __builtin_amdgcn_readfirstlane(tid >> 6);
    const int lo16 = lane & 15, g = lane >> 4;
    const int rt = w & 3, kt = w >> 2;
    STAMP_DECL; STAMP(1);
    if (dma) WAIT_VM(0);
    BARRIER();
    STAMP(2);
    const unsigned boff = WS_B + (unsigned)((cg * 72 + (tid >> 3)) * 4096 + (rt64 >> 3) * 1024 + (((tid >> 2) & 1) * 32 + ((rt64 >> 2) & 1) * 16 + (rt64 & 3) * 4 + (tid & 3)) * 16);
    u32x4 bv;
    if (w < 9) bv = ld_sc1(wsr, boff);
    const int qrow = (2 * (4 * g + (lo16 >> 2))) * 144 + kt * 6 * 4608;
    lptr qw = smem + qrow + (lane & 3) * 8, qw4 = smem + qrow + (16 + (lane & 1)) * 8;
    u32x4 wf[6][5];
#pragma unroll
    for (int i = 0; i < 6; ++i) {
#pragma unroll
        for (int j = 0; j < 4; ++j) wf[i][j] = lds_trfrag(qw + i * 4608 + 32 * j, 144);
        wf[i][4] = lds_trfrag(qw4 + i * 4608, 144);
    }
    if (w < 9) {
#pragma unroll 1
        for (unsigned spins = 0; spins < SPIN_CAP; ++spins) {
            if (__all(umax4(bv) != EMPTY)) break;
            __builtin_amdgcn_s_sleep(8);
            CFENCE();
            bv = ld_sc1(wsr, boff);
        }
        lptr bp = smem + P2_B + ((tid & 3) * 16 + ((tid >> 2) & 1) * 8) * 160 + (tid >> 3) * 2;
#pragma unroll
        for (int i = 0; i < 4; ++i) { lds_w16(bp + (2 * i) * 160, bv[i]); lds_w16(bp + (2 * i + 1) * 160, bv[i] >> 16); }
    }
    STAMP(3);
    const unsigned ooff = WS_O + (unsigned)(((rt64 * 4 + rt) * 18 + kt * 6) * 1024);
    {
        const GAS unsigned* pp = (const GAS unsigned*)(P.ws + ooff + ((lane & 3) == 3 ? 23 : 8 * (lane & 3)) * 256);
#pragma unroll 1
        for (unsigned spins = 0; spins < SPIN_CAP; ++spins) {
            if (__all(__hip_atomic_load(pp, RLX_AGENT) != EMPTY)) break;
            __builtin_amdgcn_s_sleep(2);
        }
    }
    STAMP(4);
    const int r0 = tid < 576 ? tid / 9 : 0, c0 = tid - r0 * 9;
    f32x4 acc[5];
    unsigned gm;
    {
        u32x4 of[6];
#pragma unroll
        for (int i = 0; i < 6; ++i) of[i] = ld_sc1(wsr, ooff + (unsigned)(i * 1024 + lane * 16));
        gm = __hip_atomic_load((GAS unsigned*)(P.ws + WS_GATE) + (rt64 * 64 + r0), RLX_AGENT);
#pragma unroll
        for (int j = 0; j < 5; ++j) acc[j] = f32x4{0.f, 0.f, 0.f, 0.f};
#pragma unroll
        for (int i = 0; i < 6; ++i)
#pragma unroll
            for (int j = 0; j < 5; ++j) acc[j] = mfma16(wf[i][j], of[i], acc[j]);
    }
    if (!__all(acc[0][0] == acc[0][0] && gm != EMPTY)) {
#pragma unroll
        for (int j = 0; j < 5; ++j) acc[j] = f32x4{0.f, 0.f, 0.f, 0.f};
        u32x4 of[6];
#pragma unroll 1
        for (unsigned spins = 0; spins < SPIN_CAP; ++spins) {
            CFENCE();
#pragma unroll
            for (int i = 0; i < 6; ++i) of[i] = ld_sc1(wsr, ooff + (unsigned)(i * 1024 + lane * 16));
            unsigned m = gm = __hip_atomic_load((GAS unsigned*)(P.ws + WS_GATE) + (rt64 * 64 + r0), RLX_AGENT);
#pragma unroll
            for (int i = 0; i < 6; ++i) m = max(m, umax4(of[i]));
            if (__all(m != EMPTY)) break;
        }
#pragma unroll
        for (int i = 0; i < 6; ++i) {
#pragma unroll
            for (int j = 0; j < 4; ++j) acc[j] = mfma16(lds_trfrag(qw + i * 4608 + 32 * j, 144), of[i], acc[j]);
            acc[4] = mfma16(lds_trfrag(qw4 + i * 4608, 144), of[i], acc[4]);
        }
    }
    const float gt = __uint_as_float(gm);
    lptr ap = smem + P2_ACC + kt * (64 * P2_AP) + (rt * 16 + lo16) * P2_AP + g * 16;
#pragma unroll
    for (int j = 0; j < 5; ++j) if (j < 4 || g < 2) lds_w128(ap + j * 64, __builtin_bit_cast(u32x4, acc[j]));
    STAMP(5);
    BARRIER();
    STAMP(6);
    if (w < 9) {
        lptr a = smem + P2_ACC + r0 * P2_AP + c0 * 32;
        const f32x4 s0 = __builtin_bit_cast(f32x4, lds_r128(a)) + __builtin_bit_cast(f32x4, lds_r128(a + 64 * P2_AP)) + __builtin_bit_cast(f32x4, lds_r128(a + 128 * P2_AP));
        const f32x4 s1 = __builtin_bit_cast(f32x4, lds_r128(a + 16)) + __builtin_bit_cast(f32x4, lds_r128(a + 64 * P2_AP + 16)) + __builtin_bit_cast(f32x4, lds_r128(a + 128 * P2_AP + 16));
        const u32x4 bb = lds_r128(smem + P2_B + r0 * 160 + c0 * 16);
        const float og = rbf(1.f - gt);
        u32x4 o;
#pragma unroll
        for (int i = 0; i < 4; ++i) {
            const float a0 = i < 2 ? s0[2 * i] : s1[2 * i - 4], a1 = i < 2 ? s0[2 * i + 1] : s1[2 * i - 3];
            o[i] = pk_bf16(gt * a0 + og * bf2f(bb[i]), gt * a1 + og * bfhi(bb[i]));
        }
        *(GAS u32x4*)(P.out + (unsigned)((rt64 * 64 + r0) * 1152 + cg * 144 + c0 * 16)) = o;
    }
    STAMP(7);
    if (w == 4) STAMP_OUT(blockIdx.x < 128 ? 3 : 6, blockIdx.x + 512);
}

__global__ __launch_bounds__(768, 3) void k_fused(Params P) {
    __shared__ __attribute__((aligned(16))) char smem_[LDS_BYTES];
    lptr smem = (lptr)smem_;
    const int b = blockIdx.x, xcd = b & 7, idx = b >> 3;
    const int lane = threadIdx.x & 63, w = __builtin_amdgcn_readfirstlane(threadIdx.x >> 6);
    const rsrc_t wsr = __builtin_amdgcn_make_buffer_rsrc(P.ws, 0, (int)WS_END, 0x00020000);
    const int rt64 = xcd * 4 + (idx & 3), cg2 = idx >> 2;
    if (idx < 16) {
        const int rbl = idx & 3, hgl = idx >> 2, rb = (xcd >> 1) * 4 + rbl, hg = (xcd & 1) * 4 + hgl;
        if (w >= 4) gemm_compute(P, smem, rb, hg, w - 4, wsr);
        else gemm_loader(P, smem, rb, hg, ((rbl + hgl) & 3) * 2, w, cg2, wsr);
    } else {
        if (idx < 28) ssm_role(P, smem, xcd * 12 + idx - 16, (idx - 16) & 7, wsr);
        else {
            u64 epoch = 0;
            if (w == 11) epoch = __hip_atomic_load((GAS u64*)(P.ws + WS_CTL), RLX_AGENT);
            const int cid = (idx - 28) * 8 + xcd;
#pragma unroll
            for (int i = 0; i < 14; ++i)
                st_sc1(wsr, WS_PART + (unsigned)((i * 24576 + cid * 768 + (int)threadIdx.x) * 16), u32x4{EMPTY, EMPTY, EMPTY, EMPTY});
            WAIT_VM(0);
            BARRIER();
            if (w == 11) {
                const u64 t = epoch + 1ull;
                if (lane == 0) __hip_atomic_store((GAS u64*)(P.ws + WS_CFL) + cid, t, RLX_AGENT);
                tag_post(P, smem, epoch, lane);
                if (b == 255) {
                    const GAS u64* f = (const GAS u64*)(P.ws + WS_ARR) + lane;
#pragma unroll 1
                    for (unsigned spins = 0; spins < SPIN_CAP; ++spins) {
                        const bool ok = __hip_atomic_load(f, RLX_AGENT) == t && __hip_atomic_load(f + 64, RLX_AGENT) == t &&
                                        __hip_atomic_load(f + 128, RLX_AGENT) == t && __hip_atomic_load(f + 192, RLX_AGENT) == t;
                        if (__all(ok)) break;
                        __builtin_amdgcn_s_sleep(8);
                    }
                    if (lane == 0) __hip_atomic_store((GAS u64*)(P.ws + WS_CTL), t, RLX_AGENT);
                }
                poll_flags(P, WS_CFL, 32, t, lane);
            }
        }
        if (w < 9) wo_issue<0x1ff>(P, cg2, w, 9, lane);
    }
    phase2(P, smem, rt64, cg2, idx >= 16 || w < 4, wsr);
}

__global__ __launch_bounds__(768) void k_probe(Params P) {
    asm volatile("s_waitcnt lgkmcnt(0)" :: "s"(P.x), "s"(P.wqkv), "s"(P.wo), "s"(P.logA), "s"(P.aim), "s"(P.cre), "s"(P.cim), "s"(P.logdt), "s"(P.dskip), "s"(P.gw), "s"(P.out), "s"(P.ws) : "memory");
    const u64 t_start = __builtin_amdgcn_s_memrealtime();
    while (__builtin_amdgcn_s_memrealtime() - t_start < 5000) __builtin_amdgcn_s_sleep(1);
}
extern "C" void kernel_launch(void* const* d_in, const int* in_sizes, int n_in, void* d_out, int out_size, void* d_ws, size_t ws_size,
                              hipStream_t stream) {
    Params P;
    P.x = (const char*)d_in[0]; P.wqkv = (const char*)d_in[1]; P.wo = (const char*)d_in[2];
    P.logA = (const char*)d_in[4]; P.aim = (const char*)d_in[5]; P.cre = (const char*)d_in[6]; P.cim = (const char*)d_in[7];
    P.logdt = (const char*)d_in[8]; P.dskip = (const char*)d_in[9]; P.gw = (const char*)d_in[10];
    P.out = (char*)d_out; P.ws = (char*)d_ws;
    k_fused<<<256, 768, 0, stream>>>(P);
    k_probe<<<256, 768, 0, stream>>>(P);
}
```

```cpp
#include <hip/hip_runtime.h>
#include <stdint.h>

#define GAS __attribute__((address_space(1)))
#define LAS __attribute__((address_space(3)))
#define DEVI __device__ __forceinline__
typedef unsigned short u16;
typedef unsigned long long u64;
typedef __attribute__((ext_vector_type(2))) unsigned u32x2;
typedef __attribute__((ext_vector_type(3))) unsigned u32x3;
typedef __attribute__((ext_vector_type(4))) unsigned u32x4;
typedef __attribute__((ext_vector_type(4))) short s16x4;
typedef __attribute__((ext_vector_type(2))) float f32x2;
typedef __attribute__((ext_vector_type(4))) float f32x4;
typedef __attribute__((ext_vector_type(16))) float f32x16;
typedef __attribute__((ext_vector_type(2))) __bf16 bf16x2;
typedef __attribute__((ext_vector_type(8))) __bf16 bf16x8;
typedef __attribute__((ext_vector_type(2))) _Float16 f16x2;
typedef __attribute__((ext_vector_type(8))) _Float16 f16x8;
typedef LAS char* lptr;

#define PREF 1
#define ROT 0
#define FSLEEP 2
#define NSLEEP "3"
#define LDRIFT 0
#define PSLEEP 6
#define CSLEEP 1
#define OSLEEP 2
#define RSLEEP 6
constexpr int SEQ = 2048, DM = 576;

constexpr unsigned WS_CTL = 0;
constexpr unsigned WS_ARR = 256;
constexpr unsigned WS_CF = 4096;
constexpr unsigned WS_BFL = 8192;
constexpr unsigned WS_HINT = 20480;
constexpr unsigned WS_GFL = 45056;
constexpr unsigned WS_GATE = 36864;
constexpr unsigned WS_PART = 49152;
constexpr unsigned WS_O = WS_PART + 8 * 16 * 3072;
constexpr unsigned WS_B = WS_O + SEQ * DM * 2;
constexpr unsigned WS_END = WS_B + SEQ * DM * 2;
constexpr unsigned EMPTY = 0xffffffffu;

constexpr size_t WS_STAMP = 16u << 20;
#define STAMP_DECL
#define STAMP(i)
#define CSTAMP(i)
#define STAMP_OUT(role, slot)

struct Params {
    const char* x; const char* wqkv; const char* wo; const char* logA; const char* aim; const char* cre; const char* cim;
    const char* logdt; const char* dskip; const char* gw; char* out; char* ws;
};

constexpr int LDS_BYTES = 163840, FL = 163584, LDS_TAG = FL + 64, LDS_DUMMY = FL + 128, LDS_JUNK = 150784;
enum { F_SW = 0, F_LANDED = 4, F_W1 = 20, F_CL = 24, F_B = 28, F_H = 32, F_KVP = 36, F_KVR = 40, F_Q0 = 44, F_QD = 48, F_D = 52, F_C1 = 56, F_W0 = 60 };
constexpr int CONS = LDS_JUNK;
constexpr int WO8 = 132096, P2_ACC = 73728, P2_AP = 304, P2_B = 141312, P2_G = 150528;

DEVI float bf2f(unsigned h) { return __uint_as_float(h << 16); }
DEVI float bfhi(unsigned h) { return __uint_as_float(h & 0xffff0000u); }
DEVI unsigned pk_bf16(float a, float b) { return __builtin_bit_cast(unsigned, __builtin_convertvector(f32x2{a, b}, bf16x2)); }
DEVI unsigned pk_f16(f32x2 v) { return __builtin_bit_cast(unsigned, __builtin_convertvector(v, f16x2)); }
DEVI float rbf(float a) { return bf2f(pk_bf16(a, a)); }
DEVI f32x4 mfma16(u32x4 a, u32x4 b, f32x4 c) {
    return __builtin_amdgcn_mfma_f32_16x16x32_bf16(__builtin_bit_cast(bf16x8, a), __builtin_bit_cast(bf16x8, b), c, 0, 0, 0);
}
DEVI void mfma16i(f32x4& c, u32x4 a, u32x4 b) { asm volatile("v_mfma_f32_16x16x32_bf16 %0, %1, %2, %0" : "+v"(c) : "v"(a), "v"(b)); }
#define MFMA_DONE() do { __builtin_amdgcn_sched_barrier(0); asm volatile("s_nop 7\n\ts_nop 1" ::: "memory"); __builtin_amdgcn_sched_barrier(0); } while (0)
DEVI u32x4 lds_r128(lptr p) { return *(LAS u32x4*)p; }
DEVI u32x2 lds_r64(lptr p) { return *(LAS u32x2*)p; }
DEVI float lds_rf(lptr p) { return *(LAS float*)p; }
DEVI void lds_w128(lptr p, u32x4 v) { *(LAS u32x4*)p = v; }
DEVI void lds_w64(lptr p, u32x2 v) { *(LAS u32x2*)p = v; }
DEVI void lds_w32(lptr p, unsigned v) { *(LAS unsigned*)p = v; }
DEVI void lds_w16(lptr p, unsigned v) { *(LAS u16*)p = (u16)v; }
DEVI u32x2 lds_tr(lptr p) { return __builtin_bit_cast(u32x2, __builtin_amdgcn_ds_read_tr16_b64_v4i16((LAS s16x4*)p)); }
DEVI u32x4 lds_trfrag(lptr p, int off2) { u32x2 a = lds_tr(p), b = lds_tr(p + off2); return u32x4{a.x, a.y, b.x, b.y}; }
template <int OFF> DEVI void glds16o(const char* sbase, unsigned voff) {
    asm volatile("global_load_lds_dwordx4 %0, %1 offset:%2" :: "v"(voff), "s"(sbase), "i"(OFF) : "memory");
}
DEVI void glds16(const char* sbase, unsigned voff, unsigned l) {
    asm volatile("s_mov_b32 m0, %2\n\ts_nop 0\n\tglobal_load_lds_dwordx4 %0, %1" :: "v"(voff), "s"(sbase), "s"(l) : "memory");
}
#define SET_M0(v) asm volatile("s_mov_b32 m0, %0\n\ts_nop 0" :: "s"(v) : "memory")
#define W_ADDTID(v, off) asm volatile("ds_write_addtid_b32 %0 offset:%1" :: "v"(v), "i"(off) : "memory")
#define CFENCE() asm volatile("" ::: "memory")
#define SCHED() __builtin_amdgcn_sched_barrier(0)
#define WAIT_VM(n) asm volatile("s_waitcnt vmcnt(" #n ")" ::: "memory")
#define WAIT_LGKM(n) __builtin_amdgcn_s_waitcnt(0xC07F | ((n) << 8))
#define WAIT_LGKM0() asm volatile("s_waitcnt lgkmcnt(0)" ::: "memory")
#define BARRIER() do { SCHED(); asm volatile("s_waitcnt lgkmcnt(0)\n\ts_barrier" ::: "memory"); SCHED(); } while (0)
#define RAW_BARRIER() do { SCHED(); asm volatile("s_barrier" ::: "memory"); SCHED(); } while (0)
#define RLX_AGENT __ATOMIC_RELAXED, __HIP_MEMORY_SCOPE_AGENT
constexpr unsigned SPIN_CAP = 1u << 16;
typedef __amdgpu_buffer_rsrc_t rsrc_t;
DEVI u32x4 ld_sc1(rsrc_t r, unsigned off) { return __builtin_amdgcn_raw_buffer_load_b128(r, off, 0, 16); }
DEVI void st_sc1(rsrc_t r, unsigned off, u32x4 v) { __builtin_amdgcn_raw_buffer_store_b128(v, r, off, 0, 16); }

DEVI u32x4 pack8(f32x4 a, f32x4 b) { return u32x4{pk_bf16(a[0], a[1]), pk_bf16(a[2], a[3]), pk_bf16(b[0], b[1]), pk_bf16(b[2], b[3])}; }
DEVI float phi(float t) {
    const float e = __builtin_amdgcn_fmed3f(__builtin_amdgcn_exp2f(t * 1.44269504089f), 0.f, 1.f);
    return __int_as_float(max(__float_as_int(t), 0x358637bd)) + e;
}
DEVI f32x4 phi4(f32x4 v) { return f32x4{phi(v[0]), phi(v[1]), phi(v[2]), phi(v[3])}; }
DEVI unsigned umax4(u32x4 a) { return max(max(a.x, a.y), max(a.z, a.w)); }
DEVI u32x4 not_empty(u32x4 a) { return u32x4{min(a.x, EMPTY - 1), min(a.y, EMPTY - 1), min(a.z, EMPTY - 1), min(a.w, EMPTY - 1)}; }
DEVI void tag_post(const Params& P, u64 tag) { __hip_atomic_store((GAS u64*)(P.ws + WS_ARR) + blockIdx.x, tag, RLX_AGENT); }
#define PIN_TAG() do { asm volatile("" : "+s"(tag)); tag += 1ull; } while (0)
template <int SL = PSLEEP>
DEVI void poll_addr(const Params& P, unsigned off, u64 tag) {
    const GAS u64* f = (const GAS u64*)(P.ws + off);
#pragma unroll 1
    for (unsigned spins = 0; spins < SPIN_CAP; ++spins) {
        if (__all(__hip_atomic_load(f, RLX_AGENT) == tag)) break;
        __builtin_amdgcn_s_sleep(SL);
    }
}
template <int SL = PSLEEP>
DEVI void poll_flags(const Params& P, unsigned off, int n, u64 tag, int lane) { poll_addr<SL>(P, off + (lane < n ? lane : 0) * 128, tag); }
DEVI unsigned flag_get(lptr smem, int f) { return __builtin_amdgcn_readfirstlane(__hip_atomic_load((LAS unsigned*)(smem + FL + f), __ATOMIC_RELAXED, __HIP_MEMORY_SCOPE_WORKGROUP)); }
template <int SL = FSLEEP>
DEVI void flag_wait(lptr smem, int f, unsigned v) {
#pragma unroll 1
    for (unsigned spins = 0; spins < (SPIN_CAP << 4); ++spins) { if (flag_get(smem, f) >= v) break; if (SL) __builtin_amdgcn_s_sleep(SL); }
    CFENCE();
}
DEVI void flag_add(int f) { asm volatile("s_mov_b64 exec, 1\n\tds_add_u32 %0, %1\n\ts_mov_b64 exec, -1" :: "v"(FL + f), "v"(1u) : "memory"); }
#define FLAG_ADD(f) asm volatile("s_mov_b64 exec, 1\n\tds_add_u32 %0, %1 offset:%2\n\ts_mov_b64 exec, -1" :: "v"(flp), "v"(one), "i"(f) : "memory")
DEVI void flag_set(int f, unsigned v) { asm volatile("s_mov_b64 exec, 1\n\tds_write_b32 %0, %1\n\ts_mov_b64 exec, -1" :: "v"(FL + f), "v"(v) : "memory"); }
#define NEEDP(seen, ptr, f, v) do { unsigned c_; asm volatile( \
        "v_cmp_gt_u32 vcc, %4, %1\n\ts_cbranch_vccnz 0f\n1:\n\t.subsection 1\n" \
        "0:\n\ts_mov_b32 %0, 0x100000\n2:\n\ts_sleep " NSLEEP "\n\tds_read_b32 %1, %2 offset:%3\n\ts_sub_u32 %0, %0, 1\n\ts_waitcnt lgkmcnt(0)\n\ts_cbranch_scc1 1b\n\t" \
        "v_cmp_gt_u32 vcc, %4, %1\n\ts_cbranch_vccnz 2b\n\ts_branch 1b\n\t.subsection 0" \
        : "=&s"(c_), "+v"(seen) : "v"(ptr), "i"(f), "i"(v) : "memory", "scc", "vcc"); } while (0)
#define NEED(seen, f, v) NEEDP(seen, flp, f, v)
DEVI void touch(const char* sbase, unsigned voff) {
    asm volatile("s_mov_b32 m0, %2\n\ts_nop 0\n\tglobal_load_lds_dword %0, %1" :: "v"(voff), "s"(sbase), "s"(LDS_JUNK) : "memory");
}
DEVI void touch_group(const Params& P, int xcd, int n, int lane) {
    if (n < 144) {
        const int c = 1 + n / 18, r = n - (c - 1) * 18;
        if (r < 8) touch(P.x, (unsigned)(((xcd >> 1) * 512 + r * 64 + lane) * 1152 + c * 128));
        else { const int l = r - 8; touch(P.wqkv, (unsigned)((c * 64 + lane) * 3456 + (l < 5 ? 1152 : 2304 - 640) + (xcd & 1) * 512 + l * 128)); }
    } else if (n < 189) {
        const int c = (n - 144) / 5, l = n - 144 - c * 5;
        touch(P.wqkv, (unsigned)((c * 64 + lane) * 3456 + (xcd & 1) * 512 + l * 128));
    } else touch(P.wo, (unsigned)(((n - 189) * 64 + lane) * 128));
}
template <unsigned CH>
DEVI void wo_issue(const Params& P, int cg, int first, int stride, int lane) {
#pragma unroll 1
    for (int m = first; m < 9; m += stride) {
        const int q = 64 * m + lane, rho = q / 9, pc = q - rho * 9;
        const int k = (rho & 32) + 8 * ((rho >> 3) & 3) + 4 * (rho & 1) + ((rho >> 1) & 3);
        const unsigned qo = (unsigned)(k * 1152 + cg * 144 + pc * 16);
#pragma unroll
        for (int c = 0; c < 9; ++c) if ((CH >> c) & 1) glds16(P.wo + c * (64 * 1152), qo, (unsigned)((c < 8 ? c * 9216 : WO8) + m * 1024));
    }
}

constexpr int SP = 3088;
constexpr int XT_OFF = 98816, XT_BYTES = 4608, XPAD = 256;
constexpr int TT_OFF = 69632, TC = 1216, PT_OFF = 126464, PC_OFF = 2560, ACX_OFF = 0;

DEVI f32x2 cmul(f32x2 a, f32x2 b, f32x2 bs) {
    f32x2 t = f32x2{a.x, a.x} * b;
    return __builtin_elementwise_fma(f32x2{a.y, a.y}, bs, t);
}
DEVI f32x2 swp(f32x2 b) { return f32x2{-b.y, b.x}; }
DEVI f32x2 cexp_p(float a2, float f, float p) {
    float m = __builtin_amdgcn_exp2f(a2 * p);
    float r = __builtin_amdgcn_fractf(f * p);
    return f32x2{m * __builtin_amdgcn_cosf(r), m * __builtin_amdgcn_sinf(r)};
}
DEVI u32x4 mask_frag(unsigned bits, int g, unsigned val) {
    unsigned by = bits >> (8 * g);
    u32x4 r;
#pragma unroll
    for (int i = 0; i < 4; ++i) { unsigned t = (by >> (2 * i)) & 3u; r[i] = ((t | (t << 15)) & 0x10001u) * val; }
    return r;
}
DEVI float wave_max(float v) {
    v = fmaxf(v, __int_as_float(__builtin_amdgcn_update_dpp(0, __float_as_int(v), 0xB1, 0xf, 0xf, true)));
    v = fmaxf(v, __int_as_float(__builtin_amdgcn_update_dpp(0, __float_as_int(v), 0x4E, 0xf, 0xf, true)));
    v = fmaxf(v, __int_as_float(__builtin_amdgcn_update_dpp(0, __float_as_int(v), 0x124, 0xf, 0xf, true)));
    v = fmaxf(v, __int_as_float(__builtin_amdgcn_update_dpp(0, __float_as_int(v), 0x128, 0xf, 0xf, true)));
    const int i = __float_as_int(v);
    const int a = max(__builtin_amdgcn_readlane(i, 0), __builtin_amdgcn_readlane(i, 16)), b = max(__builtin_amdgcn_readlane(i, 32), __builtin_amdgcn_readlane(i, 48));
    return __int_as_float(max(a, b));
}

DEVI void ssm_role(const Params& P, lptr smem, int xcd, int li, u64& tag, rsrc_t wsr) {
    const int cg6 = xcd * 12 + li;
    const int tid = threadIdx.x, lane = tid & 63, w = __builtin_amdgcn_readfirstlane(tid >> 6);
    const int ch = w >> 1, half = (w ^ (w >> 2)) & 1, d = cg6 * 6 + ch;
    const int lo16 = lane & 15, g = lane >> 4;
    STAMP_DECL; STAMP(1);
    __builtin_amdgcn_s_setprio(3);
    if (w >= 8) touch_group(P, xcd, (w - 8) * 16 + li, lane);
    const unsigned po = (unsigned)(d * 256 + lane * 4);
    const unsigned r_la = *(const GAS unsigned*)(P.logA + po), r_ai = *(const GAS unsigned*)(P.aim + po);
    const unsigned r_cr = *(const GAS unsigned*)(P.cre + po), r_ci = *(const GAS unsigned*)(P.cim + po);
    const unsigned r_dt = *(const GAS u16*)(P.logdt + d * 2), r_ds = *(const GAS u16*)(P.dskip + d * 2);
    const int tt = (tid + (li & 7) * 64) & 511;
    u32x3 xr[4];
    const unsigned xoff = (unsigned)(tt * 4608 + cg6 * 12);
#define GATHER(i) do { SCHED(); if (w < 8) xr[i] = *(const GAS u32x3*)(P.x + xoff + (i) * 1152); __builtin_amdgcn_s_setprio(3 - (i)); SCHED(); } while (0)
    SCHED();
    lptr xt = smem + XT_OFF + ch * XT_BYTES, tb = smem + TT_OFF + ch * 4864, pt = smem + PT_OFF + ch * 3840;
    const int j32 = lane & 31, h = lane >> 5;
    u32x4 F21, F22, F23, F31, F32, FX;
    lptr q2[4], q3[4];
    if (half) {
#pragma unroll
        for (int i = 0; i < 4; ++i) if (i < 3 || lane < 48) lds_w128(pt + i * 1024 + lane * 16, u32x4{0u, 0u, 0u, 0u});
        const int u2 = 79 - (32 + (j32 >> 1)), u3 = 127 - (64 + 32 * h + (j32 >> 1));
#pragma unroll
        for (int c = 0; c < 4; ++c) {
            const int v2 = u2 - c + 4, v3 = u3 - c + 4;
            q2[c] = pt + (j32 & 1) * 1280 + c * 320 + (v2 >> 2) * 16 + h * 8 + (v2 & 3) * 2 - 64;
            q3[c] = pt + PC_OFF + c * 320 + (v3 >> 2) * 16 + (j32 & 1) * 8 + (v3 & 3) * 2 - 64;
            asm volatile("" : "+v"(q2[c]), "+v"(q3[c]));
        }
        F21 = mask_frag(7u << (lo16 + 11), g, 0x3f80u); F22 = mask_frag(31u << (lo16 + 6), g, 0x3f80u);
        F23 = mask_frag(7u << (lo16 + 3), g, 0x3f80u); F31 = mask_frag(127u << (lo16 + 5), g, 0x3f80u);
        F32 = mask_frag(511u << (lo16 + 4), g, 0x3f80u); FX = mask_frag(3u << (lo16 + 12), g, 0xffffu);
        asm volatile("" : "+v"(F21), "+v"(F22), "+v"(F23), "+v"(F31), "+v"(F32), "+v"(FX));
    } else lds_w64(xt + lane * 8, u32x2{0u, 0u});
    int arow;
    {
        const u64 map0 = 0x0a09080803020100ull, map1 = 0x0c0b0a0a07060504ull, map2 = 0x1413110e12100f0dull;
        const u64 mm = j32 < 8 ? map0 : (j32 < 16 ? map1 : map2);
        arow = (int)((mm >> (8 * (j32 & 7))) & 0xff);
    }
    const float dt = rbf(__expf(bf2f(r_dt)));
    const float cm = wave_max(fmaxf(fmaxf(fabsf(bf2f(r_cr)), fabsf(bfhi(r_cr))), fmaxf(fabsf(bf2f(r_ci)), fabsf(bfhi(r_ci)))));
    unsigned ce = __float_as_uint(cm) >> 23; ce = ce < 16u ? 16u : (ce > 240u ? 240u : ce);
    const float cs = __uint_as_float((263u - ce) << 23), ics = __uint_as_float((ce - 9u) << 23);
    f32x16 kacc;
#pragma unroll
    for (int i = 0; i < 16; ++i) kacc[i] = 0.f;
    STAMP(2);
    {
        const unsigned sh = half * 16;
        asm volatile("" :: "v"(r_la), "v"(r_ai), "v"(r_ds));
        GATHER(0);
        PIN_TAG();
        const float la = bf2f(r_la >> sh), ai = bf2f(r_ai >> sh), cr = bf2f(r_cr >> sh) * cs, ci = bf2f(r_ci >> sh) * cs;
        const float a2 = (-rbf(__expf(la)) * dt) * 1.44269504089f, f = (ai * dt) * 0.15915494309f;
#define PUT(row, v) W_ADDTID(pk_f16(v), ((row) & 15) * SP)
        const f32x2 w1 = cexp_p(a2, -f, 1.f), w256 = cexp_p(a2, -f, 256.f);
        f32x2 u[8], v[4];
        u[1] = cexp_p(a2, f, 8.f); v[1] = cexp_p(a2, f, 64.f);
        const f32x2 w1s = swp(w1), w2s = swp(w256);
        f32x2 c[9];
        c[0] = f32x2{cr, -ci};
#pragma unroll
        for (int b = 1; b < 9; ++b) c[b] = cmul(c[b - 1], w1, w1s);
        GATHER(1);
        SET_M0(w * 256);
#pragma unroll
        for (int b = 0; b < 8; ++b) PUT(b, c[b]);
        PUT(8, cmul(c[0], w256, w2s)); PUT(9, cmul(c[2], w256, w2s)); PUT(11, cmul(c[6], w256, w2s));
        f32x2 e4 = cmul(c[4], w256, w2s), e8 = cmul(c[8], w256, w2s);
        PUT(10, e4); PUT(12, e8);
        e4 = cmul(e4, w256, w2s); e8 = cmul(e8, w256, w2s); PUT(13, e4); PUT(14, e8);
        e4 = cmul(e4, w256, w2s); e8 = cmul(e8, w256, w2s); PUT(15, e4);
        SET_M0(w * 256 + 16 * SP);
        PUT(17, e8);
        e8 = cmul(e8, w256, w2s); PUT(16, e8);
        e8 = cmul(e8, w256, w2s); PUT(18, e8);
        e8 = cmul(e8, w256, w2s); PUT(19, e8);
        e8 = cmul(e8, w256, w2s); PUT(20, e8);
        GATHER(2);
        u32x4 af[8];
        lptr ap = smem + arow * SP + w * 256 + h * 16, bp = smem + j32 * SP + w * 256 + h * 16;
#pragma unroll
        for (int s = 0; s < 8; ++s) af[s] = lds_r128(ap + 32 * s);
        { f32x2 s1 = swp(u[1]); u[2] = cmul(u[1], u[1], s1); u[3] = cmul(u[2], u[1], s1); u[4] = cmul(u[3], u[1], s1);
          f32x2 s4 = swp(u[4]); u[5] = cmul(u[1], u[4], s4); u[6] = cmul(u[2], u[4], s4); u[7] = cmul(u[3], u[4], s4); }
        f32x2 vs = swp(v[1]);
        v[2] = cmul(v[1], v[1], vs); v[3] = cmul(v[2], v[1], vs);
#pragma unroll
        for (int a = 3; a >= 1; --a) {
            if (a == 1) SET_M0(w * 256);
            if (a == 2) GATHER(3);
            vs = swp(v[a]);
            PUT(8 * a, v[a]);
#pragma unroll
            for (int b = 1; b < 8; ++b) PUT(8 * a + b, cmul(u[b], v[a], vs));
        }
        PUT(0, (f32x2{1.f, 0.f}));
#pragma unroll
        for (int b = 1; b < 8; ++b) PUT(b, u[b]);
#undef PUT
        u32x4 bfr[8];
#pragma unroll
        for (int s = 0; s < 8; ++s) bfr[s] = lds_r128(bp + 32 * s);
        SCHED();
#pragma unroll
        for (int s = 0; s < 8; ++s)
            kacc = __builtin_amdgcn_mfma_f32_32x32x16_f16(__builtin_bit_cast(f16x8, af[s]), __builtin_bit_cast(f16x8, bfr[s]), kacc, 0, 0, 0);
    }
    STAMP(3);
    const float ks = 2.f * dt * ics;
#pragma unroll
    for (int i = 0; i < 12; ++i) kacc[i] *= ks;
    if (half) {
#pragma unroll
        for (int i = 0; i < 8; ++i) W_ADDTID(kacc[i], i * SP);
    } else {
#pragma unroll
        for (int i = 0; i < 4; ++i) W_ADDTID(kacc[8 + i], i * SP);
    }
    lptr xc = smem + (w ^ 1) * 256 + lane * 4;
    if (w >= 8 && (w - 8) * 12 + li < 45) touch_group(P, xcd, 144 + (w - 8) * 12 + li, lane);
    STAMP(4);
    BARRIER();
    STAMP(5);
    const int cpy = (3 - lo16) & 3;
    lptr xf = xt + (XPAD - 16 + 16 * lo16 + 8 * g) * 2;
    lptr tf = tb + cpy * TC + 2 * (527 - lo16 + 8 * g - cpy) - 64 * 16;
    lptr xf7 = xf - 64 * 7;
    asm volatile("" : "+v"(xf7), "+v"(tf));
    auto TF = [&](int off) { u32x2 a = lds_r64(tf + off), b = lds_r64(tf + off + 8); return u32x4{a.x, a.y, b.x, b.y}; };
    f32x4 acc[8];
#pragma unroll
    for (int i = 0; i < 8; ++i) acc[i] = f32x4{0.f, 0.f, 0.f, 0.f};
    lptr axw, axr;
    if (!half) {
#pragma unroll
        for (int i = 0; i < 8; ++i) kacc[i] += lds_rf(xc + i * SP);
        unsigned dd[6];
        dd[0] = pk_bf16(kacc[7], kacc[6]); dd[1] = pk_bf16(kacc[5], kacc[4]); dd[2] = pk_bf16(kacc[3], kacc[2]);
        dd[3] = pk_bf16(kacc[1], kacc[0] + (lane == 0 ? bf2f(r_ds) : 0.f));
        dd[4] = __builtin_amdgcn_update_dpp(0, dd[0], 0x138, 0xf, 0xf, true);
        dd[5] = __builtin_amdgcn_update_dpp(0, dd[1], 0x138, 0xf, 0xf, true);
        unsigned mm[5];
#pragma unroll
        for (int i = 0; i < 5; ++i) mm[i] = __builtin_amdgcn_alignbit(dd[i + 1], dd[i], 16);
        lptr tw = tb + 2 * (536 - 8 * lane);
        lds_w128(tw, u32x4{dd[0], dd[1], dd[2], dd[3]}); lds_w128(tw + TC, u32x4{mm[0], mm[1], mm[2], mm[3]});
        lds_w128(tw + 2 * TC, u32x4{dd[1], dd[2], dd[3], dd[4]}); lds_w128(tw + 3 * TC, u32x4{mm[1], mm[2], mm[3], mm[4]});
        {
            const unsigned n0 = __builtin_amdgcn_readlane(dd[0], 63), n1 = __builtin_amdgcn_readlane(dd[1], 63);
            const unsigned kk = (n0 & 0xffffu) * 0x10001u, kh = n0 << 16, m3 = (n0 << 16) | (n0 & 0xffffu), m4 = (n0 >> 16) | (n1 << 16);
            const int c = lane & 3, bl = lane >> 2;
            u32x4 e = c == 0 ? u32x4{0u, 0u, kh, kk} : (c == 1 ? u32x4{0u, 0u, kk, m3} : (c == 2 ? u32x4{0u, kh, kk, n0} : u32x4{0u, kk, m3, m4}));
            if (bl != 3) e = u32x4{0u, 0u, 0u, 0u};
            if (bl < 6) lds_w128(tb + c * TC + (bl < 4 ? bl * 16 : 1024 + bl * 16), e);
        }
    if (w < 8) {
        lptr xb = smem + XT_OFF + (XPAD + tt * 4) * 2;
#pragma unroll
        for (int c = 0; c < 3; ++c) {
            u32x2 lo = {__builtin_amdgcn_perm(xr[1][c], xr[0][c], 0x05040100u), __builtin_amdgcn_perm(xr[3][c], xr[2][c], 0x05040100u)};
            u32x2 hi = {__builtin_amdgcn_perm(xr[1][c], xr[0][c], 0x07060302u), __builtin_amdgcn_perm(xr[3][c], xr[2][c], 0x07060302u)};
            lds_w64(xb + (2 * c) * XT_BYTES, lo); lds_w64(xb + (2 * c + 1) * XT_BYTES, hi);
        }
    }
    if (tid == 704) tag_post(P, tag);
    if (w < 7 && w * 12 + li < 81) touch_group(P, xcd, 189 + w * 12 + li, lane);
        BARRIER();
        STAMP(6);
        u32x4 X[8];
#pragma unroll
        for (int i = 0; i < 8; ++i) X[i] = lds_r128(xf7 + 64 * 6 + 512 * i);
        u32x4 k0 = TF(1024 - 64), k8 = TF(512 - 64);
#pragma unroll
        for (int i = 0; i < 8; ++i) asm volatile("" : "+v"(acc[i]));
        asm volatile("s_nop 1");
#pragma unroll
        for (int c = 1; c < 8; ++c) {
            u32x4 k0n, k8n;
            if (c < 7) { k0n = TF(1024 - 64 * (c + 1)); k8n = TF(512 - 64 * (c + 1)); }
#pragma unroll
            for (int i = 0; i < 8; ++i) {
                SCHED();
                mfma16i(acc[i], k0, X[i]);
                if (i < 7) mfma16i(acc[i + 1], k8, X[i]);
                SCHED();
                if (c < 7) X[i] = lds_r128(xf7 + 64 * (6 - c) + 512 * i);
            }
            k0 = k0n; k8 = k8n;
        }
        MFMA_DONE();
        axw = smem + ACX_OFF + ch * 8192 + lane * 16; axr = axw + 4096;
#pragma unroll
        for (int i = 0; i < 4; ++i) lds_w128(axw + i * 1024, __builtin_bit_cast(u32x4, acc[4 + i]));
    } else {
        const unsigned p89 = pk_bf16(kacc[8] + lds_rf(xc), kacc[9] + lds_rf(xc + SP)), pab = pk_bf16(kacc[10] + lds_rf(xc + 2 * SP), kacc[11] + lds_rf(xc + 3 * SP));
#pragma unroll
        for (int c = 0; c < 4; ++c) {
            lds_w16(q2[c] + 64, p89); lds_w16(q2[c], p89 >> 16);
            lds_w16(q3[c] + 64, pab); lds_w16(q3[c], pab >> 16);
        }
        CFENCE();
        FX &= ((unsigned)__builtin_amdgcn_readlane(p89, 63) >> 16) * 0x10001u;
        lptr pf2 = pt + cpy * 320 + ((79 - 64 - lo16 + 4 * g - cpy + 4) >> 2) * 16;
        lptr pf3 = pt + PC_OFF + cpy * 320 + ((127 - 112 - lo16 + 4 * g - cpy + 4) >> 2) * 16;
        u32x4 TA[3], TB[3], TCc[4];
#pragma unroll
        for (int i = 0; i < 3; ++i) { TA[i] = lds_r128(pf2 + 64 * (2 - i)); TB[i] = lds_r128(pf2 + 1280 + 64 * (2 - i)); }
#pragma unroll
        for (int i = 0; i < 4; ++i) TCc[i] = lds_r128(pf3 + 64 * (3 - i));
    if (w < 8) {
        lptr xb = smem + XT_OFF + (XPAD + tt * 4) * 2;
#pragma unroll
        for (int c = 0; c < 3; ++c) {
            u32x2 lo = {__builtin_amdgcn_perm(xr[1][c], xr[0][c], 0x05040100u), __builtin_amdgcn_perm(xr[3][c], xr[2][c], 0x05040100u)};
            u32x2 hi = {__builtin_amdgcn_perm(xr[1][c], xr[0][c], 0x07060302u), __builtin_amdgcn_perm(xr[3][c], xr[2][c], 0x07060302u)};
            lds_w64(xb + (2 * c) * XT_BYTES, lo); lds_w64(xb + (2 * c + 1) * XT_BYTES, hi);
        }
    }
    if (tid == 704) tag_post(P, tag);
    if (w < 7 && w * 12 + li < 81) touch_group(P, xcd, 189 + w * 12 + li, lane);
        BARRIER();
        STAMP(6);
        u32x4 X[8];
#pragma unroll
        for (int i = 0; i < 8; ++i) X[i] = lds_r128(xf + 512 * i);
        const f32x4 z4 = {0.f, 0.f, 0.f, 0.f};
#pragma unroll
        for (int st = 0; st < 6; ++st) {
            const u32x4 Xc = lds_r128(xf + 512 * st - 16);
            const u32x4 pa = pack8(mfma16(X[st], F21, z4), mfma16(X[st], F22, z4));
            const u32x4 pb = pack8(mfma16(X[st], F23, z4), mfma16(Xc, F22, z4));
#pragma unroll
            for (int dl = 2; dl <= 4; ++dl) if (st + dl < 8) {
                acc[st + dl] = mfma16(pa, TA[dl - 2], acc[st + dl]);
                acc[st + dl] = mfma16(pb, TB[dl - 2], acc[st + dl]);
            }
            if (st < 4) {
                const u32x4 pc = pack8(mfma16(X[st], F31, z4), mfma16(Xc, F32, z4));
#pragma unroll
                for (int dl = 4; dl <= 7; ++dl) if (st + dl < 8) acc[st + dl] = mfma16(pc, TCc[dl - 4], acc[st + dl]);
            }
        }
        const u32x4 k0 = TF(1024), k8 = TF(512), k16 = TF(0);
#pragma unroll
        for (int i = 4; i < 8; ++i) acc[i] = mfma16(FX, X[i - 4], acc[i]);
#pragma unroll
        for (int i = 0; i < 8; ++i) acc[i] = mfma16(k0, X[i], acc[i]);
#pragma unroll
        for (int i = 1; i < 8; ++i) acc[i] = mfma16(k8, X[i - 1], acc[i]);
#pragma unroll
        for (int i = 2; i < 8; ++i) acc[i] = mfma16(k16, X[i - 2], acc[i]);
        axr = smem + ACX_OFF + ch * 8192 + lane * 16; axw = axr + 4096;
#pragma unroll
        for (int i = 0; i < 4; ++i) { lds_w128(axw + i * 1024, __builtin_bit_cast(u32x4, acc[i])); acc[i] = acc[4 + i]; }
    }
    STAMP(7);
    BARRIER();
    STAMP(8);
#pragma unroll
    for (int i = 0; i < 2; ++i) {
        const f32x4 ya = acc[2 * i] + __builtin_bit_cast(f32x4, lds_r128(axr + (2 * i) * 1024));
        const f32x4 yb = acc[2 * i + 1] + __builtin_bit_cast(f32x4, lds_r128(axr + (2 * i + 1) * 1024));
        const auto sx = __builtin_amdgcn_permlane16_swap(pk_bf16(ya[0], ya[1]), pk_bf16(yb[0], yb[1]), false, false);
        const auto sy = __builtin_amdgcn_permlane16_swap(pk_bf16(ya[2], ya[3]), pk_bf16(yb[2], yb[3]), false, false);
        st_sc1(wsr, WS_B + (unsigned)(d * 4096 + half * 2048 + i * 1024 + lane * 16), u32x4{sx[0], sy[0], sx[1], sy[1]});
    }
    WAIT_VM(0);
    BARRIER();
    if (tid == 704) __hip_atomic_store((GAS u64*)(P.ws + WS_BFL + cg6 * 128), tag, RLX_AGENT);
    STAMP(9);
    if (w < 2) STAMP_OUT(2 + 2 * w, blockIdx.x + 256 * w);
}

constexpr int SLOT = 34816, XCH = 16384, KVT_OFF = 151296, KVP_OFF = 27648;
DEVI constexpr int slot_off(int s) { return (s & 3) == 0 ? SLOT : ((s & 3) == 1 ? 0 : (s & 3) * SLOT); }
DEVI constexpr int wq_off(int t) { return t == 0 ? 0 : 49152 + t * 27648; }

constexpr int quad_src(int quad) {
    const int pp = quad >= 192 ? 1 : 0, qi = quad - 192 * pp;
    const bool isz = qi >= 180;
    const int el = isz ? 0 : qi / 5, qd = qi - el * 5, hi = el >= 18 ? 1 : 0;
    const int dql = isz ? (qi < 189 ? qi - 180 : 0) : qd + 4 * hi;
    const int et = isz ? 0 : (el < 4 ? 0 : (el < 20 ? 1 : 2)), col = isz ? 0 : (el < 4 ? 12 + el : (el < 20 ? el - 4 : el - 20));
    const int tile = dql == 8 ? 5 : (et == 0 ? (dql >> 2) : (et == 1 ? 2 + (dql >> 2) : 4));
    return KVP_OFF + pp * 6144 + tile * 1024 + ((dql == 8 ? et : (dql & 3)) * 16 + col) * 4;
}
constexpr int quad_dst(int quad) {
    const int pp = quad >= 192 ? 1 : 0, qi = quad - 192 * pp;
    const bool isz = qi >= 180;
    const int el = isz ? 0 : qi / 5, qd = qi - el * 5, hi = el >= 18 ? 1 : 0;
    const int dql = isz ? (qi < 189 ? qi - 180 : 0) : qd + 4 * hi;
    const int dq = 9 * pp + dql, ks = dq >> 3, e = 36 * pp + el, ee = e >> 4;
    const int fr = ee == 0 ? 0 : (ee < 3 ? 2 * ee - 1 + ks : 2 * ee - 2 + ks);
    const int a = KVT_OFF + (isz ? (9 + ks) * 1024 + (dq & 3) * 256 + (2 * pp + (dql > 4 ? 1 : 0)) * 16 : fr * 1024 + ((dq & 3) * 16 + (e & 15)) * 16) + ((dq >> 2) & 1) * 8;
    return qi >= 189 ? LDS_DUMMY : (a | (isz ? (dql == 4 ? 0x300003 : 0x100003) : ((!hi && qd == 4) ? 1 : ((hi && qd == 0) ? 2 : 3))));
}
struct Tab { int v[2048]; };
constexpr Tab make_tab() {
    Tab t{};
    for (int c = 0; c < 192; ++c) {
        const int l = c & 63, q = 2 * ((c & 192) + (l >> 5) * 16 + (l & 15)) + ((l >> 4) & 1);
        t.v[4 * c] = quad_src(2 * c); t.v[4 * c + 1] = quad_src(2 * c + 1); t.v[4 * c + 2] = quad_dst(q); t.v[4 * c + 3] = quad_dst(q + 64);
    }
    for (int q = 0; q < 576; ++q) {
        const int rho = q / 9, pc = q % 9, k = (rho & 32) + 8 * ((rho >> 3) & 3) + 4 * (rho & 1) + ((rho >> 1) & 3), m = q >> 6;
        t.v[768 + q] = k * 3456 + pc * 16 - (m & 1) * 1024;
        t.v[1344 + q] = k * 1152 + pc * 16 - (m & 3) * 1024;
    }
    return t;
}
__device__ const Tab g_tab = make_tab();
constexpr int IDX = P2_B;

DEVI void gemm_compute(const Params& P, lptr smem, int rb, int hg, int w, u64& tag, rsrc_t wsr) {
    const int lane = threadIdx.x & 63, lo16 = lane & 15, g = lane >> 4;
    const int rg = w >> 1, p = w & 1;
    STAMP_DECL; STAMP(1);
    if (w == 0 && lane < 16) lds_w32(smem + FL + lane * 4, 0u);
    if (w >= 4) {
#pragma unroll
        for (int i = 0; i < 3; ++i) lds_w128(smem + KVT_OFF + i * 4096 + (w - 4) * 1024 + lane * 16, u32x4{0u, 0u, 0u, 0u});
    }
    PIN_TAG();
    __builtin_amdgcn_s_setprio(2);
    const int row0 = 16 * (2 * rg + p) + lo16, row1 = 16 * (2 * rg + 1 - p) + lo16;
    const int sw = (g ^ ((lo16 >> 1) & 7)) << 4;
    lptr xa0[2] = {smem + row0 * 128 + sw, smem + row0 * 128 + (sw ^ 64)}, xa1[2] = {smem + row1 * 128 + sw, smem + row1 * 128 + (sw ^ 64)};
    const int wrow = (4 * g + (lo16 >> 2)) * 288;
    lptr wa = smem + XCH + wrow + (9 * p + (lane & 3)) * 8, wa2 = smem + XCH + wrow + (9 * p + ((lane & 3) == 3 ? 18 : 8)) * 8;
    lptr flp = smem + FL;
    asm volatile("" : "+v"(flp));
    const f32x4 z4 = {0.f, 0.f, 0.f, 0.f};
    f32x4 acc[2][5];
#pragma unroll
    for (int i = 0; i < 2; ++i)
#pragma unroll
        for (int j = 0; j < 5; ++j) { const float e = j < 2 ? 1e-6f : (j == 2 && lo16 < 12 ? 1e-6f : 0.f); acc[i][j] = f32x4{e, e, e, e}; }
    u32x4 xk[18], x1[2];
    auto rel = [&](int s) { return (s & 2) ? slot_off(s) - 2 * SLOT : slot_off(s); };
    auto rebase = [&](int dlt) {
        xa0[0] += dlt; xa0[1] += dlt; xa1[0] += dlt; xa1[1] += dlt; wa += dlt; wa2 += dlt;
        asm volatile("" : "+v"(xa0[0]), "+v"(xa0[1]), "+v"(xa1[0]), "+v"(xa1[1]), "+v"(wa), "+v"(wa2));
    };
    constexpr int NB = 8;
    u32x2 wl[NB], wh[NB];
    auto rdw = [&](int f, int t) {
        const int n = f / 5, j = f % 5;
        const int o = rel(n >> 1) + (n & 1) * 9216 + t * 4608;
        const u32x2 v = j == 2 ? lds_tr(wa2 + o) : lds_tr(wa + o + (j == 0 ? 0 : (j == 1 ? 32 : (j == 3 ? 152 : 184))));
        if (t) wh[f % NB] = v; else wl[f % NB] = v;
    };
    auto peek = [&](int f) { return __hip_atomic_load((LAS unsigned*)(flp + f), __ATOMIC_RELAXED, __HIP_MEMORY_SCOPE_WORKGROUP); };
    asm volatile(".subsection 2\n.Lkf_code_end:\n\t.fill 256, 4, 0xbf800000\n\t.subsection 0");
    unsigned one = 1u;
    asm volatile("" : "+v"(one));
    rebase(0);
#pragma unroll
    for (int j = 0; j < 5; ++j) asm volatile("" : "+v"(acc[0][j]), "+v"(acc[1][j]));
    BARRIER();
    STAMP(2);
    xk[0] = lds_r128(xa0[0] + rel(0)); x1[0] = lds_r128(xa1[0] + rel(0));
#pragma unroll
    for (int f = 0; f < NB - 1; ++f) { rdw(f, 0); rdw(f, 1); }
#pragma unroll
    for (int n = 0; n < 18; ++n) {
        const int nx = n + 1, s = n >> 1;
#pragma unroll
        for (int m = 0; m < 10; ++m) {
            const int j = m >> 1, f = 5 * n + j, fn = f + NB - 1;
            SCHED();
            if (!(m & 1) && !(f & 1) && fn < 88) WAIT_LGKM(10);
            const u32x4 wv = {wl[f % NB].x, wl[f % NB].y, wh[f % NB].x, wh[f % NB].y};
            if (m & 1) mfma16i(acc[1][j], x1[n & 1], wv); else mfma16i(acc[0][j], xk[n], wv);
            SCHED();
            if (!(m & 1) && fn % 10 == 0) {
                if (s < 8 && (s & 1)) rebase((s & 2) ? -2 * SLOT : 2 * SLOT);
                if (s == 8) WAIT_LGKM0();
                RAW_BARRIER();
                if (s == 0) STAMP(3);
                if (s == 4) STAMP(4);
            }
            if (fn < 90) rdw(fn, m & 1);
            if (nx < 18) {
                if (m == 0) xk[nx] = lds_r128(xa0[nx & 1] + rel(nx >> 1));
                if (m == 2) x1[nx & 1] = lds_r128(xa1[nx & 1] + rel(nx >> 1));
            }
        }
    }
    MFMA_DONE();
    STAMP(5);
    const int qrow = (2 * (4 * g + (lo16 >> 2))) * 144;
    lptr qw = smem + qrow + (lane & 3) * 8, qw4 = smem + qrow + (16 + (lane & 1)) * 8;
    lptr qwh = qw + wq_off(1), qw4h = qw4 + wq_off(1);
    asm volatile("" : "+v"(qwh), "+v"(qw4h));
    auto rdq = [&](int ks, int j) {
        const int o = (ks < 6 ? ks : ks - 6) * 4608;
        return j < 4 ? lds_trfrag((ks < 6 ? qw : qwh) + o + 32 * j, 144) : lds_trfrag((ks < 6 ? qw4 : qw4h) + o, 144);
    };
    {
        u32x4 pk[3], vb[3];
#pragma unroll
        for (int j = 0; j < 3; ++j) pk[j] = pack8(phi4(acc[0][j]), phi4(acc[1][j]));
        vb[0] = pack8(acc[0][2], acc[1][2]);
        if (lo16 < 12) vb[0] = u32x4{0x3f803f80u, 0x3f803f80u, 0x3f803f80u, 0x3f803f80u};
        vb[1] = pack8(acc[0][3], acc[1][3]); vb[2] = pack8(acc[0][4], acc[1][4]);
        f32x4 r[6];
        const f32x4 t0 = mfma16(pk[2], vb[0], z4), t1 = mfma16(pk[2], vb[1], z4), t2 = mfma16(pk[2], vb[2], z4);
        r[0] = mfma16(pk[0], vb[0], z4); r[1] = mfma16(pk[1], vb[0], z4); r[2] = mfma16(pk[0], vb[1], z4);
        r[3] = mfma16(pk[1], vb[1], z4); r[4] = mfma16(pk[1], vb[2], z4);
        r[5] = g == 0 ? t0 : (g == 1 ? t1 : t2);
        SCHED();
        asm volatile("s_nop 7" ::: "memory");
        SET_M0(KVP_OFF - 8192 + w * 6144);
#pragma unroll
        for (int t = 5; t >= 0; --t) {
            W_ADDTID(r[t][0], 8192 + t * 1024); W_ADDTID(r[t][1], 8192 + t * 1024 + 256);
            W_ADDTID(r[t][2], 8192 + t * 1024 + 512); W_ADDTID(r[t][3], 8192 + t * 1024 + 768);
        }
        FLAG_ADD(F_KVP);
        __builtin_amdgcn_s_setprio(0);
    }
    STAMP(6);
    f32x4 qa[5];
#pragma unroll
    for (int j = 0; j < 5; ++j) qa[j] = f32x4{1e-6f, 1e-6f, 1e-6f, 1e-6f};
#pragma unroll
    for (int j = 0; j < 5; ++j) asm volatile("" : "+v"(qa[j]));
    SCHED();
    u32x4 wq[2][5];
#pragma unroll
    for (int i = 0; i < 2; ++i)
#pragma unroll
        for (int j = 0; j < 5; ++j) wq[i][j] = rdq(i, j);
    SCHED();
    flag_wait<1>(smem, F_KVR, 3);
    unsigned seenq = 0;
#pragma unroll
    for (int ks = 0; ks < 18; ++ks) {
        if (ks == 2) seenq = peek(F_LANDED);
        if (ks == 4) { SCHED(); FLAG_ADD(F_Q0); NEED(seenq, F_LANDED, 4); }
        if (ks == 16) { SCHED(); FLAG_ADD(F_QD); }
#pragma unroll
        for (int j = 0; j < 5; ++j) {
            SCHED();
            if (j == 0 && ks < 17) WAIT_LGKM(10);
            mfma16i(qa[j], wq[ks & 1][j], xk[ks]);
            SCHED();
            if (ks + 2 < 18) wq[ks & 1][j] = rdq(ks + 2, j);
        }
    }
    MFMA_DONE();
    u32x4 b0 = pack8(phi4(qa[0]), phi4(qa[1])), b1 = pack8(phi4(qa[2]), phi4(qa[3])), b2 = pack8(phi4(qa[4]), z4);
    lptr kfp = smem + KVT_OFF + lane * 16;
    unsigned ob0 = WS_O + (unsigned)(((rb * 8 + 2 * rg + p) * 72 + hg * 9 + 2 * (g & 1) + (g >> 1)) * 256 + lo16 * 16);
    asm volatile("" : "+v"(b0), "+v"(b1), "+v"(b2), "+v"(kfp), "+v"(ob0));
    STAMP(7);
    flag_wait<1>(smem, F_D, 3);
    if (w == 7 && lane == 0) __hip_atomic_store((GAS u64*)(P.ws + WS_HINT + (rb * 8 + hg) * 128), tag, RLX_AGENT);
    STAMP(8);
    {
        u32x4 kf[12];
#pragma unroll
        for (int i = 0; i < 12; ++i) kf[i] = lds_r128(kfp + (i < 3 ? 9 + i : i - 3) * 1024);
        SCHED();
        const f32x4 den = mfma16(kf[2], b2, mfma16(kf[1], b1, mfma16(kf[0], b0, f32x4{1e-6f, 1e-6f, 1e-6f, 1e-6f})));
        f32x4 o[5];
        o[0] = mfma16(kf[3], b0, z4);
        o[1] = mfma16(kf[5], b1, mfma16(kf[4], b0, z4));
        o[2] = mfma16(kf[7], b1, mfma16(kf[6], b0, z4));
        o[3] = mfma16(kf[9], b2, mfma16(kf[8], b1, z4));
        o[4] = mfma16(kf[11], b2, mfma16(kf[10], b1, z4));
        f32x4 r;
#pragma unroll
        for (int i = 0; i < 4; ++i) r[i] = __builtin_amdgcn_rcpf(den[i]);
        const float r1a = g == 0 ? r[0] : r[1], r2 = g == 0 ? r[1] : r[2], r3a = g <= 1 ? r[2] : r[3], r3b = g == 0 ? r[2] : r[3];
        u32x2 ob[5];
        ob[0] = u32x2{pk_bf16(o[0][0] * r[0], o[0][1] * r[0]), pk_bf16(o[0][2] * r[0], o[0][3] * r[0])};
        ob[1] = u32x2{pk_bf16(o[1][0] * r1a, o[1][1] * r1a), pk_bf16(o[1][2] * r[1], o[1][3] * r[1])};
        ob[2] = u32x2{pk_bf16(o[2][0] * r2, o[2][1] * r2), pk_bf16(o[2][2] * r2, o[2][3] * r2)};
        ob[3] = u32x2{pk_bf16(o[3][0] * r3a, o[3][1] * r3a), pk_bf16(o[3][2] * r3b, o[3][3] * r3b)};
        ob[4] = u32x2{pk_bf16(o[4][0] * r[3], o[4][1] * r[3]), pk_bf16(o[4][2] * r[3], o[4][3] * r[3])};
#pragma unroll
        for (int t = 0; t < 3; ++t) {
            const u32x2 a = ob[2 * t], b = ob[t < 2 ? 2 * t + 1 : 4];
            const auto sx = __builtin_amdgcn_permlane16_swap(a.x, b.x, false, false), sy = __builtin_amdgcn_permlane16_swap(a.y, b.y, false, false);
            if (t < 2 || g == 0) st_sc1(wsr, ob0 + 1024 * t, not_empty(u32x4{sx[0], sy[0], sx[1], sy[1]}));
        }
    }
    STAMP(9);
    if (w == 0) STAMP_OUT(1, blockIdx.x);
    if (w == 7) STAMP_OUT(7, blockIdx.x + 768);
}

DEVI void gemm_loader(const Params& P, lptr smem, int rb, int hg, int lw, int cg2, u64& tag, rsrc_t wsr) {
    const int lane = threadIdx.x & 63;
    STAMP_DECL; STAMP(1);
    __builtin_amdgcn_s_setprio(3);
    unsigned xo[4], wo_[5], qo[3];
    xo[0] = (unsigned)((rb * 128 + 32 * lw + (lane >> 3)) * 1152 + (((lane & 7) ^ (lane >> 4)) << 4));
    xo[1] = (xo[0] ^ 64u) + (9216u - 1024u); xo[2] = xo[0] + (18432u - 2048u); xo[3] = xo[1] + (18432u - 2048u);
    const unsigned lb = lw * 4096;
    auto issue_x = [&](int s) {
        const char* xs = P.x + s * 128;
        SET_M0(slot_off(s) + lb);
        glds16o<0>(xs, xo[0]); glds16o<1024>(xs, xo[1]); glds16o<2048>(xs, xo[2]); glds16o<3072>(xs, xo[3]);
    };
    auto issue_w = [&](int s) {
        const char* ws = P.wqkv + s * (64 * 3456);
        if ((lw >> 1) == (s & 1)) glds16(ws, wo_[4], (unsigned)(slot_off(s) + XCH + 16384 + (lw & 1) * 1024));
        SET_M0(slot_off(s) + XCH + lb);
        glds16o<0>(ws, wo_[0]); glds16o<1024>(ws, wo_[1]); glds16o<2048>(ws, wo_[2]); glds16o<3072>(ws, wo_[3]);
    };
    auto issue_q = [&](int t, int i) {
        const char* ws = P.wqkv + hg * 144 + (3 * t + i) * (64 * 3456);
        if (lw == ((i + t) & 3)) glds16(ws, qo[2], (unsigned)(wq_off(t) + i * 9216 + 8192));
        SET_M0(wq_off(t) + i * 9216 + (lb >> 1));
        glds16o<0>(ws, qo[0]); glds16o<1024>(ws, qo[1]);
    };
    issue_x(0);
    SCHED();
#pragma unroll
    for (int i = 0; i < 5; ++i) {
        const unsigned q = 64 * (i < 4 ? 4 * lw + i : 16 + (lw & 1)) + lane, rho = (q * 3641u) >> 16, pc = q - rho * 18;
        const unsigned k = (rho & 35u) + 8 * ((rho >> 2) & 3) + 4 * ((rho >> 4) & 1);
        wo_[i] = k * 3456 + (1152 + hg * 144 - (i < 4 ? i * 1024 : 0)) + pc * 16 + (pc < 9 ? 0u : 1008u);
    }
    issue_w(0); issue_w(1); issue_x(1);
    const int lo16 = lane & 15, g = lane >> 4, ct = (lw < 3 ? lw : 0) * 64 + lane;
    lptr flp = smem + FL, ixp = smem + IDX + ct * 16, irp = smem + IDX + 3072 + lane * 4, iqp = irp + lw * 512;
    unsigned one = 1u;
    u32x4 sel[2];
    sel[0] = mask_frag(lo16 < 8 ? 1u << lo16 : 0u, 0, 0x3f80u); sel[1] = mask_frag(lo16 >= 8 ? 1u << (lo16 - 8) : 0u, 0, 0x3f80u);
    unsigned sto = WS_PART + (unsigned)((hg * 16 + rb) * 3072 + ct * 16);
    unsigned off = WS_PART + (unsigned)((hg * 16 + g) * 3072 + lw * 1024 + lo16 * 16);
    asm volatile("" : "+v"(flp), "+v"(one), "+v"(ixp), "+v"(irp), "+v"(iqp));
    asm volatile("" : "+v"(sel[0]), "+v"(sel[1]), "+v"(sto), "+v"(off));
    SET_M0(IDX + (lb >> 1));
    glds16o<0>((const char*)&g_tab + (lb >> 1), (unsigned)(lane * 16)); glds16o<1024>((const char*)&g_tab + (lb >> 1), (unsigned)(lane * 16));
    PIN_TAG();
    WAIT_VM(10); BARRIER();
    issue_w(2); issue_x(2);
    if (lw == 0 && lane == 0) tag_post(P, tag);
    u32x4 ix; unsigned oo[9];
#pragma unroll
    for (int s = 0; s < 9; ++s) {
        if (s < 7) WAIT_VM(8); else WAIT_VM(6);
        RAW_BARRIER();
        if (s < 6) { issue_w(s + 3); issue_x(s + 3); } else { issue_q(s - 6, 0); issue_q(s - 6, 1); issue_q(s - 6, 2); }
        if (s == 3) {
            qo[0] = *(LAS unsigned*)iqp; qo[1] = *(LAS unsigned*)(iqp + 256); qo[2] = *(LAS unsigned*)(irp + 2048);
            ix = lds_r128(ixp);
#pragma unroll
            for (int i = 0; i < 9; ++i) oo[i] = *(LAS unsigned*)(irp + 2304 + i * 256);
            CFENCE();
        }
    }
    if (lw < 3) {
        const int dst[2] = {(int)ix.z, (int)ix.w};
        WAIT_VM(0); FLAG_ADD(F_LANDED);
        flag_wait<0>(smem, F_KVP, 9);
        STAMP(2);
        float pa[2][4][4];
#pragma unroll
        for (int i = 0; i < 2; ++i)
#pragma unroll
            for (int r = 0; r < 4; ++r)
#pragma unroll
                for (int k = 0; k < 4; ++k) pa[i][r][k] = lds_rf(smem + ix[i] + r * 12288 + k * 256);
        SCHED();
        u32x4 pv;
#pragma unroll
        for (int i = 0; i < 2; ++i)
#pragma unroll
            for (int k = 0; k < 2; ++k)
                pv[2 * i + k] = pk_bf16((pa[i][0][2 * k] + pa[i][1][2 * k]) + (pa[i][2][2 * k] + pa[i][3][2 * k]),
                                        (pa[i][0][2 * k + 1] + pa[i][1][2 * k + 1]) + (pa[i][2][2 * k + 1] + pa[i][3][2 * k + 1]));
        st_sc1(wsr, sto, not_empty(pv));
        SCHED();
        FLAG_ADD(F_KVR);
        STAMP(3);
        WAIT_VM(0);
        STAMP(4);
        f32x4 sum[2];
#pragma unroll 1
        for (unsigned spins = 0; spins < SPIN_CAP; ++spins) {
            u32x4 a[4][4];
#pragma unroll
            for (int t = 0; t < 4; ++t)
#pragma unroll
                for (int i = 0; i < 4; ++i) a[t][i] = __builtin_amdgcn_raw_buffer_load_b128(wsr, off + t * 256, i * 12288, 16);
            SCHED();
            FLAG_ADD(F_SW);
            sum[0] = sum[1] = f32x4{0.f, 0.f, 0.f, 0.f};
#pragma unroll
            for (int t = 0; t < 4; ++t)
#pragma unroll
                for (int i = 0; i < 4; ++i) sum[t >> 1] = mfma16(sel[t & 1], a[t][i], sum[t >> 1]);
            if (__all(sum[0][0] == sum[0][0] && sum[1][0] == sum[1][0])) break;
            __builtin_amdgcn_s_sleep(RSLEEP);
            if (spins < 8) { CFENCE(); continue; }
            unsigned m = 0;
#pragma unroll
            for (int t = 0; t < 4; ++t)
#pragma unroll
                for (int i = 0; i < 4; ++i) m = max(m, umax4(a[t][i]));
            if (__all(m != EMPTY)) break;
            CFENCE();
        }
#pragma unroll
        for (int i = 0; i < 2; ++i) {
            const unsigned lo = (dst[i] & 1) ? pk_bf16(sum[i][0], sum[i][1]) : 0u, hi = (dst[i] & 2) ? pk_bf16(sum[i][2], sum[i][3]) : 0u;
            lptr dp = smem + (dst[i] & 0xffffc);
            if (dst[i] & 0x100000) {
                const int hs = (dst[i] & 0x200000) ? 20 : 4;
#pragma unroll
                for (int r = 0; r < 4; ++r) { lds_w32(dp + 64 * r, lo); lds_w32(dp + 64 * r + hs, hi); }
            } else lds_w64(dp, u32x2{lo, hi});
        }
        CFENCE();
        FLAG_ADD(F_D);
        STAMP(5);
    } else {
        auto wo_chunks = [&](int c0, int c1) {
#pragma unroll 1
            for (int c = c0; c < c1; ++c) {
                const char* ws = P.wo + cg2 * 144 + c * (64 * 1152);
                const unsigned l = c < 8 ? c * 9216 : WO8;
                SET_M0(l); glds16o<0>(ws, oo[0]); glds16o<1024>(ws, oo[1]); glds16o<2048>(ws, oo[2]); glds16o<3072>(ws, oo[3]);
                SET_M0(l + 4096); glds16o<0>(ws, oo[4]); glds16o<1024>(ws, oo[5]); glds16o<2048>(ws, oo[6]); glds16o<3072>(ws, oo[7]);
                SET_M0(l + 8192); glds16o<0>(ws, oo[8]);
            }
        };
        poll_flags(P, WS_CF, 32, tag, lane);
        WAIT_VM(0); FLAG_ADD(F_LANDED); FLAG_ADD(F_KVP);
        wo_chunks(8, 9);
        flag_wait(smem, F_KVR, 3);
        wo_chunks(3, 8);
        WAIT_VM(0);
        FLAG_ADD(F_W1);
        flag_wait(smem, F_SW, 3);
        flag_wait(smem, F_Q0, 8);
        wo_chunks(0, 3);
        WAIT_VM(0);
        FLAG_ADD(F_W0);
    }
    __builtin_amdgcn_s_setprio(0);
    STAMP(6);
    if (lw == 0) STAMP_OUT(5, blockIdx.x + 256);
}

DEVI void phase2(const Params& P, lptr smem, int rt64, int cg, bool gemm, u64 tag, rsrc_t wsr) {
    const int tid = threadIdx.x, lane = tid & 63, w = __builtin_amdgcn_readfirstlane(tid >> 6);
    const int lo16 = lane & 15, g = lane >> 4;
    const int rt = w & 3, kt = w >> 2;
    STAMP_DECL; STAMP(1);
    u32x4 bv[3]; unsigned gv;
    if (w < 4) {
        if (w == 3) { poll_addr(P, lane < 12 ? WS_BFL + (cg * 12 + lane) * 128 : WS_GFL + rt64 * 128, tag); flag_set(F_B, 1); } else flag_wait(smem, F_B, 1);
        const unsigned boff = WS_B + (unsigned)((cg * 72 + (tid >> 3)) * 4096 + (rt64 >> 3) * 1024 + (((tid >> 2) & 1) * 32 + ((rt64 >> 2) & 1) * 16 + (rt64 & 3) * 4 + (tid & 3)) * 16);
        bv[0] = ld_sc1(wsr, boff); bv[1] = ld_sc1(wsr, boff + 32 * 4096); bv[2] = ld_sc1(wsr, boff + (w == 0 ? 64 * 4096 : 0));
        gv = __hip_atomic_load((GAS unsigned*)(P.ws + WS_GATE) + (rt64 * 64 + lane), RLX_AGENT);
    }
    if (gemm) flag_wait(smem, w < 4 ? F_W0 : F_W1, 1);
    STAMP(2);
    const int qrow = (2 * (4 * g + (lo16 >> 2))) * 144 + kt * 6 * 4608;
    lptr qw = smem + qrow + (lane & 3) * 8, qw4 = smem + qrow + (16 + (lane & 1)) * 8;
    const int hop = kt == 2 ? WO8 - 73728 : 0;
    auto rdo = [&](int i, int j) { return j < 4 ? lds_trfrag(qw + (i >= 4 ? hop : 0) + i * 4608 + 32 * j, 144) : lds_trfrag(qw4 + (i >= 4 ? hop : 0) + i * 4608, 144); };
    const unsigned ooff = WS_O + (unsigned)(((rt64 * 4 + rt) * 18 + kt * 6) * 1024);
    u32x4 wf[6][5];
#pragma unroll
    for (int i = 0; i < 6; ++i) {
        if (i == 5 && w < 4) {
            lptr bp = smem + P2_B + ((tid & 3) * 16 + ((tid >> 2) & 1) * 8) * 144 + (tid >> 3) * 2;
#pragma unroll
            for (int k = 0; k < 3; ++k) if (k < 2 || w == 0) {
#pragma unroll
                for (int q = 0; q < 4; ++q) { lds_w16(bp + 64 * k + (2 * q) * 144, bv[k][q]); lds_w16(bp + 64 * k + (2 * q + 1) * 144, bv[k][q] >> 16); }
            }
            if (w == 1) lds_w32(smem + P2_G + lane * 4, gv);
        }
#pragma unroll
        for (int j = 0; j < 5; ++j) wf[i][j] = rdo(i, j);
    }
    STAMP(3);
    if (w == 3) { poll_flags<OSLEEP>(P, WS_HINT + (rt64 >> 1) * 1024, 8, tag, lane); flag_set(F_H, 1); } else flag_wait<1>(smem, F_H, 1);
    STAMP(4);
    f32x4 acc[5];
    {
        u32x4 of[6];
#pragma unroll
        for (int i = 0; i < 6; ++i) of[i] = ld_sc1(wsr, ooff + (unsigned)(i * 1024 + lane * 16));
#pragma unroll
        for (int j = 0; j < 5; ++j) acc[j] = f32x4{0.f, 0.f, 0.f, 0.f};
#pragma unroll
        for (int i = 0; i < 6; ++i)
#pragma unroll
            for (int j = 0; j < 5; ++j) acc[j] = mfma16(wf[i][j], of[i], acc[j]);
    }
    if (!__all(acc[0][0] == acc[0][0])) {
#pragma unroll
        for (int j = 0; j < 5; ++j) acc[j] = f32x4{0.f, 0.f, 0.f, 0.f};
        u32x4 of[6];
#pragma unroll 1
        for (unsigned spins = 0; spins < SPIN_CAP; ++spins) {
            CFENCE();
            unsigned m = 0;
#pragma unroll
            for (int i = 0; i < 6; ++i) of[i] = ld_sc1(wsr, ooff + (unsigned)(i * 1024 + lane * 16));
#pragma unroll
            for (int i = 0; i < 6; ++i) m = max(m, umax4(of[i]));
            if (__all(m != EMPTY)) break;
        }
#pragma unroll
        for (int i = 0; i < 6; ++i)
#pragma unroll
            for (int j = 0; j < 5; ++j) acc[j] = mfma16(rdo(i, j), of[i], acc[j]);
    }
    if (gemm) flag_wait(smem, F_QD, 8);
    lptr ap = smem + P2_ACC + kt * (64 * P2_AP) + (rt * 16 + lo16) * P2_AP + g * 16;
#pragma unroll
    for (int j = 0; j < 5; ++j) if (j < 4 || g < 2) lds_w128(ap + j * 64, __builtin_bit_cast(u32x4, acc[j]));
    STAMP(5);
    BARRIER();
    STAMP(6);
    if (w < 9) {
        const int r0 = tid / 9, c0 = tid - r0 * 9;
        lptr a = smem + P2_ACC + r0 * P2_AP + c0 * 32;
        const f32x4 s0 = __builtin_bit_cast(f32x4, lds_r128(a)) + __builtin_bit_cast(f32x4, lds_r128(a + 64 * P2_AP)) + __builtin_bit_cast(f32x4, lds_r128(a + 128 * P2_AP));
        const f32x4 s1 = __builtin_bit_cast(f32x4, lds_r128(a + 16)) + __builtin_bit_cast(f32x4, lds_r128(a + 64 * P2_AP + 16)) + __builtin_bit_cast(f32x4, lds_r128(a + 128 * P2_AP + 16));
        const u32x4 bb = lds_r128(smem + P2_B + tid * 16);
        const float gt = lds_rf(smem + P2_G + r0 * 4), og = rbf(1.f - gt);
        u32x4 o;
#pragma unroll
        for (int i = 0; i < 4; ++i) {
            const float a0 = i < 2 ? s0[2 * i] : s1[2 * i - 4], a1 = i < 2 ? s0[2 * i + 1] : s1[2 * i - 3];
            o[i] = pk_bf16(gt * a0 + og * bf2f(bb[i]), gt * a1 + og * bfhi(bb[i]));
        }
        *(GAS u32x4*)(P.out + (unsigned)((rt64 * 64 + r0) * 1152 + cg * 144 + c0 * 16)) = o;
    }
    STAMP(7);
    if (w == 4) STAMP_OUT(blockIdx.x < 128 ? 3 : 6, blockIdx.x + 512);
}

DEVI void helper_role(const Params& P, lptr smem, int xcd, int hi, u64& tag, rsrc_t wsr) {
    const int tid = threadIdx.x, lane = tid & 63, w = __builtin_amdgcn_readfirstlane(tid >> 6);
    const int cid = hi * 8 + xcd, blk = (xcd >> 1) * 8 + (xcd & 1) * 4 + hi;
    const u32x4 e4 = {EMPTY, EMPTY, EMPTY, EMPTY};
    STAMP_DECL; STAMP(1);
    if (w < 8) {
        touch_group(P, xcd, w < 4 ? 12 + hi + 16 * w : 48 + hi + 4 * w, lane);
        touch_group(P, xcd, 80 + hi + 4 * w, lane);
        touch_group(P, xcd, 112 + hi + 4 * w, lane);
    }
    if (tid == 704) lds_w32(smem + FL, 0u);
    PIN_TAG();
    BARRIER();
    if (tid == 0) tag_post(P, tag);
    STAMP(2);
    if (w < 9) {
        flag_wait<8>(smem, 0, 3);
        const u32x4 gv = *(const GAS u32x4*)(P.gw + (unsigned)((tid % 72) * 16));
#pragma unroll
        for (int i = 0; i < 8; ++i) {
            const u32x4 xv = *(const GAS u32x4*)(P.x + (unsigned)(blk * 73728 + tid * 16 + i * 9216));
            float s = 0.f;
#pragma unroll
            for (int k = 0; k < 4; ++k) s += bf2f(xv[k]) * bf2f(gv[k]) + bfhi(xv[k]) * bfhi(gv[k]);
            lds_w32(smem + (i * 576 + tid) * 4, __float_as_uint(s));
        }
    } else {
#pragma unroll 1
        for (int i = 0; i < 28; ++i) {
            st_sc1(wsr, WS_PART + (unsigned)(((cid * 3 + w - 9) * 28 + i) * 1024 + lane * 16), e4);
            __builtin_amdgcn_s_sleep(CSLEEP);
        }
        STAMP(3);
        WAIT_VM(0);
        if (lane == 0 && __hip_atomic_fetch_add((LAS unsigned*)(smem + FL), 1u, __ATOMIC_RELAXED, __HIP_MEMORY_SCOPE_WORKGROUP) == 2u)
            __hip_atomic_store((GAS u64*)(P.ws + WS_CF + cid * 128), tag, RLX_AGENT);
        STAMP(4);
    }
    BARRIER();
    if (w == 11) {
        f32x4 s4 = {0.f, 0.f, 0.f, 0.f};
#pragma unroll
        for (int i = 0; i < 18; ++i) s4 += __builtin_bit_cast(f32x4, lds_r128(smem + lane * 288 + i * 16));
        const float s = (s4[0] + s4[1]) + (s4[2] + s4[3]);
        const float gate = rbf(__builtin_amdgcn_rcpf(1.f + __expf(-rbf(s))));
        __hip_atomic_store((GAS float*)(P.ws + WS_GATE) + (blk * 64 + lane), gate, RLX_AGENT);
    }
    STAMP(5);
    if (w == 11) {
        WAIT_VM(0);
        if (lane == 0) __hip_atomic_store((GAS u64*)(P.ws + WS_GFL + blk * 128), tag, RLX_AGENT);
        STAMP(6);
        STAMP_OUT(8, blockIdx.x);
    }
    BARRIER();
    if (w == 11) {
        if (cid == 31) {
            const GAS u64* f = (const GAS u64*)(P.ws + WS_ARR) + lane;
#pragma unroll 1
            for (unsigned spins = 0; spins < SPIN_CAP; ++spins) {
                const bool ok = __hip_atomic_load(f, RLX_AGENT) == tag && __hip_atomic_load(f + 64, RLX_AGENT) == tag &&
                                __hip_atomic_load(f + 128, RLX_AGENT) == tag && __hip_atomic_load(f + 192, RLX_AGENT) == tag;
                if (__all(ok)) break;
                __builtin_amdgcn_s_sleep(8);
            }
            if (lane == 0) __hip_atomic_store((GAS u64*)(P.ws + WS_CTL), tag, RLX_AGENT);
        }
    }
}

__global__ __launch_bounds__(768, 3) void k_fused(Params P) {
    __shared__ __attribute__((aligned(16))) char smem_[LDS_BYTES];
    lptr smem = (lptr)smem_;
    const int b = blockIdx.x, xcd = b & 7, idx = b >> 3;
    const int lane = threadIdx.x & 63, w = __builtin_amdgcn_readfirstlane(threadIdx.x >> 6);
    const rsrc_t wsr = __builtin_amdgcn_make_buffer_rsrc(P.ws, 0, (int)WS_END, 0x00020000);
    const int rt64 = xcd * 4 + (idx & 3), cg2 = idx >> 2;
    if (idx >= 16 && w < 6) {
        const char* pc; unsigned len;
        asm volatile("s_getpc_b64 %0\n.Lkf_code_begin:\n\ts_mov_b32 %1, .Lkf_code_end-.Lkf_code_begin" : "=s"(pc), "=s"(len));
        touch(pc, min((unsigned)(threadIdx.x * 128), len));
    }
    u64 tag = *(const __attribute__((address_space(4))) u64*)(P.ws + WS_CTL);
    if (__builtin_expect(idx < 16, 1)) {
        const int rbl = idx & 3, hgl = idx >> 2, rb = (xcd >> 1) * 4 + rbl, hg = (xcd & 1) * 4 + hgl;
        if (__builtin_expect(w < 4, 1)) gemm_loader(P, smem, rb, hg, w, cg2, tag, wsr);
        else gemm_compute(P, smem, rb, hg, w - 4, tag, wsr);
    } else {
        if (idx < 28) ssm_role(P, smem, xcd, idx - 16, tag, wsr);
        else helper_role(P, smem, xcd, idx - 28, tag, wsr);
        if (w < 9) wo_issue<0x1ff>(P, cg2, w, 9, lane);
        if (w == 10 && lane < 16) lds_w32(smem + FL + lane * 4, 0u);
        if (w == 11) poll_flags(P, WS_CF, 32, tag, lane);
        WAIT_VM(0);
        BARRIER();
    }
    phase2(P, smem, rt64, cg2, idx < 16, tag, wsr);
}

extern "C" void kernel_launch(void* const* d_in, const int* in_sizes, int n_in, void* d_out, int out_size, void* d_ws, size_t ws_size,
                              hipStream_t stream) {
    Params P;
    P.x = (const char*)d_in[0]; P.wqkv = (const char*)d_in[1]; P.wo = (const char*)d_in[2];
    P.logA = (const char*)d_in[4]; P.aim = (const char*)d_in[5]; P.cre = (const char*)d_in[6]; P.cim = (const char*)d_in[7];
    P.logdt = (const char*)d_in[8]; P.dskip = (const char*)d_in[9]; P.gw = (const char*)d_in[10];
    P.out = (char*)d_out; P.ws = (char*)d_ws;
    k_fused<<<256, 768, 0, stream>>>(P);
}
```

```cpp
#include <hip/hip_runtime.h>
#include <stdint.h>

#define GAS __attribute__((address_space(1)))
#define LAS __attribute__((address_space(3)))
#define DEVI __device__ __forceinline__
typedef unsigned short u16;
typedef unsigned long long u64;
typedef __attribute__((ext_vector_type(2))) unsigned u32x2;
typedef __attribute__((ext_vector_type(3))) unsigned u32x3;
typedef __attribute__((ext_vector_type(4))) unsigned u32x4;
typedef __attribute__((ext_vector_type(4))) short s16x4;
typedef __attribute__((ext_vector_type(2))) float f32x2;
typedef __attribute__((ext_vector_type(4))) float f32x4;
typedef __attribute__((ext_vector_type(16))) float f32x16;
typedef __attribute__((ext_vector_type(2))) __bf16 bf16x2;
typedef __attribute__((ext_vector_type(8))) __bf16 bf16x8;
typedef __attribute__((ext_vector_type(2))) _Float16 f16x2;
typedef __attribute__((ext_vector_type(8))) _Float16 f16x8;
typedef LAS char* lptr;

#define PREF 1
#define ROT 0
#define FSLEEP 2
#define NSLEEP "3"
#define LDRIFT 0
#define PSLEEP 6
#define CSLEEP 1
#define OSLEEP 2
#define RSLEEP 6
#define HOLD 1
#define GROT ((w + li) & 3)
#define NT2 4
#define OUTAUX 17
#define TEARLY 1
#define HDELAY 0
constexpr int SEQ = 2048, DM = 576;

constexpr unsigned WS_CTL = 0;
constexpr unsigned WS_ARR = 256;
constexpr unsigned WS_BFL = 4096;
constexpr unsigned WS_HINT = 20480;
constexpr unsigned WS_GATE = 36864;
constexpr unsigned WS_PFL = 49152;
constexpr unsigned WS_PART = 65536;
constexpr unsigned WS_O = WS_PART + 8 * 16 * 3072;
constexpr unsigned WS_B = WS_O + SEQ * DM * 2;
constexpr unsigned WS_END = WS_B + SEQ * DM * 2;
constexpr unsigned EMPTY = 0xffffffffu;

constexpr size_t WS_STAMP = 16u << 20;
#define STAMP_DECL
#define STAMP(i)
#define CSTAMP(i)
#define STAMP_OUT(role, slot)

struct Params {
    const char* x; const char* wqkv; const char* wo; const char* logA; const char* aim; const char* cre; const char* cim;
    const char* logdt; const char* dskip; const char* gw; char* out; char* ws;
};

constexpr int LDS_BYTES = 163840, FL = 163584, LDS_TAG = FL + 64, LDS_DUMMY = FL + 128, LDS_JUNK = 150784;
enum { F_SW = 0, F_LANDED = 4, F_W1 = 20, F_CL = 24, F_B = 28, F_H = 32, F_KVP = 36, F_KVR = 40, F_Q0 = 44, F_QD = 48, F_D = 52, F_C1 = 56, F_W0 = 60 };
constexpr int CONS = LDS_JUNK;
constexpr int WO8 = 132096, P2_ACC = 73728, P2_AP = 304, P2_B = 141312, P2_G = 150528;

DEVI float bf2f(unsigned h) { return __uint_as_float(h << 16); }
DEVI float bfhi(unsigned h) { return __uint_as_float(h & 0xffff0000u); }
DEVI unsigned pk_bf16(float a, float b) { return __builtin_bit_cast(unsigned, __builtin_convertvector(f32x2{a, b}, bf16x2)); }
DEVI unsigned pk_f16(f32x2 v) { return __builtin_bit_cast(unsigned, __builtin_convertvector(v, f16x2)); }
DEVI float rbf(float a) { return bf2f(pk_bf16(a, a)); }
DEVI f32x4 mfma16(u32x4 a, u32x4 b, f32x4 c) {
    return __builtin_amdgcn_mfma_f32_16x16x32_bf16(__builtin_bit_cast(bf16x8, a), __builtin_bit_cast(bf16x8, b), c, 0, 0, 0);
}
DEVI void mfma16i(f32x4& c, u32x4 a, u32x4 b) { asm volatile("v_mfma_f32_16x16x32_bf16 %0, %1, %2, %0" : "+v"(c) : "v"(a), "v"(b)); }
#define MFMA_DONE() do { __builtin_amdgcn_sched_barrier(0); asm volatile("s_nop 7\n\ts_nop 1" ::: "memory"); __builtin_amdgcn_sched_barrier(0); } while (0)
DEVI u32x4 lds_r128(lptr p) { return *(LAS u32x4*)p; }
DEVI u32x2 lds_r64(lptr p) { return *(LAS u32x2*)p; }
DEVI float lds_rf(lptr p) { return *(LAS float*)p; }
DEVI void lds_w128(lptr p, u32x4 v) { *(LAS u32x4*)p = v; }
DEVI void lds_w64(lptr p, u32x2 v) { *(LAS u32x2*)p = v; }
DEVI void lds_w32(lptr p, unsigned v) { *(LAS unsigned*)p = v; }
DEVI void lds_w16(lptr p, unsigned v) { *(LAS u16*)p = (u16)v; }
DEVI u32x2 lds_tr(lptr p) { return __builtin_bit_cast(u32x2, __builtin_amdgcn_ds_read_tr16_b64_v4i16((LAS s16x4*)p)); }
DEVI u32x4 lds_trfrag(lptr p, int off2) { u32x2 a = lds_tr(p), b = lds_tr(p + off2); return u32x4{a.x, a.y, b.x, b.y}; }
template <int OFF> DEVI void glds16o(const char* sbase, unsigned voff) {
    asm volatile("global_load_lds_dwordx4 %0, %1 offset:%2" :: "v"(voff), "s"(sbase), "i"(OFF) : "memory");
}
DEVI void glds16(const char* sbase, unsigned voff, unsigned l) {
    asm volatile("s_mov_b32 m0, %2\n\ts_nop 0\n\tglobal_load_lds_dwordx4 %0, %1" :: "v"(voff), "s"(sbase), "s"(l) : "memory");
}
#define SET_M0(v) asm volatile("s_mov_b32 m0, %0\n\ts_nop 0" :: "s"(v) : "memory")
#define W_ADDTID(v, off) asm volatile("ds_write_addtid_b32 %0 offset:%1" :: "v"(v), "i"(off) : "memory")
#define CFENCE() asm volatile("" ::: "memory")
#define SCHED() __builtin_amdgcn_sched_barrier(0)
#define WAIT_VM(n) asm volatile("s_waitcnt vmcnt(" #n ")" ::: "memory")
#define WAIT_LGKM(n) __builtin_amdgcn_s_waitcnt(0xC07F | ((n) << 8))
#define WAIT_LGKM0() asm volatile("s_waitcnt lgkmcnt(0)" ::: "memory")
#define BARRIER() do { SCHED(); asm volatile("s_waitcnt lgkmcnt(0)\n\ts_barrier" ::: "memory"); SCHED(); } while (0)
#define RAW_BARRIER() do { SCHED(); asm volatile("s_barrier" ::: "memory"); SCHED(); } while (0)
#define RLX_AGENT __ATOMIC_RELAXED, __HIP_MEMORY_SCOPE_AGENT
constexpr unsigned SPIN_CAP = 1u << 16;
typedef __amdgpu_buffer_rsrc_t rsrc_t;
DEVI u32x4 ld_sc1(rsrc_t r, unsigned off) { return __builtin_amdgcn_raw_buffer_load_b128(r, off, 0, 16); }
DEVI void st_sc1(rsrc_t r, unsigned off, u32x4 v) { __builtin_amdgcn_raw_buffer_store_b128(v, r, off, 0, 16); }

DEVI u32x4 pack8(f32x4 a, f32x4 b) { return u32x4{pk_bf16(a[0], a[1]), pk_bf16(a[2], a[3]), pk_bf16(b[0], b[1]), pk_bf16(b[2], b[3])}; }
DEVI float phi(float t) {
    const float e = __builtin_amdgcn_fmed3f(__builtin_amdgcn_exp2f(t * 1.44269504089f), 0.f, 1.f);
    return __int_as_float(max(__float_as_int(t), 0x358637bd)) + e;
}
DEVI f32x4 phi4(f32x4 v) { return f32x4{phi(v[0]), phi(v[1]), phi(v[2]), phi(v[3])}; }
DEVI unsigned umax4(u32x4 a) { return max(max(a.x, a.y), max(a.z, a.w)); }
DEVI u32x4 not_empty(u32x4 a) { return u32x4{min(a.x, EMPTY - 1), min(a.y, EMPTY - 1), min(a.z, EMPTY - 1), min(a.w, EMPTY - 1)}; }
DEVI void tag_post(const Params& P, u64 tag) { __hip_atomic_store((GAS u64*)(P.ws + WS_ARR) + blockIdx.x, tag, RLX_AGENT); }
#define PIN_TAG() do { asm volatile("" : "+s"(tag)); tag += 1ull; } while (0)
template <int SL = PSLEEP>
DEVI void poll_addr(const Params& P, unsigned off, u64 tag) {
    const GAS u64* f = (const GAS u64*)(P.ws + off);
#pragma unroll 1
    for (unsigned spins = 0; spins < SPIN_CAP; ++spins) {
        if (__all(__hip_atomic_load(f, RLX_AGENT) == tag)) break;
        __builtin_amdgcn_s_sleep(SL);
    }
}
template <int SL = PSLEEP>
DEVI void poll_flags(const Params& P, unsigned off, int n, u64 tag, int lane) { poll_addr<SL>(P, off + (lane < n ? lane : 0) * 128, tag); }
DEVI unsigned flag_get(lptr smem, int f) { return __builtin_amdgcn_readfirstlane(__hip_atomic_load((LAS unsigned*)(smem + FL + f), __ATOMIC_RELAXED, __HIP_MEMORY_SCOPE_WORKGROUP)); }
template <int SL = FSLEEP>
DEVI void flag_wait(lptr smem, int f, unsigned v) {
#pragma unroll 1
    for (unsigned spins = 0; spins < (SPIN_CAP << 4); ++spins) { if (flag_get(smem, f) >= v) break; if (SL) __builtin_amdgcn_s_sleep(SL); }
    CFENCE();
}
DEVI void flag_add(int f) { asm volatile("s_mov_b64 exec, 1\n\tds_add_u32 %0, %1\n\ts_mov_b64 exec, -1" :: "v"(FL + f), "v"(1u) : "memory"); }
#define FLAG_ADD(f) asm volatile("s_mov_b64 exec, 1\n\tds_add_u32 %0, %1 offset:%2\n\ts_mov_b64 exec, -1" :: "v"(flp), "v"(one), "i"(f) : "memory")
DEVI void flag_set(int f, unsigned v) { asm volatile("s_mov_b64 exec, 1\n\tds_write_b32 %0, %1\n\ts_mov_b64 exec, -1" :: "v"(FL + f), "v"(v) : "memory"); }
#define NEEDP(seen, ptr, f, v) do { unsigned c_; asm volatile( \
        "v_cmp_gt_u32 vcc, %4, %1\n\ts_cbranch_vccnz 0f\n1:\n\t.subsection 1\n" \
        "0:\n\ts_mov_b32 %0, 0x100000\n2:\n\ts_sleep " NSLEEP "\n\tds_read_b32 %1, %2 offset:%3\n\ts_sub_u32 %0, %0, 1\n\ts_waitcnt lgkmcnt(0)\n\ts_cbranch_scc1 1b\n\t" \
        "v_cmp_gt_u32 vcc, %4, %1\n\ts_cbranch_vccnz 2b\n\ts_branch 1b\n\t.subsection 0" \
        : "=&s"(c_), "+v"(seen) : "v"(ptr), "i"(f), "i"(v) : "memory", "scc", "vcc"); } while (0)
#define NEED(seen, f, v) NEEDP(seen, flp, f, v)
DEVI void touch(const char* sbase, unsigned voff) {
    asm volatile("s_mov_b32 m0, %2\n\ts_nop 0\n\tglobal_load_lds_dword %0, %1" :: "v"(voff), "s"(sbase), "s"(LDS_JUNK) : "memory");
}
DEVI void touch_group(const Params& P, int xcd, int n, int lane) {
    if (n < 126) {
        const int c0 = n / 18, r = n - c0 * 18, c = c0 + 2;
        if (r < 8) touch(P.x, (unsigned)(((xcd >> 1) * 512 + r * 64 + lane) * 1152 + c * 128));
        else { const int l = r - 8; touch(P.wqkv, (unsigned)((c * 64 + lane) * 3456 + (l < 5 ? 1152 : 2304 - 640) + (xcd & 1) * 512 + l * 128)); }
    } else if (n < 171) {
        const int c = (n - 126) / 5, l = n - 126 - c * 5;
        touch(P.wqkv, (unsigned)((c * 64 + lane) * 3456 + (xcd & 1) * 512 + l * 128));
    } else {
        const int l = (n - 171) / 9, c = n - 171 - l * 9;
        touch(P.wo, (unsigned)((c * 64 + lane) * 1152 + l * 128));
    }
}
template <unsigned CH>
DEVI void wo_issue(const Params& P, int cg, int first, int stride, int lane) {
#pragma unroll 1
    for (int m = first; m < 9; m += stride) {
        const int q = 64 * m + lane, rho = q / 9, pc = q - rho * 9;
        const int k = (rho & 32) + 8 * ((rho >> 3) & 3) + 4 * (rho & 1) + ((rho >> 1) & 3);
        const unsigned qo = (unsigned)(k * 1152 + cg * 144 + pc * 16);
#pragma unroll
        for (int c = 0; c < 9; ++c) if ((CH >> c) & 1) glds16(P.wo + c * (64 * 1152), qo, (unsigned)((c < 8 ? c * 9216 : WO8) + m * 1024));
    }
}

constexpr int SP = 3088;
constexpr int XT_OFF = 98816, XT_BYTES = 4608, XPAD = 256;
constexpr int TT_OFF = 74496, TT5_OFF = 151552, TC = 1216, PT_OFF = 126464, PC_OFF = 2560, ACX_OFF = 24704;

#define CM2(o1, p1, a1, b1, o2, p2, a2, b2) do { asm( \
    "v_pk_mul_f32 %0, %2, %3 op_sel:[1,1] op_sel_hi:[1,0] neg_lo:[0,1]\n\tv_pk_mul_f32 %1, %4, %5 op_sel:[1,1] op_sel_hi:[1,0] neg_lo:[0,1]\n\t" \
    "v_pk_fma_f32 %0, %2, %3, %0 op_sel_hi:[0,1,1]\n\tv_pk_fma_f32 %1, %4, %5, %1 op_sel_hi:[0,1,1]" \
    : "=&v"(o1), "=&v"(o2) : "v"(a1), "v"(b1), "v"(a2), "v"(b2)); \
    asm("v_cvt_pk_f16_f32 %0, %2, %3\n\tv_cvt_pk_f16_f32 %1, %4, %5" : "=&v"(p1), "=&v"(p2) : "v"((o1).x), "v"((o1).y), "v"((o2).x), "v"((o2).y)); } while (0)
#define HM2(p1, a1, p2, a2, b) asm( \
    "v_pk_mul_f16 %0, %2, %4 op_sel_hi:[0,1]\n\tv_pk_mul_f16 %1, %3, %4 op_sel_hi:[0,1]\n\t" \
    "v_pk_fma_f16 %0, %2, %4, %0 op_sel:[1,1,0] op_sel_hi:[1,0,1] neg_lo:[0,1,0]\n\tv_pk_fma_f16 %1, %3, %4, %1 op_sel:[1,1,0] op_sel_hi:[1,0,1] neg_lo:[0,1,0]" \
    : "=&v"(p1), "=&v"(p2) : "v"(a1), "v"(a2), "v"(b))
DEVI f32x2 cexp_p(float a2, float f, float p) {
    float m = __builtin_amdgcn_exp2f(a2 * p);
    float r = __builtin_amdgcn_fractf(f * p);
    return f32x2{m * __builtin_amdgcn_cosf(r), m * __builtin_amdgcn_sinf(r)};
}
DEVI u32x4 mask_frag(unsigned bits, int g, unsigned val) {
    unsigned by = bits >> (8 * g);
    u32x4 r;
#pragma unroll
    for (int i = 0; i < 4; ++i) { unsigned t = (by >> (2 * i)) & 3u; r[i] = ((t | (t << 15)) & 0x10001u) * val; }
    return r;
}
struct Tab2 { unsigned v[8][64][4]; };
constexpr Tab2 make_tab2() {
    Tab2 t{};
    for (int l = 0; l < 64; ++l) {
        const int r = l & 15, g = l >> 4, j = l & 31, h = l >> 5;
        const unsigned bits[6] = {7u << (r + 11), 31u << (r + 6), 7u << (r + 3), 127u << (r + 5), 511u << (r + 4), 3u << (r + 12)};
        for (int m = 0; m < 6; ++m)
            for (int i = 0; i < 4; ++i) {
                const unsigned q = (bits[m] >> (8 * g + 2 * i)) & 3u, val = m < 5 ? 0x3f80u : 0xffffu;
                t.v[m][l][i] = (q & 1 ? val : 0u) | (q & 2 ? val << 16 : 0u);
            }
        for (int c = 0; c < 4; ++c) {
            const int v2 = 79 - (32 + (j >> 1)) - c + 4, v3 = 127 - (64 + 32 * h + (j >> 1)) - c + 4;
            t.v[6][l][c] = PT_OFF + (j & 1) * 1280 + c * 320 + (v2 >> 2) * 16 + h * 8 + (v2 & 3) * 2 - 64;
            t.v[7][l][c] = PT_OFF + PC_OFF + c * 320 + (v3 >> 2) * 16 + (j & 1) * 8 + (v3 & 3) * 2 - 64;
        }
    }
    return t;
}
__device__ const Tab2 g_tab2 = make_tab2();
DEVI float wave_max(float v) {
    v = fmaxf(v, __int_as_float(__builtin_amdgcn_update_dpp(0, __float_as_int(v), 0xB1, 0xf, 0xf, true)));
    v = fmaxf(v, __int_as_float(__builtin_amdgcn_update_dpp(0, __float_as_int(v), 0x4E, 0xf, 0xf, true)));
    v = fmaxf(v, __int_as_float(__builtin_amdgcn_update_dpp(0, __float_as_int(v), 0x124, 0xf, 0xf, true)));
    v = fmaxf(v, __int_as_float(__builtin_amdgcn_update_dpp(0, __float_as_int(v), 0x128, 0xf, 0xf, true)));
    const int i = __float_as_int(v);
    const int a = max(__builtin_amdgcn_readlane(i, 0), __builtin_amdgcn_readlane(i, 16)), b = max(__builtin_amdgcn_readlane(i, 32), __builtin_amdgcn_readlane(i, 48));
    return __int_as_float(max(a, b));
}

DEVI void ssm_role(const Params& P, lptr smem, int xcd, int li, int gid, u64& tag, rsrc_t wsr) {
    const int gl = gid / 11, gi = gid - gl * 11, ch0 = gl * 64 + (gi < 10 ? gi * 6 : 58);
    const int tid = threadIdx.x, lane = tid & 63, w = __builtin_amdgcn_readfirstlane(tid >> 6);
    const int ch = w >> 1, half = (w ^ (w >> 2)) & 1, d = ch0 + ch;
    const bool own = gi < 10 || ch >= 2;
    const int lo16 = lane & 15, g = lane >> 4;
    STAMP_DECL; STAMP(1);
    __builtin_amdgcn_s_setprio(3);
    if (w >= 8 && li < 12) touch_group(P, xcd, (w - 8) * 12 + li, lane);
    const unsigned po = (unsigned)(d * 256 + lane * 4);
    const unsigned r_la = *(const GAS unsigned*)(P.logA + po), r_ai = *(const GAS unsigned*)(P.aim + po);
    const unsigned r_cr = *(const GAS unsigned*)(P.cre + po), r_ci = *(const GAS unsigned*)(P.cim + po);
    const unsigned r_dt = *(const GAS u16*)(P.logdt + d * 2), r_ds = *(const GAS u16*)(P.dskip + d * 2);
    u32x4 ct[8];
    if (half) {
        const char* tp = (const char*)&g_tab2;
        asm volatile("" : "+s"(tp));
#pragma unroll
        for (int i = 0; i < 8; ++i) ct[i] = *(const GAS u32x4*)(tp + (unsigned)(i * 1024 + lane * 16));
    }
    const int tt = (tid + (li & 7) * 64) & 511;
    u32x3 xr[4];
    const unsigned xoff = (unsigned)(tt * 4608 + ch0 * 2);
    const unsigned boff = own ? WS_B + (unsigned)(d * 4096 + half * 2048 + lane * 16) : WS_END;
#define GATHER(i) do { SCHED(); if (w < 8) xr[i] = *(const GAS u32x3*)(P.x + xoff + (i) * 1152); SCHED(); } while (0)
#define PRIO(i) do { SCHED(); __builtin_amdgcn_s_setprio(i); SCHED(); } while (0)
    SCHED();
    lptr xt = smem + XT_OFF + ch * XT_BYTES, tb = smem + (ch < 5 ? TT_OFF + ch * 4864 : TT5_OFF), pt = smem + PT_OFF + ch * 3840;
    const int j32 = lane & 31, h = lane >> 5;
    if (half) {
#pragma unroll
        for (int i = 0; i < 4; ++i) if (i < 3 || lane < 48) lds_w128(pt + i * 1024 + lane * 16, u32x4{0u, 0u, 0u, 0u});
    } else { lds_w64(xt + lane * 8, u32x2{0u, 0u}); lds_w32(smem + FL + 32, 0u); }
    int arow;
    {
        const u64 map0 = 0x0a09080803020100ull, map1 = 0x0c0b0a0a07060504ull, map2 = 0x1413110e12100f0dull;
        const u64 mm = j32 < 8 ? map0 : (j32 < 16 ? map1 : map2);
        arow = (int)((mm >> (8 * (j32 & 7))) & 0xff);
    }
    lptr ap = smem + arow * SP + w * 256 + h * 16, bp = smem + j32 * SP + w * 256 + h * 16;
    const int cpy = (3 - lo16) & 3;
    lptr xf7 = xt + (XPAD - 16 + 16 * lo16 + 8 * g) * 2 - 64 * 7;
    lptr tf = tb + cpy * TC + 2 * (527 - lo16 + 8 * g - cpy) - 64 * 16;
    lptr xc = smem + (w ^ 1) * 256 + lane * 4, xb = smem + XT_OFF + (XPAD + tt * 4) * 2;
    asm volatile("" : "+v"(ap), "+v"(bp), "+v"(xf7), "+v"(tf), "+v"(xc), "+v"(xb));
    const float dt = rbf(__expf(bf2f(r_dt)));
    const float cm = wave_max(fmaxf(fmaxf(fabsf(bf2f(r_cr)), fabsf(bfhi(r_cr))), fmaxf(fabsf(bf2f(r_ci)), fabsf(bfhi(r_ci)))));
    unsigned ce = __float_as_uint(cm) >> 23; ce = ce < 16u ? 16u : (ce > 240u ? 240u : ce);
    const float cs = __uint_as_float((263u - ce) << 23), ics = __uint_as_float((ce - 9u) << 23);
    f32x16 kacc;
#pragma unroll
    for (int i = 0; i < 16; ++i) kacc[i] = 0.f;
    STAMP(2);
    {
        const unsigned sh = half * 16;
        asm volatile("" :: "v"(r_la), "v"(r_ai), "v"(r_ds), "v"(ct[7]));
        if (li < 12 && (w < NT2 || (w >= 8 && !TEARLY))) touch_group(P, xcd, (w >= 8 ? w - 8 : w + 4) * 12 + li, lane);
        PIN_TAG();
        const float la = bf2f(r_la >> sh), ai = bf2f(r_ai >> sh), cr = bf2f(r_cr >> sh) * cs, ci = bf2f(r_ci >> sh) * cs;
        const float a2 = (-rbf(__expf(la)) * dt) * 1.44269504089f, f = (ai * dt) * 0.15915494309f;
#define PUT(row, v) W_ADDTID(v, ((row) & 15) * SP)
        const f32x2 w1 = cexp_p(a2, -f, 1.f), W = cexp_p(a2, -f, 256.f);
        f32x2 u[8], v[4], c[9], e4, e8, t2;
        unsigned U[8], V[4], p, q;
        u[1] = cexp_p(a2, f, 8.f); v[1] = cexp_p(a2, f, 64.f);
        c[0] = f32x2{cr, -ci};
        U[1] = pk_f16(u[1]); V[1] = pk_f16(v[1]);
        GATHER(0);
        SET_M0(w * 256);
        PUT(0, pk_f16(c[0]));
        CM2(c[1], p, c[0], w1, u[2], U[2], u[1], u[1]); PUT(1, p);
        CM2(c[2], p, c[1], w1, u[3], U[3], u[2], u[1]); PUT(2, p);
        CM2(c[3], p, c[2], w1, u[4], U[4], u[2], u[2]); PUT(3, p);
        CM2(c[4], p, c[3], w1, u[5], U[5], u[4], u[1]); PUT(4, p);
        CM2(c[5], p, c[4], w1, u[6], U[6], u[4], u[2]); PUT(5, p);
        CM2(c[6], p, c[5], w1, u[7], U[7], u[4], u[3]); PUT(6, p);
        CM2(c[7], p, c[6], w1, v[2], V[2], v[1], v[1]); PUT(7, p);
        CM2(c[8], p, c[7], w1, v[3], V[3], v[2], v[1]);
        PRIO(2);
        GATHER(1);
        CM2(e8, p, c[8], W, e4, q, c[4], W); PUT(12, p); PUT(10, q);
        CM2(c[8], p, e8, W, c[4], q, e4, W); PUT(14, p); PUT(13, q);
        CM2(t2, p, c[0], W, e4, q, c[4], W); PUT(8, p); PUT(15, q);
        CM2(t2, p, c[2], W, e4, q, c[6], W); PUT(9, p); PUT(11, q);
        SET_M0(w * 256 + 16 * SP);
        CM2(e8, p, c[8], W, t2, q, W, W); PUT(17, p);
        CM2(e4, p, c[8], t2, c[0], q, e8, t2); PUT(16, p); PUT(18, q);
        CM2(c[8], p, e4, t2, e8, q, c[0], t2); PUT(19, p); PUT(20, q);
        PRIO(1);
        GATHER(2);
        u32x4 af[8];
#pragma unroll
        for (int s = 0; s < 8; ++s) af[s] = lds_r128(ap + 32 * s);
        CFENCE();
        PUT(16, V[2]); PUT(24, V[3]);
#pragma unroll
        for (int b = 1; b < 8; ++b) { HM2(p, V[2], q, V[3], U[b]); PUT(16 + b, p); PUT(24 + b, q); }
        SET_M0(w * 256);
        PRIO(0);
        GATHER(3);
        PUT(0, 0x3c00u); PUT(8, V[1]);
#pragma unroll
        for (int b = 1; b < 8; b += 2) { HM2(p, U[b], q, U[b == 7 ? 7 : b + 1], V[1]); PUT(8 + b, p); if (b < 7) PUT(9 + b, q); PUT(b, U[b]); if (b < 7) PUT(b + 1, U[b + 1]); }
#undef PUT
        u32x4 bfr[8];
#pragma unroll
        for (int s = 0; s < 8; ++s) bfr[s] = lds_r128(bp + 32 * s);
        SCHED();
        SCHED();
#pragma unroll
        for (int s = 0; s < 8; ++s)
            kacc = __builtin_amdgcn_mfma_f32_32x32x16_f16(__builtin_bit_cast(f16x8, af[s]), __builtin_bit_cast(f16x8, bfr[s]), kacc, 0, 0, 0);
    }
    STAMP(3);
    const float ks = 2.f * dt * ics;
#pragma unroll
    for (int i = 0; i < 12; ++i) kacc[i] *= ks;
    if (half) {
#pragma unroll
        for (int i = 0; i < 8; ++i) W_ADDTID(kacc[i], i * SP);
    } else {
#pragma unroll
        for (int i = 0; i < 4; ++i) W_ADDTID(kacc[8 + i], i * SP);
    }
    STAMP(4);
    BARRIER();
    STAMP(5);
    if (tid == 704) tag_post(P, tag);
    auto cleared = [&]() {
        WAIT_VM(0);
        if (lane == 0 && __hip_atomic_fetch_add((LAS unsigned*)(smem + FL + 32), 1u, __ATOMIC_RELAXED, __HIP_MEMORY_SCOPE_WORKGROUP) == 11u)
            __hip_atomic_store((GAS u64*)(P.ws + WS_BFL + gid * 128), tag, RLX_AGENT);
    };
    auto x_in = [&]() {
        if (w < 8) {
#pragma unroll
            for (int k = 0; k < 3; ++k) {
                u32x2 lo = {__builtin_amdgcn_perm(xr[1][k], xr[0][k], 0x05040100u), __builtin_amdgcn_perm(xr[3][k], xr[2][k], 0x05040100u)};
                u32x2 hi = {__builtin_amdgcn_perm(xr[1][k], xr[0][k], 0x07060302u), __builtin_amdgcn_perm(xr[3][k], xr[2][k], 0x07060302u)};
                lds_w64(xb + (2 * k) * XT_BYTES, lo); lds_w64(xb + (2 * k + 1) * XT_BYTES, hi);
            }
        }
        SCHED();
        st_sc1(wsr, boff, u32x4{EMPTY, EMPTY, EMPTY, EMPTY}); st_sc1(wsr, boff + 1024, u32x4{EMPTY, EMPTY, EMPTY, EMPTY});
        BARRIER();
    };
    auto TF = [&](int off) { u32x2 a = lds_r64(tf + off), b = lds_r64(tf + off + 8); return u32x4{a.x, a.y, b.x, b.y}; };
    f32x4 acc[8];
#pragma unroll
    for (int i = 0; i < 8; ++i) acc[i] = f32x4{0.f, 0.f, 0.f, 0.f};
    lptr axr;
    if (!half) {
        __builtin_amdgcn_s_setprio(2);
#pragma unroll
        for (int i = 0; i < 8; ++i) kacc[i] += lds_rf(xc + i * SP);
        unsigned dd[6];
        dd[0] = pk_bf16(kacc[7], kacc[6]); dd[1] = pk_bf16(kacc[5], kacc[4]); dd[2] = pk_bf16(kacc[3], kacc[2]);
        dd[3] = pk_bf16(kacc[1], kacc[0] + (lane == 0 ? bf2f(r_ds) : 0.f));
        dd[4] = __builtin_amdgcn_update_dpp(0, dd[0], 0x138, 0xf, 0xf, true);
        dd[5] = __builtin_amdgcn_update_dpp(0, dd[1], 0x138, 0xf, 0xf, true);
        unsigned mm[5];
#pragma unroll
        for (int i = 0; i < 5; ++i) mm[i] = __builtin_amdgcn_alignbit(dd[i + 1], dd[i], 16);
        lptr tw = tb + 2 * (536 - 8 * lane);
        lds_w128(tw, u32x4{dd[0], dd[1], dd[2], dd[3]}); lds_w128(tw + TC, u32x4{mm[0], mm[1], mm[2], mm[3]});
        lds_w128(tw + 2 * TC, u32x4{dd[1], dd[2], dd[3], dd[4]}); lds_w128(tw + 3 * TC, u32x4{mm[1], mm[2], mm[3], mm[4]});
        {
            const unsigned n0 = __builtin_amdgcn_readlane(dd[0], 63), n1 = __builtin_amdgcn_readlane(dd[1], 63);
            const unsigned kk = (n0 & 0xffffu) * 0x10001u, kh = n0 << 16, m4 = (n0 >> 16) | (n1 << 16);
            const int c = lane & 3, bl = lane >> 2;
            u32x4 e;
            e.x = 0u; e.y = lane == 14 ? kh : (lane == 15 ? kk : 0u); e.z = lane == 12 ? kh : (bl == 3 ? kk : 0u);
            e.w = lane == 14 ? n0 : (lane == 15 ? m4 : (bl == 3 ? kk : 0u));
            if (bl < 6) lds_w128(tb + c * TC + bl * 16 + (bl >> 2) * 1024, e);
        }
        x_in();
        u32x4 X[8];
#pragma unroll
        for (int i = 0; i < 8; ++i) X[i] = lds_r128(xf7 + 64 * 6 + 512 * i);
        u32x4 k0 = TF(1024 - 64), k8 = TF(512 - 64);
#pragma unroll
        for (int i = 0; i < 8; ++i) asm volatile("" : "+v"(acc[i]));
        __builtin_amdgcn_s_setprio(0);
        STAMP(6);
#pragma unroll
        for (int c = 1; c < 8; ++c) {
            u32x4 k0n, k8n;
            if (c < 7) { k0n = TF(1024 - 64 * (c + 1)); k8n = TF(512 - 64 * (c + 1)); }
#pragma unroll
            for (int i = 0; i < 8; ++i) {
                SCHED();
                mfma16i(acc[i], k0, X[i]);
                if (i < 7) mfma16i(acc[i + 1], k8, X[i]);
                SCHED();
                if (c < 7) X[i] = lds_r128(xf7 + 64 * (6 - c) + 512 * i);
            }
            k0 = k0n; k8 = k8n;
            if (c == 3) { SCHED(); cleared(); SCHED(); }
        }
        MFMA_DONE();
        SET_M0(ACX_OFF - 8192 + ch * 8192);
#pragma unroll
        for (int i = 0; i < 16; ++i) W_ADDTID(acc[4 + (i >> 2)][i & 3], 8192 + i * 256);
        axr = smem + ACX_OFF + ch * 8192 + 4096 + lane * 4;
    } else {
        u32x4 X[8];
        const unsigned p89 = pk_bf16(kacc[8] + lds_rf(xc), kacc[9] + lds_rf(xc + SP)), pab = pk_bf16(kacc[10] + lds_rf(xc + 2 * SP), kacc[11] + lds_rf(xc + 3 * SP));
#pragma unroll
        for (int c = 0; c < 4; ++c) {
            lptr q2 = smem + (ct[6][c] + ch * 3840), q3 = smem + (ct[7][c] + ch * 3840);
            lds_w16(q2 + 64, p89); lds_w16(q2, p89 >> 16);
            lds_w16(q3 + 64, pab); lds_w16(q3, pab >> 16);
        }
        CFENCE();
        const u32x4 FX = ct[5] & (((unsigned)__builtin_amdgcn_readlane(p89, 63) >> 16) * 0x10001u);
        lptr pf2 = pt + cpy * 320 + ((79 - 64 - lo16 + 4 * g - cpy + 4) >> 2) * 16;
        u32x4 TA[3], TB[3], TCc[4];
#pragma unroll
        for (int i = 0; i < 3; ++i) { TA[i] = lds_r128(pf2 + 64 * (2 - i)); TB[i] = lds_r128(pf2 + 1280 + 64 * (2 - i)); }
#pragma unroll
        for (int i = 0; i < 4; ++i) TCc[i] = lds_r128(pf2 + PC_OFF + 64 * (3 - i));
        x_in();
#pragma unroll
        for (int i = 0; i < 8; ++i) X[i] = lds_r128(xf7 + 64 * 7 + 512 * i);
        STAMP(6);
        const f32x4 z4 = {0.f, 0.f, 0.f, 0.f};
#pragma unroll
        for (int st = 0; st < 6; ++st) {
            const u32x4 Xc = lds_r128(xf7 + 64 * 7 + 512 * st - 16);
            const u32x4 pa = pack8(mfma16(X[st], ct[0], z4), mfma16(X[st], ct[1], z4));
            const u32x4 pb = pack8(mfma16(X[st], ct[2], z4), mfma16(Xc, ct[1], z4));
#pragma unroll
            for (int dl = 2; dl <= 4; ++dl) if (st + dl < 8) {
                acc[st + dl] = mfma16(pa, TA[dl - 2], acc[st + dl]);
                acc[st + dl] = mfma16(pb, TB[dl - 2], acc[st + dl]);
            }
            if (st < 4) {
                const u32x4 pc = pack8(mfma16(X[st], ct[3], z4), mfma16(Xc, ct[4], z4));
#pragma unroll
                for (int dl = 4; dl <= 7; ++dl) if (st + dl < 8) acc[st + dl] = mfma16(pc, TCc[dl - 4], acc[st + dl]);
            }
        }
        SCHED(); cleared(); SCHED();
#pragma unroll
        for (int i = 4; i < 8; ++i) acc[i] = mfma16(FX, X[i - 4], acc[i]);
        const u32x4 k0 = TF(1024), k8 = TF(512), k16 = TF(0);
#pragma unroll
        for (int i = 0; i < 8; ++i) acc[i] = mfma16(k0, X[i], acc[i]);
#pragma unroll
        for (int i = 1; i < 8; ++i) acc[i] = mfma16(k8, X[i - 1], acc[i]);
#pragma unroll
        for (int i = 2; i < 8; ++i) acc[i] = mfma16(k16, X[i - 2], acc[i]);
        MFMA_DONE();
        SET_M0(ACX_OFF - 8192 + ch * 8192 + 4096);
#pragma unroll
        for (int i = 0; i < 16; ++i) W_ADDTID(acc[i >> 2][i & 3], 8192 + i * 256);
#pragma unroll
        for (int i = 0; i < 4; ++i) acc[i] = acc[4 + i];
        axr = smem + ACX_OFF + ch * 8192 + lane * 4;
    }
    STAMP(7);
    BARRIER();
    STAMP(8);
#pragma unroll
    for (int i = 0; i < 16; ++i) acc[i >> 2][i & 3] += lds_rf(axr + i * 256);
#pragma unroll
    for (int i = 0; i < 2; ++i) {
        const f32x4 ya = acc[2 * i], yb = acc[2 * i + 1];
        const auto sx = __builtin_amdgcn_permlane16_swap(pk_bf16(ya[0], ya[1]), pk_bf16(yb[0], yb[1]), false, false);
        const auto sy = __builtin_amdgcn_permlane16_swap(pk_bf16(ya[2], ya[3]), pk_bf16(yb[2], yb[3]), false, false);
        st_sc1(wsr, boff + i * 1024, not_empty(u32x4{sx[0], sy[0], sx[1], sy[1]}));
    }
    BARRIER();
    STAMP(9);
    if (w < 2) STAMP_OUT(2 + 2 * w, blockIdx.x + 256 * w);
}

constexpr int SLOT = 34816, XCH = 16384, KVT_OFF = 151296, KVP_OFF = 27648;
DEVI constexpr int slot_off(int s) { return (s & 3) == 0 ? SLOT : ((s & 3) == 1 ? 0 : (s & 3) * SLOT); }
DEVI constexpr int wq_off(int t) { return t == 0 ? 0 : 49152 + t * 27648; }

constexpr int quad_src(int quad) {
    const int pp = quad >= 192 ? 1 : 0, qi = quad - 192 * pp;
    const bool isz = qi >= 180;
    const int el = isz ? 0 : qi / 5, qd = qi - el * 5, hi = el >= 18 ? 1 : 0;
    const int dql = isz ? (qi < 189 ? qi - 180 : 0) : qd + 4 * hi;
    const int et = isz ? 0 : (el < 4 ? 0 : (el < 20 ? 1 : 2)), col = isz ? 0 : (el < 4 ? 12 + el : (el < 20 ? el - 4 : el - 20));
    const int tile = dql == 8 ? 5 : (et == 0 ? (dql >> 2) : (et == 1 ? 2 + (dql >> 2) : 4));
    return KVP_OFF + pp * 6144 + tile * 1024 + ((dql == 8 ? et : (dql & 3)) * 16 + col) * 4;
}
constexpr int quad_dst(int quad) {
    const int pp = quad >= 192 ? 1 : 0, qi = quad - 192 * pp;
    const bool isz = qi >= 180;
    const int el = isz ? 0 : qi / 5, qd = qi - el * 5, hi = el >= 18 ? 1 : 0;
    const int dql = isz ? (qi < 189 ? qi - 180 : 0) : qd + 4 * hi;
    const int dq = 9 * pp + dql, ks = dq >> 3, e = 36 * pp + el, ee = e >> 4;
    const int fr = ee == 0 ? 0 : (ee < 3 ? 2 * ee - 1 + ks : 2 * ee - 2 + ks);
    const int a = KVT_OFF + (isz ? (9 + ks) * 1024 + (dq & 3) * 256 + (2 * pp + (dql > 4 ? 1 : 0)) * 16 : fr * 1024 + ((dq & 3) * 16 + (e & 15)) * 16) + ((dq >> 2) & 1) * 8;
    return qi >= 189 ? LDS_DUMMY : (a | (isz ? (dql == 4 ? 0x300003 : 0x100003) : ((!hi && qd == 4) ? 1 : ((hi && qd == 0) ? 2 : 3))));
}
struct Tab { int v[2048]; };
constexpr Tab make_tab() {
    Tab t{};
    for (int c = 0; c < 192; ++c) {
        const int l = c & 63, q = 2 * ((c & 192) + (l >> 5) * 16 + (l & 15)) + ((l >> 4) & 1);
        t.v[4 * c] = quad_src(2 * c); t.v[4 * c + 1] = quad_src(2 * c + 1); t.v[4 * c + 2] = quad_dst(q); t.v[4 * c + 3] = quad_dst(q + 64);
    }
    for (int q = 0; q < 576; ++q) {
        const int rho = q / 9, pc = q % 9, k = (rho & 32) + 8 * ((rho >> 3) & 3) + 4 * (rho & 1) + ((rho >> 1) & 3), m = q >> 6;
        t.v[768 + q] = k * 3456 + pc * 16 - (m & 1) * 1024;
        t.v[1344 + q] = k * 1152 + pc * 16 - (m & 3) * 1024;
    }
    return t;
}
__device__ const Tab g_tab = make_tab();
constexpr int IDX = P2_B, GW_OFF = 139264;

DEVI void gemm_compute(const Params& P, lptr smem, int rb, int hg, int w, u64& tag, rsrc_t wsr) {
    const int lane = threadIdx.x & 63, lo16 = lane & 15, g = lane >> 4;
    const int rg = w >> 1, p = w & 1;
    STAMP_DECL; STAMP(1);
    if (w == 0 && lane < 16) lds_w32(smem + FL + lane * 4, 0u);
    if (w >= 4) {
#pragma unroll
        for (int i = 0; i < 3; ++i) lds_w128(smem + KVT_OFF + i * 4096 + (w - 4) * 1024 + lane * 16, u32x4{0u, 0u, 0u, 0u});
    }
    __builtin_amdgcn_s_setprio(2);
    const int row0 = 16 * (2 * rg + p) + lo16, row1 = 16 * (2 * rg + 1 - p) + lo16;
    const int sw = (g ^ ((lo16 >> 1) & 7)) << 4;
    lptr xa0[2] = {smem + row0 * 128 + sw, smem + row0 * 128 + (sw ^ 64)}, xa1[2] = {smem + row1 * 128 + sw, smem + row1 * 128 + (sw ^ 64)};
    const int wrow = (4 * g + (lo16 >> 2)) * 288;
    lptr wa = smem + XCH + wrow + (9 * p + (lane & 3)) * 8, wa2 = smem + XCH + wrow + (9 * p + ((lane & 3) == 3 ? 18 : 8)) * 8;
    lptr flp = smem + FL;
    asm volatile("" : "+v"(flp));
    const f32x4 z4 = {0.f, 0.f, 0.f, 0.f};
    f32x4 acc[2][5];
#pragma unroll
    for (int i = 0; i < 2; ++i)
#pragma unroll
        for (int j = 0; j < 5; ++j) { const float e = j < 2 ? 1e-6f : (j == 2 && lo16 < 12 ? 1e-6f : 0.f); acc[i][j] = f32x4{e, e, e, e}; }
    u32x4 xk[18], x1[2];
    auto rel = [&](int s) { return (s & 2) ? slot_off(s) - 2 * SLOT : slot_off(s); };
    auto rebase = [&](int dlt) {
        xa0[0] += dlt; xa0[1] += dlt; xa1[0] += dlt; xa1[1] += dlt; wa += dlt; wa2 += dlt;
        asm volatile("" : "+v"(xa0[0]), "+v"(xa0[1]), "+v"(xa1[0]), "+v"(xa1[1]), "+v"(wa), "+v"(wa2));
    };
    constexpr int NB = 8;
    u32x2 wl[NB], wh[NB];
    auto rdw = [&](int f, int t) {
        const int n = f / 5, j = f % 5;
        const int o = rel(n >> 1) + (n & 1) * 9216 + t * 4608;
        const u32x2 v = j == 2 ? lds_tr(wa2 + o) : lds_tr(wa + o + (j == 0 ? 0 : (j == 1 ? 32 : (j == 3 ? 152 : 184))));
        if (t) wh[f % NB] = v; else wl[f % NB] = v;
    };
    auto peek = [&](int f) { return __hip_atomic_load((LAS unsigned*)(flp + f), __ATOMIC_RELAXED, __HIP_MEMORY_SCOPE_WORKGROUP); };
    asm volatile(".subsection 2\n.Lkf_code_end:\n\t.fill 256, 4, 0xbf800000\n\t.subsection 0");
    unsigned one = 1u;
    asm volatile("" : "+v"(one));
    rebase(0);
#pragma unroll
    for (int j = 0; j < 5; ++j) asm volatile("" : "+v"(acc[0][j]), "+v"(acc[1][j]));
    BARRIER();
    STAMP(2);
    xk[0] = lds_r128(xa0[0] + rel(0)); x1[0] = lds_r128(xa1[0] + rel(0));
#pragma unroll
    for (int f = 0; f < NB - 1; ++f) { rdw(f, 0); rdw(f, 1); }
#pragma unroll
    for (int n = 0; n < 18; ++n) {
        const int nx = n + 1, s = n >> 1;
#pragma unroll
        for (int m = 0; m < 10; ++m) {
            const int j = m >> 1, f = 5 * n + j, fn = f + NB - 1;
            SCHED();
            if (!(m & 1) && !(f & 1) && fn < 88) WAIT_LGKM(10);
            const u32x4 wv = {wl[f % NB].x, wl[f % NB].y, wh[f % NB].x, wh[f % NB].y};
            if (m & 1) mfma16i(acc[1][j], x1[n & 1], wv); else mfma16i(acc[0][j], xk[n], wv);
            SCHED();
            if (!(m & 1) && fn % 10 == 0) {
                if (s < 8 && (s & 1)) rebase((s & 2) ? -2 * SLOT : 2 * SLOT);
                if (s == 8) WAIT_LGKM0();
                RAW_BARRIER();
                if (s == 0) STAMP(3);
                if (s == 4) STAMP(4);
            }
            if (fn < 90) rdw(fn, m & 1);
            if (nx < 18) {
                if (m == 0) xk[nx] = lds_r128(xa0[nx & 1] + rel(nx >> 1));
                if (m == 2) x1[nx & 1] = lds_r128(xa1[nx & 1] + rel(nx >> 1));
            }
        }
    }
    MFMA_DONE();
    STAMP(5);
    const int qrow = (2 * (4 * g + (lo16 >> 2))) * 144;
    lptr qw = smem + qrow + (lane & 3) * 8, qw4 = smem + qrow + (16 + (lane & 1)) * 8;
    lptr qwh = qw + wq_off(1), qw4h = qw4 + wq_off(1);
    asm volatile("" : "+v"(qwh), "+v"(qw4h));
    auto rdq = [&](int ks, int j) {
        const int o = (ks < 6 ? ks : ks - 6) * 4608;
        return j < 4 ? lds_trfrag((ks < 6 ? qw : qwh) + o + 32 * j, 144) : lds_trfrag((ks < 6 ? qw4 : qw4h) + o, 144);
    };
    {
        u32x4 pk[3], vb[3];
#pragma unroll
        for (int j = 0; j < 3; ++j) pk[j] = pack8(phi4(acc[0][j]), phi4(acc[1][j]));
        vb[0] = pack8(acc[0][2], acc[1][2]);
        if (lo16 < 12) vb[0] = u32x4{0x3f803f80u, 0x3f803f80u, 0x3f803f80u, 0x3f803f80u};
        vb[1] = pack8(acc[0][3], acc[1][3]); vb[2] = pack8(acc[0][4], acc[1][4]);
        f32x4 r[6];
        const f32x4 t0 = mfma16(pk[2], vb[0], z4), t1 = mfma16(pk[2], vb[1], z4), t2 = mfma16(pk[2], vb[2], z4);
        r[0] = mfma16(pk[0], vb[0], z4); r[1] = mfma16(pk[1], vb[0], z4); r[2] = mfma16(pk[0], vb[1], z4);
        r[3] = mfma16(pk[1], vb[1], z4); r[4] = mfma16(pk[1], vb[2], z4);
        r[5] = g == 0 ? t0 : (g == 1 ? t1 : t2);
        SCHED();
        asm volatile("s_nop 7" ::: "memory");
        SET_M0(KVP_OFF - 8192 + w * 6144);
#pragma unroll
        for (int t = 5; t >= 0; --t) {
            W_ADDTID(r[t][0], 8192 + t * 1024); W_ADDTID(r[t][1], 8192 + t * 1024 + 256);
            W_ADDTID(r[t][2], 8192 + t * 1024 + 512); W_ADDTID(r[t][3], 8192 + t * 1024 + 768);
        }
        FLAG_ADD(F_KVP);
        __builtin_amdgcn_s_setprio(0);
    }
    STAMP(6);
    f32x4 qa[5];
#pragma unroll
    for (int j = 0; j < 5; ++j) qa[j] = f32x4{1e-6f, 1e-6f, 1e-6f, 1e-6f};
#pragma unroll
    for (int j = 0; j < 5; ++j) asm volatile("" : "+v"(qa[j]));
    SCHED();
    u32x4 wq[2][5];
#pragma unroll
    for (int i = 0; i < 2; ++i)
#pragma unroll
        for (int j = 0; j < 5; ++j) wq[i][j] = rdq(i, j);
    SCHED();
    if (w >= 4) flag_wait<1>(smem, F_KVR, 3);
    unsigned seenq = 0;
#pragma unroll
    for (int ks = 0; ks < 18; ++ks) {
        if (ks == 2) seenq = peek(F_KVR);
        if (ks == 4) { SCHED(); FLAG_ADD(F_Q0); NEED(seenq, F_KVR, 3); seenq = peek(F_LANDED); NEED(seenq, F_LANDED, 4); }
        if (ks == 16) { SCHED(); FLAG_ADD(F_QD); }
#pragma unroll
        for (int j = 0; j < 5; ++j) {
            SCHED();
            if (j == 0 && ks < 17) WAIT_LGKM(10);
            mfma16i(qa[j], wq[ks & 1][j], xk[ks]);
            SCHED();
            if (ks + 2 < 18) wq[ks & 1][j] = rdq(ks + 2, j);
        }
    }
    __builtin_amdgcn_s_setprio(2);
    MFMA_DONE();
    u32x4 b0 = pack8(phi4(qa[0]), phi4(qa[1])), b1 = pack8(phi4(qa[2]), phi4(qa[3])), b2 = pack8(phi4(qa[4]), z4);
    lptr kfp = smem + KVT_OFF + lane * 16;
    unsigned ob0 = WS_O + (unsigned)(((rb * 8 + 2 * rg + p) * 72 + hg * 9 + 2 * (g & 1) + (g >> 1)) * 256 + lo16 * 16);
    asm volatile("" : "+v"(b0), "+v"(b1), "+v"(b2), "+v"(kfp), "+v"(ob0));
    { const u32x2 t = lds_r64(smem + LDS_TAG); tag = (u64)(unsigned)__builtin_amdgcn_readfirstlane(t.x) | ((u64)(unsigned)__builtin_amdgcn_readfirstlane(t.y) << 32); }
    STAMP(7);
    flag_wait<1>(smem, F_D, 4);
    if (w == 7 && lane == 0) __hip_atomic_store((GAS u64*)(P.ws + WS_HINT + (rb * 8 + hg) * 128), tag, RLX_AGENT);
    STAMP(8);
    {
        u32x4 kf[12];
#pragma unroll
        for (int i = 0; i < 12; ++i) kf[i] = lds_r128(kfp + (i < 3 ? 9 + i : i - 3) * 1024);
        SCHED();
        const f32x4 den = mfma16(kf[2], b2, mfma16(kf[1], b1, mfma16(kf[0], b0, f32x4{1e-6f, 1e-6f, 1e-6f, 1e-6f})));
        f32x4 o[5];
        o[0] = mfma16(kf[3], b0, z4);
        o[1] = mfma16(kf[5], b1, mfma16(kf[4], b0, z4));
        o[2] = mfma16(kf[7], b1, mfma16(kf[6], b0, z4));
        o[3] = mfma16(kf[9], b2, mfma16(kf[8], b1, z4));
        o[4] = mfma16(kf[11], b2, mfma16(kf[10], b1, z4));
        f32x4 r;
#pragma unroll
        for (int i = 0; i < 4; ++i) r[i] = __builtin_amdgcn_rcpf(den[i]);
        const float r1a = g == 0 ? r[0] : r[1], r2 = g == 0 ? r[1] : r[2], r3a = g <= 1 ? r[2] : r[3], r3b = g == 0 ? r[2] : r[3];
        u32x2 ob[5];
        ob[0] = u32x2{pk_bf16(o[0][0] * r[0], o[0][1] * r[0]), pk_bf16(o[0][2] * r[0], o[0][3] * r[0])};
        ob[1] = u32x2{pk_bf16(o[1][0] * r1a, o[1][1] * r1a), pk_bf16(o[1][2] * r[1], o[1][3] * r[1])};
        ob[2] = u32x2{pk_bf16(o[2][0] * r2, o[2][1] * r2), pk_bf16(o[2][2] * r2, o[2][3] * r2)};
        ob[3] = u32x2{pk_bf16(o[3][0] * r3a, o[3][1] * r3a), pk_bf16(o[3][2] * r3b, o[3][3] * r3b)};
        ob[4] = u32x2{pk_bf16(o[4][0] * r[3], o[4][1] * r[3]), pk_bf16(o[4][2] * r[3], o[4][3] * r[3])};
#pragma unroll
        for (int t = 0; t < 3; ++t) {
            const u32x2 a = ob[2 * t], b = ob[t < 2 ? 2 * t + 1 : 4];
            const auto sx = __builtin_amdgcn_permlane16_swap(a.x, b.x, false, false), sy = __builtin_amdgcn_permlane16_swap(a.y, b.y, false, false);
            if (t < 2 || g == 0) st_sc1(wsr, ob0 + 1024 * t, not_empty(u32x4{sx[0], sy[0], sx[1], sy[1]}));
        }
    }
    __builtin_amdgcn_s_setprio(0);
    STAMP(9);
    if (w == 0) STAMP_OUT(1, blockIdx.x);
    if (w == 7) STAMP_OUT(7, blockIdx.x + 768);
}

DEVI void gemm_loader(const Params& P, lptr smem, int rb, int hg, int lw, int cg2, u64& tag, rsrc_t wsr) {
    const int lane = threadIdx.x & 63;
    STAMP_DECL; STAMP(1);
    __builtin_amdgcn_s_setprio(3);
    unsigned xo[4], wo_[5], qo[3];
    const int rx = hg & 3, rw = rb & 3;
    unsigned mx[4], mw[4];
    const unsigned xbase = (unsigned)((rb * 128 + 8 * lw + (lane >> 3)) * 1152 + (((lane & 7) ^ ((lane >> 4) + 4 * (lw & 1))) << 4));
#pragma unroll
    for (int i = 0; i < 4; ++i) { mx[i] = (((i + rx) & 3) * 4 + lw) * 1024; mw[i] = XCH + (((i + rw) & 3) * 4 + lw) * 1024; xo[i] = xbase + ((i + rx) & 3) * 36864; }
    const unsigned lb = lw * 4096;
    auto issue_x = [&](int s, int i0 = 0, int i1 = 4) {
#pragma unroll
        for (int i = i0; i < i1; ++i) glds16(P.x + s * 128, xo[i], slot_off(s) + mx[i]);
    };
    auto issue_w = [&](int s, int i0 = 0, int i1 = 4) {
        const char* ws = P.wqkv + s * (64 * 3456);
        if (i1 == 4 && (lw >> 1) == (s & 1)) glds16(ws, wo_[4], (unsigned)(slot_off(s) + XCH + 16384 + (lw & 1) * 1024));
#pragma unroll
        for (int i = i0; i < i1; ++i) glds16(ws, wo_[i], slot_off(s) + mw[i]);
    };
    auto issue_q = [&](int t, int i) {
        const char* ws = P.wqkv + hg * 144 + (3 * t + i) * (64 * 3456);
        if (lw == ((i + t) & 3)) glds16(ws, qo[2], (unsigned)(wq_off(t) + i * 9216 + 8192));
        SET_M0(wq_off(t) + i * 9216 + (lb >> 1));
        glds16o<0>(ws, qo[0]); glds16o<1024>(ws, qo[1]);
    };
    auto w_off = [&](int i) {
        const unsigned q = 64 * (i < 4 ? ((i + rw) & 3) * 4 + lw : 16 + (lw & 1)) + lane, rho = (q * 3641u) >> 16, pc = q - rho * 18;
        const unsigned k = (rho & 35u) + 8 * ((rho >> 2) & 3) + 4 * ((rho >> 4) & 1);
        wo_[i] = k * 3456 + (1152 + hg * 144) + pc * 16 + (pc < 9 ? 0u : 1008u);
    };
    issue_x(0, 0, 1); issue_x(1, 0, 1);
    SCHED();
    w_off(0);
    issue_w(0, 0, 1); issue_w(1, 0, 1);
    SCHED();
#pragma unroll
    for (int i = 1; i < 5; ++i) w_off(i);
    if (lw == 1) { glds16(P.gw, (unsigned)(lane * 16), GW_OFF); if (lane < 8) glds16(P.gw + 1024, (unsigned)(lane * 16), GW_OFF + 1024); }
    issue_x(0, 1, 4); issue_w(0, 1, 4); issue_w(1, 1, 4); issue_x(1, 1, 4);
    const int lo16 = lane & 15, g = lane >> 4, ct = (lw < 3 ? lw : 0) * 64 + lane;
    lptr flp = smem + FL, ixp = smem + IDX + ct * 16, irp = smem + IDX + 3072 + lane * 4, iqp = irp + lw * 512;
    unsigned one = 1u;
    u32x4 sel[2];
    sel[0] = mask_frag(lo16 < 8 ? 1u << lo16 : 0u, 0, 0x3f80u); sel[1] = mask_frag(lo16 >= 8 ? 1u << (lo16 - 8) : 0u, 0, 0x3f80u);
    unsigned sto = WS_PART + (unsigned)((hg * 16 + rb) * 3072 + ct * 16);
    unsigned off = WS_PART + (unsigned)((hg * 16 + g) * 3072 + lw * 1024 + lo16 * 16);
    asm volatile("" : "+v"(flp), "+v"(one), "+v"(ixp), "+v"(irp), "+v"(iqp));
    asm volatile("" : "+v"(sel[0]), "+v"(sel[1]), "+v"(sto), "+v"(off));
    SET_M0(IDX + (lb >> 1));
    glds16o<0>((const char*)&g_tab + (lb >> 1), (unsigned)(lane * 16)); glds16o<1024>((const char*)&g_tab + (lb >> 1), (unsigned)(lane * 16));
    lptr gxp = smem + (16 * hg + lo16) * 128 + ((g ^ ((lo16 >> 1) & 7)) << 4), gwp = smem + GW_OFF + g * 16;
    f32x4 gacc = {0.f, 0.f, 0.f, 0.f};
    asm volatile("" : "+v"(gxp), "+v"(gwp));
    auto gate_step = [&](int s) {
        if (lw == 1) {
            lptr q = gxp + slot_off(s);
            gacc = mfma16(lds_r128(gwp + s * 128), lds_r128(q), gacc);
            gacc = mfma16(lds_r128(gwp + s * 128 + 64), lds_r128((lptr)((size_t)q ^ 64u)), gacc);
        }
    };
    WAIT_VM(8); BARRIER();
    if (lw == 2) {
#pragma unroll
        for (int i = 0; i < 3; ++i) st_sc1(wsr, WS_PART + (unsigned)((hg * 16 + rb) * 3072 + i * 1024 + lane * 16), u32x4{EMPTY, EMPTY, EMPTY, EMPTY});
    }
    issue_w(2); issue_x(2);
    u32x4 ix; unsigned oo[9]; u64 pf = 0;
#pragma unroll
    for (int s = 0; s < 9; ++s) {
        gate_step(s);
        if (s < 7) WAIT_VM(8); else WAIT_VM(6);
        BARRIER();
        if (s == 5 && lw == 3) asm volatile("global_load_dwordx2 %0, %1, %2 sc1" : "=v"(pf) : "v"(lo16 * 128), "s"(P.ws + WS_PFL + hg * 2048) : "memory");
        if (s < 6) { issue_w(s + 3); issue_x(s + 3); } else { issue_q(s - 6, 0); issue_q(s - 6, 1); issue_q(s - 6, 2); }
        if (s == 0) tag = *(const __attribute__((address_space(4))) u64*)(P.ws + WS_CTL);
        if (s == 1) PIN_TAG();
        if (s == 2 && lane == 0) {
            if (lw == 0) { tag_post(P, tag); lds_w64(smem + LDS_TAG, u32x2{(unsigned)tag, (unsigned)(tag >> 32)}); }
            if (lw == 2) __hip_atomic_store((GAS u64*)(P.ws + WS_PFL + (hg * 16 + rb) * 128), tag, RLX_AGENT);
        }
        if (s == 6 && lw == 3) {
            asm volatile("" : "+v"(pf));
            if (!__all(pf == tag)) poll_flags(P, WS_PFL + hg * 2048, 16, tag, lane);
        }
        if (s == 3) {
            qo[0] = *(LAS unsigned*)iqp; qo[1] = *(LAS unsigned*)(iqp + 256); qo[2] = *(LAS unsigned*)(irp + 2048);
            ix = lds_r128(ixp);
#pragma unroll
            for (int i = 0; i < 9; ++i) oo[i] = *(LAS unsigned*)(irp + 2304 + i * 256);
            CFENCE();
        }
    }
    {
        const unsigned co = WS_O + (unsigned)(((rb * 8 + 2 * lw) * 72 + hg * 9) * 256 + lane * 16);
#pragma unroll
        for (int i = 0; i < 5; ++i)
            if (i < 4 || lane < 32) st_sc1(wsr, i < 4 ? co + (i >> 1) * 18432 + (i & 1) * 1024 : co + 2048 + (lane >> 4) * (18432 - 256), u32x4{EMPTY, EMPTY, EMPTY, EMPTY});
    }
    WAIT_VM(5); FLAG_ADD(F_LANDED);
    if (lw == 1) {
        const float gate = rbf(__builtin_amdgcn_rcpf(1.f + __expf(-rbf(gacc[0]))));
        if (lane < 16) __hip_atomic_store((GAS float*)(P.ws + WS_GATE) + (rb * 128 + hg * 16 + lane), gate, RLX_AGENT);
    }
    if (lw < 3) {
        const int dst[2] = {(int)ix.z, (int)ix.w};
        flag_wait<0>(smem, F_KVP, 8);
        STAMP(2);
        float pa[2][4][4];
#pragma unroll
        for (int i = 0; i < 2; ++i)
#pragma unroll
            for (int r = 0; r < 4; ++r)
#pragma unroll
                for (int k = 0; k < 4; ++k) pa[i][r][k] = lds_rf(smem + ix[i] + r * 12288 + k * 256);
        SCHED();
        u32x4 pv;
#pragma unroll
        for (int i = 0; i < 2; ++i)
#pragma unroll
            for (int k = 0; k < 2; ++k)
                pv[2 * i + k] = pk_bf16((pa[i][0][2 * k] + pa[i][1][2 * k]) + (pa[i][2][2 * k] + pa[i][3][2 * k]),
                                        (pa[i][0][2 * k + 1] + pa[i][1][2 * k + 1]) + (pa[i][2][2 * k + 1] + pa[i][3][2 * k + 1]));
        st_sc1(wsr, sto, not_empty(pv));
        SCHED();
        FLAG_ADD(F_KVR);
        STAMP(3);
        WAIT_VM(0);
        STAMP(4);
        f32x4 sum[2];
#pragma unroll 1
        for (unsigned spins = 0; spins < SPIN_CAP; ++spins) {
            u32x4 a[4][4];
#pragma unroll
            for (int i = 0; i < 4; ++i)
#pragma unroll
                for (int t = 0; t < 4; ++t) a[t][i] = __builtin_amdgcn_raw_buffer_load_b128(wsr, off + t * 256, ((i + rb) & 3) * 12288, 16);
            SCHED();
            FLAG_ADD(F_SW);
            sum[0] = sum[1] = f32x4{0.f, 0.f, 0.f, 0.f};
#pragma unroll
            for (int i = 0; i < 4; ++i)
#pragma unroll
                for (int t = 0; t < 4; ++t) sum[t >> 1] = mfma16(sel[t & 1], a[t][i], sum[t >> 1]);
            if (__all(sum[0][0] == sum[0][0] && sum[1][0] == sum[1][0])) break;
            __builtin_amdgcn_s_sleep(RSLEEP);
            if (spins < 8) { CFENCE(); continue; }
            unsigned m = 0;
#pragma unroll
            for (int t = 0; t < 4; ++t)
#pragma unroll
                for (int i = 0; i < 4; ++i) m = max(m, umax4(a[t][i]));
            if (__all(m != EMPTY)) break;
            CFENCE();
        }
#pragma unroll
        for (int i = 0; i < 2; ++i) {
            const unsigned lo = (dst[i] & 1) ? pk_bf16(sum[i][0], sum[i][1]) : 0u, hi = (dst[i] & 2) ? pk_bf16(sum[i][2], sum[i][3]) : 0u;
            lptr dp = smem + (dst[i] & 0xffffc);
            if (dst[i] & 0x100000) {
                const int hs = (dst[i] & 0x200000) ? 20 : 4;
#pragma unroll
                for (int r = 0; r < 4; ++r) { lds_w32(dp + 64 * r, lo); lds_w32(dp + 64 * r + hs, hi); }
            } else lds_w64(dp, u32x2{lo, hi});
        }
        CFENCE();
        FLAG_ADD(F_D);
        STAMP(5);
    } else {
        auto wo_chunks = [&](int c0, int c1) {
#pragma unroll 1
            for (int c = c0; c < c1; ++c) {
                const char* ws = P.wo + cg2 * 144 + c * (64 * 1152);
                const unsigned l = c < 8 ? c * 9216 : WO8;
                SET_M0(l); glds16o<0>(ws, oo[0]); glds16o<1024>(ws, oo[1]); glds16o<2048>(ws, oo[2]); glds16o<3072>(ws, oo[3]);
                SET_M0(l + 4096); glds16o<0>(ws, oo[4]); glds16o<1024>(ws, oo[5]); glds16o<2048>(ws, oo[6]); glds16o<3072>(ws, oo[7]);
                SET_M0(l + 8192); glds16o<0>(ws, oo[8]);
            }
        };
        wo_chunks(8, 9);
        WAIT_VM(9); FLAG_ADD(F_D);
        flag_wait(smem, F_KVR, 3);
        wo_chunks(3, 8);
        WAIT_VM(0);
        FLAG_ADD(F_W1);
        flag_wait(smem, F_SW, 3);
        flag_wait(smem, F_Q0, 8);
        wo_chunks(0, 3);
        WAIT_VM(0);
        FLAG_ADD(F_W0);
    }
    __builtin_amdgcn_s_setprio(0);
    STAMP(6);
    if (lw == 0) STAMP_OUT(5, blockIdx.x + 256);
}

DEVI void phase2(const Params& P, lptr smem, int rt64, int cg, bool gemm, u64 tag, rsrc_t wsr) {
    const int tid = threadIdx.x, lane = tid & 63, w = __builtin_amdgcn_readfirstlane(tid >> 6);
    const int lo16 = lane & 15, g = lane >> 4;
    const int rt = w & 3, kt = w >> 2;
    STAMP_DECL; STAMP(1);
    u32x4 bv[3]; unsigned gv;
    const unsigned boff = WS_B + (unsigned)((cg * 72 + (tid >> 3)) * 4096 + (rt64 >> 3) * 1024 + (((tid >> 2) & 1) * 32 + ((rt64 >> 2) & 1) * 16 + (rt64 & 3) * 4 + (tid & 3)) * 16);
    auto b_load = [&]() { bv[0] = ld_sc1(wsr, boff); bv[1] = ld_sc1(wsr, boff + 32 * 4096); bv[2] = ld_sc1(wsr, boff + (w == 0 ? 64 * 4096 : 0)); };
    auto b_ok = [&]() { return (bool)__all(max(max(umax4(bv[0]), umax4(bv[1])), umax4(bv[2])) != EMPTY); };
    auto b_put = [&]() {
        lptr bp = smem + P2_B + ((tid & 3) * 16 + ((tid >> 2) & 1) * 8) * 144 + (tid >> 3) * 2;
#pragma unroll
        for (int k = 0; k < 3; ++k) if (k < 2 || w == 0) {
#pragma unroll
            for (int q = 0; q < 4; ++q) { lds_w16(bp + 64 * k + (2 * q) * 144, bv[k][q]); lds_w16(bp + 64 * k + (2 * q + 1) * 144, bv[k][q] >> 16); }
        }
    };
    bool b_todo = false;
    if (w < 4) {
        if (w == 3) { const int c0 = cg * 72, g0 = (c0 >> 6) * 11 + (c0 & 63) / 6, c1 = c0 + 71, g1 = (c1 >> 6) * 11 + (c1 & 63) / 6;
                      poll_flags(P, WS_BFL + g0 * 128, g1 - g0 + 1, tag, lane); flag_set(F_B, 1); } else flag_wait(smem, F_B, 1);
        b_load();
    }
    if (gemm) flag_wait(smem, w < 4 ? F_W0 : F_W1, 1);
    STAMP(2);
    const int qrow = (2 * (4 * g + (lo16 >> 2))) * 144;
    lptr qw = smem + qrow + (lane & 3) * 8, qw4 = smem + qrow + (16 + (lane & 1)) * 8;
    int wofs[3], oofs[3];
#pragma unroll
    for (int u = 0; u < 3; ++u) {
        const int c = kt * 3 + (u + cg) % 3;
        wofs[u] = c < 8 ? c * 9216 : WO8; oofs[u] = c * 2048;
    }
    auto rdo = [&](int i, int j) { return j < 4 ? lds_trfrag(qw + wofs[i >> 1] + (i & 1) * 4608 + 32 * j, 144) : lds_trfrag(qw4 + wofs[i >> 1] + (i & 1) * 4608, 144); };
    const unsigned ooff = WS_O + (unsigned)((rt64 * 4 + rt) * 18432 + lane * 16);
    auto ldo = [&](int i) { return __builtin_amdgcn_raw_buffer_load_b128(wsr, ooff + (i & 1) * 1024, oofs[i >> 1], 16); };
    u32x4 wf[6][5];
#pragma unroll
    for (int i = 0; i < 6; ++i) {
        if (i == 5 && w < 4) {
            if (b_ok()) b_put(); else b_todo = true;
        }
#pragma unroll
        for (int j = 0; j < 5; ++j) wf[i][j] = rdo(i, j);
    }
    STAMP(3);
    if (w == 4) { poll_flags<OSLEEP>(P, WS_HINT + (rt64 >> 1) * 1024, 8, tag, lane); flag_set(F_H, 1); } else flag_wait<1>(smem, F_H, 1);
    STAMP(4);
    if (b_todo) b_load();
    gv = __hip_atomic_load((GAS unsigned*)(P.ws + WS_GATE) + (rt64 * 64 + (tid < 576 ? tid / 9 : 0)), RLX_AGENT);
    f32x4 acc[5];
    {
        u32x4 of[6];
#pragma unroll
        for (int i = 0; i < 6; ++i) of[i] = ldo(i);
#pragma unroll
        for (int j = 0; j < 5; ++j) acc[j] = f32x4{0.f, 0.f, 0.f, 0.f};
#pragma unroll
        for (int i = 0; i < 6; ++i)
#pragma unroll
            for (int j = 0; j < 5; ++j) acc[j] = mfma16(wf[i][j], of[i], acc[j]);
    }
    if (!__all(acc[0][0] == acc[0][0])) {
#pragma unroll
        for (int j = 0; j < 5; ++j) acc[j] = f32x4{0.f, 0.f, 0.f, 0.f};
        u32x4 of[6];
#pragma unroll 1
        for (unsigned spins = 0; spins < SPIN_CAP; ++spins) {
            CFENCE();
            unsigned m = 0;
#pragma unroll
            for (int i = 0; i < 6; ++i) of[i] = ldo(i);
#pragma unroll
            for (int i = 0; i < 6; ++i) m = max(m, umax4(of[i]));
            if (__all(m != EMPTY)) break;
        }
#pragma unroll
        for (int i = 0; i < 6; ++i)
#pragma unroll
            for (int j = 0; j < 5; ++j) acc[j] = mfma16(rdo(i, j), of[i], acc[j]);
    }
    if (gemm) flag_wait(smem, F_QD, 8);
    lptr ap = smem + P2_ACC + kt * (64 * P2_AP) + (rt * 16 + lo16) * P2_AP + g * 16;
#pragma unroll
    for (int j = 0; j < 5; ++j) if (j < 4 || g < 2) lds_w128(ap + j * 64, __builtin_bit_cast(u32x4, acc[j]));
    if (b_todo) {
#pragma unroll 1
        for (unsigned spins = 0; spins < SPIN_CAP; ++spins) {
            if (b_ok()) break;
            __builtin_amdgcn_s_sleep(2);
            CFENCE();
            b_load();
        }
        b_put();
    }
    STAMP(5);
    BARRIER();
    STAMP(6);
    if (w < 9) {
        const int r0 = tid / 9, c0 = tid - r0 * 9;
        lptr a = smem + P2_ACC + r0 * P2_AP + c0 * 32;
        const f32x4 s0 = __builtin_bit_cast(f32x4, lds_r128(a)) + __builtin_bit_cast(f32x4, lds_r128(a + 64 * P2_AP)) + __builtin_bit_cast(f32x4, lds_r128(a + 128 * P2_AP));
        const f32x4 s1 = __builtin_bit_cast(f32x4, lds_r128(a + 16)) + __builtin_bit_cast(f32x4, lds_r128(a + 64 * P2_AP + 16)) + __builtin_bit_cast(f32x4, lds_r128(a + 128 * P2_AP + 16));
        const u32x4 bb = lds_r128(smem + P2_B + tid * 16);
        const float gt = __uint_as_float(gv), og = rbf(1.f - gt);
        u32x4 o;
#pragma unroll
        for (int i = 0; i < 4; ++i) {
            const float a0 = i < 2 ? s0[2 * i] : s1[2 * i - 4], a1 = i < 2 ? s0[2 * i + 1] : s1[2 * i - 3];
            o[i] = pk_bf16(gt * a0 + og * bf2f(bb[i]), gt * a1 + og * bfhi(bb[i]));
        }
        __builtin_amdgcn_raw_buffer_store_b128(o, __builtin_amdgcn_make_buffer_rsrc(P.out, 0, SEQ * DM * 2, 0x00020000), (unsigned)((rt64 * 64 + r0) * 1152 + cg * 144 + c0 * 16), 0, OUTAUX);
    }
    STAMP(7);
    if (w == 4) STAMP_OUT(blockIdx.x < 128 ? 3 : 6, blockIdx.x + 512);
}

DEVI void helper_role(const Params& P, lptr smem, int xcd, int li, u64& tag, rsrc_t wsr) {
    const int tid = threadIdx.x, lane = tid & 63, w = __builtin_amdgcn_readfirstlane(tid >> 6);
    STAMP_DECL; STAMP(1);
    if (li >= 13) {
#pragma unroll 1
        for (int z = 0; z < HDELAY; ++z) __builtin_amdgcn_s_sleep(16);
#pragma unroll 1
        for (int n = 48 + 12 * NT2 + (li - 13) + 3 * w; n < 171; n += 36) touch_group(P, xcd, n, lane);
    }
    PIN_TAG();
    BARRIER();
    if (tid == 0) tag_post(P, tag);
    if (li >= 13) {
#pragma unroll 1
        for (int n = 171 + (li - 13) + 3 * w; n < 234; n += 36) touch_group(P, xcd, n, lane);
    }
    STAMP(2);
    if (w == 11) {
        STAMP_OUT(8, blockIdx.x);
        if (blockIdx.x == 255) {
            const GAS u64* f = (const GAS u64*)(P.ws + WS_ARR) + lane;
#pragma unroll 1
            for (unsigned spins = 0; spins < SPIN_CAP; ++spins) {
                const bool ok = __hip_atomic_load(f, RLX_AGENT) == tag && __hip_atomic_load(f + 64, RLX_AGENT) == tag &&
                                __hip_atomic_load(f + 128, RLX_AGENT) == tag && __hip_atomic_load(f + 192, RLX_AGENT) == tag;
                if (__all(ok)) break;
                __builtin_amdgcn_s_sleep(8);
            }
            if (lane == 0) __hip_atomic_store((GAS u64*)(P.ws + WS_CTL), tag, RLX_AGENT);
        }
    }
}

__global__ __launch_bounds__(768, 3) void k_fused(Params P) {
    __shared__ __attribute__((aligned(16))) char smem_[LDS_BYTES];
    lptr smem = (lptr)smem_;
    const int b = blockIdx.x, xcd = b & 7, idx = b >> 3;
    const int lane = threadIdx.x & 63, w = __builtin_amdgcn_readfirstlane(threadIdx.x >> 6);
    const rsrc_t wsr = __builtin_amdgcn_make_buffer_rsrc(P.ws, 0, (int)WS_END, 0x00020000);
    const int rt64 = xcd * 4 + (idx & 3), cg2 = idx >> 2;
    if (idx >= 16 && w < 6) {
        const char* pc; unsigned len;
        asm volatile("s_getpc_b64 %0\n.Lkf_code_begin:\n\ts_mov_b32 %1, .Lkf_code_end-.Lkf_code_begin" : "=s"(pc), "=s"(len));
        touch(pc, min((unsigned)(threadIdx.x * 128), len));
    }
    u64 tag;
    if (__builtin_expect(idx < 16, 1)) {
        const int rbl = idx & 3, hgl = idx >> 2, rb = (xcd >> 1) * 4 + rbl, hg = (xcd & 1) * 4 + hgl;
        if (__builtin_expect(w < 4, 1)) gemm_loader(P, smem, rb, hg, w, cg2, tag, wsr);
        else gemm_compute(P, smem, rb, hg, w - 4, tag, wsr);
    } else {
        tag = *(const __attribute__((address_space(4))) u64*)(P.ws + WS_CTL);
        const int li = idx - 16, gid = li < 11 ? xcd * 11 + li : 88 + (li - 11) * 8 + xcd;
        if (gid < 99) ssm_role(P, smem, xcd, li, gid, tag, wsr);
        else helper_role(P, smem, xcd, li, tag, wsr);
        if (w < 9) wo_issue<0x1ff>(P, cg2, w, 9, lane);
        if (w == 10 && lane < 16) lds_w32(smem + FL + lane * 4, 0u);
        WAIT_VM(0);
        BARRIER();
    }
    phase2(P, smem, rt64, cg2, idx < 16, tag, wsr);
}

extern "C" void kernel_launch(void* const* d_in, const int* in_sizes, int n_in, void* d_out, int out_size, void* d_ws, size_t ws_size,
                              hipStream_t stream) {
    Params P;
    P.x = (const char*)d_in[0]; P.wqkv = (const char*)d_in[1]; P.wo = (const char*)d_in[2];
    P.logA = (const char*)d_in[4]; P.aim = (const char*)d_in[5]; P.cre = (const char*)d_in[6]; P.cim = (const char*)d_in[7];
    P.logdt = (const char*)d_in[8]; P.dskip = (const char*)d_in[9]; P.gw = (const char*)d_in[10];
    P.out = (char*)d_out; P.ws = (char*)d_ws;
    k_fused<<<256, 768, 0, stream>>>(P);
}
```

```cpp
#include <hip/hip_runtime.h>
#include <stdint.h>

#define GAS __attribute__((address_space(1)))
#define LAS __attribute__((address_space(3)))
#define DEVI __device__ __forceinline__
typedef unsigned short u16;
typedef unsigned long long u64;
typedef __attribute__((ext_vector_type(2))) unsigned u32x2;
typedef __attribute__((ext_vector_type(3))) unsigned u32x3;
typedef __attribute__((ext_vector_type(4))) unsigned u32x4;
typedef __attribute__((ext_vector_type(4))) short s16x4;
typedef __attribute__((ext_vector_type(2))) float f32x2;
typedef __attribute__((ext_vector_type(4))) float f32x4;
typedef __attribute__((ext_vector_type(16))) float f32x16;
typedef __attribute__((ext_vector_type(2))) __bf16 bf16x2;
typedef __attribute__((ext_vector_type(8))) __bf16 bf16x8;
typedef __attribute__((ext_vector_type(2))) _Float16 f16x2;
typedef __attribute__((ext_vector_type(8))) _Float16 f16x8;
typedef LAS char* lptr;

#define PREF 1
#define ROT 0
#define FSLEEP 2
#define NSLEEP "3"
#define LDRIFT 0
#define PSLEEP 6
#define CSLEEP 1
#define OSLEEP 2
#define RSLEEP 6
#define HOLD 1
#define GROT ((w + li) & 3)
#define NT2 4
#define OUTAUX 17
#define TEARLY 1
#define HDELAY 0
constexpr int SEQ = 2048, DM = 576;

constexpr unsigned WS_CTL = 0;
constexpr unsigned WS_ARR = 256;
constexpr unsigned WS_BFL = 4096;
constexpr unsigned WS_HINT = 20480;
constexpr unsigned WS_GATE = 36864;
constexpr unsigned WS_PFL = 49152;
constexpr unsigned WS_PART = 65536;
constexpr unsigned WS_O = WS_PART + 8 * 16 * 3072;
constexpr unsigned WS_B = WS_O + SEQ * DM * 2;
constexpr unsigned WS_END = WS_B + SEQ * DM * 2;
constexpr unsigned EMPTY = 0xffffffffu;

constexpr size_t WS_STAMP = 16u << 20;
#define STAMP_DECL
#define STAMP(i)
#define CSTAMP(i)
#define STAMP_OUT(role, slot)

struct Params {
    const char* x; const char* wqkv; const char* wo; const char* logA; const char* aim; const char* cre; const char* cim;
    const char* logdt; const char* dskip; const char* gw; char* out; char* ws;
};

constexpr int LDS_BYTES = 163840, FL = 163584, LDS_TAG = FL + 64, LDS_DUMMY = FL + 128, LDS_JUNK = 161280;
enum { F_SW = 0, F_LANDED = 4, F_W1 = 20, F_CL = 24, F_B = 28, F_H = 32, F_KVP = 36, F_KVR = 40, F_Q0 = 44, F_QD = 48, F_D = 52, F_C1 = 56, F_W0 = 60 };
constexpr int WO8 = 132096, P2_ACC = 73728, P2_AP = 304, P2_B = 141312, P2_G = 150528;

DEVI float bf2f(unsigned h) { return __uint_as_float(h << 16); }
DEVI float bfhi(unsigned h) { return __uint_as_float(h & 0xffff0000u); }
DEVI unsigned pk_bf16(float a, float b) { return __builtin_bit_cast(unsigned, __builtin_convertvector(f32x2{a, b}, bf16x2)); }
DEVI unsigned pk_f16(f32x2 v) { return __builtin_bit_cast(unsigned, __builtin_convertvector(v, f16x2)); }
DEVI float rbf(float a) { return bf2f(pk_bf16(a, a)); }
DEVI f32x4 mfma16(u32x4 a, u32x4 b, f32x4 c) {
    return __builtin_amdgcn_mfma_f32_16x16x32_bf16(__builtin_bit_cast(bf16x8, a), __builtin_bit_cast(bf16x8, b), c, 0, 0, 0);
}
DEVI void mfma16i(f32x4& c, u32x4 a, u32x4 b) { asm volatile("v_mfma_f32_16x16x32_bf16 %0, %1, %2, %0" : "+v"(c) : "v"(a), "v"(b)); }
#define MFMA_DONE() do { __builtin_amdgcn_sched_barrier(0); asm volatile("s_nop 7\n\ts_nop 1" ::: "memory"); __builtin_amdgcn_sched_barrier(0); } while (0)
DEVI u32x4 lds_r128(lptr p) { return *(LAS u32x4*)p; }
DEVI u32x2 lds_r64(lptr p) { return *(LAS u32x2*)p; }
DEVI float lds_rf(lptr p) { return *(LAS float*)p; }
DEVI void lds_w128(lptr p, u32x4 v) { *(LAS u32x4*)p = v; }
DEVI void lds_w64(lptr p, u32x2 v) { *(LAS u32x2*)p = v; }
DEVI void lds_w32(lptr p, unsigned v) { *(LAS unsigned*)p = v; }
DEVI void lds_w16(lptr p, unsigned v) { *(LAS u16*)p = (u16)v; }
DEVI u32x2 lds_tr(lptr p) { return __builtin_bit_cast(u32x2, __builtin_amdgcn_ds_read_tr16_b64_v4i16((LAS s16x4*)p)); }
DEVI u32x4 lds_trfrag(lptr p, int off2) { u32x2 a = lds_tr(p), b = lds_tr(p + off2); return u32x4{a.x, a.y, b.x, b.y}; }
template <int OFF> DEVI void glds16o(const char* sbase, unsigned voff) {
    asm volatile("global_load_lds_dwordx4 %0, %1 offset:%2" :: "v"(voff), "s"(sbase), "i"(OFF) : "memory");
}
DEVI void glds16(const char* sbase, unsigned voff, unsigned l) {
    asm volatile("s_mov_b32 m0, %2\n\ts_nop 0\n\tglobal_load_lds_dwordx4 %0, %1" :: "v"(voff), "s"(sbase), "s"(l) : "memory");
}
#define SET_M0(v) asm volatile("s_mov_b32 m0, %0\n\ts_nop 0" :: "s"(v) : "memory")
#define W_ADDTID(v, off) asm volatile("ds_write_addtid_b32 %0 offset:%1" :: "v"(v), "i"(off) : "memory")
#define CFENCE() asm volatile("" ::: "memory")
#define SCHED() __builtin_amdgcn_sched_barrier(0)
#define WAIT_VM(n) asm volatile("s_waitcnt vmcnt(" #n ")" ::: "memory")
#define WAIT_LGKM(n) __builtin_amdgcn_s_waitcnt(0xC07F | ((n) << 8))
#define WAIT_LGKM0() asm volatile("s_waitcnt lgkmcnt(0)" ::: "memory")
#define BARRIER() do { SCHED(); asm volatile("s_waitcnt lgkmcnt(0)\n\ts_barrier" ::: "memory"); SCHED(); } while (0)
#define RAW_BARRIER() do { SCHED(); asm volatile("s_barrier" ::: "memory"); SCHED(); } while (0)
#define RLX_AGENT __ATOMIC_RELAXED, __HIP_MEMORY_SCOPE_AGENT
constexpr unsigned SPIN_CAP = 1u << 16;
typedef __amdgpu_buffer_rsrc_t rsrc_t;
DEVI u32x4 ld_sc1(rsrc_t r, unsigned off) { return __builtin_amdgcn_raw_buffer_load_b128(r, off, 0, 16); }
DEVI void st_sc1(rsrc_t r, unsigned off, u32x4 v) { __builtin_amdgcn_raw_buffer_store_b128(v, r, off, 0, 16); }

DEVI u32x4 pack8(f32x4 a, f32x4 b) { return u32x4{pk_bf16(a[0], a[1]), pk_bf16(a[2], a[3]), pk_bf16(b[0], b[1]), pk_bf16(b[2], b[3])}; }
DEVI float phi(float t) {
    const float e = __builtin_amdgcn_fmed3f(__builtin_amdgcn_exp2f(t * 1.44269504089f), 0.f, 1.f);
    return __int_as_float(max(__float_as_int(t), 0x358637bd)) + e;
}
DEVI f32x4 phi4(f32x4 v) { return f32x4{phi(v[0]), phi(v[1]), phi(v[2]), phi(v[3])}; }
DEVI unsigned umax4(u32x4 a) { return max(max(a.x, a.y), max(a.z, a.w)); }
DEVI u32x4 not_empty(u32x4 a) { return u32x4{min(a.x, EMPTY - 1), min(a.y, EMPTY - 1), min(a.z, EMPTY - 1), min(a.w, EMPTY - 1)}; }
DEVI void tag_post(const Params& P, u64 tag) { __hip_atomic_store((GAS u64*)(P.ws + WS_ARR) + blockIdx.x, tag, RLX_AGENT); }
#define PIN_TAG() do { asm volatile("" : "+s"(tag)); tag += 1ull; } while (0)
template <int SL = PSLEEP>
DEVI void poll_addr(const Params& P, unsigned off, u64 tag) {
    const GAS u64* f = (const GAS u64*)(P.ws + off);
#pragma unroll 1
    for (unsigned spins = 0; spins < SPIN_CAP; ++spins) {
        if (__all(__hip_atomic_load(f, RLX_AGENT) == tag)) break;
        __builtin_amdgcn_s_sleep(SL);
    }
}
template <int SL = PSLEEP>
DEVI void poll_flags(const Params& P, unsigned off, int n, u64 tag, int lane) { poll_addr<SL>(P, off + (lane < n ? lane : 0) * 128, tag); }
DEVI void poll_b(const Params& P, int cg, u64 tag, int lane) {
    const int c0 = cg * 72, g0 = (c0 >> 6) * 11 + (c0 & 63) / 6, c1 = c0 + 71, g1 = (c1 >> 6) * 11 + (c1 & 63) / 6;
    poll_flags(P, WS_BFL + g0 * 128, g1 - g0 + 1, tag, lane);
}
DEVI unsigned flag_get(lptr smem, int f) { return __builtin_amdgcn_readfirstlane(__hip_atomic_load((LAS unsigned*)(smem + FL + f), __ATOMIC_RELAXED, __HIP_MEMORY_SCOPE_WORKGROUP)); }
template <int SL = FSLEEP>
DEVI void flag_wait(lptr smem, int f, unsigned v) {
#pragma unroll 1
    for (unsigned spins = 0; spins < (SPIN_CAP << 4); ++spins) { if (flag_get(smem, f) >= v) break; if (SL) __builtin_amdgcn_s_sleep(SL); }
    CFENCE();
}
DEVI void flag_add(int f) { asm volatile("s_mov_b64 exec, 1\n\tds_add_u32 %0, %1\n\ts_mov_b64 exec, -1" :: "v"(FL + f), "v"(1u) : "memory"); }
#define FLAG_ADD(f) asm volatile("s_mov_b64 exec, 1\n\tds_add_u32 %0, %1 offset:%2\n\ts_mov_b64 exec, -1" :: "v"(flp), "v"(one), "i"(f) : "memory")
DEVI void flag_set(int f, unsigned v) { asm volatile("s_mov_b64 exec, 1\n\tds_write_b32 %0, %1\n\ts_mov_b64 exec, -1" :: "v"(FL + f), "v"(v) : "memory"); }
#define NEEDP(seen, ptr, f, v) do { unsigned c_; asm volatile( \
        "v_cmp_gt_u32 vcc, %4, %1\n\ts_cbranch_vccnz 0f\n1:\n\t.subsection 1\n" \
        "0:\n\ts_mov_b32 %0, 0x100000\n2:\n\ts_sleep " NSLEEP "\n\tds_read_b32 %1, %2 offset:%3\n\ts_sub_u32 %0, %0, 1\n\ts_waitcnt lgkmcnt(0)\n\ts_cbranch_scc1 1b\n\t" \
        "v_cmp_gt_u32 vcc, %4, %1\n\ts_cbranch_vccnz 2b\n\ts_branch 1b\n\t.subsection 0" \
        : "=&s"(c_), "+v"(seen) : "v"(ptr), "i"(f), "i"(v) : "memory", "scc", "vcc"); } while (0)
#define NEED(seen, f, v) NEEDP(seen, flp, f, v)
DEVI void touch(const char* sbase, unsigned voff) {
    asm volatile("s_mov_b32 m0, %2\n\ts_nop 0\n\tglobal_load_lds_dword %0, %1" :: "v"(voff), "s"(sbase), "s"(LDS_JUNK) : "memory");
}
DEVI void touch_group(const Params& P, int xcd, int n, int lane) {
    if (n < 126) {
        const int c0 = n / 18, r = n - c0 * 18, c = c0 + 2;
        if (r < 8) touch(P.x, (unsigned)(((xcd >> 1) * 512 + r * 64 + lane) * 1152 + c * 128));
        else { const int l = r - 8; touch(P.wqkv, (unsigned)((c * 64 + lane) * 3456 + (l < 5 ? 1152 : 2304 - 640) + (xcd & 1) * 512 + l * 128)); }
    } else if (n < 171) {
        const int c = (n - 126) / 5, l = n - 126 - c * 5;
        touch(P.wqkv, (unsigned)((c * 64 + lane) * 3456 + (xcd & 1) * 512 + l * 128));
    } else {
        const int l = (n - 171) / 9, c = n - 171 - l * 9;
        touch(P.wo, (unsigned)((c * 64 + lane) * 1152 + l * 128));
    }
}
template <unsigned CH>
DEVI void wo_issue(const Params& P, int cg, int first, int stride, int lane) {
#pragma unroll 1
    for (int m = first; m < 9; m += stride) {
        const int q = 64 * m + lane, rho = q / 9, pc = q - rho * 9;
        const int k = (rho & 32) + 8 * ((rho >> 3) & 3) + 4 * (rho & 1) + ((rho >> 1) & 3);
        const unsigned qo = (unsigned)(k * 1152 + cg * 144 + pc * 16);
#pragma unroll
        for (int c = 0; c < 9; ++c) if ((CH >> c) & 1) glds16(P.wo + c * (64 * 1152), qo, (unsigned)((c < 8 ? c * 9216 : WO8) + m * 1024));
    }
}

constexpr int SP = 3088;
constexpr int XT_OFF = 133632, XT_BYTES = 4608, XPAD = 256;
constexpr int TT_OFF = 104448, TC = 1216, PT_OFF = 55296, PC_OFF = 2560, ACX_OFF = 55296;

#define CM2(o1, p1, a1, b1, o2, p2, a2, b2) do { asm( \
    "v_pk_mul_f32 %0, %2, %3 op_sel:[1,1] op_sel_hi:[1,0] neg_lo:[0,1]\n\tv_pk_mul_f32 %1, %4, %5 op_sel:[1,1] op_sel_hi:[1,0] neg_lo:[0,1]\n\t" \
    "v_pk_fma_f32 %0, %2, %3, %0 op_sel_hi:[0,1,1]\n\tv_pk_fma_f32 %1, %4, %5, %1 op_sel_hi:[0,1,1]" \
    : "=&v"(o1), "=&v"(o2) : "v"(a1), "v"(b1), "v"(a2), "v"(b2)); \
    asm("v_cvt_pk_f16_f32 %0, %2, %3\n\tv_cvt_pk_f16_f32 %1, %4, %5" : "=&v"(p1), "=&v"(p2) : "v"((o1).x), "v"((o1).y), "v"((o2).x), "v"((o2).y)); } while (0)
#define HM2(p1, a1, p2, a2, b) asm( \
    "v_pk_mul_f16 %0, %2, %4 op_sel_hi:[0,1]\n\tv_pk_mul_f16 %1, %3, %4 op_sel_hi:[0,1]\n\t" \
    "v_pk_fma_f16 %0, %2, %4, %0 op_sel:[1,1,0] op_sel_hi:[1,0,1] neg_lo:[0,1,0]\n\tv_pk_fma_f16 %1, %3, %4, %1 op_sel:[1,1,0] op_sel_hi:[1,0,1] neg_lo:[0,1,0]" \
    : "=&v"(p1), "=&v"(p2) : "v"(a1), "v"(a2), "v"(b))
DEVI f32x2 cexp_p(float a2, float f, float p) {
    float m = __builtin_amdgcn_exp2f(a2 * p);
    float r = __builtin_amdgcn_fractf(f * p);
    return f32x2{m * __builtin_amdgcn_cosf(r), m * __builtin_amdgcn_sinf(r)};
}
DEVI u32x4 mask_frag(unsigned bits, int g, unsigned val) {
    unsigned by = bits >> (8 * g);
    u32x4 r;
#pragma unroll
    for (int i = 0; i < 4; ++i) { unsigned t = (by >> (2 * i)) & 3u; r[i] = ((t | (t << 15)) & 0x10001u) * val; }
    return r;
}
struct Tab2 { unsigned v[8][64][4]; };
constexpr Tab2 make_tab2() {
    Tab2 t{};
    for (int l = 0; l < 64; ++l) {
        const int r = l & 15, g = l >> 4, j = l & 31, h = l >> 5;
        const unsigned bits[6] = {7u << (r + 11), 31u << (r + 6), 7u << (r + 3), 127u << (r + 5), 511u << (r + 4), 3u << (r + 12)};
        for (int m = 0; m < 6; ++m)
            for (int i = 0; i < 4; ++i) {
                const unsigned q = (bits[m] >> (8 * g + 2 * i)) & 3u, val = m < 5 ? 0x3f80u : 0xffffu;
                t.v[m][l][i] = (q & 1 ? val : 0u) | (q & 2 ? val << 16 : 0u);
            }
        for (int c = 0; c < 4; ++c) {
            const int v2 = 79 - (32 + (j >> 1)) - c + 4, v3 = 127 - (64 + 32 * h + (j >> 1)) - c + 4;
            t.v[6][l][c] = PT_OFF + (j & 1) * 1280 + c * 320 + (v2 >> 2) * 16 + h * 8 + (v2 & 3) * 2 - 64;
            t.v[7][l][c] = PT_OFF + PC_OFF + c * 320 + (v3 >> 2) * 16 + (j & 1) * 8 + (v3 & 3) * 2 - 64;
        }
    }
    return t;
}
__device__ const Tab2 g_tab2 = make_tab2();
DEVI float wave_max(float v) {
    v = fmaxf(v, __int_as_float(__builtin_amdgcn_update_dpp(0, __float_as_int(v), 0xB1, 0xf, 0xf, true)));
    v = fmaxf(v, __int_as_float(__builtin_amdgcn_update_dpp(0, __float_as_int(v), 0x4E, 0xf, 0xf, true)));
    v = fmaxf(v, __int_as_float(__builtin_amdgcn_update_dpp(0, __float_as_int(v), 0x124, 0xf, 0xf, true)));
    v = fmaxf(v, __int_as_float(__builtin_amdgcn_update_dpp(0, __float_as_int(v), 0x128, 0xf, 0xf, true)));
    const int i = __float_as_int(v);
    const int a = max(__builtin_amdgcn_readlane(i, 0), __builtin_amdgcn_readlane(i, 16)), b = max(__builtin_amdgcn_readlane(i, 32), __builtin_amdgcn_readlane(i, 48));
    return __int_as_float(max(a, b));
}

DEVI void ssm_role(const Params& P, lptr smem, int xcd, int li, int gid, int cg2, u64& tag, rsrc_t wsr, u64& tp) {
    const int gl = gid / 11, gi = gid - gl * 11, ch0 = gl * 64 + (gi < 10 ? gi * 6 : 58);
    const int tid = threadIdx.x, lane = tid & 63, w = __builtin_amdgcn_readfirstlane(tid >> 6);
    const int ch = w >> 1, half = (w ^ (w >> 2)) & 1, d = ch0 + ch;
    const bool own = gi < 10 || ch >= 2;
    const int lo16 = lane & 15, g = lane >> 4;
    STAMP_DECL; STAMP(1);
    __builtin_amdgcn_s_setprio(3);
    if (w >= 8 && li < 12) touch_group(P, xcd, (w - 8) * 12 + li, lane);
    const unsigned po = (unsigned)(d * 256 + lane * 4);
    const unsigned r_la = *(const GAS unsigned*)(P.logA + po), r_ai = *(const GAS unsigned*)(P.aim + po);
    const unsigned r_cr = *(const GAS unsigned*)(P.cre + po), r_ci = *(const GAS unsigned*)(P.cim + po);
    const unsigned r_dt = *(const GAS u16*)(P.logdt + d * 2), r_ds = *(const GAS u16*)(P.dskip + d * 2);
    u32x4 ct[8];
    if (half) {
        const char* tp = (const char*)&g_tab2;
        asm volatile("" : "+s"(tp));
#pragma unroll
        for (int i = 0; i < 8; ++i) ct[i] = *(const GAS u32x4*)(tp + (unsigned)(i * 1024 + lane * 16));
    }
    const int tt = (tid + (li & 7) * 64) & 511;
    u32x3 xr[4];
    const unsigned xoff = (unsigned)(tt * 4608 + ch0 * 2);
    const unsigned boff = own ? WS_B + (unsigned)(d * 4096 + half * 2048 + lane * 16) : WS_END;
#define GATHER(i) do { SCHED(); if (w < 8) xr[i] = *(const GAS u32x3*)(P.x + xoff + (i) * 1152); SCHED(); } while (0)
#define PRIO(i) do { SCHED(); __builtin_amdgcn_s_setprio(i); SCHED(); } while (0)
    SCHED();
    lptr xt = smem + XT_OFF + ch * XT_BYTES, tb = smem + TT_OFF + ch * 4864, pt = smem + PT_OFF + ch * 3840;
    const int j32 = lane & 31, h = lane >> 5;
    if (!half) { lds_w64(xt + lane * 8, u32x2{0u, 0u}); lds_w32(smem + FL + 32, 0u); }
    int arow;
    {
        const u64 map0 = 0x0a09080803020100ull, map1 = 0x0c0b0a0a07060504ull, map2 = 0x1413110e12100f0dull;
        const u64 mm = j32 < 8 ? map0 : (j32 < 16 ? map1 : map2);
        arow = (int)((mm >> (8 * (j32 & 7))) & 0xff);
    }
    lptr ap = smem + arow * SP + w * 256 + h * 16, bp = smem + j32 * SP + w * 256 + h * 16;
    const int cpy = (3 - lo16) & 3;
    lptr xf7 = xt + (XPAD - 16 + 16 * lo16 + 8 * g) * 2 - 64 * 7;
    lptr tf = tb + cpy * TC + 2 * (527 - lo16 + 8 * g - cpy) - 64 * 16;
    lptr xc = smem + (w ^ 1) * 256 + lane * 4, xb = smem + XT_OFF + (XPAD + tt * 4) * 2;
    asm volatile("" : "+v"(ap), "+v"(bp), "+v"(xf7), "+v"(tf), "+v"(xc), "+v"(xb));
    const float dt = rbf(__expf(bf2f(r_dt)));
    const float cm = wave_max(fmaxf(fmaxf(fabsf(bf2f(r_cr)), fabsf(bfhi(r_cr))), fmaxf(fabsf(bf2f(r_ci)), fabsf(bfhi(r_ci)))));
    unsigned ce = __float_as_uint(cm) >> 23; ce = ce < 16u ? 16u : (ce > 240u ? 240u : ce);
    const float cs = __uint_as_float((263u - ce) << 23), ics = __uint_as_float((ce - 9u) << 23);
    f32x16 kacc;
#pragma unroll
    for (int i = 0; i < 16; ++i) kacc[i] = 0.f;
    STAMP(2);
    {
        const unsigned sh = half * 16;
        asm volatile("" :: "v"(r_la), "v"(r_ai), "v"(r_ds), "v"(ct[7]));
        if (li < 12 && (w < NT2 || (w >= 8 && !TEARLY))) touch_group(P, xcd, (w >= 8 ? w - 8 : w + 4) * 12 + li, lane);
        const float la = bf2f(r_la >> sh), ai = bf2f(r_ai >> sh), cr = bf2f(r_cr >> sh) * cs, ci = bf2f(r_ci >> sh) * cs;
        const float a2 = (-rbf(__expf(la)) * dt) * 1.44269504089f, f = (ai * dt) * 0.15915494309f;
#define PUT(row, v) W_ADDTID(v, ((row) & 15) * SP)
        const f32x2 w1 = cexp_p(a2, -f, 1.f), W = cexp_p(a2, -f, 256.f);
        f32x2 u[8], v[4], c[9], e4, e8, t2;
        unsigned U[8], V[4], p, q;
        u[1] = cexp_p(a2, f, 8.f); v[1] = cexp_p(a2, f, 64.f);
        c[0] = f32x2{cr, -ci};
        U[1] = pk_f16(u[1]); V[1] = pk_f16(v[1]);
        GATHER(0);
        SET_M0(w * 256);
        PUT(0, pk_f16(c[0]));
        CM2(c[1], p, c[0], w1, u[2], U[2], u[1], u[1]); PUT(1, p);
        CM2(c[2], p, c[1], w1, u[3], U[3], u[2], u[1]); PUT(2, p);
        CM2(c[3], p, c[2], w1, u[4], U[4], u[2], u[2]); PUT(3, p);
        CM2(c[4], p, c[3], w1, u[5], U[5], u[4], u[1]); PUT(4, p);
        CM2(c[5], p, c[4], w1, u[6], U[6], u[4], u[2]); PUT(5, p);
        CM2(c[6], p, c[5], w1, u[7], U[7], u[4], u[3]); PUT(6, p);
        CM2(c[7], p, c[6], w1, v[2], V[2], v[1], v[1]); PUT(7, p);
        CM2(c[8], p, c[7], w1, v[3], V[3], v[2], v[1]);
        PRIO(2);
        GATHER(1);
        CM2(e8, p, c[8], W, e4, q, c[4], W); PUT(12, p); PUT(10, q);
        CM2(c[8], p, e8, W, c[4], q, e4, W); PUT(14, p); PUT(13, q);
        CM2(t2, p, c[0], W, e4, q, c[4], W); PUT(8, p); PUT(15, q);
        CM2(t2, p, c[2], W, e4, q, c[6], W); PUT(9, p); PUT(11, q);
        SET_M0(w * 256 + 16 * SP);
        CM2(e8, p, c[8], W, t2, q, W, W); PUT(17, p);
        CM2(e4, p, c[8], t2, c[0], q, e8, t2); PUT(16, p); PUT(18, q);
        CM2(c[8], p, e4, t2, e8, q, c[0], t2); PUT(19, p); PUT(20, q);
        PRIO(1);
        GATHER(2);
        u32x4 af[8];
#pragma unroll
        for (int s = 0; s < 8; ++s) af[s] = lds_r128(ap + 32 * s);
        CFENCE();
        PUT(16, V[2]); PUT(24, V[3]);
#pragma unroll
        for (int b = 1; b < 8; ++b) { HM2(p, V[2], q, V[3], U[b]); PUT(16 + b, p); PUT(24 + b, q); }
        SET_M0(w * 256);
        PRIO(0);
        GATHER(3);
        PUT(0, 0x3c00u); PUT(8, V[1]);
#pragma unroll
        for (int b = 1; b < 8; b += 2) { HM2(p, U[b], q, U[b == 7 ? 7 : b + 1], V[1]); PUT(8 + b, p); if (b < 7) PUT(9 + b, q); PUT(b, U[b]); if (b < 7) PUT(b + 1, U[b + 1]); }
#undef PUT
        u32x4 bfr[8];
#pragma unroll
        for (int s = 0; s < 8; ++s) bfr[s] = lds_r128(bp + 32 * s);
        SCHED();
        SCHED();
#pragma unroll
        for (int s = 0; s < 8; ++s)
            kacc = __builtin_amdgcn_mfma_f32_32x32x16_f16(__builtin_bit_cast(f16x8, af[s]), __builtin_bit_cast(f16x8, bfr[s]), kacc, 0, 0, 0);
    }
    STAMP(3);
    const float ks = 2.f * dt * ics;
#pragma unroll
    for (int i = 0; i < 12; ++i) kacc[i] *= ks;
    if (half) {
#pragma unroll
        for (int i = 0; i < 8; ++i) W_ADDTID(kacc[i], i * SP);
    } else {
#pragma unroll
        for (int i = 0; i < 4; ++i) W_ADDTID(kacc[8 + i], i * SP);
    }
    PIN_TAG();
    STAMP(4);
    BARRIER();
    STAMP(5);
    if (tid == 704) tag_post(P, tag);
    auto cleared = [&]() {
        if (w < 4) {
            WAIT_VM(0);
            if (lane == 0 && __hip_atomic_fetch_add((LAS unsigned*)(smem + FL + 32), 1u, __ATOMIC_RELAXED, __HIP_MEMORY_SCOPE_WORKGROUP) == 3u)
                __hip_atomic_store((GAS u64*)(P.ws + WS_BFL + gid * 128), tag, RLX_AGENT);
        }
    };
    auto x_in = [&]() {
        if (w < 8) {
#pragma unroll
            for (int k = 0; k < 3; ++k) {
                u32x2 lo = {__builtin_amdgcn_perm(xr[1][k], xr[0][k], 0x05040100u), __builtin_amdgcn_perm(xr[3][k], xr[2][k], 0x05040100u)};
                u32x2 hi = {__builtin_amdgcn_perm(xr[1][k], xr[0][k], 0x07060302u), __builtin_amdgcn_perm(xr[3][k], xr[2][k], 0x07060302u)};
                lds_w64(xb + (2 * k) * XT_BYTES, lo); lds_w64(xb + (2 * k + 1) * XT_BYTES, hi);
            }
        }
        if (w < 4) {
#pragma unroll
            for (int i = 0; i < 6; ++i)
                if (i >= 2 || gi < 10) __builtin_amdgcn_raw_buffer_store_b128(u32x4{EMPTY, EMPTY, EMPTY, EMPTY}, wsr, (unsigned)(tid * 16), WS_B + (ch0 + i) * 4096, 16);
        }
        BARRIER();
        if (w >= 8) wo_issue<0x03f>(P, cg2, w - 8, 4, lane);
    };
    auto TF = [&](int off) { u32x2 a = lds_r64(tf + off), b = lds_r64(tf + off + 8); return u32x4{a.x, a.y, b.x, b.y}; };
    f32x4 acc[8];
#pragma unroll
    for (int i = 0; i < 8; ++i) acc[i] = f32x4{0.f, 0.f, 0.f, 0.f};
    lptr axr;
    if (!half) {
        __builtin_amdgcn_s_setprio(2);
#pragma unroll
        for (int i = 0; i < 8; ++i) kacc[i] += lds_rf(xc + i * SP);
        unsigned dd[6];
        dd[0] = pk_bf16(kacc[7], kacc[6]); dd[1] = pk_bf16(kacc[5], kacc[4]); dd[2] = pk_bf16(kacc[3], kacc[2]);
        dd[3] = pk_bf16(kacc[1], kacc[0] + (lane == 0 ? bf2f(r_ds) : 0.f));
        dd[4] = __builtin_amdgcn_update_dpp(0, dd[0], 0x138, 0xf, 0xf, true);
        dd[5] = __builtin_amdgcn_update_dpp(0, dd[1], 0x138, 0xf, 0xf, true);
        unsigned mm[5];
#pragma unroll
        for (int i = 0; i < 5; ++i) mm[i] = __builtin_amdgcn_alignbit(dd[i + 1], dd[i], 16);
        lptr tw = tb + 2 * (536 - 8 * lane);
        lds_w128(tw, u32x4{dd[0], dd[1], dd[2], dd[3]}); lds_w128(tw + TC, u32x4{mm[0], mm[1], mm[2], mm[3]});
        lds_w128(tw + 2 * TC, u32x4{dd[1], dd[2], dd[3], dd[4]}); lds_w128(tw + 3 * TC, u32x4{mm[1], mm[2], mm[3], mm[4]});
        {
            const unsigned n0 = __builtin_amdgcn_readlane(dd[0], 63), n1 = __builtin_amdgcn_readlane(dd[1], 63);
            const unsigned kk = (n0 & 0xffffu) * 0x10001u, kh = n0 << 16, m4 = (n0 >> 16) | (n1 << 16);
            const int c = lane & 3, bl = lane >> 2;
            u32x4 e;
            e.x = 0u; e.y = lane == 14 ? kh : (lane == 15 ? kk : 0u); e.z = lane == 12 ? kh : (bl == 3 ? kk : 0u);
            e.w = lane == 14 ? n0 : (lane == 15 ? m4 : (bl == 3 ? kk : 0u));
            if (bl < 6) lds_w128(tb + c * TC + bl * 16 + (bl >> 2) * 1024, e);
        }
        x_in();
        u32x4 X[8];
#pragma unroll
        for (int i = 0; i < 8; ++i) X[i] = lds_r128(xf7 + 64 * 6 + 512 * i);
        u32x4 k0 = TF(1024 - 64), k8 = TF(512 - 64);
#pragma unroll
        for (int i = 0; i < 8; ++i) asm volatile("" : "+v"(acc[i]));
        __builtin_amdgcn_s_setprio(0);
        STAMP(6);
#pragma unroll
        for (int c = 1; c < 8; ++c) {
            u32x4 k0n, k8n;
            if (c < 7) { k0n = TF(1024 - 64 * (c + 1)); k8n = TF(512 - 64 * (c + 1)); }
#pragma unroll
            for (int i = 0; i < 8; ++i) {
                SCHED();
                mfma16i(acc[i], k0, X[i]);
                if (i < 7) mfma16i(acc[i + 1], k8, X[i]);
                SCHED();
                if (c < 7) X[i] = lds_r128(xf7 + 64 * (6 - c) + 512 * i);
            }
            k0 = k0n; k8 = k8n;
            if (c == 5) { SCHED(); cleared(); SCHED(); }
        }
        MFMA_DONE();
        SET_M0(ACX_OFF - 40960 + ch * 8192);
#pragma unroll
        for (int i = 0; i < 16; ++i) W_ADDTID(acc[4 + (i >> 2)][i & 3], 40960 + i * 256);
        axr = smem + ACX_OFF + ch * 8192 + 4096 + lane * 4;
    } else {
        u32x4 X[8];
#pragma unroll
        for (int i = 0; i < 4; ++i) if (i < 3 || lane < 48) lds_w128(pt + i * 1024 + lane * 16, u32x4{0u, 0u, 0u, 0u});
        CFENCE();
        const unsigned p89 = pk_bf16(kacc[8] + lds_rf(xc), kacc[9] + lds_rf(xc + SP)), pab = pk_bf16(kacc[10] + lds_rf(xc + 2 * SP), kacc[11] + lds_rf(xc + 3 * SP));
#pragma unroll
        for (int c = 0; c < 4; ++c) {
            lptr q2 = smem + (ct[6][c] + ch * 3840), q3 = smem + (ct[7][c] + ch * 3840);
            lds_w16(q2 + 64, p89); lds_w16(q2, p89 >> 16);
            lds_w16(q3 + 64, pab); lds_w16(q3, pab >> 16);
        }
        CFENCE();
        const u32x4 FX = ct[5] & (((unsigned)__builtin_amdgcn_readlane(p89, 63) >> 16) * 0x10001u);
        lptr pf2 = pt + cpy * 320 + ((79 - 64 - lo16 + 4 * g - cpy + 4) >> 2) * 16;
        u32x4 TA[3], TB[3], TCc[4];
#pragma unroll
        for (int i = 0; i < 3; ++i) { TA[i] = lds_r128(pf2 + 64 * (2 - i)); TB[i] = lds_r128(pf2 + 1280 + 64 * (2 - i)); }
#pragma unroll
        for (int i = 0; i < 4; ++i) TCc[i] = lds_r128(pf2 + PC_OFF + 64 * (3 - i));
        x_in();
#pragma unroll
        for (int i = 0; i < 8; ++i) X[i] = lds_r128(xf7 + 64 * 7 + 512 * i);
        STAMP(6);
        const f32x4 z4 = {0.f, 0.f, 0.f, 0.f};
#pragma unroll
        for (int st = 0; st < 6; ++st) {
            const u32x4 Xc = lds_r128(xf7 + 64 * 7 + 512 * st - 16);
            const u32x4 pa = pack8(mfma16(X[st], ct[0], z4), mfma16(X[st], ct[1], z4));
            const u32x4 pb = pack8(mfma16(X[st], ct[2], z4), mfma16(Xc, ct[1], z4));
#pragma unroll
            for (int dl = 2; dl <= 4; ++dl) if (st + dl < 8) {
                acc[st + dl] = mfma16(pa, TA[dl - 2], acc[st + dl]);
                acc[st + dl] = mfma16(pb, TB[dl - 2], acc[st + dl]);
            }
            if (st < 4) {
                const u32x4 pc = pack8(mfma16(X[st], ct[3], z4), mfma16(Xc, ct[4], z4));
#pragma unroll
                for (int dl = 4; dl <= 7; ++dl) if (st + dl < 8) acc[st + dl] = mfma16(pc, TCc[dl - 4], acc[st + dl]);
            }
        }
        SCHED(); cleared(); SCHED();
#pragma unroll
        for (int i = 4; i < 8; ++i) acc[i] = mfma16(FX, X[i - 4], acc[i]);
        const u32x4 k0 = TF(1024), k8 = TF(512), k16 = TF(0);
#pragma unroll
        for (int i = 0; i < 8; ++i) acc[i] = mfma16(k0, X[i], acc[i]);
#pragma unroll
        for (int i = 1; i < 8; ++i) acc[i] = mfma16(k8, X[i - 1], acc[i]);
#pragma unroll
        for (int i = 2; i < 8; ++i) acc[i] = mfma16(k16, X[i - 2], acc[i]);
        MFMA_DONE();
        SET_M0(ACX_OFF - 40960 + ch * 8192 + 4096);
#pragma unroll
        for (int i = 0; i < 16; ++i) W_ADDTID(acc[i >> 2][i & 3], 40960 + i * 256);
#pragma unroll
        for (int i = 0; i < 4; ++i) acc[i] = acc[4 + i];
        axr = smem + ACX_OFF + ch * 8192 + lane * 4;
    }
    STAMP(7);
    BARRIER();
    STAMP(8);
#pragma unroll
    for (int i = 0; i < 16; ++i) acc[i >> 2][i & 3] += lds_rf(axr + i * 256);
#pragma unroll
    for (int i = 0; i < 2; ++i) {
        const f32x4 ya = acc[2 * i], yb = acc[2 * i + 1];
        const auto sx = __builtin_amdgcn_permlane16_swap(pk_bf16(ya[0], ya[1]), pk_bf16(yb[0], yb[1]), false, false);
        const auto sy = __builtin_amdgcn_permlane16_swap(pk_bf16(ya[2], ya[3]), pk_bf16(yb[2], yb[3]), false, false);
        st_sc1(wsr, boff + i * 1024, not_empty(u32x4{sx[0], sy[0], sx[1], sy[1]}));
    }
    BARRIER();
    STAMP(9);
    if (w < 2) STAMP_OUT(2 + 2 * w, blockIdx.x + 256 * w);
}

constexpr int SLOT = 34816, XCH = 16384, KVT_OFF = 151296, KVP_OFF = 27648;
DEVI constexpr int slot_off(int s) { return (s & 3) == 0 ? SLOT : ((s & 3) == 1 ? 0 : (s & 3) * SLOT); }
DEVI constexpr int wq_off(int t) { return t == 0 ? 0 : 49152 + t * 27648; }

constexpr int quad_src(int quad) {
    const int pp = quad >= 192 ? 1 : 0, qi = quad - 192 * pp;
    const bool isz = qi >= 180;
    const int el = isz ? 0 : qi / 5, qd = qi - el * 5, hi = el >= 18 ? 1 : 0;
    const int dql = isz ? (qi < 189 ? qi - 180 : 0) : qd + 4 * hi;
    const int et = isz ? 0 : (el < 4 ? 0 : (el < 20 ? 1 : 2)), col = isz ? 0 : (el < 4 ? 12 + el : (el < 20 ? el - 4 : el - 20));
    const int tile = dql == 8 ? 5 : (et == 0 ? (dql >> 2) : (et == 1 ? 2 + (dql >> 2) : 4));
    return KVP_OFF + pp * 6144 + tile * 1024 + ((dql == 8 ? et : (dql & 3)) * 16 + col) * 4;
}
constexpr int quad_dst(int quad) {
    const int pp = quad >= 192 ? 1 : 0, qi = quad - 192 * pp;
    const bool isz = qi >= 180;
    const int el = isz ? 0 : qi / 5, qd = qi - el * 5, hi = el >= 18 ? 1 : 0;
    const int dql = isz ? (qi < 189 ? qi - 180 : 0) : qd + 4 * hi;
    const int dq = 9 * pp + dql, ks = dq >> 3, e = 36 * pp + el, ee = e >> 4;
    const int fr = ee == 0 ? 0 : (ee < 3 ? 2 * ee - 1 + ks : 2 * ee - 2 + ks);
    const int a = KVT_OFF + (isz ? (9 + ks) * 1024 + (dq & 3) * 256 + (2 * pp + (dql > 4 ? 1 : 0)) * 16 : fr * 1024 + ((dq & 3) * 16 + (e & 15)) * 16) + ((dq >> 2) & 1) * 8;
    return qi >= 189 ? LDS_DUMMY : (a | (isz ? (dql == 4 ? 0x300003 : 0x100003) : ((!hi && qd == 4) ? 1 : ((hi && qd == 0) ? 2 : 3))));
}
struct Tab { int v[2048]; };
constexpr Tab make_tab() {
    Tab t{};
    for (int c = 0; c < 192; ++c) {
        const int l = c & 63, q = 2 * ((c & 192) + (l >> 5) * 16 + (l & 15)) + ((l >> 4) & 1);
        t.v[4 * c] = quad_src(2 * c); t.v[4 * c + 1] = quad_src(2 * c + 1); t.v[4 * c + 2] = quad_dst(q); t.v[4 * c + 3] = quad_dst(q + 64);
    }
    for (int q = 0; q < 576; ++q) {
        const int rho = q / 9, pc = q % 9, k = (rho & 32) + 8 * ((rho >> 3) & 3) + 4 * (rho & 1) + ((rho >> 1) & 3), m = q >> 6;
        t.v[768 + q] = k * 3456 + pc * 16 - (m & 1) * 1024;
        t.v[1344 + q] = k * 1152 + pc * 16 - (m & 3) * 1024;
    }
    return t;
}
__device__ const Tab g_tab = make_tab();
constexpr int IDX = P2_B, GW_OFF = 139264;

DEVI void gemm_compute(const Params& P, lptr smem, int rb, int hg, int w, u64& tag, rsrc_t wsr, u64& tp) {
    const int lane = threadIdx.x & 63, lo16 = lane & 15, g = lane >> 4;
    const int rg = w >> 1, p = w & 1;
    STAMP_DECL; STAMP(1);
    if (w == 0 && lane < 16) lds_w32(smem + FL + lane * 4, 0u);
    if (w >= 4) {
#pragma unroll
        for (int i = 0; i < 3; ++i) lds_w128(smem + KVT_OFF + i * 4096 + (w - 4) * 1024 + lane * 16, u32x4{0u, 0u, 0u, 0u});
    }
    __builtin_amdgcn_s_setprio(2);
    const int row0 = 16 * (2 * rg + p) + lo16, row1 = 16 * (2 * rg + 1 - p) + lo16;
    const int sw = (g ^ ((lo16 >> 1) & 7)) << 4;
    lptr xa0[2] = {smem + row0 * 128 + sw, smem + row0 * 128 + (sw ^ 64)}, xa1[2] = {smem + row1 * 128 + sw, smem + row1 * 128 + (sw ^ 64)};
    const int wrow = (4 * g + (lo16 >> 2)) * 288;
    lptr wa = smem + XCH + wrow + (9 * p + (lane & 3)) * 8, wa2 = smem + XCH + wrow + (9 * p + ((lane & 3) == 3 ? 18 : 8)) * 8;
    lptr flp = smem + FL;
    asm volatile("" : "+v"(flp));
    const f32x4 z4 = {0.f, 0.f, 0.f, 0.f};
    f32x4 acc[2][5];
#pragma unroll
    for (int i = 0; i < 2; ++i)
#pragma unroll
        for (int j = 0; j < 5; ++j) { const float e = j < 2 ? 1e-6f : (j == 2 && lo16 < 12 ? 1e-6f : 0.f); acc[i][j] = f32x4{e, e, e, e}; }
    u32x4 xk[18], x1[2];
    auto rel = [&](int s) { return (s & 2) ? slot_off(s) - 2 * SLOT : slot_off(s); };
    auto rebase = [&](int dlt) {
        xa0[0] += dlt; xa0[1] += dlt; xa1[0] += dlt; xa1[1] += dlt; wa += dlt; wa2 += dlt;
        asm volatile("" : "+v"(xa0[0]), "+v"(xa0[1]), "+v"(xa1[0]), "+v"(xa1[1]), "+v"(wa), "+v"(wa2));
    };
    constexpr int NB = 8;
    u32x2 wl[NB], wh[NB];
    auto rdw = [&](int f, int t) {
        const int n = f / 5, j = f % 5;
        const int o = rel(n >> 1) + (n & 1) * 9216 + t * 4608;
        const u32x2 v = j == 2 ? lds_tr(wa2 + o) : lds_tr(wa + o + (j == 0 ? 0 : (j == 1 ? 32 : (j == 3 ? 152 : 184))));
        if (t) wh[f % NB] = v; else wl[f % NB] = v;
    };
    auto peek = [&](int f) { return __hip_atomic_load((LAS unsigned*)(flp + f), __ATOMIC_RELAXED, __HIP_MEMORY_SCOPE_WORKGROUP); };
    asm volatile(".subsection 2\n.Lkf_code_end:\n\t.fill 256, 4, 0xbf800000\n\t.subsection 0");
    unsigned one = 1u;
    asm volatile("" : "+v"(one));
    rebase(0);
#pragma unroll
    for (int j = 0; j < 5; ++j) asm volatile("" : "+v"(acc[0][j]), "+v"(acc[1][j]));
    BARRIER();
    { asm volatile("s_memrealtime %0\n\ts_waitcnt lgkmcnt(0)" : "=s"(tp) :: "memory"); }
    xk[0] = lds_r128(xa0[0] + rel(0)); x1[0] = lds_r128(xa1[0] + rel(0));
#pragma unroll
    for (int f = 0; f < NB - 1; ++f) { rdw(f, 0); rdw(f, 1); }
#pragma unroll
    for (int n = 0; n < 18; ++n) {
        const int nx = n + 1, s = n >> 1;
#pragma unroll
        for (int m = 0; m < 10; ++m) {
            const int j = m >> 1, f = 5 * n + j, fn = f + NB - 1;
            SCHED();
            if (!(m & 1) && !(f & 1) && fn < 88) WAIT_LGKM(10);
            const u32x4 wv = {wl[f % NB].x, wl[f % NB].y, wh[f % NB].x, wh[f % NB].y};
            if (m & 1) mfma16i(acc[1][j], x1[n & 1], wv); else mfma16i(acc[0][j], xk[n], wv);
            SCHED();
            if (!(m & 1) && fn % 10 == 0) {
                if (s < 8 && (s & 1)) rebase((s & 2) ? -2 * SLOT : 2 * SLOT);
                if (s == 8) WAIT_LGKM0();
                RAW_BARRIER();
                if (s == 0) STAMP(3);
                if (s == 4) STAMP(4);
            }
            if (fn < 90) rdw(fn, m & 1);
            if (nx < 18) {
                if (m == 0) xk[nx] = lds_r128(xa0[nx & 1] + rel(nx >> 1));
                if (m == 2) x1[nx & 1] = lds_r128(xa1[nx & 1] + rel(nx >> 1));
            }
        }
    }
    MFMA_DONE();
    STAMP(5);
    const int qrow = (2 * (4 * g + (lo16 >> 2))) * 144;
    lptr qw = smem + qrow + (lane & 3) * 8, qw4 = smem + qrow + (16 + (lane & 1)) * 8;
    lptr qwh = qw + wq_off(1), qw4h = qw4 + wq_off(1);
    asm volatile("" : "+v"(qwh), "+v"(qw4h));
    auto rdq = [&](int ks, int j) {
        const int o = (ks < 6 ? ks : ks - 6) * 4608;
        return j < 4 ? lds_trfrag((ks < 6 ? qw : qwh) + o + 32 * j, 144) : lds_trfrag((ks < 6 ? qw4 : qw4h) + o, 144);
    };
    {
        u32x4 pk[3], vb[3];
#pragma unroll
        for (int j = 0; j < 3; ++j) pk[j] = pack8(phi4(acc[0][j]), phi4(acc[1][j]));
        vb[0] = pack8(acc[0][2], acc[1][2]);
        if (lo16 < 12) vb[0] = u32x4{0x3f803f80u, 0x3f803f80u, 0x3f803f80u, 0x3f803f80u};
        vb[1] = pack8(acc[0][3], acc[1][3]); vb[2] = pack8(acc[0][4], acc[1][4]);
        f32x4 r[6];
        const f32x4 t0 = mfma16(pk[2], vb[0], z4), t1 = mfma16(pk[2], vb[1], z4), t2 = mfma16(pk[2], vb[2], z4);
        r[0] = mfma16(pk[0], vb[0], z4); r[1] = mfma16(pk[1], vb[0], z4); r[2] = mfma16(pk[0], vb[1], z4);
        r[3] = mfma16(pk[1], vb[1], z4); r[4] = mfma16(pk[1], vb[2], z4);
        r[5] = g == 0 ? t0 : (g == 1 ? t1 : t2);
        SCHED();
        asm volatile("s_nop 7" ::: "memory");
        SET_M0(KVP_OFF - 8192 + w * 6144);
#pragma unroll
        for (int t = 5; t >= 0; --t) {
            W_ADDTID(r[t][0], 8192 + t * 1024); W_ADDTID(r[t][1], 8192 + t * 1024 + 256);
            W_ADDTID(r[t][2], 8192 + t * 1024 + 512); W_ADDTID(r[t][3], 8192 + t * 1024 + 768);
        }
        FLAG_ADD(F_KVP);
        __builtin_amdgcn_s_setprio(0);
    }
    STAMP(6);
    f32x4 qa[5];
#pragma unroll
    for (int j = 0; j < 5; ++j) qa[j] = f32x4{1e-6f, 1e-6f, 1e-6f, 1e-6f};
#pragma unroll
    for (int j = 0; j < 5; ++j) asm volatile("" : "+v"(qa[j]));
    SCHED();
    u32x4 wq[2][5];
#pragma unroll
    for (int i = 0; i < 2; ++i)
#pragma unroll
        for (int j = 0; j < 5; ++j) wq[i][j] = rdq(i, j);
    SCHED();
    if (w >= 4) flag_wait<1>(smem, F_KVR, 3);
    unsigned seenq = 0;
#pragma unroll
    for (int ks = 0; ks < 18; ++ks) {
        if (ks == 2) seenq = peek(F_KVR);
        if (ks == 4) { SCHED(); FLAG_ADD(F_Q0); NEED(seenq, F_KVR, 3); seenq = peek(F_LANDED); NEED(seenq, F_LANDED, 4); }
        if (ks == 16) { SCHED(); FLAG_ADD(F_QD); }
#pragma unroll
        for (int j = 0; j < 5; ++j) {
            SCHED();
            if (j == 0 && ks < 17) WAIT_LGKM(10);
            mfma16i(qa[j], wq[ks & 1][j], xk[ks]);
            SCHED();
            if (ks + 2 < 18) wq[ks & 1][j] = rdq(ks + 2, j);
        }
    }
    __builtin_amdgcn_s_setprio(2);
    MFMA_DONE();
    u32x4 b0 = pack8(phi4(qa[0]), phi4(qa[1])), b1 = pack8(phi4(qa[2]), phi4(qa[3])), b2 = pack8(phi4(qa[4]), z4);
    lptr kfp = smem + KVT_OFF + lane * 16;
    unsigned ob0 = WS_O + (unsigned)(((rb * 8 + 2 * rg + p) * 72 + hg * 9 + 2 * (g & 1) + (g >> 1)) * 256 + lo16 * 16);
    asm volatile("" : "+v"(b0), "+v"(b1), "+v"(b2), "+v"(kfp), "+v"(ob0));
    { const u32x2 t = lds_r64(smem + LDS_TAG); tag = (u64)(unsigned)__builtin_amdgcn_readfirstlane(t.x) | ((u64)(unsigned)__builtin_amdgcn_readfirstlane(t.y) << 32); }
    STAMP(7);
    flag_wait<1>(smem, F_D, 4);
    if (w == 7 && lane == 0) __hip_atomic_store((GAS u64*)(P.ws + WS_HINT + (rb * 8 + hg) * 128), tag, RLX_AGENT);
    STAMP(8);
    {
        u32x4 kf[12];
#pragma unroll
        for (int i = 0; i < 12; ++i) kf[i] = lds_r128(kfp + (i < 3 ? 9 + i : i - 3) * 1024);
        SCHED();
        const f32x4 den = mfma16(kf[2], b2, mfma16(kf[1], b1, mfma16(kf[0], b0, f32x4{1e-6f, 1e-6f, 1e-6f, 1e-6f})));
        f32x4 o[5];
        o[0] = mfma16(kf[3], b0, z4);
        o[1] = mfma16(kf[5], b1, mfma16(kf[4], b0, z4));
        o[2] = mfma16(kf[7], b1, mfma16(kf[6], b0, z4));
        o[3] = mfma16(kf[9], b2, mfma16(kf[8], b1, z4));
        o[4] = mfma16(kf[11], b2, mfma16(kf[10], b1, z4));
        f32x4 r;
#pragma unroll
        for (int i = 0; i < 4; ++i) r[i] = __builtin_amdgcn_rcpf(den[i]);
        const float r1a = g == 0 ? r[0] : r[1], r2 = g == 0 ? r[1] : r[2], r3a = g <= 1 ? r[2] : r[3], r3b = g == 0 ? r[2] : r[3];
        u32x2 ob[5];
        ob[0] = u32x2{pk_bf16(o[0][0] * r[0], o[0][1] * r[0]), pk_bf16(o[0][2] * r[0], o[0][3] * r[0])};
        ob[1] = u32x2{pk_bf16(o[1][0] * r1a, o[1][1] * r1a), pk_bf16(o[1][2] * r[1], o[1][3] * r[1])};
        ob[2] = u32x2{pk_bf16(o[2][0] * r2, o[2][1] * r2), pk_bf16(o[2][2] * r2, o[2][3] * r2)};
        ob[3] = u32x2{pk_bf16(o[3][0] * r3a, o[3][1] * r3a), pk_bf16(o[3][2] * r3b, o[3][3] * r3b)};
        ob[4] = u32x2{pk_bf16(o[4][0] * r[3], o[4][1] * r[3]), pk_bf16(o[4][2] * r[3], o[4][3] * r[3])};
#pragma unroll
        for (int t = 0; t < 3; ++t) {
            const u32x2 a = ob[2 * t], b = ob[t < 2 ? 2 * t + 1 : 4];
            const auto sx = __builtin_amdgcn_permlane16_swap(a.x, b.x, false, false), sy = __builtin_amdgcn_permlane16_swap(a.y, b.y, false, false);
            if (t < 2 || g == 0) st_sc1(wsr, ob0 + 1024 * t, not_empty(u32x4{sx[0], sy[0], sx[1], sy[1]}));
        }
    }
    __builtin_amdgcn_s_setprio(0);
    STAMP(9);
    if (w == 0) STAMP_OUT(1, blockIdx.x);
    if (w == 7) STAMP_OUT(7, blockIdx.x + 768);
}

DEVI void gemm_loader(const Params& P, lptr smem, int rb, int hg, int lw, int cg2, u64& tag, rsrc_t wsr, u64& tp) {
    const int lane = threadIdx.x & 63;
    STAMP_DECL; STAMP(1);
    __builtin_amdgcn_s_setprio(3);
    unsigned xo[4], wo_[5], qo[3];
    const int rx = hg & 3, rw = rb & 3;
    unsigned mx[4], mw[4];
    const unsigned xbase = (unsigned)((rb * 128 + 8 * lw + (lane >> 3)) * 1152 + (((lane & 7) ^ ((lane >> 4) + 4 * (lw & 1))) << 4));
#pragma unroll
    for (int i = 0; i < 4; ++i) { mx[i] = (((i + rx) & 3) * 4 + lw) * 1024; mw[i] = XCH + (((i + rw) & 3) * 4 + lw) * 1024; xo[i] = xbase + ((i + rx) & 3) * 36864; }
    const unsigned lb = lw * 4096;
    auto issue_x = [&](int s, int i0 = 0, int i1 = 4) {
#pragma unroll
        for (int i = i0; i < i1; ++i) glds16(P.x + s * 128, xo[i], slot_off(s) + mx[i]);
    };
    auto issue_w = [&](int s, int i0 = 0, int i1 = 4) {
        const char* ws = P.wqkv + s * (64 * 3456);
        if (i1 == 4 && (lw >> 1) == (s & 1)) glds16(ws, wo_[4], (unsigned)(slot_off(s) + XCH + 16384 + (lw & 1) * 1024));
#pragma unroll
        for (int i = i0; i < i1; ++i) glds16(ws, wo_[i], slot_off(s) + mw[i]);
    };
    auto issue_q = [&](int t, int i) {
        const char* ws = P.wqkv + hg * 144 + (3 * t + i) * (64 * 3456);
        if (lw == ((i + t) & 3)) glds16(ws, qo[2], (unsigned)(wq_off(t) + i * 9216 + 8192));
        SET_M0(wq_off(t) + i * 9216 + (lb >> 1));
        glds16o<0>(ws, qo[0]); glds16o<1024>(ws, qo[1]);
    };
    auto w_off = [&](int i) {
        const unsigned q = 64 * (i < 4 ? ((i + rw) & 3) * 4 + lw : 16 + (lw & 1)) + lane, rho = (q * 3641u) >> 16, pc = q - rho * 18;
        const unsigned k = (rho & 35u) + 8 * ((rho >> 2) & 3) + 4 * ((rho >> 4) & 1);
        wo_[i] = k * 3456 + (1152 + hg * 144) + pc * 16 + (pc < 9 ? 0u : 1008u);
    };
    issue_x(0, 0, 1); issue_x(1, 0, 1);
    SCHED();
    w_off(0);
    issue_w(0, 0, 1); issue_w(1, 0, 1);
    SCHED();
#pragma unroll
    for (int i = 1; i < 5; ++i) w_off(i);
    if (lw == 1) { glds16(P.gw, (unsigned)(lane * 16), GW_OFF); if (lane < 8) glds16(P.gw + 1024, (unsigned)(lane * 16), GW_OFF + 1024); }
    issue_x(0, 1, 4); issue_w(0, 1, 4); issue_w(1, 1, 4); issue_x(1, 1, 4);
    const int lo16 = lane & 15, g = lane >> 4, ct = (lw < 3 ? lw : 0) * 64 + lane;
    lptr flp = smem + FL, ixp = smem + IDX + ct * 16, irp = smem + IDX + 3072 + lane * 4, iqp = irp + lw * 512;
    unsigned one = 1u;
    u32x4 sel[2];
    sel[0] = mask_frag(lo16 < 8 ? 1u << lo16 : 0u, 0, 0x3f80u); sel[1] = mask_frag(lo16 >= 8 ? 1u << (lo16 - 8) : 0u, 0, 0x3f80u);
    unsigned sto = WS_PART + (unsigned)((hg * 16 + rb) * 3072 + ct * 16);
    unsigned off = WS_PART + (unsigned)((hg * 16 + g) * 3072 + lw * 1024 + lo16 * 16);
    asm volatile("" : "+v"(flp), "+v"(one), "+v"(ixp), "+v"(irp), "+v"(iqp));
    asm volatile("" : "+v"(sel[0]), "+v"(sel[1]), "+v"(sto), "+v"(off));
    SET_M0(IDX + (lb >> 1));
    glds16o<0>((const char*)&g_tab + (lb >> 1), (unsigned)(lane * 16)); glds16o<1024>((const char*)&g_tab + (lb >> 1), (unsigned)(lane * 16));
    lptr gxp = smem + (16 * hg + lo16) * 128 + ((g ^ ((lo16 >> 1) & 7)) << 4), gwp = smem + GW_OFF + g * 16;
    f32x4 gacc = {0.f, 0.f, 0.f, 0.f};
    asm volatile("" : "+v"(gxp), "+v"(gwp));
    auto gate_step = [&](int s) {
        if (lw == 1) {
            lptr q = gxp + slot_off(s);
            gacc = mfma16(lds_r128(gwp + s * 128), lds_r128(q), gacc);
            gacc = mfma16(lds_r128(gwp + s * 128 + 64), lds_r128((lptr)((size_t)q ^ 64u)), gacc);
        }
    };
    WAIT_VM(8); BARRIER();
    issue_w(2); issue_x(2);
    u32x4 ix; unsigned oo[9]; u64 pf = 0;
#pragma unroll
    for (int s = 0; s < 9; ++s) {
        gate_step(s);
        if (s < 7) WAIT_VM(8); else WAIT_VM(6);
        BARRIER();
        if (s == 5 && lw == 3) asm volatile("global_load_dwordx2 %0, %1, %2 sc1" : "=v"(pf) : "v"((lane < 5 ? lane : 0) * 128), "s"(P.ws + WS_PFL) : "memory");
        if (s < 6) { issue_w(s + 3); issue_x(s + 3); } else { issue_q(s - 6, 0); issue_q(s - 6, 1); issue_q(s - 6, 2); }
        if (s == 0) tag = *(const __attribute__((address_space(4))) u64*)(P.ws + WS_CTL);
        if (s == 1) PIN_TAG();
        if (s == 1 && lw == 0 && lane == 0) lds_w64(smem + LDS_TAG, u32x2{(unsigned)tag, (unsigned)(tag >> 32)});
        if (s == 6 && lw == 3) {
            asm volatile("" : "+v"(pf));
            if (!__all(pf == tag)) poll_flags(P, WS_PFL, 5, tag, lane);
        }
        if (s == 3) {
            qo[0] = *(LAS unsigned*)iqp; qo[1] = *(LAS unsigned*)(iqp + 256); qo[2] = *(LAS unsigned*)(irp + 2048);
            ix = lds_r128(ixp);
#pragma unroll
            for (int i = 0; i < 9; ++i) oo[i] = *(LAS unsigned*)(irp + 2304 + i * 256);
            CFENCE();
        }
    }
    {
        const unsigned co = WS_O + (unsigned)(((rb * 8 + 2 * lw) * 72 + hg * 9) * 256 + lane * 16);
#pragma unroll
        for (int i = 0; i < 5; ++i)
            if (i < 4 || lane < 32) st_sc1(wsr, i < 4 ? co + (i >> 1) * 18432 + (i & 1) * 1024 : co + 2048 + (lane >> 4) * (18432 - 256), u32x4{EMPTY, EMPTY, EMPTY, EMPTY});
    }
    WAIT_VM(5); FLAG_ADD(F_LANDED);
    if (lw == 0 && lane == 0) tag_post(P, tag);
    if (lw == 1) {
        const float gate = rbf(__builtin_amdgcn_rcpf(1.f + __expf(-rbf(gacc[0]))));
        if (lane < 16) __hip_atomic_store((GAS float*)(P.ws + WS_GATE) + (rb * 128 + hg * 16 + lane), gate, RLX_AGENT);
    }
    if (lw < 3) {
        const int dst[2] = {(int)ix.z, (int)ix.w};
        flag_wait<0>(smem, F_KVP, 8);
        STAMP(2);
        float pa[2][4][4];
#pragma unroll
        for (int i = 0; i < 2; ++i)
#pragma unroll
            for (int r = 0; r < 4; ++r)
#pragma unroll
                for (int k = 0; k < 4; ++k) pa[i][r][k] = lds_rf(smem + ix[i] + r * 12288 + k * 256);
        SCHED();
        u32x4 pv;
#pragma unroll
        for (int i = 0; i < 2; ++i)
#pragma unroll
            for (int k = 0; k < 2; ++k)
                pv[2 * i + k] = pk_bf16((pa[i][0][2 * k] + pa[i][1][2 * k]) + (pa[i][2][2 * k] + pa[i][3][2 * k]),
                                        (pa[i][0][2 * k + 1] + pa[i][1][2 * k + 1]) + (pa[i][2][2 * k + 1] + pa[i][3][2 * k + 1]));
        st_sc1(wsr, sto, not_empty(pv));
        SCHED();
        FLAG_ADD(F_KVR);
        STAMP(3);
        WAIT_VM(0);
        STAMP(4);
        f32x4 sum[2];
#pragma unroll 1
        for (unsigned spins = 0; spins < SPIN_CAP; ++spins) {
            u32x4 a[4][4];
#pragma unroll
            for (int i = 0; i < 4; ++i)
#pragma unroll
                for (int t = 0; t < 4; ++t) a[t][i] = __builtin_amdgcn_raw_buffer_load_b128(wsr, off + t * 256, ((i + rb) & 3) * 12288, 16);
            SCHED();
            FLAG_ADD(F_SW);
            sum[0] = sum[1] = f32x4{0.f, 0.f, 0.f, 0.f};
#pragma unroll
            for (int i = 0; i < 4; ++i)
#pragma unroll
                for (int t = 0; t < 4; ++t) sum[t >> 1] = mfma16(sel[t & 1], a[t][i], sum[t >> 1]);
            if (__all(sum[0][0] == sum[0][0] && sum[1][0] == sum[1][0])) break;
            __builtin_amdgcn_s_sleep(RSLEEP);
            if (spins < 8) { CFENCE(); continue; }
            unsigned m = 0;
#pragma unroll
            for (int t = 0; t < 4; ++t)
#pragma unroll
                for (int i = 0; i < 4; ++i) m = max(m, umax4(a[t][i]));
            if (__all(m != EMPTY)) break;
            CFENCE();
        }
#pragma unroll
        for (int i = 0; i < 2; ++i) {
            const unsigned lo = (dst[i] & 1) ? pk_bf16(sum[i][0], sum[i][1]) : 0u, hi = (dst[i] & 2) ? pk_bf16(sum[i][2], sum[i][3]) : 0u;
            lptr dp = smem + (dst[i] & 0xffffc);
            if (dst[i] & 0x100000) {
                const int hs = (dst[i] & 0x200000) ? 20 : 4;
#pragma unroll
                for (int r = 0; r < 4; ++r) { lds_w32(dp + 64 * r, lo); lds_w32(dp + 64 * r + hs, hi); }
            } else lds_w64(dp, u32x2{lo, hi});
        }
        CFENCE();
        FLAG_ADD(F_D);
        STAMP(5);
    } else {
        auto wo_chunks = [&](int c0, int c1) {
#pragma unroll 1
            for (int c = c0; c < c1; ++c) {
                const char* ws = P.wo + cg2 * 144 + c * (64 * 1152);
                const unsigned l = c < 8 ? c * 9216 : WO8;
                SET_M0(l); glds16o<0>(ws, oo[0]); glds16o<1024>(ws, oo[1]); glds16o<2048>(ws, oo[2]); glds16o<3072>(ws, oo[3]);
                SET_M0(l + 4096); glds16o<0>(ws, oo[4]); glds16o<1024>(ws, oo[5]); glds16o<2048>(ws, oo[6]); glds16o<3072>(ws, oo[7]);
                SET_M0(l + 8192); glds16o<0>(ws, oo[8]);
            }
        };
        wo_chunks(8, 9);
        WAIT_VM(0); FLAG_ADD(F_D);
        flag_wait(smem, F_KVR, 3);
        wo_chunks(3, 8);
        WAIT_VM(0);
        FLAG_ADD(F_W1);
        flag_wait(smem, F_SW, 3);
        flag_wait(smem, F_Q0, 8);
        wo_chunks(0, 3);
        WAIT_VM(0);
        FLAG_ADD(F_W0);
        poll_b(P, cg2, tag, lane);
        FLAG_ADD(F_B);
    }
    __builtin_amdgcn_s_setprio(0);
    STAMP(6);
    if (lw == 0) STAMP_OUT(5, blockIdx.x + 256);
}

DEVI void phase2(const Params& P, lptr smem, int rt64, int cg, bool gemm, u64 tag, rsrc_t wsr, u64& tp) {
    const int tid = threadIdx.x, lane = tid & 63, w = __builtin_amdgcn_readfirstlane(tid >> 6);
    const int lo16 = lane & 15, g = lane >> 4;
    const int rt = w & 3, kt = w >> 2;
    STAMP_DECL; STAMP(1);
    u32x4 bv[3]; unsigned gv;
    const unsigned boff = WS_B + (unsigned)((cg * 72 + (tid >> 3)) * 4096 + (rt64 >> 3) * 1024 + (((tid >> 2) & 1) * 32 + ((rt64 >> 2) & 1) * 16 + (rt64 & 3) * 4 + (tid & 3)) * 16);
    auto b_load = [&]() { bv[0] = ld_sc1(wsr, boff); bv[1] = ld_sc1(wsr, boff + 32 * 4096); bv[2] = ld_sc1(wsr, boff + (w == 0 ? 64 * 4096 : 0)); };
    auto b_ok = [&]() { return (bool)__all(max(max(umax4(bv[0]), umax4(bv[1])), umax4(bv[2])) != EMPTY); };
    auto b_put = [&]() {
        lptr bp = smem + P2_B + ((tid & 3) * 16 + ((tid >> 2) & 1) * 8) * 144 + (tid >> 3) * 2;
#pragma unroll
        for (int k = 0; k < 3; ++k) if (k < 2 || w == 0) {
#pragma unroll
            for (int q = 0; q < 4; ++q) { lds_w16(bp + 64 * k + (2 * q) * 144, bv[k][q]); lds_w16(bp + 64 * k + (2 * q + 1) * 144, bv[k][q] >> 16); }
        }
    };
    bool b_todo = false;
    if (w < 4) {
        if (gemm) flag_wait(smem, F_B, 1);
        b_load();
    }
    if (gemm) flag_wait(smem, w < 4 ? F_W0 : F_W1, 1);
    STAMP(2);
    const int qrow = (2 * (4 * g + (lo16 >> 2))) * 144;
    lptr qw = smem + qrow + (lane & 3) * 8, qw4 = smem + qrow + (16 + (lane & 1)) * 8;
    int wofs[3], oofs[3];
#pragma unroll
    for (int u = 0; u < 3; ++u) {
        const int c = kt * 3 + (u + cg) % 3;
        wofs[u] = c < 8 ? c * 9216 : WO8; oofs[u] = c * 2048;
    }
    auto rdo = [&](int i, int j) { return j < 4 ? lds_trfrag(qw + wofs[i >> 1] + (i & 1) * 4608 + 32 * j, 144) : lds_trfrag(qw4 + wofs[i >> 1] + (i & 1) * 4608, 144); };
    const unsigned ooff = WS_O + (unsigned)((rt64 * 4 + rt) * 18432 + lane * 16);
    auto ldo = [&](int i) { return __builtin_amdgcn_raw_buffer_load_b128(wsr, ooff + (i & 1) * 1024, oofs[i >> 1], 16); };
    u32x4 wf[6][5];
#pragma unroll
    for (int i = 0; i < 6; ++i) {
        if (i == 5 && w < 4) {
            if (b_ok()) b_put(); else b_todo = true;
        }
#pragma unroll
        for (int j = 0; j < 5; ++j) wf[i][j] = rdo(i, j);
    }
    STAMP(3);
    if (w == 4) { poll_flags<OSLEEP>(P, WS_HINT + (rt64 >> 1) * 1024, 8, tag, lane); flag_set(F_H, 1); } else flag_wait<1>(smem, F_H, 1);
    STAMP(4);
    if (b_todo) b_load();
    gv = __hip_atomic_load((GAS unsigned*)(P.ws + WS_GATE) + (rt64 * 64 + (tid < 576 ? tid / 9 : 0)), RLX_AGENT);
    f32x4 acc[5];
    {
        u32x4 of[6];
#pragma unroll
        for (int i = 0; i < 6; ++i) of[i] = ldo(i);
#pragma unroll
        for (int j = 0; j < 5; ++j) acc[j] = f32x4{0.f, 0.f, 0.f, 0.f};
#pragma unroll
        for (int i = 0; i < 6; ++i)
#pragma unroll
            for (int j = 0; j < 5; ++j) acc[j] = mfma16(wf[i][j], of[i], acc[j]);
    }
    if (!__all(acc[0][0] == acc[0][0])) {
#pragma unroll
        for (int j = 0; j < 5; ++j) acc[j] = f32x4{0.f, 0.f, 0.f, 0.f};
        u32x4 of[6];
#pragma unroll 1
        for (unsigned spins = 0; spins < SPIN_CAP; ++spins) {
            CFENCE();
            unsigned m = 0;
#pragma unroll
            for (int i = 0; i < 6; ++i) of[i] = ldo(i);
#pragma unroll
            for (int i = 0; i < 6; ++i) m = max(m, umax4(of[i]));
            if (__all(m != EMPTY)) break;
        }
#pragma unroll
        for (int i = 0; i < 6; ++i)
#pragma unroll
            for (int j = 0; j < 5; ++j) acc[j] = mfma16(rdo(i, j), of[i], acc[j]);
    }
    if (gemm) flag_wait(smem, F_QD, 8);
    lptr ap = smem + P2_ACC + kt * (64 * P2_AP) + (rt * 16 + lo16) * P2_AP + g * 16;
#pragma unroll
    for (int j = 0; j < 5; ++j) if (j < 4 || g < 2) lds_w128(ap + j * 64, __builtin_bit_cast(u32x4, acc[j]));
    if (b_todo) {
#pragma unroll 1
        for (unsigned spins = 0; spins < SPIN_CAP; ++spins) {
            if (b_ok()) break;
            __builtin_amdgcn_s_sleep(2);
            CFENCE();
            b_load();
        }
        b_put();
    }
    STAMP(5);
    BARRIER();
    STAMP(6);
    if (w < 9) {
        const int r0 = tid / 9, c0 = tid - r0 * 9;
        lptr a = smem + P2_ACC + r0 * P2_AP + c0 * 32;
        const f32x4 s0 = __builtin_bit_cast(f32x4, lds_r128(a)) + __builtin_bit_cast(f32x4, lds_r128(a + 64 * P2_AP)) + __builtin_bit_cast(f32x4, lds_r128(a + 128 * P2_AP));
        const f32x4 s1 = __builtin_bit_cast(f32x4, lds_r128(a + 16)) + __builtin_bit_cast(f32x4, lds_r128(a + 64 * P2_AP + 16)) + __builtin_bit_cast(f32x4, lds_r128(a + 128 * P2_AP + 16));
        const u32x4 bb = lds_r128(smem + P2_B + tid * 16);
        const float gt = __uint_as_float(gv), og = rbf(1.f - gt);
        u32x4 o;
#pragma unroll
        for (int i = 0; i < 4; ++i) {
            const float a0 = i < 2 ? s0[2 * i] : s1[2 * i - 4], a1 = i < 2 ? s0[2 * i + 1] : s1[2 * i - 3];
            o[i] = pk_bf16(gt * a0 + og * bf2f(bb[i]), gt * a1 + og * bfhi(bb[i]));
        }
        __builtin_amdgcn_raw_buffer_store_b128(o, __builtin_amdgcn_make_buffer_rsrc(P.out, 0, SEQ * DM * 2, 0x00020000), (unsigned)((rt64 * 64 + r0) * 1152 + cg * 144 + c0 * 16), 0, OUTAUX);
    }
    STAMP(7);
    if (w == 4) STAMP_OUT(blockIdx.x < 128 ? 3 : 6, blockIdx.x + 512);
}

DEVI void helper_role(const Params& P, lptr smem, int xcd, int li, u64& tag, rsrc_t wsr, u64& tp) {
    const int tid = threadIdx.x, lane = tid & 63, w = __builtin_amdgcn_readfirstlane(tid >> 6);
    STAMP_DECL; STAMP(1);
    if (li >= 13) {
#pragma unroll 1
        for (int n = 48 + 12 * NT2 + (li - 13) + 3 * w; n < 171; n += 36) touch_group(P, xcd, n, lane);
    }
    if (li == 12) {
#pragma unroll
        for (int i = 0; i < 7; ++i) {
            const int u = (i * 5 + xcd - 3) * 12 + w;
            if (u < 384) st_sc1(wsr, WS_PART + (unsigned)(u * 1024 + lane * 16), u32x4{EMPTY, EMPTY, EMPTY, EMPTY});
        }
        WAIT_VM(0);
    }
    PIN_TAG();
    BARRIER();
    if (tid == 0) {
        if (li == 12) __hip_atomic_store((GAS u64*)(P.ws + WS_PFL + (xcd - 3) * 128), tag, RLX_AGENT);
        tag_post(P, tag);
    }
    if (li >= 13) {
#pragma unroll 1
        for (int n = 171 + (li - 13) + 3 * w; n < 234; n += 36) touch_group(P, xcd, n, lane);
    }
    STAMP(2);
    if (w == 11) {
        STAMP_OUT(8, blockIdx.x);
        if (blockIdx.x == 255) {
            const GAS u64* f = (const GAS u64*)(P.ws + WS_ARR) + lane;
#pragma unroll 1
            for (unsigned spins = 0; spins < SPIN_CAP; ++spins) {
                const bool ok = __hip_atomic_load(f, RLX_AGENT) == tag && __hip_atomic_load(f + 64, RLX_AGENT) == tag &&
                                __hip_atomic_load(f + 128, RLX_AGENT) == tag && __hip_atomic_load(f + 192, RLX_AGENT) == tag;
                if (__all(ok)) break;
                __builtin_amdgcn_s_sleep(8);
            }
            if (lane == 0) __hip_atomic_store((GAS u64*)(P.ws + WS_CTL), tag, RLX_AGENT);
        }
    }
}

__global__ __launch_bounds__(768, 3) void k_fused(Params P) {
    __shared__ __attribute__((aligned(16))) char smem_[LDS_BYTES];
    lptr smem = (lptr)smem_;
    const int b = blockIdx.x, xcd = b & 7, idx = b >> 3;
    const int lane = threadIdx.x & 63, w = __builtin_amdgcn_readfirstlane(threadIdx.x >> 6);
    const rsrc_t wsr = __builtin_amdgcn_make_buffer_rsrc(P.ws, 0, (int)WS_END, 0x00020000);
    const int rt64 = xcd * 4 + (idx & 3), cg2 = idx >> 2;
    if (idx >= 16 && w < 6) {
        const char* pc; unsigned len;
        asm volatile("s_getpc_b64 %0\n.Lkf_code_begin:\n\ts_mov_b32 %1, .Lkf_code_end-.Lkf_code_begin" : "=s"(pc), "=s"(len));
        touch(pc, min((unsigned)(threadIdx.x * 128), len));
    }
    u64 tag; u64 tp = 0;
    if (__builtin_expect(idx < 16, 1)) {
        const int rbl = idx & 3, hgl = idx >> 2, rb = (xcd >> 1) * 4 + rbl, hg = (xcd & 1) * 4 + hgl;
        if (__builtin_expect(w < 4, 1)) gemm_loader(P, smem, rb, hg, w, cg2, tag, wsr, tp);
        else gemm_compute(P, smem, rb, hg, w - 4, tag, wsr, tp);
    } else {
        tag = *(const __attribute__((address_space(4))) u64*)(P.ws + WS_CTL);
        const int li = idx - 16, gid = li < 11 ? xcd * 11 + li : 88 + (li - 11) * 8 + xcd;
        if (gid < 99) ssm_role(P, smem, xcd, li, gid, cg2, tag, wsr, tp);
        else { helper_role(P, smem, xcd, li, tag, wsr, tp); if (w < 9) wo_issue<0x03f>(P, cg2, w, 9, lane); }
        if (w < 9) wo_issue<0x1c0>(P, cg2, w, 9, lane);
        if (w == 9) poll_b(P, cg2, tag, lane);
        if (w == 10 && lane < 16) lds_w32(smem + FL + lane * 4, 0u);
        WAIT_VM(0);
        BARRIER();
    }
    phase2(P, smem, rt64, cg2, idx < 16, tag, wsr, tp);
    if (tp) while (__builtin_amdgcn_s_memrealtime() - tp < 5000) __builtin_amdgcn_s_sleep(1);
}

extern "C" void kernel_launch(void* const* d_in, const int* in_sizes, int n_in, void* d_out, int out_size, void* d_ws, size_t ws_size,
                              hipStream_t stream) {
    Params P;
    P.x = (const char*)d_in[0]; P.wqkv = (const char*)d_in[1]; P.wo = (const char*)d_in[2];
    P.logA = (const char*)d_in[4]; P.aim = (const char*)d_in[5]; P.cre = (const char*)d_in[6]; P.cim = (const char*)d_in[7];
    P.logdt = (const char*)d_in[8]; P.dskip = (const char*)d_in[9]; P.gw = (const char*)d_in[10];
    P.out = (char*)d_out; P.ws = (char*)d_ws;
    k_fused<<<256, 768, 0, stream>>>(P);
}
```

```cpp
#include <hip/hip_runtime.h>
#include <stdint.h>

#define GAS __attribute__((address_space(1)))
#define LAS __attribute__((address_space(3)))
#define DEVI __device__ __forceinline__
typedef unsigned short u16;
typedef unsigned long long u64;
typedef __attribute__((ext_vector_type(2))) unsigned u32x2;
typedef __attribute__((ext_vector_type(3))) unsigned u32x3;
typedef __attribute__((ext_vector_type(4))) unsigned u32x4;
typedef __attribute__((ext_vector_type(4))) short s16x4;
typedef __attribute__((ext_vector_type(2))) float f32x2;
typedef __attribute__((ext_vector_type(4))) float f32x4;
typedef __attribute__((ext_vector_type(16))) float f32x16;
typedef __attribute__((ext_vector_type(2))) __bf16 bf16x2;
typedef __attribute__((ext_vector_type(8))) __bf16 bf16x8;
typedef __attribute__((ext_vector_type(2))) _Float16 f16x2;
typedef __attribute__((ext_vector_type(8))) _Float16 f16x8;
typedef LAS char* lptr;

#define PREF 1
#define ROT 0
#define FSLEEP 2
#define NSLEEP "3"
#define LDRIFT 0
#define PSLEEP 6
#define CSLEEP 1
#define OSLEEP 2
#define RSLEEP 6
#define HOLD 1
#define GROT ((w + li) & 3)
#define NTW 8
#define OUTAUX 17
#define TEARLY 1
#define HDELAY 0
#define CPF 16
constexpr int SEQ = 2048, DM = 576;

constexpr unsigned WS_CTL = 0;
constexpr unsigned WS_ARR = 256;
constexpr unsigned WS_BFL = 4096;
constexpr unsigned WS_HINT = 20480;
constexpr unsigned WS_GATE = 36864;
constexpr unsigned WS_PFL = 49152;
constexpr unsigned WS_PART = 65536;
constexpr unsigned WS_O = WS_PART + 8 * 16 * 3072;
constexpr unsigned WS_B = WS_O + SEQ * DM * 2;
constexpr unsigned WS_END = WS_B + SEQ * DM * 2;
constexpr unsigned EMPTY = 0xffffffffu;

constexpr size_t WS_STAMP = 16u << 20;
#define STAMP_DECL
#define STAMP(i)
#define CSTAMP(i)
#define STAMP_OUT(role, slot)

struct Params {
    const char* x; const char* wqkv; const char* wo; const char* logA; const char* aim; const char* cre; const char* cim;
    const char* logdt; const char* dskip; const char* gw; char* out; char* ws;
};

constexpr int LDS_BYTES = 163840, FL = 163584, LDS_TAG = FL + 64, LDS_DUMMY = FL + 128, LDS_JUNK = 161280;
enum { F_SW = 0, F_LANDED = 4, F_W1 = 20, F_B = 28, F_H = 24, F_KVP = 36, F_KVR = 40, F_Q0 = 44, F_QD = 48, F_D = 52, F_C1 = 56, F_W0 = 60 };
constexpr int WO8 = 132096, P2_ACC = 73728, P2_AP = 304, P2_B = 141312, P2_G = 150528;

DEVI float bf2f(unsigned h) { return __uint_as_float(h << 16); }
DEVI float bfhi(unsigned h) { return __uint_as_float(h & 0xffff0000u); }
DEVI unsigned pk_bf16(float a, float b) { return __builtin_bit_cast(unsigned, __builtin_convertvector(f32x2{a, b}, bf16x2)); }
DEVI unsigned pk_f16(f32x2 v) { return __builtin_bit_cast(unsigned, __builtin_convertvector(v, f16x2)); }
DEVI float rbf(float a) { return bf2f(pk_bf16(a, a)); }
DEVI f32x4 mfma16(u32x4 a, u32x4 b, f32x4 c) {
    return __builtin_amdgcn_mfma_f32_16x16x32_bf16(__builtin_bit_cast(bf16x8, a), __builtin_bit_cast(bf16x8, b), c, 0, 0, 0);
}
DEVI void mfma16i(f32x4& c, u32x4 a, u32x4 b) { asm volatile("v_mfma_f32_16x16x32_bf16 %0, %1, %2, %0" : "+v"(c) : "v"(a), "v"(b)); }
#define MFMA_DONE() do { __builtin_amdgcn_sched_barrier(0); asm volatile("s_nop 7\n\ts_nop 1" ::: "memory"); __builtin_amdgcn_sched_barrier(0); } while (0)
DEVI u32x4 lds_r128(lptr p) { return *(LAS u32x4*)p; }
DEVI u32x2 lds_r64(lptr p) { return *(LAS u32x2*)p; }
DEVI float lds_rf(lptr p) { return *(LAS float*)p; }
DEVI void lds_w128(lptr p, u32x4 v) { *(LAS u32x4*)p = v; }
DEVI void lds_w64(lptr p, u32x2 v) { *(LAS u32x2*)p = v; }
DEVI void lds_w32(lptr p, unsigned v) { *(LAS unsigned*)p = v; }
DEVI void lds_w16(lptr p, unsigned v) { *(LAS u16*)p = (u16)v; }
DEVI u32x2 lds_tr(lptr p) { return __builtin_bit_cast(u32x2, __builtin_amdgcn_ds_read_tr16_b64_v4i16((LAS s16x4*)p)); }
DEVI u32x4 lds_trfrag(lptr p, int off2) { u32x2 a = lds_tr(p), b = lds_tr(p + off2); return u32x4{a.x, a.y, b.x, b.y}; }
template <int OFF> DEVI void glds16o(const char* sbase, unsigned voff) {
    asm volatile("global_load_lds_dwordx4 %0, %1 offset:%2" :: "v"(voff), "s"(sbase), "i"(OFF) : "memory");
}
DEVI void glds16(const char* sbase, unsigned voff, unsigned l) {
    asm volatile("s_mov_b32 m0, %2\n\ts_nop 0\n\tglobal_load_lds_dwordx4 %0, %1" :: "v"(voff), "s"(sbase), "s"(l) : "memory");
}
#define SET_M0(v) asm volatile("s_mov_b32 m0, %0\n\ts_nop 0" :: "s"(v) : "memory")
#define W_ADDTID(v, off) asm volatile("ds_write_addtid_b32 %0 offset:%1" :: "v"(v), "i"(off) : "memory")
#define CFENCE() asm volatile("" ::: "memory")
#define SCHED() __builtin_amdgcn_sched_barrier(0)
#define WAIT_VM(n) asm volatile("s_waitcnt vmcnt(" #n ")" ::: "memory")
#define WAIT_LGKM(n) __builtin_amdgcn_s_waitcnt(0xC07F | ((n) << 8))
#define WAIT_LGKM0() asm volatile("s_waitcnt lgkmcnt(0)" ::: "memory")
#define BARRIER() do { SCHED(); asm volatile("s_waitcnt lgkmcnt(0)\n\ts_barrier" ::: "memory"); SCHED(); } while (0)
#define RAW_BARRIER() do { SCHED(); asm volatile("s_barrier" ::: "memory"); SCHED(); } while (0)
#define RLX_AGENT __ATOMIC_RELAXED, __HIP_MEMORY_SCOPE_AGENT
constexpr unsigned SPIN_CAP = 1u << 16;
typedef __amdgpu_buffer_rsrc_t rsrc_t;
DEVI u32x4 ld_sc1(rsrc_t r, unsigned off) { return __builtin_amdgcn_raw_buffer_load_b128(r, off, 0, 16); }
DEVI void st_sc1(rsrc_t r, unsigned off, u32x4 v) { __builtin_amdgcn_raw_buffer_store_b128(v, r, off, 0, 16); }

DEVI u32x4 pack8(f32x4 a, f32x4 b) { return u32x4{pk_bf16(a[0], a[1]), pk_bf16(a[2], a[3]), pk_bf16(b[0], b[1]), pk_bf16(b[2], b[3])}; }
DEVI float phi(float t) {
    const float e = __builtin_amdgcn_fmed3f(__builtin_amdgcn_exp2f(t * 1.44269504089f), 0.f, 1.f);
    return __int_as_float(max(__float_as_int(t), 0x358637bd)) + e;
}
DEVI f32x4 phi4(f32x4 v) { return f32x4{phi(v[0]), phi(v[1]), phi(v[2]), phi(v[3])}; }
DEVI unsigned umax4(u32x4 a) { return max(max(a.x, a.y), max(a.z, a.w)); }
DEVI u32x4 not_empty(u32x4 a) { return u32x4{min(a.x, EMPTY - 1), min(a.y, EMPTY - 1), min(a.z, EMPTY - 1), min(a.w, EMPTY - 1)}; }
DEVI void tag_post(const Params& P, u64 tag) { __hip_atomic_store((GAS u64*)(P.ws + WS_ARR) + blockIdx.x, tag, RLX_AGENT); }
#define PIN_TAG() do { asm volatile("" : "+s"(tag)); tag += 1ull; } while (0)
template <int SL = PSLEEP>
DEVI void poll_addr(const Params& P, unsigned off, u64 tag) {
    const GAS u64* f = (const GAS u64*)(P.ws + off);
#pragma unroll 1
    for (unsigned spins = 0; spins < SPIN_CAP; ++spins) {
        if (__all(__hip_atomic_load(f, RLX_AGENT) == tag)) break;
        __builtin_amdgcn_s_sleep(SL);
    }
}
template <int SL = PSLEEP>
DEVI void poll_flags(const Params& P, unsigned off, int n, u64 tag, int lane) { poll_addr<SL>(P, off + (lane < n ? lane : 0) * 128, tag); }
DEVI unsigned flag_get(lptr smem, int f) { return __builtin_amdgcn_readfirstlane(__hip_atomic_load((LAS unsigned*)(smem + FL + f), __ATOMIC_RELAXED, __HIP_MEMORY_SCOPE_WORKGROUP)); }
template <int SL = FSLEEP>
DEVI void flag_wait(lptr smem, int f, unsigned v) {
#pragma unroll 1
    for (unsigned spins = 0; spins < (SPIN_CAP << 4); ++spins) { if (flag_get(smem, f) >= v) break; if (SL) __builtin_amdgcn_s_sleep(SL); }
    CFENCE();
}
DEVI void flag_add(int f) { asm volatile("s_mov_b64 exec, 1\n\tds_add_u32 %0, %1\n\ts_mov_b64 exec, -1" :: "v"(FL + f), "v"(1u) : "memory"); }
#define FLAG_ADD(f) asm volatile("s_mov_b64 exec, 1\n\tds_add_u32 %0, %1 offset:%2\n\ts_mov_b64 exec, -1" :: "v"(flp), "v"(one), "i"(f) : "memory")
DEVI void flag_set(int f, unsigned v) { asm volatile("s_mov_b64 exec, 1\n\tds_write_b32 %0, %1\n\ts_mov_b64 exec, -1" :: "v"(FL + f), "v"(v) : "memory"); }
#define NEEDP(seen, ptr, f, v) do { unsigned c_; asm volatile( \
        "v_cmp_gt_u32 vcc, %4, %1\n\ts_cbranch_vccnz 0f\n1:\n\t.subsection 1\n" \
        "0:\n\ts_mov_b32 %0, 0x100000\n2:\n\ts_sleep " NSLEEP "\n\tds_read_b32 %1, %2 offset:%3\n\ts_sub_u32 %0, %0, 1\n\ts_waitcnt lgkmcnt(0)\n\ts_cbranch_scc1 1b\n\t" \
        "v_cmp_gt_u32 vcc, %4, %1\n\ts_cbranch_vccnz 2b\n\ts_branch 1b\n\t.subsection 0" \
        : "=&s"(c_), "+v"(seen) : "v"(ptr), "i"(f), "i"(v) : "memory", "scc", "vcc"); } while (0)
#define NEED(seen, f, v) NEEDP(seen, flp, f, v)
DEVI void touch(const char* sbase, unsigned voff) {
    asm volatile("s_mov_b32 m0, %2\n\ts_nop 0\n\tglobal_load_lds_dword %0, %1" :: "v"(voff), "s"(sbase), "s"(LDS_JUNK) : "memory");
}
DEVI const char* group_addr(const Params& P, int xcd, int n, int lane) {
    if (n < 126) {
        const int c0 = n / 18, r = n - c0 * 18, c = c0 + 2, l = r - 8;
        return r < 8 ? P.x + (unsigned)(((xcd >> 1) * 512 + r * 64 + lane) * 1152 + c * 128)
                     : P.wqkv + (unsigned)((c * 64 + lane) * 3456 + (l < 5 ? 1152 : 2304 - 640) + (xcd & 1) * 512 + l * 128);
    }
    const int c = (n - 126) / 5, l = n - 126 - c * 5;
    return P.wqkv + (unsigned)((c * 64 + lane) * 3456 + (xcd & 1) * 512 + l * 128);
}
DEVI void touch_group(const Params& P, int xcd, int n, int lane) {
    if (n < 126) {
        const int c0 = n / 18, r = n - c0 * 18, c = c0 + 2;
        if (r < 8) touch(P.x, (unsigned)(((xcd >> 1) * 512 + r * 64 + lane) * 1152 + c * 128));
        else { const int l = r - 8; touch(P.wqkv, (unsigned)((c * 64 + lane) * 3456 + (l < 5 ? 1152 : 2304 - 640) + (xcd & 1) * 512 + l * 128)); }
    } else if (n < 171) {
        const int c = (n - 126) / 5, l = n - 126 - c * 5;
        touch(P.wqkv, (unsigned)((c * 64 + lane) * 3456 + (xcd & 1) * 512 + l * 128));
    } else {
        const int l = (n - 171) / 9, c = n - 171 - l * 9;
        touch(P.wo, (unsigned)((c * 64 + lane) * 1152 + l * 128));
    }
}
template <unsigned CH>
DEVI void wo_issue(const Params& P, int cg, int first, int stride, int lane) {
#pragma unroll 1
    for (int m = first; m < 9; m += stride) {
        const int q = 64 * m + lane, rho = q / 9, pc = q - rho * 9;
        const int k = (rho & 32) + 8 * ((rho >> 3) & 3) + 4 * (rho & 1) + ((rho >> 1) & 3);
        const unsigned qo = (unsigned)(k * 1152 + cg * 144 + pc * 16);
#pragma unroll
        for (int c = 0; c < 9; ++c) if ((CH >> c) & 1) glds16(P.wo + c * (64 * 1152), qo, (unsigned)((c < 6 ? c * 9216 : WO8 + (c - 6) * 9216) + m * 1024));
    }
}

constexpr int SP = 3088;
constexpr int XT_OFF = 133632, XT_BYTES = 4608, XPAD = 256;
constexpr int TT_OFF = 104448, TC = 1216, PT_OFF = 55296, PC_OFF = 2560, ACX_OFF = 55296;

#define CM2(o1, p1, a1, b1, o2, p2, a2, b2) do { asm( \
    "v_pk_mul_f32 %0, %2, %3 op_sel:[1,1] op_sel_hi:[1,0] neg_lo:[0,1]\n\tv_pk_mul_f32 %1, %4, %5 op_sel:[1,1] op_sel_hi:[1,0] neg_lo:[0,1]\n\t" \
    "v_pk_fma_f32 %0, %2, %3, %0 op_sel_hi:[0,1,1]\n\tv_pk_fma_f32 %1, %4, %5, %1 op_sel_hi:[0,1,1]" \
    : "=&v"(o1), "=&v"(o2) : "v"(a1), "v"(b1), "v"(a2), "v"(b2)); \
    asm("v_cvt_pk_f16_f32 %0, %2, %3\n\tv_cvt_pk_f16_f32 %1, %4, %5" : "=&v"(p1), "=&v"(p2) : "v"((o1).x), "v"((o1).y), "v"((o2).x), "v"((o2).y)); } while (0)
#define HM2(p1, a1, p2, a2, b) asm( \
    "v_pk_mul_f16 %0, %2, %4 op_sel_hi:[0,1]\n\tv_pk_mul_f16 %1, %3, %4 op_sel_hi:[0,1]\n\t" \
    "v_pk_fma_f16 %0, %2, %4, %0 op_sel:[1,1,0] op_sel_hi:[1,0,1] neg_lo:[0,1,0]\n\tv_pk_fma_f16 %1, %3, %4, %1 op_sel:[1,1,0] op_sel_hi:[1,0,1] neg_lo:[0,1,0]" \
    : "=&v"(p1), "=&v"(p2) : "v"(a1), "v"(a2), "v"(b))
DEVI f32x2 cexp_p(float a2, float f, float p) {
    float m = __builtin_amdgcn_exp2f(a2 * p);
    float r = __builtin_amdgcn_fractf(f * p);
    return f32x2{m * __builtin_amdgcn_cosf(r), m * __builtin_amdgcn_sinf(r)};
}
DEVI u32x4 mask_frag(unsigned bits, int g, unsigned val) {
    unsigned by = bits >> (8 * g);
    u32x4 r;
#pragma unroll
    for (int i = 0; i < 4; ++i) { unsigned t = (by >> (2 * i)) & 3u; r[i] = ((t | (t << 15)) & 0x10001u) * val; }
    return r;
}
struct Tab2 { unsigned v[8][64][4]; };
constexpr Tab2 make_tab2() {
    Tab2 t{};
    for (int l = 0; l < 64; ++l) {
        const int r = l & 15, g = l >> 4, j = l & 31, h = l >> 5;
        const unsigned bits[6] = {7u << (r + 11), 31u << (r + 6), 7u << (r + 3), 127u << (r + 5), 511u << (r + 4), 3u << (r + 12)};
        for (int m = 0; m < 6; ++m)
            for (int i = 0; i < 4; ++i) {
                const unsigned q = (bits[m] >> (8 * g + 2 * i)) & 3u, val = m < 5 ? 0x3f80u : 0xffffu;
                t.v[m][l][i] = (q & 1 ? val : 0u) | (q & 2 ? val << 16 : 0u);
            }
        for (int c = 0; c < 4; ++c) {
            const int v2 = 79 - (32 + (j >> 1)) - c + 4, v3 = 127 - (64 + 32 * h + (j >> 1)) - c + 4;
            t.v[6][l][c] = PT_OFF + (j & 1) * 1280 + c * 320 + (v2 >> 2) * 16 + h * 8 + (v2 & 3) * 2 - 64;
            t.v[7][l][c] = PT_OFF + PC_OFF + c * 320 + (v3 >> 2) * 16 + (j & 1) * 8 + (v3 & 3) * 2 - 64;
        }
    }
    return t;
}
__device__ const Tab2 g_tab2 = make_tab2();
DEVI float wave_max(float v) {
    v = fmaxf(v, __int_as_float(__builtin_amdgcn_update_dpp(0, __float_as_int(v), 0xB1, 0xf, 0xf, true)));
    v = fmaxf(v, __int_as_float(__builtin_amdgcn_update_dpp(0, __float_as_int(v), 0x4E, 0xf, 0xf, true)));
    v = fmaxf(v, __int_as_float(__builtin_amdgcn_update_dpp(0, __float_as_int(v), 0x124, 0xf, 0xf, true)));
    v = fmaxf(v, __int_as_float(__builtin_amdgcn_update_dpp(0, __float_as_int(v), 0x128, 0xf, 0xf, true)));
    const int i = __float_as_int(v);
    const int a = max(__builtin_amdgcn_readlane(i, 0), __builtin_amdgcn_readlane(i, 16)), b = max(__builtin_amdgcn_readlane(i, 32), __builtin_amdgcn_readlane(i, 48));
    return __int_as_float(max(a, b));
}

DEVI void ssm_role(const Params& P, lptr smem, int xcd, int li, int gid, int cg2, u64& tag, rsrc_t wsr) {
    const int gl = gid / 11, gi = gid - gl * 11, ch0 = gl * 64 + (gi < 10 ? gi * 6 : 58);
    const int tid = threadIdx.x, lane = tid & 63, w = __builtin_amdgcn_readfirstlane(tid >> 6);
    const int ch = w >> 1, half = (w ^ (w >> 2)) & 1, d = ch0 + ch;
    const bool own = gi < 10 || ch >= 2;
    const int lo16 = lane & 15, g = lane >> 4;
    STAMP_DECL; STAMP(1);
    __builtin_amdgcn_s_setprio(3);
    const unsigned po = (unsigned)(d * 256 + lane * 4);
    const unsigned r_la = *(const GAS unsigned*)(P.logA + po), r_ai = *(const GAS unsigned*)(P.aim + po);
    const unsigned r_cr = *(const GAS unsigned*)(P.cre + po), r_ci = *(const GAS unsigned*)(P.cim + po);
    const unsigned r_dt = *(const GAS u16*)(P.logdt + d * 2), r_ds = *(const GAS u16*)(P.dskip + d * 2);
    u32x4 ct[8];
    if (half) {
        const char* tp = (const char*)&g_tab2;
        asm volatile("" : "+s"(tp));
#pragma unroll
        for (int i = 0; i < 8; ++i) ct[i] = *(const GAS u32x4*)(tp + (unsigned)(i * 1024 + lane * 16));
    }
    const char* tga = (w < NTW && li < 12) ? group_addr(P, xcd, w * 12 + li, lane) : P.logA + po;
    RAW_BARRIER();
    const unsigned junk = *(const GAS unsigned*)tga;
    SCHED();
    const int tt = (tid + (li & 7) * 64) & 511;
    u32x3 xr[4];
    const unsigned xoff = (unsigned)(tt * 4608 + ch0 * 2);
    const unsigned boff = own ? WS_B + (unsigned)(d * 4096 + half * 2048 + lane * 16) : WS_END;
#define GATHER(i) do { SCHED(); if (w < 8) xr[i] = *(const GAS u32x3*)(P.x + xoff + (i) * 1152); SCHED(); } while (0)
#define PRIO(i) do { SCHED(); __builtin_amdgcn_s_setprio(i); SCHED(); } while (0)
    SCHED();
    lptr xt = smem + XT_OFF + ch * XT_BYTES, tb = smem + TT_OFF + ch * 4864, pt = smem + PT_OFF + ch * 3840;
    const int j32 = lane & 31, h = lane >> 5;
    if (!half) { lds_w64(xt + lane * 8, u32x2{0u, 0u}); lds_w32(smem + FL + 32, 0u); }
    int arow;
    {
        const u64 map0 = 0x0a09080803020100ull, map1 = 0x0c0b0a0a07060504ull, map2 = 0x1413110e12100f0dull;
        const u64 mm = j32 < 8 ? map0 : (j32 < 16 ? map1 : map2);
        arow = (int)((mm >> (8 * (j32 & 7))) & 0xff);
    }
    lptr ap = smem + arow * SP + w * 256 + h * 16, bp = smem + j32 * SP + w * 256 + h * 16;
    const int cpy = (3 - lo16) & 3;
    lptr xf7 = xt + (XPAD - 16 + 16 * lo16 + 8 * g) * 2 - 64 * 7;
    lptr tf = tb + cpy * TC + 2 * (527 - lo16 + 8 * g - cpy) - 64 * 16;
    lptr xc = smem + (w ^ 1) * 256 + lane * 4, xb = smem + XT_OFF + (XPAD + tt * 4) * 2;
    asm volatile("" : "+v"(ap), "+v"(bp), "+v"(xf7), "+v"(tf), "+v"(xc), "+v"(xb));
    const float dt = rbf(__expf(bf2f(r_dt)));
    const float cm = wave_max(fmaxf(fmaxf(fabsf(bf2f(r_cr)), fabsf(bfhi(r_cr))), fmaxf(fabsf(bf2f(r_ci)), fabsf(bfhi(r_ci)))));
    unsigned ce = __float_as_uint(cm) >> 23; ce = ce < 16u ? 16u : (ce > 240u ? 240u : ce);
    const float cs = __uint_as_float((263u - ce) << 23), ics = __uint_as_float((ce - 9u) << 23);
    f32x16 kacc;
#pragma unroll
    for (int i = 0; i < 16; ++i) kacc[i] = 0.f;
    STAMP(2);
    {
        const unsigned sh = half * 16;
        asm volatile("" :: "v"(r_la), "v"(r_ai), "v"(r_ds), "v"(ct[7]));
        const float la = bf2f(r_la >> sh), ai = bf2f(r_ai >> sh), cr = bf2f(r_cr >> sh) * cs, ci = bf2f(r_ci >> sh) * cs;
        const float a2 = (-rbf(__expf(la)) * dt) * 1.44269504089f, f = (ai * dt) * 0.15915494309f;
#define PUT(row, v) W_ADDTID(v, ((row) & 15) * SP)
        const f32x2 w1 = cexp_p(a2, -f, 1.f), W = cexp_p(a2, -f, 256.f);
        f32x2 u[8], v[4], c[9], e4, e8, t2;
        unsigned U[8], V[4], p, q;
        u[1] = cexp_p(a2, f, 8.f); v[1] = cexp_p(a2, f, 64.f);
        c[0] = f32x2{cr, -ci};
        U[1] = pk_f16(u[1]); V[1] = pk_f16(v[1]);
        GATHER(0);
        SET_M0(w * 256);
        PUT(0, pk_f16(c[0]));
        CM2(c[1], p, c[0], w1, u[2], U[2], u[1], u[1]); PUT(1, p);
        CM2(c[2], p, c[1], w1, u[3], U[3], u[2], u[1]); PUT(2, p);
        CM2(c[3], p, c[2], w1, u[4], U[4], u[2], u[2]); PUT(3, p);
        CM2(c[4], p, c[3], w1, u[5], U[5], u[4], u[1]); PUT(4, p);
        CM2(c[5], p, c[4], w1, u[6], U[6], u[4], u[2]); PUT(5, p);
        CM2(c[6], p, c[5], w1, u[7], U[7], u[4], u[3]); PUT(6, p);
        CM2(c[7], p, c[6], w1, v[2], V[2], v[1], v[1]); PUT(7, p);
        CM2(c[8], p, c[7], w1, v[3], V[3], v[2], v[1]);
        PRIO(2);
        GATHER(1);
        CM2(e8, p, c[8], W, e4, q, c[4], W); PUT(12, p); PUT(10, q);
        CM2(c[8], p, e8, W, c[4], q, e4, W); PUT(14, p); PUT(13, q);
        CM2(t2, p, c[0], W, e4, q, c[4], W); PUT(8, p); PUT(15, q);
        CM2(t2, p, c[2], W, e4, q, c[6], W); PUT(9, p); PUT(11, q);
        SET_M0(w * 256 + 16 * SP);
        CM2(e8, p, c[8], W, t2, q, W, W); PUT(17, p);
        CM2(e4, p, c[8], t2, c[0], q, e8, t2); PUT(16, p); PUT(18, q);
        CM2(c[8], p, e4, t2, e8, q, c[0], t2); PUT(19, p); PUT(20, q);
        PRIO(1);
        GATHER(2);
        u32x4 af[8];
#pragma unroll
        for (int s = 0; s < 8; ++s) af[s] = lds_r128(ap + 32 * s);
        CFENCE();
        PUT(16, V[2]); PUT(24, V[3]);
#pragma unroll
        for (int b = 1; b < 8; ++b) { HM2(p, V[2], q, V[3], U[b]); PUT(16 + b, p); PUT(24 + b, q); }
        SET_M0(w * 256);
        PRIO(0);
        GATHER(3);
        PUT(0, 0x3c00u); PUT(8, V[1]);
#pragma unroll
        for (int b = 1; b < 8; b += 2) { HM2(p, U[b], q, U[b == 7 ? 7 : b + 1], V[1]); PUT(8 + b, p); if (b < 7) PUT(9 + b, q); PUT(b, U[b]); if (b < 7) PUT(b + 1, U[b + 1]); }
#undef PUT
        u32x4 bfr[8];
#pragma unroll
        for (int s = 0; s < 8; ++s) bfr[s] = lds_r128(bp + 32 * s);
        SCHED();
        SCHED();
#pragma unroll
        for (int s = 0; s < 8; ++s)
            kacc = __builtin_amdgcn_mfma_f32_32x32x16_f16(__builtin_bit_cast(f16x8, af[s]), __builtin_bit_cast(f16x8, bfr[s]), kacc, 0, 0, 0);
    }
    STAMP(3);
    const float ks = 2.f * dt * ics;
#pragma unroll
    for (int i = 0; i < 12; ++i) kacc[i] *= ks;
    if (half) {
#pragma unroll
        for (int i = 0; i < 8; ++i) W_ADDTID(kacc[i], i * SP);
    } else {
#pragma unroll
        for (int i = 0; i < 4; ++i) W_ADDTID(kacc[8 + i], i * SP);
    }
    PIN_TAG();
    STAMP(4);
    BARRIER();
    STAMP(5);
    if (tid == 704) tag_post(P, tag);
    auto cleared = [&]() {
        if (w < 4) {
            WAIT_VM(0);
            if (lane == 0 && __hip_atomic_fetch_add((LAS unsigned*)(smem + FL + 32), 1u, __ATOMIC_RELAXED, __HIP_MEMORY_SCOPE_WORKGROUP) == 3u)
                __hip_atomic_store((GAS u64*)(P.ws + WS_BFL + gid * 128), tag, RLX_AGENT);
        }
    };
    auto x_in = [&]() {
        asm volatile("" :: "v"(junk));
        if (w < 8) {
#pragma unroll
            for (int k = 0; k < 3; ++k) {
                u32x2 lo = {__builtin_amdgcn_perm(xr[1][k], xr[0][k], 0x05040100u), __builtin_amdgcn_perm(xr[3][k], xr[2][k], 0x05040100u)};
                u32x2 hi = {__builtin_amdgcn_perm(xr[1][k], xr[0][k], 0x07060302u), __builtin_amdgcn_perm(xr[3][k], xr[2][k], 0x07060302u)};
                lds_w64(xb + (2 * k) * XT_BYTES, lo); lds_w64(xb + (2 * k + 1) * XT_BYTES, hi);
            }
        }
        if (w < 4) {
#pragma unroll
            for (int i = 0; i < 6; ++i)
                if (i >= 2 || gi < 10) __builtin_amdgcn_raw_buffer_store_b128(u32x4{EMPTY, EMPTY, EMPTY, EMPTY}, wsr, (unsigned)(tid * 16), WS_B + (ch0 + i) * 4096, 16);
        }
        BARRIER();
        if (w >= 8) wo_issue<0x03f>(P, cg2, w - 8, 4, lane);
    };
    auto TF = [&](int off) { u32x2 a = lds_r64(tf + off), b = lds_r64(tf + off + 8); return u32x4{a.x, a.y, b.x, b.y}; };
    f32x4 acc[8];
#pragma unroll
    for (int i = 0; i < 8; ++i) acc[i] = f32x4{0.f, 0.f, 0.f, 0.f};
    lptr axr;
    if (!half) {
        __builtin_amdgcn_s_setprio(2);
#pragma unroll
        for (int i = 0; i < 8; ++i) kacc[i] += lds_rf(xc + i * SP);
        unsigned dd[6];
        dd[0] = pk_bf16(kacc[7], kacc[6]); dd[1] = pk_bf16(kacc[5], kacc[4]); dd[2] = pk_bf16(kacc[3], kacc[2]);
        dd[3] = pk_bf16(kacc[1], kacc[0] + (lane == 0 ? bf2f(r_ds) : 0.f));
        dd[4] = __builtin_amdgcn_update_dpp(0, dd[0], 0x138, 0xf, 0xf, true);
        dd[5] = __builtin_amdgcn_update_dpp(0, dd[1], 0x138, 0xf, 0xf, true);
        unsigned mm[5];
#pragma unroll
        for (int i = 0; i < 5; ++i) mm[i] = __builtin_amdgcn_alignbit(dd[i + 1], dd[i], 16);
        lptr tw = tb + 2 * (536 - 8 * lane);
        lds_w128(tw, u32x4{dd[0], dd[1], dd[2], dd[3]}); lds_w128(tw + TC, u32x4{mm[0], mm[1], mm[2], mm[3]});
        lds_w128(tw + 2 * TC, u32x4{dd[1], dd[2], dd[3], dd[4]}); lds_w128(tw + 3 * TC, u32x4{mm[1], mm[2], mm[3], mm[4]});
        {
            const unsigned n0 = __builtin_amdgcn_readlane(dd[0], 63), n1 = __builtin_amdgcn_readlane(dd[1], 63);
            const unsigned kk = (n0 & 0xffffu) * 0x10001u, kh = n0 << 16, m4 = (n0 >> 16) | (n1 << 16);
            const int c = lane & 3, bl = lane >> 2;
            u32x4 e;
            e.x = 0u; e.y = lane == 14 ? kh : (lane == 15 ? kk : 0u); e.z = lane == 12 ? kh : (bl == 3 ? kk : 0u);
            e.w = lane == 14 ? n0 : (lane == 15 ? m4 : (bl == 3 ? kk : 0u));
            if (bl < 6) lds_w128(tb + c * TC + bl * 16 + (bl >> 2) * 1024, e);
        }
        x_in();
        u32x4 X[8];
#pragma unroll
        for (int i = 0; i < 8; ++i) X[i] = lds_r128(xf7 + 64 * 6 + 512 * i);
        u32x4 k0 = TF(1024 - 64), k8 = TF(512 - 64);
#pragma unroll
        for (int i = 0; i < 8; ++i) asm volatile("" : "+v"(acc[i]));
        __builtin_amdgcn_s_setprio(0);
        STAMP(6);
#pragma unroll
        for (int c = 1; c < 8; ++c) {
            u32x4 k0n, k8n;
            if (c < 7) { k0n = TF(1024 - 64 * (c + 1)); k8n = TF(512 - 64 * (c + 1)); }
#pragma unroll
            for (int i = 0; i < 8; ++i) {
                SCHED();
                mfma16i(acc[i], k0, X[i]);
                if (i < 7) mfma16i(acc[i + 1], k8, X[i]);
                SCHED();
                if (c < 7) X[i] = lds_r128(xf7 + 64 * (6 - c) + 512 * i);
            }
            k0 = k0n; k8 = k8n;
            if (c == 5) { SCHED(); cleared(); SCHED(); }
        }
        MFMA_DONE();
        SET_M0(ACX_OFF - 40960 + ch * 8192);
#pragma unroll
        for (int i = 0; i < 16; ++i) W_ADDTID(acc[4 + (i >> 2)][i & 3], 40960 + i * 256);
        axr = smem + ACX_OFF + ch * 8192 + 4096 + lane * 4;
    } else {
        u32x4 X[8];
#pragma unroll
        for (int i = 0; i < 4; ++i) if (i < 3 || lane < 48) lds_w128(pt + i * 1024 + lane * 16, u32x4{0u, 0u, 0u, 0u});
        CFENCE();
        const unsigned p89 = pk_bf16(kacc[8] + lds_rf(xc), kacc[9] + lds_rf(xc + SP)), pab = pk_bf16(kacc[10] + lds_rf(xc + 2 * SP), kacc[11] + lds_rf(xc + 3 * SP));
#pragma unroll
        for (int c = 0; c < 4; ++c) {
            lptr q2 = smem + (ct[6][c] + ch * 3840), q3 = smem + (ct[7][c] + ch * 3840);
            lds_w16(q2 + 64, p89); lds_w16(q2, p89 >> 16);
            lds_w16(q3 + 64, pab); lds_w16(q3, pab >> 16);
        }
        CFENCE();
        const u32x4 FX = ct[5] & (((unsigned)__builtin_amdgcn_readlane(p89, 63) >> 16) * 0x10001u);
        lptr pf2 = pt + cpy * 320 + ((79 - 64 - lo16 + 4 * g - cpy + 4) >> 2) * 16;
        u32x4 TA[3], TB[3], TCc[4];
#pragma unroll
        for (int i = 0; i < 3; ++i) { TA[i] = lds_r128(pf2 + 64 * (2 - i)); TB[i] = lds_r128(pf2 + 1280 + 64 * (2 - i)); }
#pragma unroll
        for (int i = 0; i < 4; ++i) TCc[i] = lds_r128(pf2 + PC_OFF + 64 * (3 - i));
        x_in();
#pragma unroll
        for (int i = 0; i < 8; ++i) X[i] = lds_r128(xf7 + 64 * 7 + 512 * i);
        STAMP(6);
        const f32x4 z4 = {0.f, 0.f, 0.f, 0.f};
#pragma unroll
        for (int st = 0; st < 6; ++st) {
            const u32x4 Xc = lds_r128(xf7 + 64 * 7 + 512 * st - 16);
            const u32x4 pa = pack8(mfma16(X[st], ct[0], z4), mfma16(X[st], ct[1], z4));
            const u32x4 pb = pack8(mfma16(X[st], ct[2], z4), mfma16(Xc, ct[1], z4));
#pragma unroll
            for (int dl = 2; dl <= 4; ++dl) if (st + dl < 8) {
                acc[st + dl] = mfma16(pa, TA[dl - 2], acc[st + dl]);
                acc[st + dl] = mfma16(pb, TB[dl - 2], acc[st + dl]);
            }
            if (st < 4) {
                const u32x4 pc = pack8(mfma16(X[st], ct[3], z4), mfma16(Xc, ct[4], z4));
#pragma unroll
                for (int dl = 4; dl <= 7; ++dl) if (st + dl < 8) acc[st + dl] = mfma16(pc, TCc[dl - 4], acc[st + dl]);
            }
        }
        SCHED(); cleared(); SCHED();
#pragma unroll
        for (int i = 4; i < 8; ++i) acc[i] = mfma16(FX, X[i - 4], acc[i]);
        const u32x4 k0 = TF(1024), k8 = TF(512), k16 = TF(0);
#pragma unroll
        for (int i = 0; i < 8; ++i) acc[i] = mfma16(k0, X[i], acc[i]);
#pragma unroll
        for (int i = 1; i < 8; ++i) acc[i] = mfma16(k8, X[i - 1], acc[i]);
#pragma unroll
        for (int i = 2; i < 8; ++i) acc[i] = mfma16(k16, X[i - 2], acc[i]);
        MFMA_DONE();
        SET_M0(ACX_OFF - 40960 + ch * 8192 + 4096);
#pragma unroll
        for (int i = 0; i < 16; ++i) W_ADDTID(acc[i >> 2][i & 3], 40960 + i * 256);
#pragma unroll
        for (int i = 0; i < 4; ++i) acc[i] = acc[4 + i];
        axr = smem + ACX_OFF + ch * 8192 + lane * 4;
    }
    STAMP(7);
    if (w == 0 && lane < 8) lds_w32(smem + FL + lane * 4, 0u);
    BARRIER();
    STAMP(8);
    if (w >= 8) wo_issue<0x1c0>(P, cg2, w - 8, 4, lane);
#pragma unroll
    for (int i = 0; i < 16; ++i) acc[i >> 2][i & 3] += lds_rf(axr + i * 256);
#pragma unroll
    for (int i = 0; i < 2; ++i) {
        const f32x4 ya = acc[2 * i], yb = acc[2 * i + 1];
        const auto sx = __builtin_amdgcn_permlane16_swap(pk_bf16(ya[0], ya[1]), pk_bf16(yb[0], yb[1]), false, false);
        const auto sy = __builtin_amdgcn_permlane16_swap(pk_bf16(ya[2], ya[3]), pk_bf16(yb[2], yb[3]), false, false);
        st_sc1(wsr, boff + i * 1024, not_empty(u32x4{sx[0], sy[0], sx[1], sy[1]}));
    }
    WAIT_VM(2);
    BARRIER();
    STAMP(9);
    if (w < 2) STAMP_OUT(2 + 2 * w, blockIdx.x + 256 * w);
}

constexpr int SLOT = 34816, XCH = 16384, KVT_OFF = 151296, KVP_OFF = 27648;
DEVI constexpr int slot_off(int s) { return (s & 3) == 0 ? SLOT : ((s & 3) == 1 ? 0 : (s & 3) * SLOT); }
DEVI constexpr int wq_off(int t) { return t == 0 ? 0 : 49152 + t * 27648; }

constexpr int quad_src(int quad) {
    const int pp = quad >= 192 ? 1 : 0, qi = quad - 192 * pp;
    const bool isz = qi >= 180;
    const int el = isz ? 0 : qi / 5, qd = qi - el * 5, hi = el >= 18 ? 1 : 0;
    const int dql = isz ? (qi < 189 ? qi - 180 : 0) : qd + 4 * hi;
    const int et = isz ? 0 : (el < 4 ? 0 : (el < 20 ? 1 : 2)), col = isz ? 0 : (el < 4 ? 12 + el : (el < 20 ? el - 4 : el - 20));
    const int tile = dql == 8 ? 5 : (et == 0 ? (dql >> 2) : (et == 1 ? 2 + (dql >> 2) : 4));
    return KVP_OFF + pp * 6144 + tile * 1024 + ((dql == 8 ? et : (dql & 3)) * 16 + col) * 4;
}
constexpr int quad_dst(int quad) {
    const int pp = quad >= 192 ? 1 : 0, qi = quad - 192 * pp;
    const bool isz = qi >= 180;
    const int el = isz ? 0 : qi / 5, qd = qi - el * 5, hi = el >= 18 ? 1 : 0;
    const int dql = isz ? (qi < 189 ? qi - 180 : 0) : qd + 4 * hi;
    const int dq = 9 * pp + dql, ks = dq >> 3, e = 36 * pp + el, ee = e >> 4;
    const int fr = ee == 0 ? 0 : (ee < 3 ? 2 * ee - 1 + ks : 2 * ee - 2 + ks);
    const int a = KVT_OFF + (isz ? (9 + ks) * 1024 + (dq & 3) * 256 + (2 * pp + (dql > 4 ? 1 : 0)) * 16 : fr * 1024 + ((dq & 3) * 16 + (e & 15)) * 16) + ((dq >> 2) & 1) * 8;
    return qi >= 189 ? LDS_DUMMY : (a | (isz ? (dql == 4 ? 0x300003 : 0x100003) : ((!hi && qd == 4) ? 1 : ((hi && qd == 0) ? 2 : 3))));
}
struct Tab { int v[2048]; };
constexpr Tab make_tab() {
    Tab t{};
    for (int c = 0; c < 192; ++c) {
        const int l = c & 63, q = 2 * ((c & 192) + (l >> 5) * 16 + (l & 15)) + ((l >> 4) & 1);
        t.v[4 * c] = quad_src(2 * c); t.v[4 * c + 1] = quad_src(2 * c + 1); t.v[4 * c + 2] = quad_dst(q); t.v[4 * c + 3] = quad_dst(q + 64);
    }
    for (int q = 0; q < 576; ++q) {
        const int rho = q / 9, pc = q % 9, k = (rho & 32) + 8 * ((rho >> 3) & 3) + 4 * (rho & 1) + ((rho >> 1) & 3), m = q >> 6;
        t.v[768 + q] = k * 3456 + pc * 16 - (m & 1) * 1024;
        t.v[1344 + q] = k * 1152 + pc * 16 - (m & 3) * 1024;
    }
    return t;
}
__device__ const Tab g_tab = make_tab();
constexpr int IDX = P2_B, GW_OFF = 139264;

DEVI void gemm_compute(const Params& P, lptr smem, int rb, int hg, int w, u64& tag, rsrc_t wsr) {
    const int lane = threadIdx.x & 63, lo16 = lane & 15, g = lane >> 4;
    const int rg = w >> 1, p = w & 1;
    STAMP_DECL; STAMP(1);
    if (w == 0 && lane < 16) lds_w32(smem + FL + lane * 4, 0u);
    if (w >= 4) {
#pragma unroll
        for (int i = 0; i < 3; ++i) lds_w128(smem + KVT_OFF + i * 4096 + (w - 4) * 1024 + lane * 16, u32x4{0u, 0u, 0u, 0u});
    }
    __builtin_amdgcn_s_setprio(2);
    const int row0 = 16 * (2 * rg + p) + lo16, row1 = 16 * (2 * rg + 1 - p) + lo16;
    const int sw = (g ^ ((lo16 >> 1) & 7)) << 4;
    lptr xa0[2] = {smem + row0 * 128 + sw, smem + row0 * 128 + (sw ^ 64)}, xa1[2] = {smem + row1 * 128 + sw, smem + row1 * 128 + (sw ^ 64)};
    const int wrow = (4 * g + (lo16 >> 2)) * 288;
    lptr wa = smem + XCH + wrow + (9 * p + (lane & 3)) * 8, wa2 = smem + XCH + wrow + (9 * p + ((lane & 3) == 3 ? 18 : 8)) * 8;
    lptr flp = smem + FL;
    asm volatile("" : "+v"(flp));
    const f32x4 z4 = {0.f, 0.f, 0.f, 0.f};
    f32x4 acc[2][5];
#pragma unroll
    for (int i = 0; i < 2; ++i)
#pragma unroll
        for (int j = 0; j < 5; ++j) { const float e = j < 2 ? 1e-6f : (j == 2 && lo16 < 12 ? 1e-6f : 0.f); acc[i][j] = f32x4{e, e, e, e}; }
    u32x4 xk[18], x1[2];
    auto rel = [&](int s) { return (s & 2) ? slot_off(s) - 2 * SLOT : slot_off(s); };
    auto rebase = [&](int dlt) {
        xa0[0] += dlt; xa0[1] += dlt; xa1[0] += dlt; xa1[1] += dlt; wa += dlt; wa2 += dlt;
        asm volatile("" : "+v"(xa0[0]), "+v"(xa0[1]), "+v"(xa1[0]), "+v"(xa1[1]), "+v"(wa), "+v"(wa2));
    };
    constexpr int NB = 8;
    u32x2 wl[NB], wh[NB];
    auto rdw = [&](int f, int t) {
        const int n = f / 5, j = f % 5;
        const int o = rel(n >> 1) + (n & 1) * 9216 + t * 4608;
        const u32x2 v = j == 2 ? lds_tr(wa2 + o) : lds_tr(wa + o + (j == 0 ? 0 : (j == 1 ? 32 : (j == 3 ? 152 : 184))));
        if (t) wh[f % NB] = v; else wl[f % NB] = v;
    };
    auto peek = [&](int f) { return __hip_atomic_load((LAS unsigned*)(flp + f), __ATOMIC_RELAXED, __HIP_MEMORY_SCOPE_WORKGROUP); };
    asm volatile(".subsection 2\n.Lkf_code_end:\n\t.fill 256, 4, 0xbf800000\n\t.subsection 0");
    unsigned one = 1u;
    asm volatile("" : "+v"(one));
    rebase(0);
#pragma unroll
    for (int j = 0; j < 5; ++j) asm volatile("" : "+v"(acc[0][j]), "+v"(acc[1][j]));
    BARRIER();
    STAMP(2);
    xk[0] = lds_r128(xa0[0] + rel(0)); x1[0] = lds_r128(xa1[0] + rel(0));
#pragma unroll
    for (int f = 0; f < NB - 1; ++f) { rdw(f, 0); rdw(f, 1); }
#pragma unroll
    for (int n = 0; n < 18; ++n) {
        const int nx = n + 1, s = n >> 1;
#pragma unroll
        for (int m = 0; m < 10; ++m) {
            const int j = m >> 1, f = 5 * n + j, fn = f + NB - 1;
            SCHED();
            if (!(m & 1) && !(f & 1) && fn < 88) WAIT_LGKM(10);
            const u32x4 wv = {wl[f % NB].x, wl[f % NB].y, wh[f % NB].x, wh[f % NB].y};
            if (m & 1) mfma16i(acc[1][j], x1[n & 1], wv); else mfma16i(acc[0][j], xk[n], wv);
            SCHED();
            if (!(m & 1) && fn % 10 == 0) {
                if (s < 8 && (s & 1)) rebase((s & 2) ? -2 * SLOT : 2 * SLOT);
                if (s == 8) WAIT_LGKM0();
                RAW_BARRIER();
                if (s == 0) STAMP(3);
                if (s == 4) STAMP(4);
            }
            if (fn < 90) rdw(fn, m & 1);
            if (nx < 18) {
                if (m == 0) xk[nx] = lds_r128(xa0[nx & 1] + rel(nx >> 1));
                if (m == 2) x1[nx & 1] = lds_r128(xa1[nx & 1] + rel(nx >> 1));
            }
        }
    }
    MFMA_DONE();
    STAMP(5);
    const int qrow = (2 * (4 * g + (lo16 >> 2))) * 144;
    lptr qw = smem + qrow + (lane & 3) * 8, qw4 = smem + qrow + (16 + (lane & 1)) * 8;
    lptr qwh = qw + wq_off(1), qw4h = qw4 + wq_off(1);
    asm volatile("" : "+v"(qwh), "+v"(qw4h));
    auto rdq = [&](int ks, int j) {
        const int o = (ks < 6 ? ks : ks - 6) * 4608;
        return j < 4 ? lds_trfrag((ks < 6 ? qw : qwh) + o + 32 * j, 144) : lds_trfrag((ks < 6 ? qw4 : qw4h) + o, 144);
    };
    {
        u32x4 pk[3], vb[3];
#pragma unroll
        for (int j = 0; j < 3; ++j) pk[j] = pack8(phi4(acc[0][j]), phi4(acc[1][j]));
        vb[0] = pack8(acc[0][2], acc[1][2]);
        if (lo16 < 12) vb[0] = u32x4{0x3f803f80u, 0x3f803f80u, 0x3f803f80u, 0x3f803f80u};
        vb[1] = pack8(acc[0][3], acc[1][3]); vb[2] = pack8(acc[0][4], acc[1][4]);
        f32x4 r[6];
        const f32x4 t0 = mfma16(pk[2], vb[0], z4), t1 = mfma16(pk[2], vb[1], z4), t2 = mfma16(pk[2], vb[2], z4);
        r[0] = mfma16(pk[0], vb[0], z4); r[1] = mfma16(pk[1], vb[0], z4); r[2] = mfma16(pk[0], vb[1], z4);
        r[3] = mfma16(pk[1], vb[1], z4); r[4] = mfma16(pk[1], vb[2], z4);
        r[5] = g == 0 ? t0 : (g == 1 ? t1 : t2);
        SCHED();
        asm volatile("s_nop 7" ::: "memory");
        SET_M0(KVP_OFF - 8192 + w * 6144);
#pragma unroll
        for (int t = 5; t >= 0; --t) {
            W_ADDTID(r[t][0], 8192 + t * 1024); W_ADDTID(r[t][1], 8192 + t * 1024 + 256);
            W_ADDTID(r[t][2], 8192 + t * 1024 + 512); W_ADDTID(r[t][3], 8192 + t * 1024 + 768);
        }
        FLAG_ADD(F_KVP);
        __builtin_amdgcn_s_setprio(0);
    }
    STAMP(6);
    f32x4 qa[5];
#pragma unroll
    for (int j = 0; j < 5; ++j) qa[j] = f32x4{1e-6f, 1e-6f, 1e-6f, 1e-6f};
#pragma unroll
    for (int j = 0; j < 5; ++j) asm volatile("" : "+v"(qa[j]));
    SCHED();
    u32x4 wq[2][5];
#pragma unroll
    for (int i = 0; i < 2; ++i)
#pragma unroll
        for (int j = 0; j < 5; ++j) wq[i][j] = rdq(i, j);
    SCHED();
    if (w >= 4) flag_wait<1>(smem, F_KVR, 3);
    unsigned seenq = 0;
#pragma unroll
    for (int ks = 0; ks < 18; ++ks) {
        if (ks == 2) seenq = peek(F_KVR);
        if (ks == 4) { SCHED(); FLAG_ADD(F_Q0); NEED(seenq, F_KVR, 3); seenq = peek(F_LANDED); NEED(seenq, F_LANDED, 4); }
        if (ks == 16) { SCHED(); FLAG_ADD(F_QD); }
#pragma unroll
        for (int j = 0; j < 5; ++j) {
            SCHED();
            if (j == 0 && ks < 17) WAIT_LGKM(10);
            mfma16i(qa[j], wq[ks & 1][j], xk[ks]);
            SCHED();
            if (ks + 2 < 18) wq[ks & 1][j] = rdq(ks + 2, j);
        }
    }
    __builtin_amdgcn_s_setprio(2);
    MFMA_DONE();
    u32x4 b0 = pack8(phi4(qa[0]), phi4(qa[1])), b1 = pack8(phi4(qa[2]), phi4(qa[3])), b2 = pack8(phi4(qa[4]), z4);
    lptr kfp = smem + KVT_OFF + lane * 16;
    unsigned ob0 = WS_O + (unsigned)(((rb * 8 + 2 * rg + p) * 72 + hg * 9 + 2 * (g & 1) + (g >> 1)) * 256 + lo16 * 16);
    asm volatile("" : "+v"(b0), "+v"(b1), "+v"(b2), "+v"(kfp), "+v"(ob0));
    { const u32x2 t = lds_r64(smem + LDS_TAG); tag = (u64)(unsigned)__builtin_amdgcn_readfirstlane(t.x) | ((u64)(unsigned)__builtin_amdgcn_readfirstlane(t.y) << 32); }
    STAMP(7);
    flag_wait<1>(smem, F_D, 4);
    STAMP(8);
    {
        u32x4 kf[12];
#pragma unroll
        for (int i = 0; i < 12; ++i) kf[i] = lds_r128(kfp + (i < 3 ? 9 + i : i - 3) * 1024);
        SCHED();
        const f32x4 den = mfma16(kf[2], b2, mfma16(kf[1], b1, mfma16(kf[0], b0, f32x4{1e-6f, 1e-6f, 1e-6f, 1e-6f})));
        f32x4 o[5];
        o[0] = mfma16(kf[3], b0, z4);
        o[1] = mfma16(kf[5], b1, mfma16(kf[4], b0, z4));
        o[2] = mfma16(kf[7], b1, mfma16(kf[6], b0, z4));
        o[3] = mfma16(kf[9], b2, mfma16(kf[8], b1, z4));
        o[4] = mfma16(kf[11], b2, mfma16(kf[10], b1, z4));
        f32x4 r;
#pragma unroll
        for (int i = 0; i < 4; ++i) r[i] = __builtin_amdgcn_rcpf(den[i]);
        const float r1a = g == 0 ? r[0] : r[1], r2 = g == 0 ? r[1] : r[2], r3a = g <= 1 ? r[2] : r[3], r3b = g == 0 ? r[2] : r[3];
        u32x2 ob[5];
        ob[0] = u32x2{pk_bf16(o[0][0] * r[0], o[0][1] * r[0]), pk_bf16(o[0][2] * r[0], o[0][3] * r[0])};
        ob[1] = u32x2{pk_bf16(o[1][0] * r1a, o[1][1] * r1a), pk_bf16(o[1][2] * r[1], o[1][3] * r[1])};
        ob[2] = u32x2{pk_bf16(o[2][0] * r2, o[2][1] * r2), pk_bf16(o[2][2] * r2, o[2][3] * r2)};
        ob[3] = u32x2{pk_bf16(o[3][0] * r3a, o[3][1] * r3a), pk_bf16(o[3][2] * r3b, o[3][3] * r3b)};
        ob[4] = u32x2{pk_bf16(o[4][0] * r[3], o[4][1] * r[3]), pk_bf16(o[4][2] * r[3], o[4][3] * r[3])};
        SCHED();
        if (w == 7 && lane == 0) __hip_atomic_store((GAS u64*)(P.ws + WS_HINT + (rb * 8 + hg) * 128), tag, RLX_AGENT);
        SCHED();
#pragma unroll
        for (int t = 0; t < 3; ++t) {
            const u32x2 a = ob[2 * t], b = ob[t < 2 ? 2 * t + 1 : 4];
            const auto sx = __builtin_amdgcn_permlane16_swap(a.x, b.x, false, false), sy = __builtin_amdgcn_permlane16_swap(a.y, b.y, false, false);
            if (t < 2 || g == 0) st_sc1(wsr, ob0 + 1024 * t, not_empty(u32x4{sx[0], sy[0], sx[1], sy[1]}));
        }
    }
    __builtin_amdgcn_s_setprio(0);
    STAMP(9);
    if (w == 0) STAMP_OUT(1, blockIdx.x);
    if (w == 7) STAMP_OUT(7, blockIdx.x + 768);
}

DEVI void gemm_loader(const Params& P, lptr smem, int rb, int hg, int lw, int cg2, u64& tag, rsrc_t wsr) {
    const int lane = threadIdx.x & 63;
    STAMP_DECL; STAMP(1);
    __builtin_amdgcn_s_setprio(3);
    unsigned xo[4], wo_[5], qo[3];
    const int rx = hg & 3, rw = rb & 3;
    unsigned mx[4], mw[4];
    const unsigned xbase = (unsigned)((rb * 128 + 8 * lw + (lane >> 3)) * 1152 + (((lane & 7) ^ ((lane >> 4) + 4 * (lw & 1))) << 4));
#pragma unroll
    for (int i = 0; i < 4; ++i) { mx[i] = (((i + rx) & 3) * 4 + lw) * 1024; mw[i] = XCH + (((i + rw) & 3) * 4 + lw) * 1024; xo[i] = xbase + ((i + rx) & 3) * 36864; }
    const unsigned lb = lw * 4096;
    auto issue_x = [&](int s, int i0 = 0, int i1 = 4) {
#pragma unroll
        for (int i = i0; i < i1; ++i) glds16(P.x + s * 128, xo[i], slot_off(s) + mx[i]);
    };
    auto issue_w = [&](int s, int i0 = 0, int i1 = 4) {
        const char* ws = P.wqkv + s * (64 * 3456);
        if (i1 == 4 && (lw >> 1) == (s & 1)) glds16(ws, wo_[4], (unsigned)(slot_off(s) + XCH + 16384 + (lw & 1) * 1024));
#pragma unroll
        for (int i = i0; i < i1; ++i) glds16(ws, wo_[i], slot_off(s) + mw[i]);
    };
    auto issue_q = [&](int t, int i) {
        const char* ws = P.wqkv + hg * 144 + (3 * t + i) * (64 * 3456);
        if (lw == ((i + t) & 3)) glds16(ws, qo[2], (unsigned)(wq_off(t) + i * 9216 + 8192));
        SET_M0(wq_off(t) + i * 9216 + (lb >> 1));
        glds16o<0>(ws, qo[0]); glds16o<1024>(ws, qo[1]);
    };
    auto w_off = [&](int i) {
        const unsigned q = 64 * (i < 4 ? ((i + rw) & 3) * 4 + lw : 16 + (lw & 1)) + lane, rho = (q * 3641u) >> 16, pc = q - rho * 18;
        const unsigned k = (rho & 35u) + 8 * ((rho >> 2) & 3) + 4 * ((rho >> 4) & 1);
        wo_[i] = k * 3456 + (1152 + hg * 144) + pc * 16 + (pc < 9 ? 0u : 1008u);
    };
    issue_x(0, 0, 1); issue_x(1, 0, 1);
    SCHED();
    w_off(0);
    issue_w(0, 0, 1); issue_w(1, 0, 1);
    SCHED();
#pragma unroll
    for (int i = 1; i < 5; ++i) w_off(i);
    if (lw == 1) { glds16(P.gw, (unsigned)(lane * 16), GW_OFF); if (lane < 8) glds16(P.gw + 1024, (unsigned)(lane * 16), GW_OFF + 1024); }
    issue_x(0, 1, 4); issue_w(0, 1, 4); issue_w(1, 1, 4); issue_x(1, 1, 4);
    const int lo16 = lane & 15, g = lane >> 4, ct = (lw < 3 ? lw : 0) * 64 + lane;
    lptr flp = smem + FL, ixp = smem + IDX + ct * 16, irp = smem + IDX + 3072 + lane * 4, iqp = irp + lw * 512;
    unsigned one = 1u;
    u32x4 sel[2];
    sel[0] = mask_frag(lo16 < 8 ? 1u << lo16 : 0u, 0, 0x3f80u); sel[1] = mask_frag(lo16 >= 8 ? 1u << (lo16 - 8) : 0u, 0, 0x3f80u);
    unsigned sto = WS_PART + (unsigned)((hg * 16 + rb) * 3072 + ct * 16);
    unsigned off = WS_PART + (unsigned)((hg * 16 + g) * 3072 + lw * 1024 + lo16 * 16);
    asm volatile("" : "+v"(flp), "+v"(one), "+v"(ixp), "+v"(irp), "+v"(iqp));
    asm volatile("" : "+v"(sel[0]), "+v"(sel[1]), "+v"(sto), "+v"(off));
    SET_M0(IDX + (lb >> 1));
    glds16o<0>((const char*)&g_tab + (lb >> 1), (unsigned)(lane * 16)); glds16o<1024>((const char*)&g_tab + (lb >> 1), (unsigned)(lane * 16));
    lptr gxp = smem + (16 * hg + lo16) * 128 + ((g ^ ((lo16 >> 1) & 7)) << 4), gwp = smem + GW_OFF + g * 16;
    f32x4 gacc = {0.f, 0.f, 0.f, 0.f};
    asm volatile("" : "+v"(gxp), "+v"(gwp));
    auto gate_step = [&](int s) {
        if (lw == 1) {
            lptr q = gxp + slot_off(s);
            gacc = mfma16(lds_r128(gwp + s * 128), lds_r128(q), gacc);
            gacc = mfma16(lds_r128(gwp + s * 128 + 64), lds_r128((lptr)((size_t)q ^ 64u)), gacc);
        }
    };
    WAIT_VM(8); BARRIER();
    issue_w(2); issue_x(2);
    u32x4 ix; unsigned oo[9]; u64 pf = 0;
#pragma unroll
    for (int s = 0; s < 9; ++s) {
        gate_step(s);
        if (s < 7) WAIT_VM(8); else WAIT_VM(6);
        BARRIER();
        if (s == 5 && lw == 3) asm volatile("global_load_dwordx2 %0, %1, %2 sc1" : "=v"(pf) : "v"((lane < 5 ? lane : 0) * 128), "s"(P.ws + WS_PFL) : "memory");
        if (s < 6) { issue_w(s + 3); issue_x(s + 3); } else { issue_q(s - 6, 0); issue_q(s - 6, 1); issue_q(s - 6, 2); }
        if (s == 0) tag = *(const __attribute__((address_space(4))) u64*)(P.ws + WS_CTL);
        if (s == 1) PIN_TAG();
        if (s == 1 && lw == 0 && lane == 0) lds_w64(smem + LDS_TAG, u32x2{(unsigned)tag, (unsigned)(tag >> 32)});
        if (s == 6 && lw == 3) {
            asm volatile("" : "+v"(pf));
            if (!__all(pf == tag)) poll_flags(P, WS_PFL, 5, tag, lane);
        }
        if (s == 3) {
            qo[0] = *(LAS unsigned*)iqp; qo[1] = *(LAS unsigned*)(iqp + 256); qo[2] = *(LAS unsigned*)(irp + 2048);
            ix = lds_r128(ixp);
#pragma unroll
            for (int i = 0; i < 9; ++i) oo[i] = *(LAS unsigned*)(irp + 2304 + i * 256);
            CFENCE();
        }
    }
    {
        const unsigned co = WS_O + (unsigned)(((rb * 8 + 2 * lw) * 72 + hg * 9) * 256 + lane * 16);
#pragma unroll
        for (int i = 0; i < 5; ++i)
            if (i < 4 || lane < 32) st_sc1(wsr, i < 4 ? co + (i >> 1) * 18432 + (i & 1) * 1024 : co + 2048 + (lane >> 4) * (18432 - 256), u32x4{EMPTY, EMPTY, EMPTY, EMPTY});
    }
    WAIT_VM(5); FLAG_ADD(F_LANDED);
    if (lw == 0 && lane == 0) tag_post(P, tag);
    if (lw == 1) {
        const float gate = rbf(__builtin_amdgcn_rcpf(1.f + __expf(-rbf(gacc[0]))));
        if (lane < 16) __hip_atomic_store((GAS float*)(P.ws + WS_GATE) + (rb * 128 + hg * 16 + lane), gate, RLX_AGENT);
    }
    if (lw < 3) {
        const int dst[2] = {(int)ix.z, (int)ix.w};
        flag_wait<0>(smem, F_KVP, 8);
        STAMP(2);
        float pa[2][4][4];
#pragma unroll
        for (int i = 0; i < 2; ++i)
#pragma unroll
            for (int r = 0; r < 4; ++r)
#pragma unroll
                for (int k = 0; k < 4; ++k) pa[i][r][k] = lds_rf(smem + ix[i] + r * 12288 + k * 256);
        SCHED();
        u32x4 pv;
#pragma unroll
        for (int i = 0; i < 2; ++i)
#pragma unroll
            for (int k = 0; k < 2; ++k)
                pv[2 * i + k] = pk_bf16((pa[i][0][2 * k] + pa[i][1][2 * k]) + (pa[i][2][2 * k] + pa[i][3][2 * k]),
                                        (pa[i][0][2 * k + 1] + pa[i][1][2 * k + 1]) + (pa[i][2][2 * k + 1] + pa[i][3][2 * k + 1]));
        st_sc1(wsr, sto, not_empty(pv));
        SCHED();
        FLAG_ADD(F_KVR);
        STAMP(3);
        WAIT_VM(0);
        STAMP(4);
        f32x4 sum[2];
#pragma unroll 1
        for (unsigned spins = 0; spins < SPIN_CAP; ++spins) {
            u32x4 a[4][4];
#pragma unroll
            for (int i = 0; i < 4; ++i)
#pragma unroll
                for (int t = 0; t < 4; ++t) a[t][i] = __builtin_amdgcn_raw_buffer_load_b128(wsr, off + t * 256, ((i + rb) & 3) * 12288, 16);
            SCHED();
            FLAG_ADD(F_SW);
            sum[0] = sum[1] = f32x4{0.f, 0.f, 0.f, 0.f};
#pragma unroll
            for (int i = 0; i < 4; ++i)
#pragma unroll
                for (int t = 0; t < 4; ++t) sum[t >> 1] = mfma16(sel[t & 1], a[t][i], sum[t >> 1]);
            if (__all(sum[0][0] == sum[0][0] && sum[1][0] == sum[1][0])) break;
            __builtin_amdgcn_s_sleep(RSLEEP);
            if (spins < 8) { CFENCE(); continue; }
            unsigned m = 0;
#pragma unroll
            for (int t = 0; t < 4; ++t)
#pragma unroll
                for (int i = 0; i < 4; ++i) m = max(m, umax4(a[t][i]));
            if (__all(m != EMPTY)) break;
            CFENCE();
        }
#pragma unroll
        for (int i = 0; i < 2; ++i) {
            const unsigned lo = (dst[i] & 1) ? pk_bf16(sum[i][0], sum[i][1]) : 0u, hi = (dst[i] & 2) ? pk_bf16(sum[i][2], sum[i][3]) : 0u;
            lptr dp = smem + (dst[i] & 0xffffc);
            if (dst[i] & 0x100000) {
                const int hs = (dst[i] & 0x200000) ? 20 : 4;
#pragma unroll
                for (int r = 0; r < 4; ++r) { lds_w32(dp + 64 * r, lo); lds_w32(dp + 64 * r + hs, hi); }
            } else lds_w64(dp, u32x2{lo, hi});
        }
        CFENCE();
        FLAG_ADD(F_D);
        STAMP(5);
    } else {
        auto wo_chunks = [&](int c0, int c1) {
#pragma unroll 1
            for (int c = c0; c < c1; ++c) {
                const char* ws = P.wo + cg2 * 144 + c * (64 * 1152);
                const unsigned l = c < 8 ? c * 9216 : WO8;
                SET_M0(l); glds16o<0>(ws, oo[0]); glds16o<1024>(ws, oo[1]); glds16o<2048>(ws, oo[2]); glds16o<3072>(ws, oo[3]);
                SET_M0(l + 4096); glds16o<0>(ws, oo[4]); glds16o<1024>(ws, oo[5]); glds16o<2048>(ws, oo[6]); glds16o<3072>(ws, oo[7]);
                SET_M0(l + 8192); glds16o<0>(ws, oo[8]);
            }
        };
        wo_chunks(8, 9);
        flag_wait(smem, F_KVR, 3);
        wo_chunks(3, 8);
        WAIT_VM(0);
        FLAG_ADD(F_D);
        flag_wait(smem, F_SW, 3);
        flag_wait(smem, F_Q0, 8);
        wo_chunks(0, 3);
        WAIT_VM(0);
        FLAG_ADD(F_W0);
    }
    __builtin_amdgcn_s_setprio(0);
    STAMP(6);
    if (lw == 0) STAMP_OUT(5, blockIdx.x + 256);
}

DEVI void phase2(const Params& P, lptr smem, int rt64, int cg, bool gemm, u64 tag, rsrc_t wsr) {
    const int tid = threadIdx.x, lane = tid & 63, w = __builtin_amdgcn_readfirstlane(tid >> 6);
    const int lo16 = lane & 15, g = lane >> 4;
    const int rt = w & 3, kt = w >> 2;
    STAMP_DECL; STAMP(1);
    const int pb = gemm ? P2_B : 55296;
    if (gemm && w < 4) flag_wait(smem, F_W0, 1);
    STAMP(2);
    const int qrow = (2 * (4 * g + (lo16 >> 2))) * 144;
    lptr qw = smem + qrow + (lane & 3) * 8, qw4 = smem + qrow + (16 + (lane & 1)) * 8;
    int wofs[3], oofs[3];
#pragma unroll
    for (int u = 0; u < 3; ++u) {
        const int c = kt * 3 + (u + cg) % 3;
        wofs[u] = c < (gemm ? 8 : 6) ? c * 9216 : WO8 + (gemm ? 0 : (c - 6) * 9216); oofs[u] = c * 2048;
    }
    auto rdo = [&](int i, int j) { return j < 4 ? lds_trfrag(qw + wofs[i >> 1] + (i & 1) * 4608 + 32 * j, 144) : lds_trfrag(qw4 + wofs[i >> 1] + (i & 1) * 4608, 144); };
    const unsigned ooff = WS_O + (unsigned)((rt64 * 4 + rt) * 18432 + lane * 16);
    auto ldo = [&](int i) { return __builtin_amdgcn_raw_buffer_load_b128(wsr, ooff + (i & 1) * 1024, oofs[i >> 1], 16); };
    u32x4 wf[6][5];
#pragma unroll
    for (int i = 0; i < 6; ++i)
#pragma unroll
        for (int j = 0; j < 5; ++j) wf[i][j] = rdo(i, j);
    const int r0 = tid < 576 ? tid / 9 : 0, c0 = tid - r0 * 9;
    unsigned boff = tid < 576 ? WS_B + (unsigned)((cg * 72 + (tid >> 3)) * 4096 + (rt64 >> 3) * 1024 + (((tid >> 2) & 1) * 32 + ((rt64 >> 2) & 1) * 16 + (rt64 & 3) * 4 + (tid & 3)) * 16) : WS_END;
    unsigned goff = WS_GATE + (unsigned)((rt64 * 64 + r0) * 4);
    unsigned qd = gemm ? __hip_atomic_load((LAS unsigned*)(smem + FL + F_QD), __ATOMIC_RELAXED, __HIP_MEMORY_SCOPE_WORKGROUP) : 8u;
    asm volatile("" : "+v"(boff), "+v"(goff));
    STAMP(3);
    if (w == 3) {
        const int a0 = cg * 72, g0 = (a0 >> 6) * 11 + (a0 & 63) / 6, a1 = a0 + 71, g1 = (a1 >> 6) * 11 + (a1 & 63) / 6;
        poll_addr<OSLEEP>(P, lane < 8 ? WS_HINT + ((rt64 >> 1) * 8 + lane) * 128 : WS_BFL + min(g0 + lane - 8, g1) * 128, tag);
        flag_set(F_H, 1);
    } else flag_wait<1>(smem, F_H, 1);
    STAMP(4);
    u32x4 bv; unsigned gv;
    f32x4 acc[5];
    {
        u32x4 of[6];
#pragma unroll
        for (int i = 0; i < 6; ++i) of[i] = ldo(i);
        bv = ld_sc1(wsr, boff); gv = __builtin_amdgcn_raw_buffer_load_b32(wsr, goff, 0, 16);
        SCHED();
#pragma unroll
        for (int j = 0; j < 5; ++j) acc[j] = f32x4{0.f, 0.f, 0.f, 0.f};
#pragma unroll
        for (int i = 0; i < 6; ++i)
#pragma unroll
            for (int j = 0; j < 5; ++j) acc[j] = mfma16(wf[i][j], of[i], acc[j]);
    }
    if (!__all(acc[0][0] == acc[0][0])) {
#pragma unroll
        for (int j = 0; j < 5; ++j) acc[j] = f32x4{0.f, 0.f, 0.f, 0.f};
        u32x4 of[6];
#pragma unroll 1
        for (unsigned spins = 0; spins < SPIN_CAP; ++spins) {
            CFENCE();
            unsigned m = 0;
#pragma unroll
            for (int i = 0; i < 6; ++i) of[i] = ldo(i);
#pragma unroll
            for (int i = 0; i < 6; ++i) m = max(m, umax4(of[i]));
            if (__all(m != EMPTY)) break;
        }
#pragma unroll
        for (int i = 0; i < 6; ++i)
#pragma unroll
            for (int j = 0; j < 5; ++j) acc[j] = mfma16(rdo(i, j), of[i], acc[j]);
    }
    if ((unsigned)__builtin_amdgcn_readfirstlane(qd) < 8u) flag_wait(smem, F_QD, 8);
    lptr ap = smem + P2_ACC + kt * (64 * P2_AP) + (rt * 16 + lo16) * P2_AP + g * 16;
#pragma unroll
    for (int j = 0; j < 5; ++j) if (j < 4 || g < 2) lds_w128(ap + j * 64, __builtin_bit_cast(u32x4, acc[j]));
    if (w < 9) {
#pragma unroll 1
        for (unsigned spins = 0; spins < SPIN_CAP; ++spins) {
            if (__all(umax4(bv) != EMPTY)) break;
            __builtin_amdgcn_s_sleep(2);
            CFENCE();
            bv = ld_sc1(wsr, boff);
        }
        lptr bp = smem + pb + ((tid & 3) * 16 + ((tid >> 2) & 1) * 8) * 144 + (tid >> 3) * 2;
#pragma unroll
        for (int q = 0; q < 4; ++q) { lds_w16(bp + (2 * q) * 144, bv[q]); lds_w16(bp + (2 * q + 1) * 144, bv[q] >> 16); }
    }
    STAMP(5);
    BARRIER();
    STAMP(6);
    if (w < 9) {
        lptr a = smem + P2_ACC + r0 * P2_AP + c0 * 32;
        const f32x4 s0 = __builtin_bit_cast(f32x4, lds_r128(a)) + __builtin_bit_cast(f32x4, lds_r128(a + 64 * P2_AP)) + __builtin_bit_cast(f32x4, lds_r128(a + 128 * P2_AP));
        const f32x4 s1 = __builtin_bit_cast(f32x4, lds_r128(a + 16)) + __builtin_bit_cast(f32x4, lds_r128(a + 64 * P2_AP + 16)) + __builtin_bit_cast(f32x4, lds_r128(a + 128 * P2_AP + 16));
        const u32x4 bb = lds_r128(smem + pb + tid * 16);
        const float gt = __uint_as_float(gv), og = rbf(1.f - gt);
        u32x4 o;
#pragma unroll
        for (int i = 0; i < 4; ++i) {
            const float a0 = i < 2 ? s0[2 * i] : s1[2 * i - 4], a1 = i < 2 ? s0[2 * i + 1] : s1[2 * i - 3];
            o[i] = pk_bf16(gt * a0 + og * bf2f(bb[i]), gt * a1 + og * bfhi(bb[i]));
        }
        __builtin_amdgcn_raw_buffer_store_b128(o, __builtin_amdgcn_make_buffer_rsrc(P.out, 0, SEQ * DM * 2, 0x00020000), (unsigned)((rt64 * 64 + r0) * 1152 + cg * 144 + c0 * 16), 0, OUTAUX);
    }
    STAMP(7);
    if (w == 4) STAMP_OUT(blockIdx.x < 128 ? 3 : 6, blockIdx.x + 512);
}

DEVI void helper_role(const Params& P, lptr smem, int xcd, int li, u64& tag, rsrc_t wsr) {
    const int tid = threadIdx.x, lane = tid & 63, w = __builtin_amdgcn_readfirstlane(tid >> 6);
    STAMP_DECL; STAMP(1);
    if (li >= 13) {
#pragma unroll 1
        for (int n = 12 * NTW + (li - 13) + 3 * w; n < 171; n += 36) touch_group(P, xcd, n, lane);
    }
    if (li == 12) {
#pragma unroll
        for (int i = 0; i < 7; ++i) {
            const int u = (i * 5 + xcd - 3) * 12 + w;
            if (u < 384) st_sc1(wsr, WS_PART + (unsigned)(u * 1024 + lane * 16), u32x4{EMPTY, EMPTY, EMPTY, EMPTY});
        }
        WAIT_VM(0);
    }
    PIN_TAG();
    BARRIER();
    if (tid == 0) {
        if (li == 12) __hip_atomic_store((GAS u64*)(P.ws + WS_PFL + (xcd - 3) * 128), tag, RLX_AGENT);
        tag_post(P, tag);
    }
    if (li >= 13) {
#pragma unroll 1
        for (int n = 171 + (li - 13) + 3 * w; n < 234; n += 36) touch_group(P, xcd, n, lane);
    }
    STAMP(2);
    if (w == 11) {
        STAMP_OUT(8, blockIdx.x);
        if (blockIdx.x == 255) {
            const GAS u64* f = (const GAS u64*)(P.ws + WS_ARR) + lane;
#pragma unroll 1
            for (unsigned spins = 0; spins < SPIN_CAP; ++spins) {
                const bool ok = __hip_atomic_load(f, RLX_AGENT) == tag && __hip_atomic_load(f + 64, RLX_AGENT) == tag &&
                                __hip_atomic_load(f + 128, RLX_AGENT) == tag && __hip_atomic_load(f + 192, RLX_AGENT) == tag;
                if (__all(ok)) break;
                __builtin_amdgcn_s_sleep(8);
            }
            if (lane == 0) __hip_atomic_store((GAS u64*)(P.ws + WS_CTL), tag, RLX_AGENT);
        }
    }
}

__global__ __launch_bounds__(768, 3) void k_fused(Params P) {
    __shared__ __attribute__((aligned(16))) char smem_[LDS_BYTES];
    lptr smem = (lptr)smem_;
    const int b = blockIdx.x, xcd = b & 7, idx = b >> 3;
    const int lane = threadIdx.x & 63, w = __builtin_amdgcn_readfirstlane(threadIdx.x >> 6);
    const rsrc_t wsr = __builtin_amdgcn_make_buffer_rsrc(P.ws, 0, (int)WS_END, 0x00020000);
    const int rt64 = xcd * 4 + (idx & 3), cg2 = idx >> 2;
    if (idx >= CPF && w < 6) {
        const char* pc; unsigned len;
        asm volatile("s_getpc_b64 %0\n.Lkf_code_begin:\n\ts_mov_b32 %1, .Lkf_code_end-.Lkf_code_begin" : "=s"(pc), "=s"(len));
        touch(pc, min((unsigned)(threadIdx.x * 128), len));
    }
    u64 tag;
    if (__builtin_expect(idx < 16, 1)) {
        const int rbl = idx & 3, hgl = idx >> 2, rb = (xcd >> 1) * 4 + rbl, hg = (xcd & 1) * 4 + hgl;
        if (__builtin_expect(w < 4, 1)) gemm_loader(P, smem, rb, hg, w, cg2, tag, wsr);
        else gemm_compute(P, smem, rb, hg, w - 4, tag, wsr);
    } else {
        tag = *(const __attribute__((address_space(4))) u64*)(P.ws + WS_CTL);
        const int li = idx - 16, gid = li < 11 ? xcd * 11 + li : 88 + (li - 11) * 8 + xcd;
        if (gid < 99) ssm_role(P, smem, xcd, li, gid, cg2, tag, wsr);
        else {
            helper_role(P, smem, xcd, li, tag, wsr);
            if (w < 9) wo_issue<0x1ff>(P, cg2, w, 9, lane);
            if (w == 10 && lane < 16) lds_w32(smem + FL + lane * 4, 0u);
            WAIT_VM(0);
            BARRIER();
        }
    }
    phase2(P, smem, rt64, cg2, idx < 16, tag, wsr);
}

extern "C" void kernel_launch(void* const* d_in, const int* in_sizes, int n_in, void* d_out, int out_size, void* d_ws, size_t ws_size,
                              hipStream_t stream) {
    Params P;
    P.x = (const char*)d_in[0]; P.wqkv = (const char*)d_in[1]; P.wo = (const char*)d_in[2];
    P.logA = (const char*)d_in[4]; P.aim = (const char*)d_in[5]; P.cre = (const char*)d_in[6]; P.cim = (const char*)d_in[7];
    P.logdt = (const char*)d_in[8]; P.dskip = (const char*)d_in[9]; P.gw = (const char*)d_in[10];
    P.out = (char*)d_out; P.ws = (char*)d_ws;
    k_fused<<<256, 768, 0, stream>>>(P);
}
```

```cpp
#include <hip/hip_runtime.h>
#include <stdint.h>

#define GAS __attribute__((address_space(1)))
#define LAS __attribute__((address_space(3)))
#define DEVI __device__ __forceinline__
typedef unsigned short u16;
typedef unsigned long long u64;
typedef __attribute__((ext_vector_type(2))) unsigned u32x2;
typedef __attribute__((ext_vector_type(3))) unsigned u32x3;
typedef __attribute__((ext_vector_type(4))) unsigned u32x4;
typedef __attribute__((ext_vector_type(4))) short s16x4;
typedef __attribute__((ext_vector_type(2))) float f32x2;
typedef __attribute__((ext_vector_type(4))) float f32x4;
typedef __attribute__((ext_vector_type(16))) float f32x16;
typedef __attribute__((ext_vector_type(2))) __bf16 bf16x2;
typedef __attribute__((ext_vector_type(8))) __bf16 bf16x8;
typedef __attribute__((ext_vector_type(2))) _Float16 f16x2;
typedef __attribute__((ext_vector_type(8))) _Float16 f16x8;
typedef LAS char* lptr;

#define PREF 1
#define ROT 0
#define FSLEEP 2
#define NSLEEP "3"
#define LDRIFT 0
#define PSLEEP 6
#define CSLEEP 1
#define OSLEEP 2
#define RSLEEP 6
#define HOLD 1
#define GROT ((w + li) & 3)
#define NTW 8
#define OUTAUX 17
#define TEARLY 1
#define HDELAY 0
#define CPF 16
constexpr int SEQ = 2048, DM = 576;

constexpr unsigned WS_CTL = 0;
constexpr unsigned WS_ARR = 256;
constexpr unsigned WS_BFL = 4096;
constexpr unsigned WS_HINT = 20480;
constexpr unsigned WS_GATE = 36864;
constexpr unsigned WS_PFL = 49152;
constexpr unsigned WS_PART = 65536;
constexpr unsigned WS_O = WS_PART + 8 * 16 * 3072;
constexpr unsigned WS_B = WS_O + SEQ * DM * 2;
constexpr unsigned WS_END = WS_B + SEQ * DM * 2;
constexpr unsigned EMPTY = 0xffffffffu;

constexpr size_t WS_STAMP = 16u << 20;
#define STAMP_DECL
#define STAMP(i)
#define CSTAMP(i)
#define STAMP_OUT(role, slot)

struct Params {
    const char* x; const char* wqkv; const char* wo; const char* logA; const char* aim; const char* cre; const char* cim;
    const char* logdt; const char* dskip; const char* gw; char* out; char* ws;
};

constexpr int LDS_BYTES = 163840, FL = 163584, LDS_TAG = FL + 64, LDS_DUMMY = FL + 128, LDS_JUNK = 161280;
enum { F_SW = 0, F_LANDED = 4, F_W1 = 20, F_B = 28, F_H = 24, F_KVP = 36, F_KVR = 40, F_Q0 = 44, F_QD = 48, F_D = 52, F_C1 = 56, F_W0 = 60 };
constexpr int WO8 = 132096, P2_ACC = 73728, P2_AP = 304, P2_B = 141312, P2_G = 150528;

DEVI float bf2f(unsigned h) { return __uint_as_float(h << 16); }
DEVI float bfhi(unsigned h) { return __uint_as_float(h & 0xffff0000u); }
DEVI unsigned pk_bf16(float a, float b) { return __builtin_bit_cast(unsigned, __builtin_convertvector(f32x2{a, b}, bf16x2)); }
DEVI unsigned pk_f16(f32x2 v) { return __builtin_bit_cast(unsigned, __builtin_convertvector(v, f16x2)); }
DEVI float rbf(float a) { return bf2f(pk_bf16(a, a)); }
DEVI f32x4 mfma16(u32x4 a, u32x4 b, f32x4 c) {
    return __builtin_amdgcn_mfma_f32_16x16x32_bf16(__builtin_bit_cast(bf16x8, a), __builtin_bit_cast(bf16x8, b), c, 0, 0, 0);
}
DEVI void mfma16i(f32x4& c, u32x4 a, u32x4 b) { asm volatile("v_mfma_f32_16x16x32_bf16 %0, %1, %2, %0" : "+v"(c) : "v"(a), "v"(b)); }
#define MFMA_DONE() do { __builtin_amdgcn_sched_barrier(0); asm volatile("s_nop 7\n\ts_nop 1" ::: "memory"); __builtin_amdgcn_sched_barrier(0); } while (0)
DEVI u32x4 lds_r128(lptr p) { return *(LAS u32x4*)p; }
DEVI u32x2 lds_r64(lptr p) { return *(LAS u32x2*)p; }
DEVI float lds_rf(lptr p) { return *(LAS float*)p; }
DEVI void lds_w128(lptr p, u32x4 v) { *(LAS u32x4*)p = v; }
DEVI void lds_w64(lptr p, u32x2 v) { *(LAS u32x2*)p = v; }
DEVI void lds_w32(lptr p, unsigned v) { *(LAS unsigned*)p = v; }
DEVI void lds_w16(lptr p, unsigned v) { *(LAS u16*)p = (u16)v; }
DEVI u32x2 lds_tr(lptr p) { return __builtin_bit_cast(u32x2, __builtin_amdgcn_ds_read_tr16_b64_v4i16((LAS s16x4*)p)); }
DEVI u32x4 lds_trfrag(lptr p, int off2) { u32x2 a = lds_tr(p), b = lds_tr(p + off2); return u32x4{a.x, a.y, b.x, b.y}; }
template <int OFF> DEVI void glds16o(const char* sbase, unsigned voff) {
    asm volatile("global_load_lds_dwordx4 %0, %1 offset:%2" :: "v"(voff), "s"(sbase), "i"(OFF) : "memory");
}
DEVI void glds16(const char* sbase, unsigned voff, unsigned l) {
    asm volatile("s_mov_b32 m0, %2\n\ts_nop 0\n\tglobal_load_lds_dwordx4 %0, %1" :: "v"(voff), "s"(sbase), "s"(l) : "memory");
}
#define SET_M0(v) asm volatile("s_mov_b32 m0, %0\n\ts_nop 0" :: "s"(v) : "memory")
#define W_ADDTID(v, off) asm volatile("ds_write_addtid_b32 %0 offset:%1" :: "v"(v), "i"(off) : "memory")
#define CFENCE() asm volatile("" ::: "memory")
#define SCHED() __builtin_amdgcn_sched_barrier(0)
#define WAIT_VM(n) asm volatile("s_waitcnt vmcnt(" #n ")" ::: "memory")
#define WAIT_LGKM(n) __builtin_amdgcn_s_waitcnt(0xC07F | ((n) << 8))
#define WAIT_LGKM0() asm volatile("s_waitcnt lgkmcnt(0)" ::: "memory")
#define BARRIER() do { SCHED(); asm volatile("s_waitcnt lgkmcnt(0)\n\ts_barrier" ::: "memory"); SCHED(); } while (0)
#define RAW_BARRIER() do { SCHED(); asm volatile("s_barrier" ::: "memory"); SCHED(); } while (0)
#define RLX_AGENT __ATOMIC_RELAXED, __HIP_MEMORY_SCOPE_AGENT
constexpr unsigned SPIN_CAP = 1u << 16;
typedef __amdgpu_buffer_rsrc_t rsrc_t;
DEVI u32x4 ld_sc1(rsrc_t r, unsigned off) { return __builtin_amdgcn_raw_buffer_load_b128(r, off, 0, 16); }
DEVI void st_sc1(rsrc_t r, unsigned off, u32x4 v) { __builtin_amdgcn_raw_buffer_store_b128(v, r, off, 0, 16); }

DEVI u32x4 pack8(f32x4 a, f32x4 b) { return u32x4{pk_bf16(a[0], a[1]), pk_bf16(a[2], a[3]), pk_bf16(b[0], b[1]), pk_bf16(b[2], b[3])}; }
DEVI float phi(float t) {
    const float e = __builtin_amdgcn_fmed3f(__builtin_amdgcn_exp2f(t * 1.44269504089f), 0.f, 1.f);
    return __int_as_float(max(__float_as_int(t), 0x358637bd)) + e;
}
DEVI f32x4 phi4(f32x4 v) { return f32x4{phi(v[0]), phi(v[1]), phi(v[2]), phi(v[3])}; }
DEVI unsigned umax4(u32x4 a) { return max(max(a.x, a.y), max(a.z, a.w)); }
DEVI u32x4 not_empty(u32x4 a) { return u32x4{min(a.x, EMPTY - 1), min(a.y, EMPTY - 1), min(a.z, EMPTY - 1), min(a.w, EMPTY - 1)}; }
DEVI void tag_post(const Params& P, u64 tag) { __hip_atomic_store((GAS u64*)(P.ws + WS_ARR) + blockIdx.x, tag, RLX_AGENT); }
#define PIN_TAG() do { asm volatile("" : "+s"(tag)); tag += 1ull; } while (0)
template <int SL = PSLEEP>
DEVI void poll_addr(const Params& P, unsigned off, u64 tag) {
    const GAS u64* f = (const GAS u64*)(P.ws + off);
#pragma unroll 1
    for (unsigned spins = 0; spins < SPIN_CAP; ++spins) {
        if (__all(__hip_atomic_load(f, RLX_AGENT) == tag)) break;
        __builtin_amdgcn_s_sleep(SL);
    }
}
template <int SL = PSLEEP>
DEVI void poll_flags(const Params& P, unsigned off, int n, u64 tag, int lane) { poll_addr<SL>(P, off + (lane < n ? lane : 0) * 128, tag); }
DEVI unsigned flag_get(lptr smem, int f) { return __builtin_amdgcn_readfirstlane(__hip_atomic_load((LAS unsigned*)(smem + FL + f), __ATOMIC_RELAXED, __HIP_MEMORY_SCOPE_WORKGROUP)); }
template <int SL = FSLEEP>
DEVI void flag_wait(lptr smem, int f, unsigned v) {
#pragma unroll 1
    for (unsigned spins = 0; spins < (SPIN_CAP << 4); ++spins) { if (flag_get(smem, f) >= v) break; if (SL) __builtin_amdgcn_s_sleep(SL); }
    CFENCE();
}
DEVI void flag_add(int f) { asm volatile("s_mov_b64 exec, 1\n\tds_add_u32 %0, %1\n\ts_mov_b64 exec, -1" :: "v"(FL + f), "v"(1u) : "memory"); }
#define FLAG_ADD(f) asm volatile("s_mov_b64 exec, 1\n\tds_add_u32 %0, %1 offset:%2\n\ts_mov_b64 exec, -1" :: "v"(flp), "v"(one), "i"(f) : "memory")
DEVI void flag_set(int f, unsigned v) { asm volatile("s_mov_b64 exec, 1\n\tds_write_b32 %0, %1\n\ts_mov_b64 exec, -1" :: "v"(FL + f), "v"(v) : "memory"); }
#define NEEDP(seen, ptr, f, v) do { unsigned c_; asm volatile( \
        "v_cmp_gt_u32 vcc, %4, %1\n\ts_cbranch_vccnz 0f\n1:\n\t.subsection 1\n" \
        "0:\n\ts_mov_b32 %0, 0x100000\n2:\n\ts_sleep " NSLEEP "\n\tds_read_b32 %1, %2 offset:%3\n\ts_sub_u32 %0, %0, 1\n\ts_waitcnt lgkmcnt(0)\n\ts_cbranch_scc1 1b\n\t" \
        "v_cmp_gt_u32 vcc, %4, %1\n\ts_cbranch_vccnz 2b\n\ts_branch 1b\n\t.subsection 0" \
        : "=&s"(c_), "+v"(seen) : "v"(ptr), "i"(f), "i"(v) : "memory", "scc", "vcc"); } while (0)
#define NEED(seen, f, v) NEEDP(seen, flp, f, v)
DEVI void touch(const char* sbase, unsigned voff) {
    asm volatile("s_mov_b32 m0, %2\n\ts_nop 0\n\tglobal_load_lds_dword %0, %1" :: "v"(voff), "s"(sbase), "s"(LDS_JUNK) : "memory");
}
DEVI const char* group_addr(const Params& P, int xcd, int n, int lane) {
    if (n < 126) {
        const int c0 = n / 18, r = n - c0 * 18, c = c0 + 2, l = r - 8;
        return r < 8 ? P.x + (unsigned)(((xcd >> 1) * 512 + r * 64 + lane) * 1152 + c * 128)
                     : P.wqkv + (unsigned)((c * 64 + lane) * 3456 + (l < 5 ? 1152 : 2304 - 640) + (xcd & 1) * 512 + l * 128);
    }
    const int c = (n - 126) / 5, l = n - 126 - c * 5;
    return P.wqkv + (unsigned)((c * 64 + lane) * 3456 + (xcd & 1) * 512 + l * 128);
}
DEVI void touch_group(const Params& P, int xcd, int n, int lane) {
    if (n < 126) {
        const int c0 = n / 18, r = n - c0 * 18, c = c0 + 2;
        if (r < 8) touch(P.x, (unsigned)(((xcd >> 1) * 512 + r * 64 + lane) * 1152 + c * 128));
        else { const int l = r - 8; touch(P.wqkv, (unsigned)((c * 64 + lane) * 3456 + (l < 5 ? 1152 : 2304 - 640) + (xcd & 1) * 512 + l * 128)); }
    } else if (n < 171) {
        const int c = (n - 126) / 5, l = n - 126 - c * 5;
        touch(P.wqkv, (unsigned)((c * 64 + lane) * 3456 + (xcd & 1) * 512 + l * 128));
    } else {
        const int l = (n - 171) / 9, c = n - 171 - l * 9;
        touch(P.wo, (unsigned)((c * 64 + lane) * 1152 + l * 128));
    }
}
template <unsigned CH>
DEVI void wo_issue(const Params& P, int cg, int first, int stride, int lane) {
#pragma unroll 1
    for (int m = first; m < 9; m += stride) {
        const int q = 64 * m + lane, rho = q / 9, pc = q - rho * 9;
        const int k = (rho & 32) + 8 * ((rho >> 3) & 3) + 4 * (rho & 1) + ((rho >> 1) & 3);
        const unsigned qo = (unsigned)(k * 1152 + cg * 144 + pc * 16);
#pragma unroll
        for (int c = 0; c < 9; ++c) if ((CH >> c) & 1) glds16(P.wo + c * (64 * 1152), qo, (unsigned)((c < 6 ? c * 9216 : WO8 + (c - 6) * 9216) + m * 1024));
    }
}

constexpr int SP = 3088;
constexpr int XT_OFF = 133632, XT_BYTES = 4608, XPAD = 256;
constexpr int TT_OFF = 104448, TC = 1216, PT_OFF = 55296, PC_OFF = 2560, ACX_OFF = 55296;

#define CM2(o1, p1, a1, b1, o2, p2, a2, b2) do { asm( \
    "v_pk_mul_f32 %0, %2, %3 op_sel:[1,1] op_sel_hi:[1,0] neg_lo:[0,1]\n\tv_pk_mul_f32 %1, %4, %5 op_sel:[1,1] op_sel_hi:[1,0] neg_lo:[0,1]\n\t" \
    "v_pk_fma_f32 %0, %2, %3, %0 op_sel_hi:[0,1,1]\n\tv_pk_fma_f32 %1, %4, %5, %1 op_sel_hi:[0,1,1]" \
    : "=&v"(o1), "=&v"(o2) : "v"(a1), "v"(b1), "v"(a2), "v"(b2)); \
    asm("v_cvt_pk_f16_f32 %0, %2, %3\n\tv_cvt_pk_f16_f32 %1, %4, %5" : "=&v"(p1), "=&v"(p2) : "v"((o1).x), "v"((o1).y), "v"((o2).x), "v"((o2).y)); } while (0)
#define HM2(p1, a1, p2, a2, b) asm( \
    "v_pk_mul_f16 %0, %2, %4 op_sel_hi:[0,1]\n\tv_pk_mul_f16 %1, %3, %4 op_sel_hi:[0,1]\n\t" \
    "v_pk_fma_f16 %0, %2, %4, %0 op_sel:[1,1,0] op_sel_hi:[1,0,1] neg_lo:[0,1,0]\n\tv_pk_fma_f16 %1, %3, %4, %1 op_sel:[1,1,0] op_sel_hi:[1,0,1] neg_lo:[0,1,0]" \
    : "=&v"(p1), "=&v"(p2) : "v"(a1), "v"(a2), "v"(b))
DEVI f32x2 cexp_p(float a2, float f, float p) {
    float m = __builtin_amdgcn_exp2f(a2 * p);
    float r = __builtin_amdgcn_fractf(f * p);
    return f32x2{m * __builtin_amdgcn_cosf(r), m * __builtin_amdgcn_sinf(r)};
}
DEVI u32x4 mask_frag(unsigned bits, int g, unsigned val) {
    unsigned by = bits >> (8 * g);
    u32x4 r;
#pragma unroll
    for (int i = 0; i < 4; ++i) { unsigned t = (by >> (2 * i)) & 3u; r[i] = ((t | (t << 15)) & 0x10001u) * val; }
    return r;
}
struct Tab2 { unsigned v[8][64][4]; };
constexpr Tab2 make_tab2() {
    Tab2 t{};
    for (int l = 0; l < 64; ++l) {
        const int r = l & 15, g = l >> 4, j = l & 31, h = l >> 5;
        const unsigned bits[6] = {7u << (r + 11), 31u << (r + 6), 7u << (r + 3), 127u << (r + 5), 511u << (r + 4), 3u << (r + 12)};
        for (int m = 0; m < 6; ++m)
            for (int i = 0; i < 4; ++i) {
                const unsigned q = (bits[m] >> (8 * g + 2 * i)) & 3u, val = m < 5 ? 0x3f80u : 0xffffu;
                t.v[m][l][i] = (q & 1 ? val : 0u) | (q & 2 ? val << 16 : 0u);
            }
        for (int c = 0; c < 4; ++c) {
            const int v2 = 79 - (32 + (j >> 1)) - c + 4, v3 = 127 - (64 + 32 * h + (j >> 1)) - c + 4;
            t.v[6][l][c] = PT_OFF + (j & 1) * 1280 + c * 320 + (v2 >> 2) * 16 + h * 8 + (v2 & 3) * 2 - 64;
            t.v[7][l][c] = PT_OFF + PC_OFF + c * 320 + (v3 >> 2) * 16 + (j & 1) * 8 + (v3 & 3) * 2 - 64;
        }
    }
    return t;
}
__device__ const Tab2 g_tab2 = make_tab2();
DEVI float wave_max(float v) {
    v = fmaxf(v, __int_as_float(__builtin_amdgcn_update_dpp(0, __float_as_int(v), 0xB1, 0xf, 0xf, true)));
    v = fmaxf(v, __int_as_float(__builtin_amdgcn_update_dpp(0, __float_as_int(v), 0x4E, 0xf, 0xf, true)));
    v = fmaxf(v, __int_as_float(__builtin_amdgcn_update_dpp(0, __float_as_int(v), 0x124, 0xf, 0xf, true)));
    v = fmaxf(v, __int_as_float(__builtin_amdgcn_update_dpp(0, __float_as_int(v), 0x128, 0xf, 0xf, true)));
    const int i = __float_as_int(v);
    const int a = max(__builtin_amdgcn_readlane(i, 0), __builtin_amdgcn_readlane(i, 16)), b = max(__builtin_amdgcn_readlane(i, 32), __builtin_amdgcn_readlane(i, 48));
    return __int_as_float(max(a, b));
}

DEVI void ssm_role(const Params& P, lptr smem, int xcd, int li, int gid, int cg2, u64& tag, rsrc_t wsr, u64& tp) {
    const int gl = gid / 11, gi = gid - gl * 11, ch0 = gl * 64 + (gi < 10 ? gi * 6 : 58);
    const int tid = threadIdx.x, lane = tid & 63, w = __builtin_amdgcn_readfirstlane(tid >> 6);
    const int ch = w >> 1, half = (w ^ (w >> 2)) & 1, d = ch0 + ch;
    const bool own = gi < 10 || ch >= 2;
    const int lo16 = lane & 15, g = lane >> 4;
    STAMP_DECL; STAMP(1);
    __builtin_amdgcn_s_setprio(3);
    const unsigned po = (unsigned)(d * 256 + lane * 4);
    const unsigned r_la = *(const GAS unsigned*)(P.logA + po), r_ai = *(const GAS unsigned*)(P.aim + po);
    const unsigned r_cr = *(const GAS unsigned*)(P.cre + po), r_ci = *(const GAS unsigned*)(P.cim + po);
    const unsigned r_dt = *(const GAS u16*)(P.logdt + d * 2), r_ds = *(const GAS u16*)(P.dskip + d * 2);
    u32x4 ct[8];
    if (half) {
        const char* tp = (const char*)&g_tab2;
        asm volatile("" : "+s"(tp));
#pragma unroll
        for (int i = 0; i < 8; ++i) ct[i] = *(const GAS u32x4*)(tp + (unsigned)(i * 1024 + lane * 16));
    }
    const char* tga = (w < NTW && li < 12) ? group_addr(P, xcd, w * 12 + li, lane) : P.logA + po;
    RAW_BARRIER();
    const unsigned junk = *(const GAS unsigned*)tga;
    SCHED();
    const int tt = (tid + (li & 7) * 64) & 511;
    u32x3 xr[4];
    const unsigned xoff = (unsigned)(tt * 4608 + ch0 * 2);
    const unsigned boff = own ? WS_B + (unsigned)(d * 4096 + half * 2048 + lane * 16) : WS_END;
#define GATHER(i) do { SCHED(); if (w < 8) xr[i] = *(const GAS u32x3*)(P.x + xoff + (i) * 1152); SCHED(); } while (0)
#define PRIO(i) do { SCHED(); __builtin_amdgcn_s_setprio(i); SCHED(); } while (0)
    SCHED();
    lptr xt = smem + XT_OFF + ch * XT_BYTES, tb = smem + TT_OFF + ch * 4864, pt = smem + PT_OFF + ch * 3840;
    const int j32 = lane & 31, h = lane >> 5;
    if (!half) { lds_w64(xt + lane * 8, u32x2{0u, 0u}); lds_w32(smem + FL + 32, 0u); }
    int arow;
    {
        const u64 map0 = 0x0a09080803020100ull, map1 = 0x0c0b0a0a07060504ull, map2 = 0x1413110e12100f0dull;
        const u64 mm = j32 < 8 ? map0 : (j32 < 16 ? map1 : map2);
        arow = (int)((mm >> (8 * (j32 & 7))) & 0xff);
    }
    lptr ap = smem + arow * SP + w * 256 + h * 16, bp = smem + j32 * SP + w * 256 + h * 16;
    const int cpy = (3 - lo16) & 3;
    lptr xf7 = xt + (XPAD - 16 + 16 * lo16 + 8 * g) * 2 - 64 * 7;
    lptr tf = tb + cpy * TC + 2 * (527 - lo16 + 8 * g - cpy) - 64 * 16;
    lptr xc = smem + (w ^ 1) * 256 + lane * 4, xb = smem + XT_OFF + (XPAD + tt * 4) * 2;
    asm volatile("" : "+v"(ap), "+v"(bp), "+v"(xf7), "+v"(tf), "+v"(xc), "+v"(xb));
    const float dt = rbf(__expf(bf2f(r_dt)));
    const float cm = wave_max(fmaxf(fmaxf(fabsf(bf2f(r_cr)), fabsf(bfhi(r_cr))), fmaxf(fabsf(bf2f(r_ci)), fabsf(bfhi(r_ci)))));
    unsigned ce = __float_as_uint(cm) >> 23; ce = ce < 16u ? 16u : (ce > 240u ? 240u : ce);
    const float cs = __uint_as_float((263u - ce) << 23), ics = __uint_as_float((ce - 9u) << 23);
    f32x16 kacc;
#pragma unroll
    for (int i = 0; i < 16; ++i) kacc[i] = 0.f;
    STAMP(2);
    {
        const unsigned sh = half * 16;
        asm volatile("" :: "v"(r_la), "v"(r_ai), "v"(r_ds), "v"(ct[7]));
        const float la = bf2f(r_la >> sh), ai = bf2f(r_ai >> sh), cr = bf2f(r_cr >> sh) * cs, ci = bf2f(r_ci >> sh) * cs;
        const float a2 = (-rbf(__expf(la)) * dt) * 1.44269504089f, f = (ai * dt) * 0.15915494309f;
#define PUT(row, v) W_ADDTID(v, ((row) & 15) * SP)
        const f32x2 w1 = cexp_p(a2, -f, 1.f), W = cexp_p(a2, -f, 256.f);
        f32x2 u[8], v[4], c[9], e4, e8, t2;
        unsigned U[8], V[4], p, q;
        u[1] = cexp_p(a2, f, 8.f); v[1] = cexp_p(a2, f, 64.f);
        c[0] = f32x2{cr, -ci};
        U[1] = pk_f16(u[1]); V[1] = pk_f16(v[1]);
        GATHER(0);
        SET_M0(w * 256);
        PUT(0, pk_f16(c[0]));
        CM2(c[1], p, c[0], w1, u[2], U[2], u[1], u[1]); PUT(1, p);
        CM2(c[2], p, c[1], w1, u[3], U[3], u[2], u[1]); PUT(2, p);
        CM2(c[3], p, c[2], w1, u[4], U[4], u[2], u[2]); PUT(3, p);
        CM2(c[4], p, c[3], w1, u[5], U[5], u[4], u[1]); PUT(4, p);
        CM2(c[5], p, c[4], w1, u[6], U[6], u[4], u[2]); PUT(5, p);
        CM2(c[6], p, c[5], w1, u[7], U[7], u[4], u[3]); PUT(6, p);
        CM2(c[7], p, c[6], w1, v[2], V[2], v[1], v[1]); PUT(7, p);
        CM2(c[8], p, c[7], w1, v[3], V[3], v[2], v[1]);
        PRIO(2);
        GATHER(1);
        CM2(e8, p, c[8], W, e4, q, c[4], W); PUT(12, p); PUT(10, q);
        CM2(c[8], p, e8, W, c[4], q, e4, W); PUT(14, p); PUT(13, q);
        CM2(t2, p, c[0], W, e4, q, c[4], W); PUT(8, p); PUT(15, q);
        CM2(t2, p, c[2], W, e4, q, c[6], W); PUT(9, p); PUT(11, q);
        SET_M0(w * 256 + 16 * SP);
        CM2(e8, p, c[8], W, t2, q, W, W); PUT(17, p);
        CM2(e4, p, c[8], t2, c[0], q, e8, t2); PUT(16, p); PUT(18, q);
        CM2(c[8], p, e4, t2, e8, q, c[0], t2); PUT(19, p); PUT(20, q);
        PRIO(1);
        GATHER(2);
        u32x4 af[8];
#pragma unroll
        for (int s = 0; s < 8; ++s) af[s] = lds_r128(ap + 32 * s);
        CFENCE();
        PUT(16, V[2]); PUT(24, V[3]);
#pragma unroll
        for (int b = 1; b < 8; ++b) { HM2(p, V[2], q, V[3], U[b]); PUT(16 + b, p); PUT(24 + b, q); }
        SET_M0(w * 256);
        PRIO(0);
        GATHER(3);
        PUT(0, 0x3c00u); PUT(8, V[1]);
#pragma unroll
        for (int b = 1; b < 8; b += 2) { HM2(p, U[b], q, U[b == 7 ? 7 : b + 1], V[1]); PUT(8 + b, p); if (b < 7) PUT(9 + b, q); PUT(b, U[b]); if (b < 7) PUT(b + 1, U[b + 1]); }
#undef PUT
        u32x4 bfr[8];
#pragma unroll
        for (int s = 0; s < 8; ++s) bfr[s] = lds_r128(bp + 32 * s);
        SCHED();
        SCHED();
#pragma unroll
        for (int s = 0; s < 8; ++s)
            kacc = __builtin_amdgcn_mfma_f32_32x32x16_f16(__builtin_bit_cast(f16x8, af[s]), __builtin_bit_cast(f16x8, bfr[s]), kacc, 0, 0, 0);
    }
    STAMP(3);
    const float ks = 2.f * dt * ics;
#pragma unroll
    for (int i = 0; i < 12; ++i) kacc[i] *= ks;
    if (half) {
#pragma unroll
        for (int i = 0; i < 8; ++i) W_ADDTID(kacc[i], i * SP);
    } else {
#pragma unroll
        for (int i = 0; i < 4; ++i) W_ADDTID(kacc[8 + i], i * SP);
    }
    PIN_TAG();
    STAMP(4);
    BARRIER();
    STAMP(5);
    if (tid == 704) tag_post(P, tag);
    auto cleared = [&]() {
        if (w < 4) {
            WAIT_VM(0);
            if (lane == 0 && __hip_atomic_fetch_add((LAS unsigned*)(smem + FL + 32), 1u, __ATOMIC_RELAXED, __HIP_MEMORY_SCOPE_WORKGROUP) == 3u)
                __hip_atomic_store((GAS u64*)(P.ws + WS_BFL + gid * 128), tag, RLX_AGENT);
        }
    };
    auto x_in = [&]() {
        asm volatile("" :: "v"(junk));
        if (w < 8) {
#pragma unroll
            for (int k = 0; k < 3; ++k) {
                u32x2 lo = {__builtin_amdgcn_perm(xr[1][k], xr[0][k], 0x05040100u), __builtin_amdgcn_perm(xr[3][k], xr[2][k], 0x05040100u)};
                u32x2 hi = {__builtin_amdgcn_perm(xr[1][k], xr[0][k], 0x07060302u), __builtin_amdgcn_perm(xr[3][k], xr[2][k], 0x07060302u)};
                lds_w64(xb + (2 * k) * XT_BYTES, lo); lds_w64(xb + (2 * k + 1) * XT_BYTES, hi);
            }
        }
        if (w < 4) {
#pragma unroll
            for (int i = 0; i < 6; ++i)
                if (i >= 2 || gi < 10) __builtin_amdgcn_raw_buffer_store_b128(u32x4{EMPTY, EMPTY, EMPTY, EMPTY}, wsr, (unsigned)(tid * 16), WS_B + (ch0 + i) * 4096, 16);
        }
        BARRIER();
        if (w >= 8) wo_issue<0x03f>(P, cg2, w - 8, 4, lane);
    };
    auto TF = [&](int off) { u32x2 a = lds_r64(tf + off), b = lds_r64(tf + off + 8); return u32x4{a.x, a.y, b.x, b.y}; };
    f32x4 acc[8];
#pragma unroll
    for (int i = 0; i < 8; ++i) acc[i] = f32x4{0.f, 0.f, 0.f, 0.f};
    lptr axr;
    if (!half) {
        __builtin_amdgcn_s_setprio(2);
#pragma unroll
        for (int i = 0; i < 8; ++i) kacc[i] += lds_rf(xc + i * SP);
        unsigned dd[6];
        dd[0] = pk_bf16(kacc[7], kacc[6]); dd[1] = pk_bf16(kacc[5], kacc[4]); dd[2] = pk_bf16(kacc[3], kacc[2]);
        dd[3] = pk_bf16(kacc[1], kacc[0] + (lane == 0 ? bf2f(r_ds) : 0.f));
        dd[4] = __builtin_amdgcn_update_dpp(0, dd[0], 0x138, 0xf, 0xf, true);
        dd[5] = __builtin_amdgcn_update_dpp(0, dd[1], 0x138, 0xf, 0xf, true);
        unsigned mm[5];
#pragma unroll
        for (int i = 0; i < 5; ++i) mm[i] = __builtin_amdgcn_alignbit(dd[i + 1], dd[i], 16);
        lptr tw = tb + 2 * (536 - 8 * lane);
        lds_w128(tw, u32x4{dd[0], dd[1], dd[2], dd[3]}); lds_w128(tw + TC, u32x4{mm[0], mm[1], mm[2], mm[3]});
        lds_w128(tw + 2 * TC, u32x4{dd[1], dd[2], dd[3], dd[4]}); lds_w128(tw + 3 * TC, u32x4{mm[1], mm[2], mm[3], mm[4]});
        {
            const unsigned n0 = __builtin_amdgcn_readlane(dd[0], 63), n1 = __builtin_amdgcn_readlane(dd[1], 63);
            const unsigned kk = (n0 & 0xffffu) * 0x10001u, kh = n0 << 16, m4 = (n0 >> 16) | (n1 << 16);
            const int c = lane & 3, bl = lane >> 2;
            u32x4 e;
            e.x = 0u; e.y = lane == 14 ? kh : (lane == 15 ? kk : 0u); e.z = lane == 12 ? kh : (bl == 3 ? kk : 0u);
            e.w = lane == 14 ? n0 : (lane == 15 ? m4 : (bl == 3 ? kk : 0u));
            if (bl < 6) lds_w128(tb + c * TC + bl * 16 + (bl >> 2) * 1024, e);
        }
        x_in();
        u32x4 X[8];
#pragma unroll
        for (int i = 0; i < 8; ++i) X[i] = lds_r128(xf7 + 64 * 6 + 512 * i);
        u32x4 k0 = TF(1024 - 64), k8 = TF(512 - 64);
#pragma unroll
        for (int i = 0; i < 8; ++i) asm volatile("" : "+v"(acc[i]));
        __builtin_amdgcn_s_setprio(0);
        STAMP(6);
#pragma unroll
        for (int c = 1; c < 8; ++c) {
            u32x4 k0n, k8n;
            if (c < 7) { k0n = TF(1024 - 64 * (c + 1)); k8n = TF(512 - 64 * (c + 1)); }
#pragma unroll
            for (int i = 0; i < 8; ++i) {
                SCHED();
                mfma16i(acc[i], k0, X[i]);
                if (i < 7) mfma16i(acc[i + 1], k8, X[i]);
                SCHED();
                if (c < 7) X[i] = lds_r128(xf7 + 64 * (6 - c) + 512 * i);
            }
            k0 = k0n; k8 = k8n;
            if (c == 5) { SCHED(); cleared(); SCHED(); }
        }
        MFMA_DONE();
        SET_M0(ACX_OFF - 40960 + ch * 8192);
#pragma unroll
        for (int i = 0; i < 16; ++i) W_ADDTID(acc[4 + (i >> 2)][i & 3], 40960 + i * 256);
        axr = smem + ACX_OFF + ch * 8192 + 4096 + lane * 4;
    } else {
        u32x4 X[8];
#pragma unroll
        for (int i = 0; i < 4; ++i) if (i < 3 || lane < 48) lds_w128(pt + i * 1024 + lane * 16, u32x4{0u, 0u, 0u, 0u});
        CFENCE();
        const unsigned p89 = pk_bf16(kacc[8] + lds_rf(xc), kacc[9] + lds_rf(xc + SP)), pab = pk_bf16(kacc[10] + lds_rf(xc + 2 * SP), kacc[11] + lds_rf(xc + 3 * SP));
#pragma unroll
        for (int c = 0; c < 4; ++c) {
            lptr q2 = smem + (ct[6][c] + ch * 3840), q3 = smem + (ct[7][c] + ch * 3840);
            lds_w16(q2 + 64, p89); lds_w16(q2, p89 >> 16);
            lds_w16(q3 + 64, pab); lds_w16(q3, pab >> 16);
        }
        CFENCE();
        const u32x4 FX = ct[5] & (((unsigned)__builtin_amdgcn_readlane(p89, 63) >> 16) * 0x10001u);
        lptr pf2 = pt + cpy * 320 + ((79 - 64 - lo16 + 4 * g - cpy + 4) >> 2) * 16;
        u32x4 TA[3], TB[3], TCc[4];
#pragma unroll
        for (int i = 0; i < 3; ++i) { TA[i] = lds_r128(pf2 + 64 * (2 - i)); TB[i] = lds_r128(pf2 + 1280 + 64 * (2 - i)); }
#pragma unroll
        for (int i = 0; i < 4; ++i) TCc[i] = lds_r128(pf2 + PC_OFF + 64 * (3 - i));
        x_in();
#pragma unroll
        for (int i = 0; i < 8; ++i) X[i] = lds_r128(xf7 + 64 * 7 + 512 * i);
        STAMP(6);
        const f32x4 z4 = {0.f, 0.f, 0.f, 0.f};
#pragma unroll
        for (int st = 0; st < 6; ++st) {
            const u32x4 Xc = lds_r128(xf7 + 64 * 7 + 512 * st - 16);
            const u32x4 pa = pack8(mfma16(X[st], ct[0], z4), mfma16(X[st], ct[1], z4));
            const u32x4 pb = pack8(mfma16(X[st], ct[2], z4), mfma16(Xc, ct[1], z4));
#pragma unroll
            for (int dl = 2; dl <= 4; ++dl) if (st + dl < 8) {
                acc[st + dl] = mfma16(pa, TA[dl - 2], acc[st + dl]);
                acc[st + dl] = mfma16(pb, TB[dl - 2], acc[st + dl]);
            }
            if (st < 4) {
                const u32x4 pc = pack8(mfma16(X[st], ct[3], z4), mfma16(Xc, ct[4], z4));
#pragma unroll
                for (int dl = 4; dl <= 7; ++dl) if (st + dl < 8) acc[st + dl] = mfma16(pc, TCc[dl - 4], acc[st + dl]);
            }
        }
        SCHED(); cleared(); SCHED();
#pragma unroll
        for (int i = 4; i < 8; ++i) acc[i] = mfma16(FX, X[i - 4], acc[i]);
        const u32x4 k0 = TF(1024), k8 = TF(512), k16 = TF(0);
#pragma unroll
        for (int i = 0; i < 8; ++i) acc[i] = mfma16(k0, X[i], acc[i]);
#pragma unroll
        for (int i = 1; i < 8; ++i) acc[i] = mfma16(k8, X[i - 1], acc[i]);
#pragma unroll
        for (int i = 2; i < 8; ++i) acc[i] = mfma16(k16, X[i - 2], acc[i]);
        MFMA_DONE();
        SET_M0(ACX_OFF - 40960 + ch * 8192 + 4096);
#pragma unroll
        for (int i = 0; i < 16; ++i) W_ADDTID(acc[i >> 2][i & 3], 40960 + i * 256);
#pragma unroll
        for (int i = 0; i < 4; ++i) acc[i] = acc[4 + i];
        axr = smem + ACX_OFF + ch * 8192 + lane * 4;
    }
    STAMP(7);
    if (w == 0 && lane < 8) lds_w32(smem + FL + lane * 4, 0u);
    BARRIER();
    STAMP(8);
    if (w >= 8) wo_issue<0x1c0>(P, cg2, w - 8, 4, lane);
#pragma unroll
    for (int i = 0; i < 16; ++i) acc[i >> 2][i & 3] += lds_rf(axr + i * 256);
#pragma unroll
    for (int i = 0; i < 2; ++i) {
        const f32x4 ya = acc[2 * i], yb = acc[2 * i + 1];
        const auto sx = __builtin_amdgcn_permlane16_swap(pk_bf16(ya[0], ya[1]), pk_bf16(yb[0], yb[1]), false, false);
        const auto sy = __builtin_amdgcn_permlane16_swap(pk_bf16(ya[2], ya[3]), pk_bf16(yb[2], yb[3]), false, false);
        st_sc1(wsr, boff + i * 1024, not_empty(u32x4{sx[0], sy[0], sx[1], sy[1]}));
    }
    WAIT_VM(2);
    BARRIER();
    STAMP(9);
    if (w < 2) STAMP_OUT(2 + 2 * w, blockIdx.x + 256 * w);
}

constexpr int SLOT = 34816, XCH = 16384, KVT_OFF = 151296, KVP_OFF = 27648;
DEVI constexpr int slot_off(int s) { return (s & 3) == 0 ? SLOT : ((s & 3) == 1 ? 0 : (s & 3) * SLOT); }
DEVI constexpr int wq_off(int t) { return t == 0 ? 0 : 49152 + t * 27648; }

constexpr int quad_src(int quad) {
    const int pp = quad >= 192 ? 1 : 0, qi = quad - 192 * pp;
    const bool isz = qi >= 180;
    const int el = isz ? 0 : qi / 5, qd = qi - el * 5, hi = el >= 18 ? 1 : 0;
    const int dql = isz ? (qi < 189 ? qi - 180 : 0) : qd + 4 * hi;
    const int et = isz ? 0 : (el < 4 ? 0 : (el < 20 ? 1 : 2)), col = isz ? 0 : (el < 4 ? 12 + el : (el < 20 ? el - 4 : el - 20));
    const int tile = dql == 8 ? 5 : (et == 0 ? (dql >> 2) : (et == 1 ? 2 + (dql >> 2) : 4));
    return KVP_OFF + pp * 6144 + tile * 1024 + ((dql == 8 ? et : (dql & 3)) * 16 + col) * 4;
}
constexpr int quad_dst(int quad) {
    const int pp = quad >= 192 ? 1 : 0, qi = quad - 192 * pp;
    const bool isz = qi >= 180;
    const int el = isz ? 0 : qi / 5, qd = qi - el * 5, hi = el >= 18 ? 1 : 0;
    const int dql = isz ? (qi < 189 ? qi - 180 : 0) : qd + 4 * hi;
    const int dq = 9 * pp + dql, ks = dq >> 3, e = 36 * pp + el, ee = e >> 4;
    const int fr = ee == 0 ? 0 : (ee < 3 ? 2 * ee - 1 + ks : 2 * ee - 2 + ks);
    const int a = KVT_OFF + (isz ? (9 + ks) * 1024 + (dq & 3) * 256 + (2 * pp + (dql > 4 ? 1 : 0)) * 16 : fr * 1024 + ((dq & 3) * 16 + (e & 15)) * 16) + ((dq >> 2) & 1) * 8;
    return qi >= 189 ? LDS_DUMMY : (a | (isz ? (dql == 4 ? 0x300003 : 0x100003) : ((!hi && qd == 4) ? 1 : ((hi && qd == 0) ? 2 : 3))));
}
struct Tab { int v[2048]; };
constexpr Tab make_tab() {
    Tab t{};
    for (int c = 0; c < 192; ++c) {
        const int l = c & 63, q = 2 * ((c & 192) + (l >> 5) * 16 + (l & 15)) + ((l >> 4) & 1);
        t.v[4 * c] = quad_src(2 * c); t.v[4 * c + 1] = quad_src(2 * c + 1); t.v[4 * c + 2] = quad_dst(q); t.v[4 * c + 3] = quad_dst(q + 64);
    }
    for (int q = 0; q < 576; ++q) {
        const int rho = q / 9, pc = q % 9, k = (rho & 32) + 8 * ((rho >> 3) & 3) + 4 * (rho & 1) + ((rho >> 1) & 3), m = q >> 6;
        t.v[768 + q] = k * 3456 + pc * 16 - (m & 1) * 1024;
        t.v[1344 + q] = k * 1152 + pc * 16 - (m & 3) * 1024;
    }
    return t;
}
__device__ const Tab g_tab = make_tab();
constexpr int IDX = P2_B, GW_OFF = 139264;

DEVI void gemm_compute(const Params& P, lptr smem, int rb, int hg, int w, u64& tag, rsrc_t wsr, u64& tp) {
    const int lane = threadIdx.x & 63, lo16 = lane & 15, g = lane >> 4;
    const int rg = w >> 1, p = w & 1;
    STAMP_DECL; STAMP(1);
    if (w == 0 && lane < 16) lds_w32(smem + FL + lane * 4, 0u);
    if (w >= 4) {
#pragma unroll
        for (int i = 0; i < 3; ++i) lds_w128(smem + KVT_OFF + i * 4096 + (w - 4) * 1024 + lane * 16, u32x4{0u, 0u, 0u, 0u});
    }
    __builtin_amdgcn_s_setprio(2);
    const int row0 = 16 * (2 * rg + p) + lo16, row1 = 16 * (2 * rg + 1 - p) + lo16;
    const int sw = (g ^ ((lo16 >> 1) & 7)) << 4;
    lptr xa0[2] = {smem + row0 * 128 + sw, smem + row0 * 128 + (sw ^ 64)}, xa1[2] = {smem + row1 * 128 + sw, smem + row1 * 128 + (sw ^ 64)};
    const int wrow = (4 * g + (lo16 >> 2)) * 288;
    lptr wa = smem + XCH + wrow + (9 * p + (lane & 3)) * 8, wa2 = smem + XCH + wrow + (9 * p + ((lane & 3) == 3 ? 18 : 8)) * 8;
    lptr flp = smem + FL;
    asm volatile("" : "+v"(flp));
    const f32x4 z4 = {0.f, 0.f, 0.f, 0.f};
    f32x4 acc[2][5];
#pragma unroll
    for (int i = 0; i < 2; ++i)
#pragma unroll
        for (int j = 0; j < 5; ++j) { const float e = j < 2 ? 1e-6f : (j == 2 && lo16 < 12 ? 1e-6f : 0.f); acc[i][j] = f32x4{e, e, e, e}; }
    u32x4 xk[18], x1[2];
    auto rel = [&](int s) { return (s & 2) ? slot_off(s) - 2 * SLOT : slot_off(s); };
    auto rebase = [&](int dlt) {
        xa0[0] += dlt; xa0[1] += dlt; xa1[0] += dlt; xa1[1] += dlt; wa += dlt; wa2 += dlt;
        asm volatile("" : "+v"(xa0[0]), "+v"(xa0[1]), "+v"(xa1[0]), "+v"(xa1[1]), "+v"(wa), "+v"(wa2));
    };
    constexpr int NB = 8;
    u32x2 wl[NB], wh[NB];
    auto rdw = [&](int f, int t) {
        const int n = f / 5, j = f % 5;
        const int o = rel(n >> 1) + (n & 1) * 9216 + t * 4608;
        const u32x2 v = j == 2 ? lds_tr(wa2 + o) : lds_tr(wa + o + (j == 0 ? 0 : (j == 1 ? 32 : (j == 3 ? 152 : 184))));
        if (t) wh[f % NB] = v; else wl[f % NB] = v;
    };
    auto peek = [&](int f) { return __hip_atomic_load((LAS unsigned*)(flp + f), __ATOMIC_RELAXED, __HIP_MEMORY_SCOPE_WORKGROUP); };
    asm volatile(".subsection 2\n.Lkf_code_end:\n\t.fill 256, 4, 0xbf800000\n\t.subsection 0");
    unsigned one = 1u;
    asm volatile("" : "+v"(one));
    rebase(0);
#pragma unroll
    for (int j = 0; j < 5; ++j) asm volatile("" : "+v"(acc[0][j]), "+v"(acc[1][j]));
    BARRIER();
    STAMP(2);
    xk[0] = lds_r128(xa0[0] + rel(0)); x1[0] = lds_r128(xa1[0] + rel(0));
#pragma unroll
    for (int f = 0; f < NB - 1; ++f) { rdw(f, 0); rdw(f, 1); }
#pragma unroll
    for (int n = 0; n < 18; ++n) {
        const int nx = n + 1, s = n >> 1;
#pragma unroll
        for (int m = 0; m < 10; ++m) {
            const int j = m >> 1, f = 5 * n + j, fn = f + NB - 1;
            SCHED();
            if (!(m & 1) && !(f & 1) && fn < 88) WAIT_LGKM(10);
            const u32x4 wv = {wl[f % NB].x, wl[f % NB].y, wh[f % NB].x, wh[f % NB].y};
            if (m & 1) mfma16i(acc[1][j], x1[n & 1], wv); else mfma16i(acc[0][j], xk[n], wv);
            SCHED();
            if (!(m & 1) && fn % 10 == 0) {
                if (s < 8 && (s & 1)) rebase((s & 2) ? -2 * SLOT : 2 * SLOT);
                if (s == 8) WAIT_LGKM0();
                RAW_BARRIER();
                if (s == 0) STAMP(3);
                if (s == 4) STAMP(4);
            }
            if (fn < 90) rdw(fn, m & 1);
            if (nx < 18) {
                if (m == 0) xk[nx] = lds_r128(xa0[nx & 1] + rel(nx >> 1));
                if (m == 2) x1[nx & 1] = lds_r128(xa1[nx & 1] + rel(nx >> 1));
            }
        }
    }
    MFMA_DONE();
    STAMP(5);
    const int qrow = (2 * (4 * g + (lo16 >> 2))) * 144;
    lptr qw = smem + qrow + (lane & 3) * 8, qw4 = smem + qrow + (16 + (lane & 1)) * 8;
    lptr qwh = qw + wq_off(1), qw4h = qw4 + wq_off(1);
    asm volatile("" : "+v"(qwh), "+v"(qw4h));
    auto rdq = [&](int ks, int j) {
        const int o = (ks < 6 ? ks : ks - 6) * 4608;
        return j < 4 ? lds_trfrag((ks < 6 ? qw : qwh) + o + 32 * j, 144) : lds_trfrag((ks < 6 ? qw4 : qw4h) + o, 144);
    };
    {
        u32x4 pk[3], vb[3];
#pragma unroll
        for (int j = 0; j < 3; ++j) pk[j] = pack8(phi4(acc[0][j]), phi4(acc[1][j]));
        vb[0] = pack8(acc[0][2], acc[1][2]);
        if (lo16 < 12) vb[0] = u32x4{0x3f803f80u, 0x3f803f80u, 0x3f803f80u, 0x3f803f80u};
        vb[1] = pack8(acc[0][3], acc[1][3]); vb[2] = pack8(acc[0][4], acc[1][4]);
        f32x4 r[6];
        const f32x4 t0 = mfma16(pk[2], vb[0], z4), t1 = mfma16(pk[2], vb[1], z4), t2 = mfma16(pk[2], vb[2], z4);
        r[0] = mfma16(pk[0], vb[0], z4); r[1] = mfma16(pk[1], vb[0], z4); r[2] = mfma16(pk[0], vb[1], z4);
        r[3] = mfma16(pk[1], vb[1], z4); r[4] = mfma16(pk[1], vb[2], z4);
        r[5] = g == 0 ? t0 : (g == 1 ? t1 : t2);
        SCHED();
        asm volatile("s_nop 7" ::: "memory");
        SET_M0(KVP_OFF - 8192 + w * 6144);
#pragma unroll
        for (int t = 5; t >= 0; --t) {
            W_ADDTID(r[t][0], 8192 + t * 1024); W_ADDTID(r[t][1], 8192 + t * 1024 + 256);
            W_ADDTID(r[t][2], 8192 + t * 1024 + 512); W_ADDTID(r[t][3], 8192 + t * 1024 + 768);
        }
        FLAG_ADD(F_KVP);
        __builtin_amdgcn_s_setprio(0);
    }
    { asm volatile("s_memrealtime %0\n\ts_waitcnt lgkmcnt(0)" : "=s"(tp) :: "memory"); }
    f32x4 qa[5];
#pragma unroll
    for (int j = 0; j < 5; ++j) qa[j] = f32x4{1e-6f, 1e-6f, 1e-6f, 1e-6f};
#pragma unroll
    for (int j = 0; j < 5; ++j) asm volatile("" : "+v"(qa[j]));
    SCHED();
    u32x4 wq[2][5];
#pragma unroll
    for (int i = 0; i < 2; ++i)
#pragma unroll
        for (int j = 0; j < 5; ++j) wq[i][j] = rdq(i, j);
    SCHED();
    if (w >= 4) flag_wait<1>(smem, F_KVR, 3);
    unsigned seenq = 0;
#pragma unroll
    for (int ks = 0; ks < 18; ++ks) {
        if (ks == 2) seenq = peek(F_KVR);
        if (ks == 4) { SCHED(); FLAG_ADD(F_Q0); NEED(seenq, F_KVR, 3); seenq = peek(F_LANDED); NEED(seenq, F_LANDED, 4); }
        if (ks == 16) { SCHED(); FLAG_ADD(F_QD); }
#pragma unroll
        for (int j = 0; j < 5; ++j) {
            SCHED();
            if (j == 0 && ks < 17) WAIT_LGKM(10);
            mfma16i(qa[j], wq[ks & 1][j], xk[ks]);
            SCHED();
            if (ks + 2 < 18) wq[ks & 1][j] = rdq(ks + 2, j);
        }
    }
    __builtin_amdgcn_s_setprio(2);
    MFMA_DONE();
    u32x4 b0 = pack8(phi4(qa[0]), phi4(qa[1])), b1 = pack8(phi4(qa[2]), phi4(qa[3])), b2 = pack8(phi4(qa[4]), z4);
    lptr kfp = smem + KVT_OFF + lane * 16;
    unsigned ob0 = WS_O + (unsigned)(((rb * 8 + 2 * rg + p) * 72 + hg * 9 + 2 * (g & 1) + (g >> 1)) * 256 + lo16 * 16);
    asm volatile("" : "+v"(b0), "+v"(b1), "+v"(b2), "+v"(kfp), "+v"(ob0));
    { const u32x2 t = lds_r64(smem + LDS_TAG); tag = (u64)(unsigned)__builtin_amdgcn_readfirstlane(t.x) | ((u64)(unsigned)__builtin_amdgcn_readfirstlane(t.y) << 32); }
    STAMP(7);
    flag_wait<1>(smem, F_D, 4);
    STAMP(8);
    {
        u32x4 kf[12];
#pragma unroll
        for (int i = 0; i < 12; ++i) kf[i] = lds_r128(kfp + (i < 3 ? 9 + i : i - 3) * 1024);
        SCHED();
        const f32x4 den = mfma16(kf[2], b2, mfma16(kf[1], b1, mfma16(kf[0], b0, f32x4{1e-6f, 1e-6f, 1e-6f, 1e-6f})));
        f32x4 o[5];
        o[0] = mfma16(kf[3], b0, z4);
        o[1] = mfma16(kf[5], b1, mfma16(kf[4], b0, z4));
        o[2] = mfma16(kf[7], b1, mfma16(kf[6], b0, z4));
        o[3] = mfma16(kf[9], b2, mfma16(kf[8], b1, z4));
        o[4] = mfma16(kf[11], b2, mfma16(kf[10], b1, z4));
        f32x4 r;
#pragma unroll
        for (int i = 0; i < 4; ++i) r[i] = __builtin_amdgcn_rcpf(den[i]);
        const float r1a = g == 0 ? r[0] : r[1], r2 = g == 0 ? r[1] : r[2], r3a = g <= 1 ? r[2] : r[3], r3b = g == 0 ? r[2] : r[3];
        u32x2 ob[5];
        ob[0] = u32x2{pk_bf16(o[0][0] * r[0], o[0][1] * r[0]), pk_bf16(o[0][2] * r[0], o[0][3] * r[0])};
        ob[1] = u32x2{pk_bf16(o[1][0] * r1a, o[1][1] * r1a), pk_bf16(o[1][2] * r[1], o[1][3] * r[1])};
        ob[2] = u32x2{pk_bf16(o[2][0] * r2, o[2][1] * r2), pk_bf16(o[2][2] * r2, o[2][3] * r2)};
        ob[3] = u32x2{pk_bf16(o[3][0] * r3a, o[3][1] * r3a), pk_bf16(o[3][2] * r3b, o[3][3] * r3b)};
        ob[4] = u32x2{pk_bf16(o[4][0] * r[3], o[4][1] * r[3]), pk_bf16(o[4][2] * r[3], o[4][3] * r[3])};
        SCHED();
        if (w == 7 && lane == 0) __hip_atomic_store((GAS u64*)(P.ws + WS_HINT + (rb * 8 + hg) * 128), tag, RLX_AGENT);
        SCHED();
#pragma unroll
        for (int t = 0; t < 3; ++t) {
            const u32x2 a = ob[2 * t], b = ob[t < 2 ? 2 * t + 1 : 4];
            const auto sx = __builtin_amdgcn_permlane16_swap(a.x, b.x, false, false), sy = __builtin_amdgcn_permlane16_swap(a.y, b.y, false, false);
            if (t < 2 || g == 0) st_sc1(wsr, ob0 + 1024 * t, not_empty(u32x4{sx[0], sy[0], sx[1], sy[1]}));
        }
    }
    __builtin_amdgcn_s_setprio(0);
    STAMP(9);
    if (w == 0) STAMP_OUT(1, blockIdx.x);
    if (w == 7) STAMP_OUT(7, blockIdx.x + 768);
}

DEVI void gemm_loader(const Params& P, lptr smem, int rb, int hg, int lw, int cg2, u64& tag, rsrc_t wsr, u64& tp) {
    const int lane = threadIdx.x & 63;
    STAMP_DECL; STAMP(1);
    __builtin_amdgcn_s_setprio(3);
    unsigned xo[4], wo_[5], qo[3];
    const int rx = hg & 3, rw = rb & 3;
    unsigned mx[4], mw[4];
    const unsigned xbase = (unsigned)((rb * 128 + 8 * lw + (lane >> 3)) * 1152 + (((lane & 7) ^ ((lane >> 4) + 4 * (lw & 1))) << 4));
#pragma unroll
    for (int i = 0; i < 4; ++i) { mx[i] = (((i + rx) & 3) * 4 + lw) * 1024; mw[i] = XCH + (((i + rw) & 3) * 4 + lw) * 1024; xo[i] = xbase + ((i + rx) & 3) * 36864; }
    const unsigned lb = lw * 4096;
    auto issue_x = [&](int s, int i0 = 0, int i1 = 4) {
#pragma unroll
        for (int i = i0; i < i1; ++i) glds16(P.x + s * 128, xo[i], slot_off(s) + mx[i]);
    };
    auto issue_w = [&](int s, int i0 = 0, int i1 = 4) {
        const char* ws = P.wqkv + s * (64 * 3456);
        if (i1 == 4 && (lw >> 1) == (s & 1)) glds16(ws, wo_[4], (unsigned)(slot_off(s) + XCH + 16384 + (lw & 1) * 1024));
#pragma unroll
        for (int i = i0; i < i1; ++i) glds16(ws, wo_[i], slot_off(s) + mw[i]);
    };
    auto issue_q = [&](int t, int i) {
        const char* ws = P.wqkv + hg * 144 + (3 * t + i) * (64 * 3456);
        if (lw == ((i + t) & 3)) glds16(ws, qo[2], (unsigned)(wq_off(t) + i * 9216 + 8192));
        SET_M0(wq_off(t) + i * 9216 + (lb >> 1));
        glds16o<0>(ws, qo[0]); glds16o<1024>(ws, qo[1]);
    };
    auto w_off = [&](int i) {
        const unsigned q = 64 * (i < 4 ? ((i + rw) & 3) * 4 + lw : 16 + (lw & 1)) + lane, rho = (q * 3641u) >> 16, pc = q - rho * 18;
        const unsigned k = (rho & 35u) + 8 * ((rho >> 2) & 3) + 4 * ((rho >> 4) & 1);
        wo_[i] = k * 3456 + (1152 + hg * 144) + pc * 16 + (pc < 9 ? 0u : 1008u);
    };
    issue_x(0, 0, 1); issue_x(1, 0, 1);
    SCHED();
    w_off(0);
    issue_w(0, 0, 1); issue_w(1, 0, 1);
    SCHED();
#pragma unroll
    for (int i = 1; i < 5; ++i) w_off(i);
    if (lw == 1) { glds16(P.gw, (unsigned)(lane * 16), GW_OFF); if (lane < 8) glds16(P.gw + 1024, (unsigned)(lane * 16), GW_OFF + 1024); }
    issue_x(0, 1, 4); issue_w(0, 1, 4); issue_w(1, 1, 4); issue_x(1, 1, 4);
    const int lo16 = lane & 15, g = lane >> 4, ct = (lw < 3 ? lw : 0) * 64 + lane;
    lptr flp = smem + FL, ixp = smem + IDX + ct * 16, irp = smem + IDX + 3072 + lane * 4, iqp = irp + lw * 512;
    unsigned one = 1u;
    u32x4 sel[2];
    sel[0] = mask_frag(lo16 < 8 ? 1u << lo16 : 0u, 0, 0x3f80u); sel[1] = mask_frag(lo16 >= 8 ? 1u << (lo16 - 8) : 0u, 0, 0x3f80u);
    unsigned sto = WS_PART + (unsigned)((hg * 16 + rb) * 3072 + ct * 16);
    unsigned off = WS_PART + (unsigned)((hg * 16 + g) * 3072 + lw * 1024 + lo16 * 16);
    asm volatile("" : "+v"(flp), "+v"(one), "+v"(ixp), "+v"(irp), "+v"(iqp));
    asm volatile("" : "+v"(sel[0]), "+v"(sel[1]), "+v"(sto), "+v"(off));
    SET_M0(IDX + (lb >> 1));
    glds16o<0>((const char*)&g_tab + (lb >> 1), (unsigned)(lane * 16)); glds16o<1024>((const char*)&g_tab + (lb >> 1), (unsigned)(lane * 16));
    lptr gxp = smem + (16 * hg + lo16) * 128 + ((g ^ ((lo16 >> 1) & 7)) << 4), gwp = smem + GW_OFF + g * 16;
    f32x4 gacc = {0.f, 0.f, 0.f, 0.f};
    asm volatile("" : "+v"(gxp), "+v"(gwp));
    auto gate_step = [&](int s) {
        if (lw == 1) {
            lptr q = gxp + slot_off(s);
            gacc = mfma16(lds_r128(gwp + s * 128), lds_r128(q), gacc);
            gacc = mfma16(lds_r128(gwp + s * 128 + 64), lds_r128((lptr)((size_t)q ^ 64u)), gacc);
        }
    };
    WAIT_VM(8); BARRIER();
    issue_w(2); issue_x(2);
    u32x4 ix; unsigned oo[9]; u64 pf = 0;
#pragma unroll
    for (int s = 0; s < 9; ++s) {
        gate_step(s);
        if (s < 7) WAIT_VM(8); else WAIT_VM(6);
        BARRIER();
        if (s == 5 && lw == 3) asm volatile("global_load_dwordx2 %0, %1, %2 sc1" : "=v"(pf) : "v"((lane < 5 ? lane : 0) * 128), "s"(P.ws + WS_PFL) : "memory");
        if (s < 6) { issue_w(s + 3); issue_x(s + 3); } else { issue_q(s - 6, 0); issue_q(s - 6, 1); issue_q(s - 6, 2); }
        if (s == 0) tag = *(const __attribute__((address_space(4))) u64*)(P.ws + WS_CTL);
        if (s == 1) PIN_TAG();
        if (s == 1 && lw == 0 && lane == 0) lds_w64(smem + LDS_TAG, u32x2{(unsigned)tag, (unsigned)(tag >> 32)});
        if (s == 6 && lw == 3) {
            asm volatile("" : "+v"(pf));
            if (!__all(pf == tag)) poll_flags(P, WS_PFL, 5, tag, lane);
        }
        if (s == 3) {
            qo[0] = *(LAS unsigned*)iqp; qo[1] = *(LAS unsigned*)(iqp + 256); qo[2] = *(LAS unsigned*)(irp + 2048);
            ix = lds_r128(ixp);
#pragma unroll
            for (int i = 0; i < 9; ++i) oo[i] = *(LAS unsigned*)(irp + 2304 + i * 256);
            CFENCE();
        }
    }
    {
        const unsigned co = WS_O + (unsigned)(((rb * 8 + 2 * lw) * 72 + hg * 9) * 256 + lane * 16);
#pragma unroll
        for (int i = 0; i < 5; ++i)
            if (i < 4 || lane < 32) st_sc1(wsr, i < 4 ? co + (i >> 1) * 18432 + (i & 1) * 1024 : co + 2048 + (lane >> 4) * (18432 - 256), u32x4{EMPTY, EMPTY, EMPTY, EMPTY});
    }
    WAIT_VM(5); FLAG_ADD(F_LANDED);
    if (lw == 0 && lane == 0) tag_post(P, tag);
    if (lw == 1) {
        const float gate = rbf(__builtin_amdgcn_rcpf(1.f + __expf(-rbf(gacc[0]))));
        if (lane < 16) __hip_atomic_store((GAS float*)(P.ws + WS_GATE) + (rb * 128 + hg * 16 + lane), gate, RLX_AGENT);
    }
    if (lw < 3) {
        const int dst[2] = {(int)ix.z, (int)ix.w};
        flag_wait<0>(smem, F_KVP, 8);
        STAMP(2);
        float pa[2][4][4];
#pragma unroll
        for (int i = 0; i < 2; ++i)
#pragma unroll
            for (int r = 0; r < 4; ++r)
#pragma unroll
                for (int k = 0; k < 4; ++k) pa[i][r][k] = lds_rf(smem + ix[i] + r * 12288 + k * 256);
        SCHED();
        u32x4 pv;
#pragma unroll
        for (int i = 0; i < 2; ++i)
#pragma unroll
            for (int k = 0; k < 2; ++k)
                pv[2 * i + k] = pk_bf16((pa[i][0][2 * k] + pa[i][1][2 * k]) + (pa[i][2][2 * k] + pa[i][3][2 * k]),
                                        (pa[i][0][2 * k + 1] + pa[i][1][2 * k + 1]) + (pa[i][2][2 * k + 1] + pa[i][3][2 * k + 1]));
        st_sc1(wsr, sto, not_empty(pv));
        SCHED();
        FLAG_ADD(F_KVR);
        STAMP(3);
        WAIT_VM(0);
        STAMP(4);
        f32x4 sum[2];
#pragma unroll 1
        for (unsigned spins = 0; spins < SPIN_CAP; ++spins) {
            u32x4 a[4][4];
#pragma unroll
            for (int i = 0; i < 4; ++i)
#pragma unroll
                for (int t = 0; t < 4; ++t) a[t][i] = __builtin_amdgcn_raw_buffer_load_b128(wsr, off + t * 256, ((i + rb) & 3) * 12288, 16);
            SCHED();
            FLAG_ADD(F_SW);
            sum[0] = sum[1] = f32x4{0.f, 0.f, 0.f, 0.f};
#pragma unroll
            for (int i = 0; i < 4; ++i)
#pragma unroll
                for (int t = 0; t < 4; ++t) sum[t >> 1] = mfma16(sel[t & 1], a[t][i], sum[t >> 1]);
            if (__all(sum[0][0] == sum[0][0] && sum[1][0] == sum[1][0])) break;
            __builtin_amdgcn_s_sleep(RSLEEP);
            if (spins < 8) { CFENCE(); continue; }
            unsigned m = 0;
#pragma unroll
            for (int t = 0; t < 4; ++t)
#pragma unroll
                for (int i = 0; i < 4; ++i) m = max(m, umax4(a[t][i]));
            if (__all(m != EMPTY)) break;
            CFENCE();
        }
#pragma unroll
        for (int i = 0; i < 2; ++i) {
            const unsigned lo = (dst[i] & 1) ? pk_bf16(sum[i][0], sum[i][1]) : 0u, hi = (dst[i] & 2) ? pk_bf16(sum[i][2], sum[i][3]) : 0u;
            lptr dp = smem + (dst[i] & 0xffffc);
            if (dst[i] & 0x100000) {
                const int hs = (dst[i] & 0x200000) ? 20 : 4;
#pragma unroll
                for (int r = 0; r < 4; ++r) { lds_w32(dp + 64 * r, lo); lds_w32(dp + 64 * r + hs, hi); }
            } else lds_w64(dp, u32x2{lo, hi});
        }
        CFENCE();
        FLAG_ADD(F_D);
        STAMP(5);
    } else {
        auto wo_chunks = [&](int c0, int c1) {
#pragma unroll 1
            for (int c = c0; c < c1; ++c) {
                const char* ws = P.wo + cg2 * 144 + c * (64 * 1152);
                const unsigned l = c < 8 ? c * 9216 : WO8;
                SET_M0(l); glds16o<0>(ws, oo[0]); glds16o<1024>(ws, oo[1]); glds16o<2048>(ws, oo[2]); glds16o<3072>(ws, oo[3]);
                SET_M0(l + 4096); glds16o<0>(ws, oo[4]); glds16o<1024>(ws, oo[5]); glds16o<2048>(ws, oo[6]); glds16o<3072>(ws, oo[7]);
                SET_M0(l + 8192); glds16o<0>(ws, oo[8]);
            }
        };
        wo_chunks(8, 9);
        flag_wait(smem, F_KVR, 3);
        wo_chunks(3, 8);
        WAIT_VM(0);
        FLAG_ADD(F_D);
        flag_wait(smem, F_SW, 3);
        flag_wait(smem, F_Q0, 8);
        wo_chunks(0, 3);
        WAIT_VM(0);
        FLAG_ADD(F_W0);
    }
    __builtin_amdgcn_s_setprio(0);
    STAMP(6);
    if (lw == 0) STAMP_OUT(5, blockIdx.x + 256);
}

DEVI void phase2(const Params& P, lptr smem, int rt64, int cg, bool gemm, u64 tag, rsrc_t wsr, u64& tp) {
    const int tid = threadIdx.x, lane = tid & 63, w = __builtin_amdgcn_readfirstlane(tid >> 6);
    const int lo16 = lane & 15, g = lane >> 4;
    const int rt = w & 3, kt = w >> 2;
    STAMP_DECL; STAMP(1);
    const int pb = gemm ? P2_B : 55296;
    if (gemm && w < 4) flag_wait(smem, F_W0, 1);
    STAMP(2);
    const int qrow = (2 * (4 * g + (lo16 >> 2))) * 144;
    lptr qw = smem + qrow + (lane & 3) * 8, qw4 = smem + qrow + (16 + (lane & 1)) * 8;
    int wofs[3], oofs[3];
#pragma unroll
    for (int u = 0; u < 3; ++u) {
        const int c = kt * 3 + (u + cg) % 3;
        wofs[u] = c < (gemm ? 8 : 6) ? c * 9216 : WO8 + (gemm ? 0 : (c - 6) * 9216); oofs[u] = c * 2048;
    }
    auto rdo = [&](int i, int j) { return j < 4 ? lds_trfrag(qw + wofs[i >> 1] + (i & 1) * 4608 + 32 * j, 144) : lds_trfrag(qw4 + wofs[i >> 1] + (i & 1) * 4608, 144); };
    const unsigned ooff = WS_O + (unsigned)((rt64 * 4 + rt) * 18432 + lane * 16);
    auto ldo = [&](int i) { return __builtin_amdgcn_raw_buffer_load_b128(wsr, ooff + (i & 1) * 1024, oofs[i >> 1], 16); };
    u32x4 wf[6][5];
#pragma unroll
    for (int i = 0; i < 6; ++i)
#pragma unroll
        for (int j = 0; j < 5; ++j) wf[i][j] = rdo(i, j);
    const int r0 = tid < 576 ? tid / 9 : 0, c0 = tid - r0 * 9;
    unsigned boff = tid < 576 ? WS_B + (unsigned)((cg * 72 + (tid >> 3)) * 4096 + (rt64 >> 3) * 1024 + (((tid >> 2) & 1) * 32 + ((rt64 >> 2) & 1) * 16 + (rt64 & 3) * 4 + (tid & 3)) * 16) : WS_END;
    unsigned goff = WS_GATE + (unsigned)((rt64 * 64 + r0) * 4);
    unsigned qd = gemm ? __hip_atomic_load((LAS unsigned*)(smem + FL + F_QD), __ATOMIC_RELAXED, __HIP_MEMORY_SCOPE_WORKGROUP) : 8u;
    asm volatile("" : "+v"(boff), "+v"(goff));
    STAMP(3);
    if (w == 3) {
        const int a0 = cg * 72, g0 = (a0 >> 6) * 11 + (a0 & 63) / 6, a1 = a0 + 71, g1 = (a1 >> 6) * 11 + (a1 & 63) / 6;
        poll_addr<OSLEEP>(P, lane < 8 ? WS_HINT + ((rt64 >> 1) * 8 + lane) * 128 : WS_BFL + min(g0 + lane - 8, g1) * 128, tag);
        flag_set(F_H, 1);
    } else flag_wait<1>(smem, F_H, 1);
    STAMP(4);
    u32x4 bv; unsigned gv;
    f32x4 acc[5];
    {
        u32x4 of[6];
#pragma unroll
        for (int i = 0; i < 6; ++i) of[i] = ldo(i);
        bv = ld_sc1(wsr, boff); gv = __builtin_amdgcn_raw_buffer_load_b32(wsr, goff, 0, 16);
        SCHED();
#pragma unroll
        for (int j = 0; j < 5; ++j) acc[j] = f32x4{0.f, 0.f, 0.f, 0.f};
#pragma unroll
        for (int i = 0; i < 6; ++i)
#pragma unroll
            for (int j = 0; j < 5; ++j) acc[j] = mfma16(wf[i][j], of[i], acc[j]);
    }
    if (!__all(acc[0][0] == acc[0][0])) {
#pragma unroll
        for (int j = 0; j < 5; ++j) acc[j] = f32x4{0.f, 0.f, 0.f, 0.f};
        u32x4 of[6];
#pragma unroll 1
        for (unsigned spins = 0; spins < SPIN_CAP; ++spins) {
            CFENCE();
            unsigned m = 0;
#pragma unroll
            for (int i = 0; i < 6; ++i) of[i] = ldo(i);
#pragma unroll
            for (int i = 0; i < 6; ++i) m = max(m, umax4(of[i]));
            if (__all(m != EMPTY)) break;
        }
#pragma unroll
        for (int i = 0; i < 6; ++i)
#pragma unroll
            for (int j = 0; j < 5; ++j) acc[j] = mfma16(rdo(i, j), of[i], acc[j]);
    }
    if ((unsigned)__builtin_amdgcn_readfirstlane(qd) < 8u) flag_wait(smem, F_QD, 8);
    lptr ap = smem + P2_ACC + kt * (64 * P2_AP) + (rt * 16 + lo16) * P2_AP + g * 16;
#pragma unroll
    for (int j = 0; j < 5; ++j) if (j < 4 || g < 2) lds_w128(ap + j * 64, __builtin_bit_cast(u32x4, acc[j]));
    if (w < 9) {
#pragma unroll 1
        for (unsigned spins = 0; spins < SPIN_CAP; ++spins) {
            if (__all(umax4(bv) != EMPTY)) break;
            __builtin_amdgcn_s_sleep(2);
            CFENCE();
            bv = ld_sc1(wsr, boff);
        }
        lptr bp = smem + pb + ((tid & 3) * 16 + ((tid >> 2) & 1) * 8) * 144 + (tid >> 3) * 2;
#pragma unroll
        for (int q = 0; q < 4; ++q) { lds_w16(bp + (2 * q) * 144, bv[q]); lds_w16(bp + (2 * q + 1) * 144, bv[q] >> 16); }
    }
    STAMP(5);
    BARRIER();
    STAMP(6);
    if (w < 9) {
        lptr a = smem + P2_ACC + r0 * P2_AP + c0 * 32;
        const f32x4 s0 = __builtin_bit_cast(f32x4, lds_r128(a)) + __builtin_bit_cast(f32x4, lds_r128(a + 64 * P2_AP)) + __builtin_bit_cast(f32x4, lds_r128(a + 128 * P2_AP));
        const f32x4 s1 = __builtin_bit_cast(f32x4, lds_r128(a + 16)) + __builtin_bit_cast(f32x4, lds_r128(a + 64 * P2_AP + 16)) + __builtin_bit_cast(f32x4, lds_r128(a + 128 * P2_AP + 16));
        const u32x4 bb = lds_r128(smem + pb + tid * 16);
        const float gt = __uint_as_float(gv), og = rbf(1.f - gt);
        u32x4 o;
#pragma unroll
        for (int i = 0; i < 4; ++i) {
            const float a0 = i < 2 ? s0[2 * i] : s1[2 * i - 4], a1 = i < 2 ? s0[2 * i + 1] : s1[2 * i - 3];
            o[i] = pk_bf16(gt * a0 + og * bf2f(bb[i]), gt * a1 + og * bfhi(bb[i]));
        }
        __builtin_amdgcn_raw_buffer_store_b128(o, __builtin_amdgcn_make_buffer_rsrc(P.out, 0, SEQ * DM * 2, 0x00020000), (unsigned)((rt64 * 64 + r0) * 1152 + cg * 144 + c0 * 16), 0, OUTAUX);
    }
    STAMP(7);
    if (w == 4) STAMP_OUT(blockIdx.x < 128 ? 3 : 6, blockIdx.x + 512);
}

DEVI void helper_role(const Params& P, lptr smem, int xcd, int li, u64& tag, rsrc_t wsr, u64& tp) {
    const int tid = threadIdx.x, lane = tid & 63, w = __builtin_amdgcn_readfirstlane(tid >> 6);
    STAMP_DECL; STAMP(1);
    if (li >= 13) {
#pragma unroll 1
        for (int n = 12 * NTW + (li - 13) + 3 * w; n < 171; n += 36) touch_group(P, xcd, n, lane);
    }
    if (li == 12) {
#pragma unroll
        for (int i = 0; i < 7; ++i) {
            const int u = (i * 5 + xcd - 3) * 12 + w;
            if (u < 384) st_sc1(wsr, WS_PART + (unsigned)(u * 1024 + lane * 16), u32x4{EMPTY, EMPTY, EMPTY, EMPTY});
        }
        WAIT_VM(0);
    }
    PIN_TAG();
    BARRIER();
    if (tid == 0) {
        if (li == 12) __hip_atomic_store((GAS u64*)(P.ws + WS_PFL + (xcd - 3) * 128), tag, RLX_AGENT);
        tag_post(P, tag);
    }
    if (li >= 13) {
#pragma unroll 1
        for (int n = 171 + (li - 13) + 3 * w; n < 234; n += 36) touch_group(P, xcd, n, lane);
    }
    STAMP(2);
    if (w == 11) {
        STAMP_OUT(8, blockIdx.x);
        if (blockIdx.x == 255) {
            const GAS u64* f = (const GAS u64*)(P.ws + WS_ARR) + lane;
#pragma unroll 1
            for (unsigned spins = 0; spins < SPIN_CAP; ++spins) {
                const bool ok = __hip_atomic_load(f, RLX_AGENT) == tag && __hip_atomic_load(f + 64, RLX_AGENT) == tag &&
                                __hip_atomic_load(f + 128, RLX_AGENT) == tag && __hip_atomic_load(f + 192, RLX_AGENT) == tag;
                if (__all(ok)) break;
                __builtin_amdgcn_s_sleep(8);
            }
            if (lane == 0) __hip_atomic_store((GAS u64*)(P.ws + WS_CTL), tag, RLX_AGENT);
        }
    }
}

__global__ __launch_bounds__(768, 3) void k_fused(Params P) {
    __shared__ __attribute__((aligned(16))) char smem_[LDS_BYTES];
    lptr smem = (lptr)smem_;
    const int b = blockIdx.x, xcd = b & 7, idx = b >> 3;
    const int lane = threadIdx.x & 63, w = __builtin_amdgcn_readfirstlane(threadIdx.x >> 6);
    const rsrc_t wsr = __builtin_amdgcn_make_buffer_rsrc(P.ws, 0, (int)WS_END, 0x00020000);
    const int rt64 = xcd * 4 + (idx & 3), cg2 = idx >> 2;
    if (idx >= CPF && w < 6) {
        const char* pc; unsigned len;
        asm volatile("s_getpc_b64 %0\n.Lkf_code_begin:\n\ts_mov_b32 %1, .Lkf_code_end-.Lkf_code_begin" : "=s"(pc), "=s"(len));
        touch(pc, min((unsigned)(threadIdx.x * 128), len));
    }
    u64 tag; u64 tp = 0;
    if (__builtin_expect(idx < 16, 1)) {
        const int rbl = idx & 3, hgl = idx >> 2, rb = (xcd >> 1) * 4 + rbl, hg = (xcd & 1) * 4 + hgl;
        if (__builtin_expect(w < 4, 1)) gemm_loader(P, smem, rb, hg, w, cg2, tag, wsr, tp);
        else gemm_compute(P, smem, rb, hg, w - 4, tag, wsr, tp);
    } else {
        tag = *(const __attribute__((address_space(4))) u64*)(P.ws + WS_CTL);
        const int li = idx - 16, gid = li < 11 ? xcd * 11 + li : 88 + (li - 11) * 8 + xcd;
        if (gid < 99) ssm_role(P, smem, xcd, li, gid, cg2, tag, wsr, tp);
        else {
            helper_role(P, smem, xcd, li, tag, wsr, tp);
            if (w < 9) wo_issue<0x1ff>(P, cg2, w, 9, lane);
            if (w == 10 && lane < 16) lds_w32(smem + FL + lane * 4, 0u);
            WAIT_VM(0);
            BARRIER();
        }
    }
    phase2(P, smem, rt64, cg2, idx < 16, tag, wsr, tp);
    if (tp) while (__builtin_amdgcn_s_memrealtime() - tp < 5000) __builtin_amdgcn_s_sleep(1);
}

extern "C" void kernel_launch(void* const* d_in, const int* in_sizes, int n_in, void* d_out, int out_size, void* d_ws, size_t ws_size,
                              hipStream_t stream) {
    Params P;
    P.x = (const char*)d_in[0]; P.wqkv = (const char*)d_in[1]; P.wo = (const char*)d_in[2];
    P.logA = (const char*)d_in[4]; P.aim = (const char*)d_in[5]; P.cre = (const char*)d_in[6]; P.cim = (const char*)d_in[7];
    P.logdt = (const char*)d_in[8]; P.dskip = (const char*)d_in[9]; P.gw = (const char*)d_in[10];
    P.out = (char*)d_out; P.ws = (char*)d_ws;
    k_fused<<<256, 768, 0, stream>>>(P);
}
```

```cpp
#include <hip/hip_runtime.h>
#include <stdint.h>

#define GAS __attribute__((address_space(1)))
#define LAS __attribute__((address_space(3)))
#define DEVI __device__ __forceinline__
typedef unsigned short u16;
typedef unsigned long long u64;
typedef __attribute__((ext_vector_type(2))) unsigned u32x2;
typedef __attribute__((ext_vector_type(3))) unsigned u32x3;
typedef __attribute__((ext_vector_type(4))) unsigned u32x4;
typedef __attribute__((ext_vector_type(4))) short s16x4;
typedef __attribute__((ext_vector_type(2))) float f32x2;
typedef __attribute__((ext_vector_type(4))) float f32x4;
typedef __attribute__((ext_vector_type(16))) float f32x16;
typedef __attribute__((ext_vector_type(2))) __bf16 bf16x2;
typedef __attribute__((ext_vector_type(8))) __bf16 bf16x8;
typedef __attribute__((ext_vector_type(2))) _Float16 f16x2;
typedef __attribute__((ext_vector_type(8))) _Float16 f16x8;
typedef LAS char* lptr;

#define PREF 1
#define ROT 0
#define FSLEEP 2
#define NSLEEP "3"
#define LDRIFT 0
#define PSLEEP 6
#define CSLEEP 1
#define OSLEEP 2
#define RSLEEP 14
#define HOLD 1
#define GROT ((w + li) & 3)
#define NTW 8
#define OUTAUX 17
#define TEARLY 1
#define HDELAY 0
#define CPF 16
#define LA3 0
constexpr int SEQ = 2048, DM = 576;

constexpr unsigned WS_CTL = 0;
constexpr unsigned WS_ARR = 256;
constexpr unsigned WS_BFL = 4096;
constexpr unsigned WS_HINT = 20480;
constexpr unsigned WS_GATE = 36864;
constexpr unsigned WS_PFL = 49152;
constexpr unsigned WS_PART = 65536;
constexpr unsigned WS_O = WS_PART + 8 * 16 * 3072;
constexpr unsigned WS_B = WS_O + SEQ * DM * 2;
constexpr unsigned WS_END = WS_B + SEQ * DM * 2;
constexpr unsigned EMPTY = 0xffffffffu;

constexpr size_t WS_STAMP = 16u << 20;
#define STAMP_DECL
#define STAMP(i)
#define CSTAMP(i)
#define STAMP_OUT(role, slot)

struct Params {
    const char* x; const char* wqkv; const char* wo; const char* logA; const char* aim; const char* cre; const char* cim;
    const char* logdt; const char* dskip; const char* gw; char* out; char* ws;
};

constexpr int LDS_BYTES = 163840, FL = 163584, LDS_TAG = FL + 64, LDS_DUMMY = FL + 128, LDS_JUNK = 161280;
enum { F_SW = 0, F_LANDED = 4, F_W1 = 20, F_B = 28, F_H = 24, F_KVP = 36, F_KVR = 40, F_Q0 = 44, F_QD = 48, F_D = 52, F_C1 = 56, F_W0 = 60 };
constexpr int WO8 = 132096, P2_ACC = 73728, P2_AP = 304, P2_B = 141312, P2_G = 150528;

DEVI float bf2f(unsigned h) { return __uint_as_float(h << 16); }
DEVI float bfhi(unsigned h) { return __uint_as_float(h & 0xffff0000u); }
DEVI unsigned pk_bf16(float a, float b) { return __builtin_bit_cast(unsigned, __builtin_convertvector(f32x2{a, b}, bf16x2)); }
DEVI unsigned pk_f16(f32x2 v) { return __builtin_bit_cast(unsigned, __builtin_convertvector(v, f16x2)); }
DEVI float rbf(float a) { return bf2f(pk_bf16(a, a)); }
DEVI f32x4 mfma16(u32x4 a, u32x4 b, f32x4 c) {
    return __builtin_amdgcn_mfma_f32_16x16x32_bf16(__builtin_bit_cast(bf16x8, a), __builtin_bit_cast(bf16x8, b), c, 0, 0, 0);
}
DEVI void mfma16i(f32x4& c, u32x4 a, u32x4 b) { asm volatile("v_mfma_f32_16x16x32_bf16 %0, %1, %2, %0" : "+v"(c) : "v"(a), "v"(b)); }
#define MFMA_DONE() do { __builtin_amdgcn_sched_barrier(0); asm volatile("s_nop 7\n\ts_nop 1" ::: "memory"); __builtin_amdgcn_sched_barrier(0); } while (0)
DEVI u32x4 lds_r128(lptr p) { return *(LAS u32x4*)p; }
DEVI u32x2 lds_r64(lptr p) { return *(LAS u32x2*)p; }
DEVI float lds_rf(lptr p) { return *(LAS float*)p; }
DEVI void lds_w128(lptr p, u32x4 v) { *(LAS u32x4*)p = v; }
DEVI void lds_w64(lptr p, u32x2 v) { *(LAS u32x2*)p = v; }
DEVI void lds_w32(lptr p, unsigned v) { *(LAS unsigned*)p = v; }
DEVI void lds_w16(lptr p, unsigned v) { *(LAS u16*)p = (u16)v; }
DEVI u32x2 lds_tr(lptr p) { return __builtin_bit_cast(u32x2, __builtin_amdgcn_ds_read_tr16_b64_v4i16((LAS s16x4*)p)); }
DEVI u32x4 lds_trfrag(lptr p, int off2) { u32x2 a = lds_tr(p), b = lds_tr(p + off2); return u32x4{a.x, a.y, b.x, b.y}; }
template <int OFF> DEVI void glds16o(const char* sbase, unsigned voff) {
    asm volatile("global_load_lds_dwordx4 %0, %1 offset:%2" :: "v"(voff), "s"(sbase), "i"(OFF) : "memory");
}
DEVI void glds16(const char* sbase, unsigned voff, unsigned l) {
    asm volatile("s_mov_b32 m0, %2\n\ts_nop 0\n\tglobal_load_lds_dwordx4 %0, %1" :: "v"(voff), "s"(sbase), "s"(l) : "memory");
}
#define SET_M0(v) asm volatile("s_mov_b32 m0, %0\n\ts_nop 0" :: "s"(v) : "memory")
#define W_ADDTID(v, off) asm volatile("ds_write_addtid_b32 %0 offset:%1" :: "v"(v), "i"(off) : "memory")
#define CFENCE() asm volatile("" ::: "memory")
#define SCHED() __builtin_amdgcn_sched_barrier(0)
#define WAIT_VM(n) asm volatile("s_waitcnt vmcnt(" #n ")" ::: "memory")
#define WAIT_LGKM(n) __builtin_amdgcn_s_waitcnt(0xC07F | ((n) << 8))
#define WAIT_LGKM0() asm volatile("s_waitcnt lgkmcnt(0)" ::: "memory")
#define BARRIER() do { SCHED(); asm volatile("s_waitcnt lgkmcnt(0)\n\ts_barrier" ::: "memory"); SCHED(); } while (0)
#define RAW_BARRIER() do { SCHED(); asm volatile("s_barrier" ::: "memory"); SCHED(); } while (0)
#define RLX_AGENT __ATOMIC_RELAXED, __HIP_MEMORY_SCOPE_AGENT
constexpr unsigned SPIN_CAP = 1u << 16;
typedef __amdgpu_buffer_rsrc_t rsrc_t;
DEVI u32x4 ld_sc1(rsrc_t r, unsigned off) { return __builtin_amdgcn_raw_buffer_load_b128(r, off, 0, 16); }
DEVI void st_sc1(rsrc_t r, unsigned off, u32x4 v) { __builtin_amdgcn_raw_buffer_store_b128(v, r, off, 0, 16); }

DEVI u32x4 pack8(f32x4 a, f32x4 b) { return u32x4{pk_bf16(a[0], a[1]), pk_bf16(a[2], a[3]), pk_bf16(b[0], b[1]), pk_bf16(b[2], b[3])}; }
DEVI float phi(float t) {
    const float e = __builtin_amdgcn_fmed3f(__builtin_amdgcn_exp2f(t * 1.44269504089f), 0.f, 1.f);
    return __int_as_float(max(__float_as_int(t), 0x358637bd)) + e;
}
DEVI f32x4 phi4(f32x4 v) { return f32x4{phi(v[0]), phi(v[1]), phi(v[2]), phi(v[3])}; }
DEVI unsigned umax4(u32x4 a) { return max(max(a.x, a.y), max(a.z, a.w)); }
DEVI u32x4 not_empty(u32x4 a) { return u32x4{min(a.x, EMPTY - 1), min(a.y, EMPTY - 1), min(a.z, EMPTY - 1), min(a.w, EMPTY - 1)}; }
DEVI void tag_post(const Params& P, u64 tag) { __hip_atomic_store((GAS u64*)(P.ws + WS_ARR) + blockIdx.x, tag, RLX_AGENT); }
#define PIN_TAG() do { asm volatile("" : "+s"(tag)); tag += 1ull; } while (0)
template <int SL = PSLEEP>
DEVI void poll_addr(const Params& P, unsigned off, u64 tag) {
    const GAS u64* f = (const GAS u64*)(P.ws + off);
#pragma unroll 1
    for (unsigned spins = 0; spins < SPIN_CAP; ++spins) {
        if (__all(__hip_atomic_load(f, RLX_AGENT) == tag)) break;
        __builtin_amdgcn_s_sleep(SL);
    }
}
template <int SL = PSLEEP>
DEVI void poll_flags(const Params& P, unsigned off, int n, u64 tag, int lane) { poll_addr<SL>(P, off + (lane < n ? lane : 0) * 128, tag); }
DEVI unsigned flag_get(lptr smem, int f) { return __builtin_amdgcn_readfirstlane(__hip_atomic_load((LAS unsigned*)(smem + FL + f), __ATOMIC_RELAXED, __HIP_MEMORY_SCOPE_WORKGROUP)); }
template <int SL = FSLEEP>
DEVI void flag_wait(lptr smem, int f, unsigned v) {
#pragma unroll 1
    for (unsigned spins = 0; spins < (SPIN_CAP << 4); ++spins) { if (flag_get(smem, f) >= v) break; if (SL) __builtin_amdgcn_s_sleep(SL); }
    CFENCE();
}
DEVI void flag_add(int f) { asm volatile("s_mov_b64 exec, 1\n\tds_add_u32 %0, %1\n\ts_mov_b64 exec, -1" :: "v"(FL + f), "v"(1u) : "memory"); }
#define FLAG_ADD(f) asm volatile("s_mov_b64 exec, 1\n\tds_add_u32 %0, %1 offset:%2\n\ts_mov_b64 exec, -1" :: "v"(flp), "v"(one), "i"(f) : "memory")
DEVI void flag_set(int f, unsigned v) { asm volatile("s_mov_b64 exec, 1\n\tds_write_b32 %0, %1\n\ts_mov_b64 exec, -1" :: "v"(FL + f), "v"(v) : "memory"); }
#define NEEDP(seen, ptr, f, v) do { unsigned c_; asm volatile( \
        "v_cmp_gt_u32 vcc, %4, %1\n\ts_cbranch_vccnz 0f\n1:\n\t.subsection 1\n" \
        "0:\n\ts_mov_b32 %0, 0x100000\n2:\n\ts_sleep " NSLEEP "\n\tds_read_b32 %1, %2 offset:%3\n\ts_sub_u32 %0, %0, 1\n\ts_waitcnt lgkmcnt(0)\n\ts_cbranch_scc1 1b\n\t" \
        "v_cmp_gt_u32 vcc, %4, %1\n\ts_cbranch_vccnz 2b\n\ts_branch 1b\n\t.subsection 0" \
        : "=&s"(c_), "+v"(seen) : "v"(ptr), "i"(f), "i"(v) : "memory", "scc", "vcc"); } while (0)
#define NEED(seen, f, v) NEEDP(seen, flp, f, v)
DEVI void touch(const char* sbase, unsigned voff) {
    asm volatile("s_mov_b32 m0, %2\n\ts_nop 0\n\tglobal_load_lds_dword %0, %1" :: "v"(voff), "s"(sbase), "s"(LDS_JUNK) : "memory");
}
DEVI const char* group_addr(const Params& P, int xcd, int n, int lane) {
    if (n < 126) {
        const int c0 = n / 18, r = n - c0 * 18, c = c0 + 2, l = r - 8;
        return r < 8 ? P.x + (unsigned)(((xcd >> 1) * 512 + r * 64 + lane) * 1152 + c * 128)
                     : P.wqkv + (unsigned)((c * 64 + lane) * 3456 + (l < 5 ? 1152 : 2304 - 640) + (xcd & 1) * 512 + l * 128);
    }
    const int c = (n - 126) / 5, l = n - 126 - c * 5;
    return P.wqkv + (unsigned)((c * 64 + lane) * 3456 + (xcd & 1) * 512 + l * 128);
}
DEVI void touch_group(const Params& P, int xcd, int n, int lane) {
    if (n < 126) {
        const int c0 = n / 18, r = n - c0 * 18, c = c0 + 2;
        if (r < 8) touch(P.x, (unsigned)(((xcd >> 1) * 512 + r * 64 + lane) * 1152 + c * 128));
        else { const int l = r - 8; touch(P.wqkv, (unsigned)((c * 64 + lane) * 3456 + (l < 5 ? 1152 : 2304 - 640) + (xcd & 1) * 512 + l * 128)); }
    } else if (n < 171) {
        const int c = (n - 126) / 5, l = n - 126 - c * 5;
        touch(P.wqkv, (unsigned)((c * 64 + lane) * 3456 + (xcd & 1) * 512 + l * 128));
    } else {
        const int l = (n - 171) / 9, c = n - 171 - l * 9;
        touch(P.wo, (unsigned)((c * 64 + lane) * 1152 + l * 128));
    }
}
template <unsigned CH>
DEVI void wo_issue(const Params& P, int cg, int first, int stride, int lane) {
#pragma unroll 1
    for (int m = first; m < 9; m += stride) {
        const int q = 64 * m + lane, rho = q / 9, pc = q - rho * 9;
        const int k = (rho & 32) + 8 * ((rho >> 3) & 3) + 4 * (rho & 1) + ((rho >> 1) & 3);
        const unsigned qo = (unsigned)(k * 1152 + cg * 144 + pc * 16);
#pragma unroll
        for (int c = 0; c < 9; ++c) if ((CH >> c) & 1) glds16(P.wo + c * (64 * 1152), qo, (unsigned)((c < 6 ? c * 9216 : WO8 + (c - 6) * 9216) + m * 1024));
    }
}

constexpr int SP = 3088;
constexpr int XT_OFF = 133632, XT_BYTES = 4608, XPAD = 256;
constexpr int TT_OFF = 104448, TC = 1216, PT_OFF = 55296, PC_OFF = 2560, ACX_OFF = 55296;

#define CM2(o1, p1, a1, b1, o2, p2, a2, b2) do { asm( \
    "v_pk_mul_f32 %0, %2, %3 op_sel:[1,1] op_sel_hi:[1,0] neg_lo:[0,1]\n\tv_pk_mul_f32 %1, %4, %5 op_sel:[1,1] op_sel_hi:[1,0] neg_lo:[0,1]\n\t" \
    "v_pk_fma_f32 %0, %2, %3, %0 op_sel_hi:[0,1,1]\n\tv_pk_fma_f32 %1, %4, %5, %1 op_sel_hi:[0,1,1]" \
    : "=&v"(o1), "=&v"(o2) : "v"(a1), "v"(b1), "v"(a2), "v"(b2)); \
    asm("v_cvt_pk_f16_f32 %0, %2, %3\n\tv_cvt_pk_f16_f32 %1, %4, %5" : "=&v"(p1), "=&v"(p2) : "v"((o1).x), "v"((o1).y), "v"((o2).x), "v"((o2).y)); } while (0)
#define HM2(p1, a1, p2, a2, b) asm( \
    "v_pk_mul_f16 %0, %2, %4 op_sel_hi:[0,1]\n\tv_pk_mul_f16 %1, %3, %4 op_sel_hi:[0,1]\n\t" \
    "v_pk_fma_f16 %0, %2, %4, %0 op_sel:[1,1,0] op_sel_hi:[1,0,1] neg_lo:[0,1,0]\n\tv_pk_fma_f16 %1, %3, %4, %1 op_sel:[1,1,0] op_sel_hi:[1,0,1] neg_lo:[0,1,0]" \
    : "=&v"(p1), "=&v"(p2) : "v"(a1), "v"(a2), "v"(b))
DEVI f32x2 cexp_p(float a2, float f, float p) {
    float m = __builtin_amdgcn_exp2f(a2 * p);
    float r = __builtin_amdgcn_fractf(f * p);
    return f32x2{m * __builtin_amdgcn_cosf(r), m * __builtin_amdgcn_sinf(r)};
}
DEVI u32x4 mask_frag(unsigned bits, int g, unsigned val) {
    unsigned by = bits >> (8 * g);
    u32x4 r;
#pragma unroll
    for (int i = 0; i < 4; ++i) { unsigned t = (by >> (2 * i)) & 3u; r[i] = ((t | (t << 15)) & 0x10001u) * val; }
    return r;
}
struct Tab2 { unsigned v[8][64][4]; };
constexpr Tab2 make_tab2() {
    Tab2 t{};
    for (int l = 0; l < 64; ++l) {
        const int r = l & 15, g = l >> 4, j = l & 31, h = l >> 5;
        const unsigned bits[6] = {7u << (r + 11), 31u << (r + 6), 7u << (r + 3), 127u << (r + 5), 511u << (r + 4), 3u << (r + 12)};
        for (int m = 0; m < 6; ++m)
            for (int i = 0; i < 4; ++i) {
                const unsigned q = (bits[m] >> (8 * g + 2 * i)) & 3u, val = m < 5 ? 0x3f80u : 0xffffu;
                t.v[m][l][i] = (q & 1 ? val : 0u) | (q & 2 ? val << 16 : 0u);
            }
        for (int c = 0; c < 4; ++c) {
            const int v2 = 79 - (32 + (j >> 1)) - c + 4, v3 = 127 - (64 + 32 * h + (j >> 1)) - c + 4;
            t.v[6][l][c] = PT_OFF + (j & 1) * 1280 + c * 320 + (v2 >> 2) * 16 + h * 8 + (v2 & 3) * 2 - 64;
            t.v[7][l][c] = PT_OFF + PC_OFF + c * 320 + (v3 >> 2) * 16 + (j & 1) * 8 + (v3 & 3) * 2 - 64;
        }
    }
    return t;
}
__device__ const Tab2 g_tab2 = make_tab2();
DEVI float wave_max(float v) {
    v = fmaxf(v, __int_as_float(__builtin_amdgcn_update_dpp(0, __float_as_int(v), 0xB1, 0xf, 0xf, true)));
    v = fmaxf(v, __int_as_float(__builtin_amdgcn_update_dpp(0, __float_as_int(v), 0x4E, 0xf, 0xf, true)));
    v = fmaxf(v, __int_as_float(__builtin_amdgcn_update_dpp(0, __float_as_int(v), 0x124, 0xf, 0xf, true)));
    v = fmaxf(v, __int_as_float(__builtin_amdgcn_update_dpp(0, __float_as_int(v), 0x128, 0xf, 0xf, true)));
    const int i = __float_as_int(v);
    const int a = max(__builtin_amdgcn_readlane(i, 0), __builtin_amdgcn_readlane(i, 16)), b = max(__builtin_amdgcn_readlane(i, 32), __builtin_amdgcn_readlane(i, 48));
    return __int_as_float(max(a, b));
}

DEVI void ssm_role(const Params& P, lptr smem, int xcd, int li, int gid, int cg2, u64& tag, rsrc_t wsr) {
    const int gl = gid / 11, gi = gid - gl * 11, ch0 = gl * 64 + (gi < 10 ? gi * 6 : 58);
    const int tid = threadIdx.x, lane = tid & 63, w = __builtin_amdgcn_readfirstlane(tid >> 6);
    const int ch = w >> 1, half = (w ^ (w >> 2)) & 1, d = ch0 + ch;
    const bool own = gi < 10 || ch >= 2;
    const int lo16 = lane & 15, g = lane >> 4;
    STAMP_DECL; STAMP(1);
    __builtin_amdgcn_s_setprio(3);
    const unsigned po = (unsigned)(d * 256 + lane * 4);
    const unsigned r_la = *(const GAS unsigned*)(P.logA + po), r_ai = *(const GAS unsigned*)(P.aim + po);
    const unsigned r_cr = *(const GAS unsigned*)(P.cre + po), r_ci = *(const GAS unsigned*)(P.cim + po);
    const unsigned r_dt = *(const GAS u16*)(P.logdt + d * 2), r_ds = *(const GAS u16*)(P.dskip + d * 2);
    u32x4 ct[8];
    if (half) {
        const char* tp = (const char*)&g_tab2;
        asm volatile("" : "+s"(tp));
#pragma unroll
        for (int i = 0; i < 8; ++i) ct[i] = *(const GAS u32x4*)(tp + (unsigned)(i * 1024 + lane * 16));
    }
    const char* tga = (w < NTW && li < 12) ? group_addr(P, xcd, w * 12 + li, lane) : P.logA + po;
    RAW_BARRIER();
    const unsigned junk = *(const GAS unsigned*)tga;
    SCHED();
    const int tt = (tid + (li & 7) * 64) & 511;
    u32x3 xr[4];
    const unsigned xoff = (unsigned)(tt * 4608 + ch0 * 2);
    const unsigned boff = own ? WS_B + (unsigned)(d * 4096 + half * 2048 + lane * 16) : WS_END;
#define GATHER(i) do { SCHED(); if (w < 8) xr[i] = *(const GAS u32x3*)(P.x + xoff + (i) * 1152); SCHED(); } while (0)
#define PRIO(i) do { SCHED(); __builtin_amdgcn_s_setprio(i); SCHED(); } while (0)
    SCHED();
    lptr xt = smem + XT_OFF + ch * XT_BYTES, tb = smem + TT_OFF + ch * 4864, pt = smem + PT_OFF + ch * 3840;
    const int j32 = lane & 31, h = lane >> 5;
    if (!half) { lds_w64(xt + lane * 8, u32x2{0u, 0u}); lds_w32(smem + FL + 32, 0u); }
    int arow;
    {
        const u64 map0 = 0x0a09080803020100ull, map1 = 0x0c0b0a0a07060504ull, map2 = 0x1413110e12100f0dull;
        const u64 mm = j32 < 8 ? map0 : (j32 < 16 ? map1 : map2);
        arow = (int)((mm >> (8 * (j32 & 7))) & 0xff);
    }
    lptr ap = smem + arow * SP + w * 256 + h * 16, bp = smem + j32 * SP + w * 256 + h * 16;
    const int cpy = (3 - lo16) & 3;
    lptr xf7 = xt + (XPAD - 16 + 16 * lo16 + 8 * g) * 2 - 64 * 7;
    lptr tf = tb + cpy * TC + 2 * (527 - lo16 + 8 * g - cpy) - 64 * 16;
    lptr xc = smem + (w ^ 1) * 256 + lane * 4, xb = smem + XT_OFF + (XPAD + tt * 4) * 2;
    asm volatile("" : "+v"(ap), "+v"(bp), "+v"(xf7), "+v"(tf), "+v"(xc), "+v"(xb));
    const float dt = rbf(__expf(bf2f(r_dt)));
    const float cm = wave_max(fmaxf(fmaxf(fabsf(bf2f(r_cr)), fabsf(bfhi(r_cr))), fmaxf(fabsf(bf2f(r_ci)), fabsf(bfhi(r_ci)))));
    unsigned ce = __float_as_uint(cm) >> 23; ce = ce < 16u ? 16u : (ce > 240u ? 240u : ce);
    const float cs = __uint_as_float((263u - ce) << 23), ics = __uint_as_float((ce - 9u) << 23);
    f32x16 kacc;
#pragma unroll
    for (int i = 0; i < 16; ++i) kacc[i] = 0.f;
    STAMP(2);
    {
        const unsigned sh = half * 16;
        asm volatile("" :: "v"(r_la), "v"(r_ai), "v"(r_ds), "v"(ct[7]));
        const float la = bf2f(r_la >> sh), ai = bf2f(r_ai >> sh), cr = bf2f(r_cr >> sh) * cs, ci = bf2f(r_ci >> sh) * cs;
        const float a2 = (-rbf(__expf(la)) * dt) * 1.44269504089f, f = (ai * dt) * 0.15915494309f;
#define PUT(row, v) W_ADDTID(v, ((row) & 15) * SP)
        const f32x2 w1 = cexp_p(a2, -f, 1.f), W = cexp_p(a2, -f, 256.f);
        f32x2 u[8], v[4], c[9], e4, e8, t2;
        unsigned U[8], V[4], p, q;
        u[1] = cexp_p(a2, f, 8.f); v[1] = cexp_p(a2, f, 64.f);
        c[0] = f32x2{cr, -ci};
        U[1] = pk_f16(u[1]); V[1] = pk_f16(v[1]);
        GATHER(0);
        SET_M0(w * 256);
        PUT(0, pk_f16(c[0]));
        CM2(c[1], p, c[0], w1, u[2], U[2], u[1], u[1]); PUT(1, p);
        CM2(c[2], p, c[1], w1, u[3], U[3], u[2], u[1]); PUT(2, p);
        CM2(c[3], p, c[2], w1, u[4], U[4], u[2], u[2]); PUT(3, p);
        CM2(c[4], p, c[3], w1, u[5], U[5], u[4], u[1]); PUT(4, p);
        CM2(c[5], p, c[4], w1, u[6], U[6], u[4], u[2]); PUT(5, p);
        CM2(c[6], p, c[5], w1, u[7], U[7], u[4], u[3]); PUT(6, p);
        CM2(c[7], p, c[6], w1, v[2], V[2], v[1], v[1]); PUT(7, p);
        CM2(c[8], p, c[7], w1, v[3], V[3], v[2], v[1]);
        PRIO(2);
        GATHER(1);
        CM2(e8, p, c[8], W, e4, q, c[4], W); PUT(12, p); PUT(10, q);
        CM2(c[8], p, e8, W, c[4], q, e4, W); PUT(14, p); PUT(13, q);
        CM2(t2, p, c[0], W, e4, q, c[4], W); PUT(8, p); PUT(15, q);
        CM2(t2, p, c[2], W, e4, q, c[6], W); PUT(9, p); PUT(11, q);
        SET_M0(w * 256 + 16 * SP);
        CM2(e8, p, c[8], W, t2, q, W, W); PUT(17, p);
        CM2(e4, p, c[8], t2, c[0], q, e8, t2); PUT(16, p); PUT(18, q);
        CM2(c[8], p, e4, t2, e8, q, c[0], t2); PUT(19, p); PUT(20, q);
        PRIO(1);
        GATHER(2);
        u32x4 af[8];
#pragma unroll
        for (int s = 0; s < 8; ++s) af[s] = lds_r128(ap + 32 * s);
        CFENCE();
        PUT(16, V[2]); PUT(24, V[3]);
#pragma unroll
        for (int b = 1; b < 8; ++b) { HM2(p, V[2], q, V[3], U[b]); PUT(16 + b, p); PUT(24 + b, q); }
        SET_M0(w * 256);
        PRIO(0);
        GATHER(3);
        PUT(0, 0x3c00u); PUT(8, V[1]);
#pragma unroll
        for (int b = 1; b < 8; b += 2) { HM2(p, U[b], q, U[b == 7 ? 7 : b + 1], V[1]); PUT(8 + b, p); if (b < 7) PUT(9 + b, q); PUT(b, U[b]); if (b < 7) PUT(b + 1, U[b + 1]); }
#undef PUT
        u32x4 bfr[8];
#pragma unroll
        for (int s = 0; s < 8; ++s) bfr[s] = lds_r128(bp + 32 * s);
        SCHED();
        SCHED();
#pragma unroll
        for (int s = 0; s < 8; ++s)
            kacc = __builtin_amdgcn_mfma_f32_32x32x16_f16(__builtin_bit_cast(f16x8, af[s]), __builtin_bit_cast(f16x8, bfr[s]), kacc, 0, 0, 0);
    }
    STAMP(3);
    const float ks = 2.f * dt * ics;
#pragma unroll
    for (int i = 0; i < 12; ++i) kacc[i] *= ks;
    if (half) {
#pragma unroll
        for (int i = 0; i < 8; ++i) W_ADDTID(kacc[i], i * SP);
    } else {
#pragma unroll
        for (int i = 0; i < 4; ++i) W_ADDTID(kacc[8 + i], i * SP);
    }
    PIN_TAG();
    STAMP(4);
    BARRIER();
    STAMP(5);
    if (tid == 704) tag_post(P, tag);
    auto cleared = [&]() {
        if (w < 4) {
            WAIT_VM(0);
            if (lane == 0 && __hip_atomic_fetch_add((LAS unsigned*)(smem + FL + 32), 1u, __ATOMIC_RELAXED, __HIP_MEMORY_SCOPE_WORKGROUP) == 3u)
                __hip_atomic_store((GAS u64*)(P.ws + WS_BFL + gid * 128), tag, RLX_AGENT);
        }
    };
    auto x_in = [&]() {
        asm volatile("" :: "v"(junk));
        if (w < 8) {
#pragma unroll
            for (int k = 0; k < 3; ++k) {
                u32x2 lo = {__builtin_amdgcn_perm(xr[1][k], xr[0][k], 0x05040100u), __builtin_amdgcn_perm(xr[3][k], xr[2][k], 0x05040100u)};
                u32x2 hi = {__builtin_amdgcn_perm(xr[1][k], xr[0][k], 0x07060302u), __builtin_amdgcn_perm(xr[3][k], xr[2][k], 0x07060302u)};
                lds_w64(xb + (2 * k) * XT_BYTES, lo); lds_w64(xb + (2 * k + 1) * XT_BYTES, hi);
            }
        }
        if (w < 4) {
#pragma unroll
            for (int i = 0; i < 6; ++i)
                if (i >= 2 || gi < 10) __builtin_amdgcn_raw_buffer_store_b128(u32x4{EMPTY, EMPTY, EMPTY, EMPTY}, wsr, (unsigned)(tid * 16), WS_B + (ch0 + i) * 4096, 16);
        }
        BARRIER();
        if (w >= 8) wo_issue<0x03f>(P, cg2, w - 8, 4, lane);
    };
    auto TF = [&](int off) { u32x2 a = lds_r64(tf + off), b = lds_r64(tf + off + 8); return u32x4{a.x, a.y, b.x, b.y}; };
    f32x4 acc[8];
#pragma unroll
    for (int i = 0; i < 8; ++i) acc[i] = f32x4{0.f, 0.f, 0.f, 0.f};
    lptr axr;
    if (!half) {
        __builtin_amdgcn_s_setprio(2);
#pragma unroll
        for (int i = 0; i < 8; ++i) kacc[i] += lds_rf(xc + i * SP);
        unsigned dd[6];
        dd[0] = pk_bf16(kacc[7], kacc[6]); dd[1] = pk_bf16(kacc[5], kacc[4]); dd[2] = pk_bf16(kacc[3], kacc[2]);
        dd[3] = pk_bf16(kacc[1], kacc[0] + (lane == 0 ? bf2f(r_ds) : 0.f));
        dd[4] = __builtin_amdgcn_update_dpp(0, dd[0], 0x138, 0xf, 0xf, true);
        dd[5] = __builtin_amdgcn_update_dpp(0, dd[1], 0x138, 0xf, 0xf, true);
        unsigned mm[5];
#pragma unroll
        for (int i = 0; i < 5; ++i) mm[i] = __builtin_amdgcn_alignbit(dd[i + 1], dd[i], 16);
        lptr tw = tb + 2 * (536 - 8 * lane);
        lds_w128(tw, u32x4{dd[0], dd[1], dd[2], dd[3]}); lds_w128(tw + TC, u32x4{mm[0], mm[1], mm[2], mm[3]});
        lds_w128(tw + 2 * TC, u32x4{dd[1], dd[2], dd[3], dd[4]}); lds_w128(tw + 3 * TC, u32x4{mm[1], mm[2], mm[3], mm[4]});
        {
            const unsigned n0 = __builtin_amdgcn_readlane(dd[0], 63), n1 = __builtin_amdgcn_readlane(dd[1], 63);
            const unsigned kk = (n0 & 0xffffu) * 0x10001u, kh = n0 << 16, m4 = (n0 >> 16) | (n1 << 16);
            const int c = lane & 3, bl = lane >> 2;
            u32x4 e;
            e.x = 0u; e.y = lane == 14 ? kh : (lane == 15 ? kk : 0u); e.z = lane == 12 ? kh : (bl == 3 ? kk : 0u);
            e.w = lane == 14 ? n0 : (lane == 15 ? m4 : (bl == 3 ? kk : 0u));
            if (bl < 6) lds_w128(tb + c * TC + bl * 16 + (bl >> 2) * 1024, e);
        }
        x_in();
        u32x4 X[8];
#pragma unroll
        for (int i = 0; i < 8; ++i) X[i] = lds_r128(xf7 + 64 * 6 + 512 * i);
        u32x4 k0 = TF(1024 - 64), k8 = TF(512 - 64);
#pragma unroll
        for (int i = 0; i < 8; ++i) asm volatile("" : "+v"(acc[i]));
        __builtin_amdgcn_s_setprio(0);
        STAMP(6);
#pragma unroll
        for (int c = 1; c < 8; ++c) {
            u32x4 k0n, k8n;
            if (c < 7) { k0n = TF(1024 - 64 * (c + 1)); k8n = TF(512 - 64 * (c + 1)); }
#pragma unroll
            for (int i = 0; i < 8; ++i) {
                SCHED();
                mfma16i(acc[i], k0, X[i]);
                if (i < 7) mfma16i(acc[i + 1], k8, X[i]);
                SCHED();
                if (c < 7) X[i] = lds_r128(xf7 + 64 * (6 - c) + 512 * i);
            }
            k0 = k0n; k8 = k8n;
            if (c == 5) { SCHED(); cleared(); SCHED(); }
        }
        MFMA_DONE();
        SET_M0(ACX_OFF - 40960 + ch * 8192);
#pragma unroll
        for (int i = 0; i < 16; ++i) W_ADDTID(acc[4 + (i >> 2)][i & 3], 40960 + i * 256);
        axr = smem + ACX_OFF + ch * 8192 + 4096 + lane * 4;
    } else {
        u32x4 X[8];
#pragma unroll
        for (int i = 0; i < 4; ++i) if (i < 3 || lane < 48) lds_w128(pt + i * 1024 + lane * 16, u32x4{0u, 0u, 0u, 0u});
        CFENCE();
        const unsigned p89 = pk_bf16(kacc[8] + lds_rf(xc), kacc[9] + lds_rf(xc + SP)), pab = pk_bf16(kacc[10] + lds_rf(xc + 2 * SP), kacc[11] + lds_rf(xc + 3 * SP));
#pragma unroll
        for (int c = 0; c < 4; ++c) {
            lptr q2 = smem + (ct[6][c] + ch * 3840), q3 = smem + (ct[7][c] + ch * 3840);
            lds_w16(q2 + 64, p89); lds_w16(q2, p89 >> 16);
            lds_w16(q3 + 64, pab); lds_w16(q3, pab >> 16);
        }
        CFENCE();
        const u32x4 FX = ct[5] & (((unsigned)__builtin_amdgcn_readlane(p89, 63) >> 16) * 0x10001u);
        lptr pf2 = pt + cpy * 320 + ((79 - 64 - lo16 + 4 * g - cpy + 4) >> 2) * 16;
        u32x4 TA[3], TB[3], TCc[4];
#pragma unroll
        for (int i = 0; i < 3; ++i) { TA[i] = lds_r128(pf2 + 64 * (2 - i)); TB[i] = lds_r128(pf2 + 1280 + 64 * (2 - i)); }
#pragma unroll
        for (int i = 0; i < 4; ++i) TCc[i] = lds_r128(pf2 + PC_OFF + 64 * (3 - i));
        x_in();
#pragma unroll
        for (int i = 0; i < 8; ++i) X[i] = lds_r128(xf7 + 64 * 7 + 512 * i);
        STAMP(6);
        const f32x4 z4 = {0.f, 0.f, 0.f, 0.f};
#pragma unroll
        for (int st = 0; st < 6; ++st) {
            const u32x4 Xc = lds_r128(xf7 + 64 * 7 + 512 * st - 16);
            const u32x4 pa = pack8(mfma16(X[st], ct[0], z4), mfma16(X[st], ct[1], z4));
            const u32x4 pb = pack8(mfma16(X[st], ct[2], z4), mfma16(Xc, ct[1], z4));
#pragma unroll
            for (int dl = 2; dl <= 4; ++dl) if (st + dl < 8) {
                acc[st + dl] = mfma16(pa, TA[dl - 2], acc[st + dl]);
                acc[st + dl] = mfma16(pb, TB[dl - 2], acc[st + dl]);
            }
            if (st < 4) {
                const u32x4 pc = pack8(mfma16(X[st], ct[3], z4), mfma16(Xc, ct[4], z4));
#pragma unroll
                for (int dl = 4; dl <= 7; ++dl) if (st + dl < 8) acc[st + dl] = mfma16(pc, TCc[dl - 4], acc[st + dl]);
            }
        }
        SCHED(); cleared(); SCHED();
#pragma unroll
        for (int i = 4; i < 8; ++i) acc[i] = mfma16(FX, X[i - 4], acc[i]);
        const u32x4 k0 = TF(1024), k8 = TF(512), k16 = TF(0);
#pragma unroll
        for (int i = 0; i < 8; ++i) acc[i] = mfma16(k0, X[i], acc[i]);
#pragma unroll
        for (int i = 1; i < 8; ++i) acc[i] = mfma16(k8, X[i - 1], acc[i]);
#pragma unroll
        for (int i = 2; i < 8; ++i) acc[i] = mfma16(k16, X[i - 2], acc[i]);
        MFMA_DONE();
        SET_M0(ACX_OFF - 40960 + ch * 8192 + 4096);
#pragma unroll
        for (int i = 0; i < 16; ++i) W_ADDTID(acc[i >> 2][i & 3], 40960 + i * 256);
#pragma unroll
        for (int i = 0; i < 4; ++i) acc[i] = acc[4 + i];
        axr = smem + ACX_OFF + ch * 8192 + lane * 4;
    }
    STAMP(7);
    if (w == 0 && lane < 8) lds_w32(smem + FL + lane * 4, 0u);
    BARRIER();
    STAMP(8);
    if (w >= 8) wo_issue<0x1c0>(P, cg2, w - 8, 4, lane);
#pragma unroll
    for (int i = 0; i < 16; ++i) acc[i >> 2][i & 3] += lds_rf(axr + i * 256);
#pragma unroll
    for (int i = 0; i < 2; ++i) {
        const f32x4 ya = acc[2 * i], yb = acc[2 * i + 1];
        const auto sx = __builtin_amdgcn_permlane16_swap(pk_bf16(ya[0], ya[1]), pk_bf16(yb[0], yb[1]), false, false);
        const auto sy = __builtin_amdgcn_permlane16_swap(pk_bf16(ya[2], ya[3]), pk_bf16(yb[2], yb[3]), false, false);
        st_sc1(wsr, boff + i * 1024, not_empty(u32x4{sx[0], sy[0], sx[1], sy[1]}));
    }
    WAIT_VM(2);
    BARRIER();
    STAMP(9);
    if (w < 2) STAMP_OUT(2 + 2 * w, blockIdx.x + 256 * w);
}

constexpr int SLOT = 34816, XCH = 16384, KVT_OFF = 151296, KVP_OFF = 27648;
DEVI constexpr int slot_off(int s) { return (s & 3) == 0 ? SLOT : ((s & 3) == 1 ? 0 : (s & 3) * SLOT); }
DEVI constexpr int wq_off(int t) { return t == 0 ? 0 : 49152 + t * 27648; }

constexpr int quad_src(int quad) {
    const int pp = quad >= 192 ? 1 : 0, qi = quad - 192 * pp;
    const bool isz = qi >= 180;
    const int el = isz ? 0 : qi / 5, qd = qi - el * 5, hi = el >= 18 ? 1 : 0;
    const int dql = isz ? (qi < 189 ? qi - 180 : 0) : qd + 4 * hi;
    const int et = isz ? 0 : (el < 4 ? 0 : (el < 20 ? 1 : 2)), col = isz ? 0 : (el < 4 ? 12 + el : (el < 20 ? el - 4 : el - 20));
    const int tile = dql == 8 ? 5 : (et == 0 ? (dql >> 2) : (et == 1 ? 2 + (dql >> 2) : 4));
    return KVP_OFF + pp * 6144 + tile * 1024 + ((dql == 8 ? et : (dql & 3)) * 16 + col) * 4;
}
constexpr int quad_dst(int quad) {
    const int pp = quad >= 192 ? 1 : 0, qi = quad - 192 * pp;
    const bool isz = qi >= 180;
    const int el = isz ? 0 : qi / 5, qd = qi - el * 5, hi = el >= 18 ? 1 : 0;
    const int dql = isz ? (qi < 189 ? qi - 180 : 0) : qd + 4 * hi;
    const int dq = 9 * pp + dql, ks = dq >> 3, e = 36 * pp + el, ee = e >> 4;
    const int fr = ee == 0 ? 0 : (ee < 3 ? 2 * ee - 1 + ks : 2 * ee - 2 + ks);
    const int a = KVT_OFF + (isz ? (9 + ks) * 1024 + (dq & 3) * 256 + (2 * pp + (dql > 4 ? 1 : 0)) * 16 : fr * 1024 + ((dq & 3) * 16 + (e & 15)) * 16) + ((dq >> 2) & 1) * 8;
    return qi >= 189 ? LDS_DUMMY : (a | (isz ? (dql == 4 ? 0x300003 : 0x100003) : ((!hi && qd == 4) ? 1 : ((hi && qd == 0) ? 2 : 3))));
}
struct Tab { int v[2048]; };
constexpr Tab make_tab() {
    Tab t{};
    for (int c = 0; c < 192; ++c) {
        const int l = c & 63, q = 2 * ((c & 192) + (l >> 5) * 16 + (l & 15)) + ((l >> 4) & 1);
        t.v[4 * c] = quad_src(2 * c); t.v[4 * c + 1] = quad_src(2 * c + 1); t.v[4 * c + 2] = quad_dst(q); t.v[4 * c + 3] = quad_dst(q + 64);
    }
    for (int q = 0; q < 576; ++q) {
        const int rho = q / 9, pc = q % 9, k = (rho & 32) + 8 * ((rho >> 3) & 3) + 4 * (rho & 1) + ((rho >> 1) & 3), m = q >> 6;
        t.v[768 + q] = k * 3456 + pc * 16 - (m & 1) * 1024;
        t.v[1344 + q] = k * 1152 + pc * 16 - (m & 3) * 1024;
    }
    return t;
}
__device__ const Tab g_tab = make_tab();
constexpr int IDX = P2_B, GW_OFF = 139264;

#define GET_TAG() do { const u32x2 t_ = lds_r64(smem + LDS_TAG); tag = (u64)(unsigned)__builtin_amdgcn_readfirstlane(t_.x) | ((u64)(unsigned)__builtin_amdgcn_readfirstlane(t_.y) << 32); } while (0)
DEVI void gemm_kv(const Params& P, lptr smem, int rb, int hg, int kw, int cg2, u64& tag, rsrc_t wsr) {
    const int lane = threadIdx.x & 63, lo16 = lane & 15, g = lane >> 4;
    const int p = kw & 1, ct = (kw < 3 ? kw : 0) * 64 + lane;
    STAMP_DECL; STAMP(1);
    __builtin_amdgcn_s_setprio(2);
    const int xrow = ((kw >> 1) * 64 + lo16) * 128, sw = (g ^ ((lo16 >> 1) & 7)) << 4;
    lptr xa[2] = {smem + xrow + sw, smem + xrow + (sw ^ 64)};
    const int wrow = (4 * g + (lo16 >> 2)) * 288;
    lptr wa = smem + XCH + wrow + (9 * p + (lane & 3)) * 8, wa2 = smem + XCH + wrow + (9 * p + ((lane & 3) == 3 ? 18 : 8)) * 8;
    lptr flp = smem + FL, ixp = smem + IDX + ct * 16, irp = smem + IDX + 3072 + 2304 + lane * 4;
    unsigned one = 1u;
    u32x4 sel[2];
    sel[0] = mask_frag(lo16 < 8 ? 1u << lo16 : 0u, 0, 0x3f80u); sel[1] = mask_frag(lo16 >= 8 ? 1u << (lo16 - 8) : 0u, 0, 0x3f80u);
    unsigned sto = WS_PART + (unsigned)((hg * 16 + rb) * 3072 + ct * 16);
    unsigned off = WS_PART + (unsigned)((hg * 16 + g) * 3072 + kw * 1024 + lo16 * 16);
    asm volatile("" : "+v"(flp), "+v"(one), "+v"(ixp), "+v"(irp), "+v"(sel[0]), "+v"(sel[1]), "+v"(sto), "+v"(off));
    const f32x4 z4 = {0.f, 0.f, 0.f, 0.f};
    f32x4 acc[4][5];
#pragma unroll
    for (int i = 0; i < 4; ++i)
#pragma unroll
        for (int j = 0; j < 5; ++j) { const float e = j < 2 ? 1e-6f : (j == 2 && lo16 < 12 ? 1e-6f : 0.f); acc[i][j] = f32x4{e, e, e, e}; asm volatile("" : "+v"(acc[i][j])); }
    auto rel = [&](int s) { return (s & 2) ? slot_off(s) - 2 * SLOT : slot_off(s); };
    auto rebase = [&](int dlt) {
        xa[0] += dlt; xa[1] += dlt; wa += dlt; wa2 += dlt;
        asm volatile("" : "+v"(xa[0]), "+v"(xa[1]), "+v"(wa), "+v"(wa2));
    };
    constexpr int NB = 6;
    u32x2 wl[NB], wh[NB];
    u32x4 xf[2][4];
    auto rdw = [&](int f, int t) {
        const int n = f / 5, j = f % 5;
        const int o = rel(n >> 1) + (n & 1) * 9216 + t * 4608;
        const u32x2 v = j == 2 ? lds_tr(wa2 + o) : lds_tr(wa + o + (j == 0 ? 0 : (j == 1 ? 32 : (j == 3 ? 152 : 184))));
        if (t) wh[f % NB] = v; else wl[f % NB] = v;
    };
    auto rdx = [&](int n, int r) { xf[n & 1][r] = lds_r128(xa[n & 1] + rel(n >> 1) + r * 2048); };
    rebase(0);
    BARRIER();
    STAMP(2);
    rdw(0, 0); rdw(0, 1);
#pragma unroll
    for (int r = 0; r < 4; ++r) rdx(0, r);
#pragma unroll
    for (int f = 1; f < NB - 1; ++f) { rdw(f, 0); rdw(f, 1); }
    auto kstep = [&](const int n) __attribute__((always_inline)) {
        const int s = n >> 1;
#pragma unroll
        for (int m = 0; m < 20; ++m) {
            const int j = m >> 2, r = m & 3, f = 5 * n + j, fn = f + NB - 1;
            SCHED();
            const u32x4 wv = {wl[f % NB].x, wl[f % NB].y, wh[f % NB].x, wh[f % NB].y};
            mfma16i(acc[r][j], xf[n & 1][r], wv);
            SCHED();
            if (r < 2 && fn < 90) rdw(fn, r);
            if (r == 2 && j < 4 && n < 17) rdx(n + 1, j);
            if (m == 15 && n < 17) WAIT_LGKM(12);
            if (m == 18 && !(n & 1)) {
                if (s < 8 && (s & 1)) rebase((s & 2) ? -2 * SLOT : 2 * SLOT);
                if (s == 8) WAIT_LGKM0();
                RAW_BARRIER();
                if (s == 0) STAMP(3);
                if (s == 4) STAMP(4);
            }
            if (m == 19 && n < 17) WAIT_LGKM(2);
        }
    };
    kstep(0); kstep(1); kstep(2); kstep(3); kstep(4); kstep(5); kstep(6); kstep(7); kstep(8);
    kstep(9); kstep(10); kstep(11); kstep(12); kstep(13); kstep(14); kstep(15); kstep(16); kstep(17);
    __builtin_amdgcn_s_setprio(3);
    const u32x4 ix = lds_r128(ixp);
    unsigned oo[9];
#pragma unroll
    for (int i = 0; i < 9; ++i) oo[i] = *(LAS unsigned*)(irp + i * 256);
    MFMA_DONE();
    STAMP(5);
    {
        f32x4 r[6], t0 = z4, t1 = z4, t2 = z4;
#pragma unroll
        for (int i = 0; i < 5; ++i) r[i] = z4;
#pragma unroll
        for (int u = 0; u < 2; ++u) {
            u32x4 pk[3], vb[3];
#pragma unroll
            for (int j = 0; j < 3; ++j) pk[j] = pack8(phi4(acc[2 * u][j]), phi4(acc[2 * u + 1][j]));
            vb[0] = pack8(acc[2 * u][2], acc[2 * u + 1][2]);
            if (lo16 < 12) vb[0] = u32x4{0x3f803f80u, 0x3f803f80u, 0x3f803f80u, 0x3f803f80u};
            vb[1] = pack8(acc[2 * u][3], acc[2 * u + 1][3]); vb[2] = pack8(acc[2 * u][4], acc[2 * u + 1][4]);
            t0 = mfma16(pk[2], vb[0], t0); t1 = mfma16(pk[2], vb[1], t1); t2 = mfma16(pk[2], vb[2], t2);
            r[0] = mfma16(pk[0], vb[0], r[0]); r[1] = mfma16(pk[1], vb[0], r[1]); r[2] = mfma16(pk[0], vb[1], r[2]);
            r[3] = mfma16(pk[1], vb[1], r[3]); r[4] = mfma16(pk[1], vb[2], r[4]);
        }
        r[5] = g == 0 ? t0 : (g == 1 ? t1 : t2);
        MFMA_DONE();
        SET_M0(KVP_OFF + kw * 6144);
#pragma unroll
        for (int t = 5; t >= 0; --t) {
            W_ADDTID(r[t][0], t * 1024); W_ADDTID(r[t][1], t * 1024 + 256);
            W_ADDTID(r[t][2], t * 1024 + 512); W_ADDTID(r[t][3], t * 1024 + 768);
        }
        FLAG_ADD(F_KVP);
    }
    STAMP(6);
    if (kw < 3) {
        const int dst[2] = {(int)ix.z, (int)ix.w};
        flag_wait<0>(smem, F_KVP, 4);
        STAMP(7);
        float pa[2][2][4];
#pragma unroll
        for (int i = 0; i < 2; ++i)
#pragma unroll
            for (int r = 0; r < 2; ++r)
#pragma unroll
                for (int k = 0; k < 4; ++k) pa[i][r][k] = lds_rf(smem + ix[i] + r * 12288 + k * 256);
        SCHED();
        u32x4 pv;
#pragma unroll
        for (int i = 0; i < 2; ++i)
#pragma unroll
            for (int k = 0; k < 2; ++k)
                pv[2 * i + k] = pk_bf16(pa[i][0][2 * k] + pa[i][1][2 * k], pa[i][0][2 * k + 1] + pa[i][1][2 * k + 1]);
        st_sc1(wsr, sto, not_empty(pv));
        SCHED();
        FLAG_ADD(F_KVR);
        STAMP(8);
        WAIT_VM(0);
        STAMP(9);
        f32x4 sum[2];
#pragma unroll 1
        for (unsigned spins = 0; spins < SPIN_CAP; ++spins) {
            u32x4 a[4][4];
#pragma unroll
            for (int i = 0; i < 4; ++i)
#pragma unroll
                for (int t = 0; t < 4; ++t) a[t][i] = __builtin_amdgcn_raw_buffer_load_b128(wsr, off + t * 256, ((i + rb) & 3) * 12288, 16);
            SCHED();
            FLAG_ADD(F_SW);
            sum[0] = sum[1] = f32x4{0.f, 0.f, 0.f, 0.f};
#pragma unroll
            for (int i = 0; i < 4; ++i)
#pragma unroll
                for (int t = 0; t < 4; ++t) sum[t >> 1] = mfma16(sel[t & 1], a[t][i], sum[t >> 1]);
            if (__all(sum[0][0] == sum[0][0] && sum[1][0] == sum[1][0])) break;
            __builtin_amdgcn_s_sleep(RSLEEP);
            if (spins < 8) { CFENCE(); continue; }
            unsigned m = 0;
#pragma unroll
            for (int t = 0; t < 4; ++t)
#pragma unroll
                for (int i = 0; i < 4; ++i) m = max(m, umax4(a[t][i]));
            if (__all(m != EMPTY)) break;
            CFENCE();
        }
#pragma unroll
        for (int i = 0; i < 2; ++i) {
            const unsigned lo = (dst[i] & 1) ? pk_bf16(sum[i][0], sum[i][1]) : 0u, hi = (dst[i] & 2) ? pk_bf16(sum[i][2], sum[i][3]) : 0u;
            lptr dp = smem + (dst[i] & 0xffffc);
            if (dst[i] & 0x100000) {
                const int hs = (dst[i] & 0x200000) ? 20 : 4;
#pragma unroll
                for (int r = 0; r < 4; ++r) { lds_w32(dp + 64 * r, lo); lds_w32(dp + 64 * r + hs, hi); }
            } else lds_w64(dp, u32x2{lo, hi});
        }
        CFENCE();
        FLAG_ADD(F_D);
        STAMP(10);
    } else {
        auto wo_chunks = [&](int c0, int c1) {
#pragma unroll 1
            for (int c = c0; c < c1; ++c) {
                const char* ws = P.wo + cg2 * 144 + c * (64 * 1152);
                const unsigned l = c < 8 ? c * 9216 : WO8;
                SET_M0(l); glds16o<0>(ws, oo[0]); glds16o<1024>(ws, oo[1]); glds16o<2048>(ws, oo[2]); glds16o<3072>(ws, oo[3]);
                SET_M0(l + 4096); glds16o<0>(ws, oo[4]); glds16o<1024>(ws, oo[5]); glds16o<2048>(ws, oo[6]); glds16o<3072>(ws, oo[7]);
                SET_M0(l + 8192); glds16o<0>(ws, oo[8]);
            }
        };
        flag_wait(smem, F_KVR, 3);
        wo_chunks(3, 9);
        WAIT_VM(0);
        FLAG_ADD(F_D);
        flag_wait(smem, F_SW, 3);
        flag_wait(smem, F_Q0, 8);
        wo_chunks(0, 3);
        WAIT_VM(0);
        FLAG_ADD(F_W0);
    }
    GET_TAG();
    __builtin_amdgcn_s_setprio(0);
    if (kw == 0) STAMP_OUT(1, blockIdx.x);
}

DEVI void gemm_q(const Params& P, lptr smem, int rb, int hg, int t8, u64& tag, rsrc_t wsr) {
    const int lane = threadIdx.x & 63, lw = t8 & 3;
    const bool ld = t8 < 4;
    STAMP_DECL; STAMP(1);
    __builtin_amdgcn_s_setprio(3);
    unsigned xo[4], wo_[5], qo[3];
    const int rx = hg & 3, rw = rb & 3;
    unsigned mx[4], mw[4];
    const unsigned xbase = (unsigned)((rb * 128 + 8 * lw + (lane >> 3)) * 1152 + (((lane & 7) ^ ((lane >> 4) + 4 * (lw & 1))) << 4));
#pragma unroll
    for (int i = 0; i < 4; ++i) { mx[i] = (((i + rx) & 3) * 4 + lw) * 1024; mw[i] = XCH + (((i + rw) & 3) * 4 + lw) * 1024; xo[i] = xbase + ((i + rx) & 3) * 36864; }
    const unsigned lb = lw * 4096;
    auto issue_x = [&](int s, int i0 = 0, int i1 = 4) {
#pragma unroll
        for (int i = i0; i < i1; ++i) glds16(P.x + s * 128, xo[i], slot_off(s) + mx[i]);
    };
    auto issue_w = [&](int s, int i0 = 0, int i1 = 4) {
        const char* ws = P.wqkv + s * (64 * 3456);
        if (i1 == 4 && (lw >> 1) == (s & 1)) glds16(ws, wo_[4], (unsigned)(slot_off(s) + XCH + 16384 + (lw & 1) * 1024));
#pragma unroll
        for (int i = i0; i < i1; ++i) glds16(ws, wo_[i], slot_off(s) + mw[i]);
    };
    auto issue_q = [&](int t, int i) {
        const char* ws = P.wqkv + hg * 144 + (3 * t + i) * (64 * 3456);
        if (lw == ((i + t) & 3)) glds16(ws, qo[2], (unsigned)(wq_off(t) + i * 9216 + 8192));
        SET_M0(wq_off(t) + i * 9216 + (lb >> 1));
        glds16o<0>(ws, qo[0]); glds16o<1024>(ws, qo[1]);
    };
    auto w_off = [&](int i) {
        const unsigned q = 64 * (i < 4 ? ((i + rw) & 3) * 4 + lw : 16 + (lw & 1)) + lane, rho = (q * 3641u) >> 16, pc = q - rho * 18;
        const unsigned k = (rho & 35u) + 8 * ((rho >> 2) & 3) + 4 * ((rho >> 4) & 1);
        wo_[i] = k * 3456 + (1152 + hg * 144) + pc * 16 + (pc < 9 ? 0u : 1008u);
    };
    if (ld) {
        issue_x(0, 0, 1); issue_x(1, 0, 1);
        SCHED();
        w_off(0);
        issue_w(0, 0, 1); issue_w(1, 0, 1);
        SCHED();
#pragma unroll
        for (int i = 1; i < 5; ++i) w_off(i);
        if (lw == 1) { glds16(P.gw, (unsigned)(lane * 16), GW_OFF); if (lane < 8) glds16(P.gw + 1024, (unsigned)(lane * 16), GW_OFF + 1024); }
        issue_x(0, 1, 4); issue_w(0, 1, 4); issue_w(1, 1, 4); issue_x(1, 1, 4);
        SET_M0(IDX + (lb >> 1));
        glds16o<0>((const char*)&g_tab + (lb >> 1), (unsigned)(lane * 16)); glds16o<1024>((const char*)&g_tab + (lb >> 1), (unsigned)(lane * 16));
    } else {
        if (lw == 0 && lane < 16) lds_w32(smem + FL + lane * 4, 0u);
#pragma unroll
        for (int i = 0; i < 3; ++i) lds_w128(smem + KVT_OFF + i * 4096 + lw * 1024 + lane * 16, u32x4{0u, 0u, 0u, 0u});
    }
    const int lo16 = lane & 15, g = lane >> 4;
    const int sw = (g ^ ((lo16 >> 1) & 7)) << 4;
    lptr xa[2] = {smem + (16 * t8 + lo16) * 128 + sw, smem + (16 * t8 + lo16) * 128 + (sw ^ 64)}, xb[2] = {xa[0] + 2 * SLOT, xa[1] + 2 * SLOT};
    lptr flp = smem + FL, iqp = smem + IDX + 3072 + lane * 4, gwp = smem + GW_OFF + g * 16;
    const int qrow = (2 * (4 * g + (lo16 >> 2))) * 144;
    lptr qw = smem + qrow + (lane & 3) * 8, qw4 = smem + qrow + (16 + (lane & 1)) * 8;
    lptr qwh = qw + wq_off(1), qw4h = qw4 + wq_off(1), kfp = smem + KVT_OFF + lane * 16;
    unsigned one = 1u, ob0 = WS_O + (unsigned)(((rb * 8 + t8) * 72 + hg * 9 + 2 * (g & 1) + (g >> 1)) * 256 + lo16 * 16);
    asm volatile("" : "+v"(flp), "+v"(one), "+v"(iqp), "+v"(gwp), "+v"(xa[0]), "+v"(xa[1]), "+v"(xb[0]), "+v"(xb[1]));
    asm volatile("" : "+v"(qw), "+v"(qw4), "+v"(qwh), "+v"(qw4h), "+v"(kfp), "+v"(ob0));
    asm volatile(".subsection 2\n.Lkf_code_end:\n\t.fill 256, 4, 0xbf800000\n\t.subsection 0");
    const f32x4 z4 = {0.f, 0.f, 0.f, 0.f};
    f32x4 gacc = z4, qa[5];
#pragma unroll
    for (int j = 0; j < 5; ++j) { qa[j] = f32x4{1e-6f, 1e-6f, 1e-6f, 1e-6f}; asm volatile("" : "+v"(qa[j])); }
    u32x4 xk[18];
    u64 pf = 0;
    WAIT_VM(8); BARRIER();
    if (ld) { issue_w(2); issue_x(2); if (LA3) { issue_w(3); issue_x(3); } }
#pragma unroll
    for (int s = 0; s < 9; ++s) {
#pragma unroll
        for (int k = 0; k < 2; ++k) xk[2 * s + k] = lds_r128(((s & 2) ? xb[k] - 2 * SLOT : xa[k]) + slot_off(s));
        if (t8 == hg) {
            gacc = mfma16(lds_r128(gwp + s * 128), xk[2 * s], gacc);
            gacc = mfma16(lds_r128(gwp + s * 128 + 64), xk[2 * s + 1], gacc);
        }
        if (t8 == 4) {
            if (s == 1) { tag = *(const __attribute__((address_space(4))) u64*)(P.ws + WS_CTL); PIN_TAG(); if (lane == 0) lds_w64(smem + LDS_TAG, u32x2{(unsigned)tag, (unsigned)(tag >> 32)}); }
            if (s == 4) asm volatile("global_load_dwordx2 %0, %1, %2 sc1" : "=v"(pf) : "v"((lane < 5 ? lane : 0) * 128), "s"(P.ws + WS_PFL) : "memory");
            if (s == 7) {
                asm volatile("s_waitcnt vmcnt(0)" : "+v"(pf) :: "memory");
                if (!__all(pf == tag)) poll_flags(P, WS_PFL, 5, tag, lane);
            }
        }
        if (LA3) { if (s < 6) WAIT_VM(17); else if (s == 6) WAIT_VM(14); else WAIT_VM(12); }
        else if (s < 7) WAIT_VM(8); else WAIT_VM(6);
        BARRIER();
        if (ld) {
            const int sn = s + 3 + LA3;
            if (sn < 9) { issue_w(sn); issue_x(sn); } else if (sn < 12) { issue_q(sn - 9, 0); issue_q(sn - 9, 1); issue_q(sn - 9, 2); }
            if (s == 3) { qo[0] = *(LAS unsigned*)(iqp + lw * 512); qo[1] = *(LAS unsigned*)(iqp + lw * 512 + 256); qo[2] = *(LAS unsigned*)(iqp + 2048); CFENCE(); }
        }
    }
    __builtin_amdgcn_s_setprio(0);
    STAMP(5);
    if (!ld) {
        const unsigned co = WS_O + (unsigned)(((rb * 8 + 2 * lw) * 72 + hg * 9) * 256 + lane * 16);
#pragma unroll
        for (int i = 0; i < 5; ++i)
            if (i < 4 || lane < 32) st_sc1(wsr, i < 4 ? co + (i >> 1) * 18432 + (i & 1) * 1024 : co + 2048 + (lane >> 4) * (18432 - 256), u32x4{EMPTY, EMPTY, EMPTY, EMPTY});
    }
    if (t8 == 4 && lane == 0) tag_post(P, tag);
    if (t8 == hg) {
        const float gate = rbf(__builtin_amdgcn_rcpf(1.f + __expf(-rbf(gacc[0]))));
        if (lane < 16) __hip_atomic_store((GAS float*)(P.ws + WS_GATE) + (rb * 128 + hg * 16 + lane), gate, RLX_AGENT);
    }
    auto peek = [&](int f) { return __hip_atomic_load((LAS unsigned*)(flp + f), __ATOMIC_RELAXED, __HIP_MEMORY_SCOPE_WORKGROUP); };
    auto rdq = [&](int ks, int j) {
        const int o = (ks < 6 ? ks : ks - 6) * 4608;
        return j < 4 ? lds_trfrag((ks < 6 ? qw : qwh) + o + 32 * j, 144) : lds_trfrag((ks < 6 ? qw4 : qw4h) + o, 144);
    };
    u32x4 wq[2][5];
#pragma unroll
    for (int i = 0; i < 2; ++i)
#pragma unroll
        for (int j = 0; j < 5; ++j) wq[i][j] = rdq(i, j);
    unsigned seenq = 0;
#pragma unroll
    for (int ks = 0; ks < 18; ++ks) {
        if (ks == 4) {
            SCHED();
            WAIT_VM(0); FLAG_ADD(F_Q0);
            seenq = peek(F_KVR); NEED(seenq, F_KVR, 3);
            seenq = peek(F_Q0); NEED(seenq, F_Q0, 8);
            STAMP(6);
        }
        if (ks == 16) { SCHED(); FLAG_ADD(F_QD); }
#pragma unroll
        for (int j = 0; j < 5; ++j) {
            SCHED();
            if (j == 0 && ks < 17) WAIT_LGKM(10);
            mfma16i(qa[j], wq[ks & 1][j], xk[ks]);
            SCHED();
            if (ks + 2 < 18) wq[ks & 1][j] = rdq(ks + 2, j);
        }
    }
    __builtin_amdgcn_s_setprio(2);
    MFMA_DONE();
    u32x4 b0 = pack8(phi4(qa[0]), phi4(qa[1])), b1 = pack8(phi4(qa[2]), phi4(qa[3])), b2 = pack8(phi4(qa[4]), z4);
    asm volatile("" : "+v"(b0), "+v"(b1), "+v"(b2));
    GET_TAG();
    STAMP(7);
    flag_wait<1>(smem, F_D, 4);
    STAMP(8);
    {
        u32x4 kf[12];
#pragma unroll
        for (int i = 0; i < 12; ++i) kf[i] = lds_r128(kfp + (i < 3 ? 9 + i : i - 3) * 1024);
        SCHED();
        const f32x4 den = mfma16(kf[2], b2, mfma16(kf[1], b1, mfma16(kf[0], b0, f32x4{1e-6f, 1e-6f, 1e-6f, 1e-6f})));
        f32x4 o[5];
        o[0] = mfma16(kf[3], b0, z4);
        o[1] = mfma16(kf[5], b1, mfma16(kf[4], b0, z4));
        o[2] = mfma16(kf[7], b1, mfma16(kf[6], b0, z4));
        o[3] = mfma16(kf[9], b2, mfma16(kf[8], b1, z4));
        o[4] = mfma16(kf[11], b2, mfma16(kf[10], b1, z4));
        f32x4 r;
#pragma unroll
        for (int i = 0; i < 4; ++i) r[i] = __builtin_amdgcn_rcpf(den[i]);
        const float r1a = g == 0 ? r[0] : r[1], r2 = g == 0 ? r[1] : r[2], r3a = g <= 1 ? r[2] : r[3], r3b = g == 0 ? r[2] : r[3];
        u32x2 ob[5];
        ob[0] = u32x2{pk_bf16(o[0][0] * r[0], o[0][1] * r[0]), pk_bf16(o[0][2] * r[0], o[0][3] * r[0])};
        ob[1] = u32x2{pk_bf16(o[1][0] * r1a, o[1][1] * r1a), pk_bf16(o[1][2] * r[1], o[1][3] * r[1])};
        ob[2] = u32x2{pk_bf16(o[2][0] * r2, o[2][1] * r2), pk_bf16(o[2][2] * r2, o[2][3] * r2)};
        ob[3] = u32x2{pk_bf16(o[3][0] * r3a, o[3][1] * r3a), pk_bf16(o[3][2] * r3b, o[3][3] * r3b)};
        ob[4] = u32x2{pk_bf16(o[4][0] * r[3], o[4][1] * r[3]), pk_bf16(o[4][2] * r[3], o[4][3] * r[3])};
        SCHED();
        if (t8 == 7 && lane == 0) __hip_atomic_store((GAS u64*)(P.ws + WS_HINT + (rb * 8 + hg) * 128), tag, RLX_AGENT);
        SCHED();
#pragma unroll
        for (int t = 0; t < 3; ++t) {
            const u32x2 a = ob[2 * t], b = ob[t < 2 ? 2 * t + 1 : 4];
            const auto sx = __builtin_amdgcn_permlane16_swap(a.x, b.x, false, false), sy = __builtin_amdgcn_permlane16_swap(a.y, b.y, false, false);
            if (t < 2 || g == 0) st_sc1(wsr, ob0 + 1024 * t, not_empty(u32x4{sx[0], sy[0], sx[1], sy[1]}));
        }
    }
    __builtin_amdgcn_s_setprio(0);
    STAMP(9);
    if (t8 == 0) STAMP_OUT(5, blockIdx.x + 256);
    if (t8 == 7) STAMP_OUT(7, blockIdx.x + 768);
}

DEVI void phase2(const Params& P, lptr smem, int rt64, int cg, bool gemm, u64 tag, rsrc_t wsr) {
    const int lane = threadIdx.x & 63, w0 = __builtin_amdgcn_readfirstlane(threadIdx.x >> 6);
    const int w = gemm ? (w0 + 8) % 12 : w0, tid = w * 64 + lane;
    const int lo16 = lane & 15, g = lane >> 4;
    const int rt = w & 3, kt = w >> 2;
    STAMP_DECL; STAMP(1);
    const int pb = gemm ? P2_B : 55296;
    if (gemm && w < 4) flag_wait(smem, F_W0, 1);
    STAMP(2);
    const int qrow = (2 * (4 * g + (lo16 >> 2))) * 144;
    lptr qw = smem + qrow + (lane & 3) * 8, qw4 = smem + qrow + (16 + (lane & 1)) * 8;
    int wofs[3], oofs[3];
#pragma unroll
    for (int u = 0; u < 3; ++u) {
        const int c = kt * 3 + (u + cg) % 3;
        wofs[u] = c < (gemm ? 8 : 6) ? c * 9216 : WO8 + (gemm ? 0 : (c - 6) * 9216); oofs[u] = c * 2048;
    }
    auto rdo = [&](int i, int j) { return j < 4 ? lds_trfrag(qw + wofs[i >> 1] + (i & 1) * 4608 + 32 * j, 144) : lds_trfrag(qw4 + wofs[i >> 1] + (i & 1) * 4608, 144); };
    const unsigned ooff = WS_O + (unsigned)((rt64 * 4 + rt) * 18432 + lane * 16);
    auto ldo = [&](int i) { return __builtin_amdgcn_raw_buffer_load_b128(wsr, ooff + (i & 1) * 1024, oofs[i >> 1], 16); };
    u32x4 wf[6][5];
#pragma unroll
    for (int i = 0; i < 6; ++i)
#pragma unroll
        for (int j = 0; j < 5; ++j) wf[i][j] = rdo(i, j);
    const int r0 = tid < 576 ? tid / 9 : 0, c0 = tid - r0 * 9;
    unsigned boff = tid < 576 ? WS_B + (unsigned)((cg * 72 + (tid >> 3)) * 4096 + (rt64 >> 3) * 1024 + (((tid >> 2) & 1) * 32 + ((rt64 >> 2) & 1) * 16 + (rt64 & 3) * 4 + (tid & 3)) * 16) : WS_END;
    unsigned goff = WS_GATE + (unsigned)((rt64 * 64 + r0) * 4);
    unsigned qd = gemm ? __hip_atomic_load((LAS unsigned*)(smem + FL + F_QD), __ATOMIC_RELAXED, __HIP_MEMORY_SCOPE_WORKGROUP) : 8u;
    asm volatile("" : "+v"(boff), "+v"(goff));
    STAMP(3);
    if (w == 3) {
        const int a0 = cg * 72, g0 = (a0 >> 6) * 11 + (a0 & 63) / 6, a1 = a0 + 71, g1 = (a1 >> 6) * 11 + (a1 & 63) / 6;
        poll_addr<OSLEEP>(P, lane < 8 ? WS_HINT + ((rt64 >> 1) * 8 + lane) * 128 : WS_BFL + min(g0 + lane - 8, g1) * 128, tag);
        flag_set(F_H, 1);
    } else flag_wait<1>(smem, F_H, 1);
    STAMP(4);
    u32x4 bv; unsigned gv;
    f32x4 acc[5];
    {
        u32x4 of[6];
#pragma unroll
        for (int i = 0; i < 6; ++i) of[i] = ldo(i);
        bv = ld_sc1(wsr, boff); gv = __builtin_amdgcn_raw_buffer_load_b32(wsr, goff, 0, 16);
        SCHED();
#pragma unroll
        for (int j = 0; j < 5; ++j) acc[j] = f32x4{0.f, 0.f, 0.f, 0.f};
#pragma unroll
        for (int i = 0; i < 6; ++i)
#pragma unroll
            for (int j = 0; j < 5; ++j) acc[j] = mfma16(wf[i][j], of[i], acc[j]);
    }
    if (!__all(acc[0][0] == acc[0][0])) {
#pragma unroll
        for (int j = 0; j < 5; ++j) acc[j] = f32x4{0.f, 0.f, 0.f, 0.f};
        u32x4 of[6];
#pragma unroll 1
        for (unsigned spins = 0; spins < SPIN_CAP; ++spins) {
            CFENCE();
            unsigned m = 0;
#pragma unroll
            for (int i = 0; i < 6; ++i) of[i] = ldo(i);
#pragma unroll
            for (int i = 0; i < 6; ++i) m = max(m, umax4(of[i]));
            if (__all(m != EMPTY)) break;
        }
#pragma unroll
        for (int i = 0; i < 6; ++i)
#pragma unroll
            for (int j = 0; j < 5; ++j) acc[j] = mfma16(rdo(i, j), of[i], acc[j]);
    }
    if ((unsigned)__builtin_amdgcn_readfirstlane(qd) < 8u) flag_wait(smem, F_QD, 8);
    lptr ap = smem + P2_ACC + kt * (64 * P2_AP) + (rt * 16 + lo16) * P2_AP + g * 16;
#pragma unroll
    for (int j = 0; j < 5; ++j) if (j < 4 || g < 2) lds_w128(ap + j * 64, __builtin_bit_cast(u32x4, acc[j]));
    if (w < 9) {
#pragma unroll 1
        for (unsigned spins = 0; spins < SPIN_CAP; ++spins) {
            if (__all(umax4(bv) != EMPTY)) break;
            __builtin_amdgcn_s_sleep(2);
            CFENCE();
            bv = ld_sc1(wsr, boff);
        }
        lptr bp = smem + pb + ((tid & 3) * 16 + ((tid >> 2) & 1) * 8) * 144 + (tid >> 3) * 2;
#pragma unroll
        for (int q = 0; q < 4; ++q) { lds_w16(bp + (2 * q) * 144, bv[q]); lds_w16(bp + (2 * q + 1) * 144, bv[q] >> 16); }
    }
    STAMP(5);
    BARRIER();
    STAMP(6);
    if (w < 9) {
        lptr a = smem + P2_ACC + r0 * P2_AP + c0 * 32;
        const f32x4 s0 = __builtin_bit_cast(f32x4, lds_r128(a)) + __builtin_bit_cast(f32x4, lds_r128(a + 64 * P2_AP)) + __builtin_bit_cast(f32x4, lds_r128(a + 128 * P2_AP));
        const f32x4 s1 = __builtin_bit_cast(f32x4, lds_r128(a + 16)) + __builtin_bit_cast(f32x4, lds_r128(a + 64 * P2_AP + 16)) + __builtin_bit_cast(f32x4, lds_r128(a + 128 * P2_AP + 16));
        const u32x4 bb = lds_r128(smem + pb + tid * 16);
        const float gt = __uint_as_float(gv), og = rbf(1.f - gt);
        u32x4 o;
#pragma unroll
        for (int i = 0; i < 4; ++i) {
            const float a0 = i < 2 ? s0[2 * i] : s1[2 * i - 4], a1 = i < 2 ? s0[2 * i + 1] : s1[2 * i - 3];
            o[i] = pk_bf16(gt * a0 + og * bf2f(bb[i]), gt * a1 + og * bfhi(bb[i]));
        }
        __builtin_amdgcn_raw_buffer_store_b128(o, __builtin_amdgcn_make_buffer_rsrc(P.out, 0, SEQ * DM * 2, 0x00020000), (unsigned)((rt64 * 64 + r0) * 1152 + cg * 144 + c0 * 16), 0, OUTAUX);
    }
    STAMP(7);
    if (w == 4) STAMP_OUT(blockIdx.x < 128 ? 3 : 6, blockIdx.x + 512);
}

DEVI void helper_role(const Params& P, lptr smem, int xcd, int li, u64& tag, rsrc_t wsr) {
    const int tid = threadIdx.x, lane = tid & 63, w = __builtin_amdgcn_readfirstlane(tid >> 6);
    STAMP_DECL; STAMP(1);
    if (li >= 13) {
#pragma unroll 1
        for (int n = 12 * NTW + (li - 13) + 3 * w; n < 171; n += 36) touch_group(P, xcd, n, lane);
    }
    if (li == 12) {
#pragma unroll
        for (int i = 0; i < 7; ++i) {
            const int u = (i * 5 + xcd - 3) * 12 + w;
            if (u < 384) st_sc1(wsr, WS_PART + (unsigned)(u * 1024 + lane * 16), u32x4{EMPTY, EMPTY, EMPTY, EMPTY});
        }
        WAIT_VM(0);
    }
    PIN_TAG();
    BARRIER();
    if (tid == 0) {
        if (li == 12) __hip_atomic_store((GAS u64*)(P.ws + WS_PFL + (xcd - 3) * 128), tag, RLX_AGENT);
        tag_post(P, tag);
    }
    if (li >= 13) {
#pragma unroll 1
        for (int n = 171 + (li - 13) + 3 * w; n < 234; n += 36) touch_group(P, xcd, n, lane);
    }
    STAMP(2);
    if (w == 11) {
        STAMP_OUT(8, blockIdx.x);
        if (blockIdx.x == 255) {
            const GAS u64* f = (const GAS u64*)(P.ws + WS_ARR) + lane;
#pragma unroll 1
            for (unsigned spins = 0; spins < SPIN_CAP; ++spins) {
                const bool ok = __hip_atomic_load(f, RLX_AGENT) == tag && __hip_atomic_load(f + 64, RLX_AGENT) == tag &&
                                __hip_atomic_load(f + 128, RLX_AGENT) == tag && __hip_atomic_load(f + 192, RLX_AGENT) == tag;
                if (__all(ok)) break;
                __builtin_amdgcn_s_sleep(8);
            }
            if (lane == 0) __hip_atomic_store((GAS u64*)(P.ws + WS_CTL), tag, RLX_AGENT);
        }
    }
}

__global__ __launch_bounds__(768, 3) void k_fused(Params P) {
    __shared__ __attribute__((aligned(16))) char smem_[LDS_BYTES];
    lptr smem = (lptr)smem_;
    const int b = blockIdx.x, xcd = b & 7, idx = b >> 3;
    const int lane = threadIdx.x & 63, w = __builtin_amdgcn_readfirstlane(threadIdx.x >> 6);
    const rsrc_t wsr = __builtin_amdgcn_make_buffer_rsrc(P.ws, 0, (int)WS_END, 0x00020000);
    const int rt64 = xcd * 4 + (idx & 3), cg2 = idx >> 2;
    if (idx >= CPF && w < 6) {
        const char* pc; unsigned len;
        asm volatile("s_getpc_b64 %0\n.Lkf_code_begin:\n\ts_mov_b32 %1, .Lkf_code_end-.Lkf_code_begin" : "=s"(pc), "=s"(len));
        touch(pc, min((unsigned)(threadIdx.x * 128), len));
    }
    u64 tag;
    if (__builtin_expect(idx < 16, 1)) {
        const int rbl = idx & 3, hgl = idx >> 2, rb = (xcd >> 1) * 4 + rbl, hg = (xcd & 1) * 4 + hgl;
        if (__builtin_expect((w & 12) != 4, 1)) gemm_q(P, smem, rb, hg, w < 4 ? w : w - 4, tag, wsr);
        else gemm_kv(P, smem, rb, hg, w - 4, cg2, tag, wsr);
    } else {
        tag = *(const __attribute__((address_space(4))) u64*)(P.ws + WS_CTL);
        const int li = idx - 16, gid = li < 11 ? xcd * 11 + li : 88 + (li - 11) * 8 + xcd;
        if (gid < 99) ssm_role(P, smem, xcd, li, gid, cg2, tag, wsr);
        else {
            helper_role(P, smem, xcd, li, tag, wsr);
            if (w < 9) wo_issue<0x1ff>(P, cg2, w, 9, lane);
            if (w == 10 && lane < 16) lds_w32(smem + FL + lane * 4, 0u);
            WAIT_VM(0);
            BARRIER();
        }
    }
    phase2(P, smem, rt64, cg2, idx < 16, tag, wsr);
}

extern "C" void kernel_launch(void* const* d_in, const int* in_sizes, int n_in, void* d_out, int out_size, void* d_ws, size_t ws_size,
                              hipStream_t stream) {
    Params P;
    P.x = (const char*)d_in[0]; P.wqkv = (const char*)d_in[1]; P.wo = (const char*)d_in[2];
    P.logA = (const char*)d_in[4]; P.aim = (const char*)d_in[5]; P.cre = (const char*)d_in[6]; P.cim = (const char*)d_in[7];
    P.logdt = (const char*)d_in[8]; P.dskip = (const char*)d_in[9]; P.gw = (const char*)d_in[10];
    P.out = (char*)d_out; P.ws = (char*)d_ws;
    k_fused<<<256, 768, 0, stream>>>(P);
}
```

```cpp
#include <hip/hip_runtime.h>
#include <stdint.h>

#define GAS __attribute__((address_space(1)))
#define LAS __attribute__((address_space(3)))
#define DEVI __device__ __forceinline__
typedef unsigned short u16;
typedef unsigned long long u64;
typedef __attribute__((ext_vector_type(2))) unsigned u32x2;
typedef __attribute__((ext_vector_type(3))) unsigned u32x3;
typedef __attribute__((ext_vector_type(4))) unsigned u32x4;
typedef __attribute__((ext_vector_type(4))) short s16x4;
typedef __attribute__((ext_vector_type(2))) float f32x2;
typedef __attribute__((ext_vector_type(4))) float f32x4;
typedef __attribute__((ext_vector_type(16))) float f32x16;
typedef __attribute__((ext_vector_type(2))) __bf16 bf16x2;
typedef __attribute__((ext_vector_type(8))) __bf16 bf16x8;
typedef __attribute__((ext_vector_type(2))) _Float16 f16x2;
typedef __attribute__((ext_vector_type(8))) _Float16 f16x8;
typedef LAS char* lptr;

#define PREF 1
#define ROT 0
#define FSLEEP 2
#define NSLEEP "3"
#define LDRIFT 0
#define PSLEEP 6
#define CSLEEP 1
#define OSLEEP 2
#define RSLEEP 12
#define HOLD 1
#define GROT ((w + li) & 3)
#define NTW 8
#define OUTAUX 17
#define TEARLY 1
#define HDELAY 0
#define CPF 16
#define LA3 0
#define KSLEEP 3
#define QH 6
constexpr int SEQ = 2048, DM = 576;

constexpr unsigned WS_CTL = 0;
constexpr unsigned WS_ARR = 256;
constexpr unsigned WS_BFL = 4096;
constexpr unsigned WS_HINT = 20480;
constexpr unsigned WS_GATE = 36864;
constexpr unsigned WS_PFL = 49152;
constexpr unsigned WS_KH = 65536;
constexpr unsigned WS_PART = 81920;
constexpr unsigned WS_O = WS_PART + 8 * 16 * 3072;
constexpr unsigned WS_B = WS_O + SEQ * DM * 2;
constexpr unsigned WS_END = WS_B + SEQ * DM * 2;
constexpr unsigned EMPTY = 0xffffffffu;

constexpr size_t WS_STAMP = 16u << 20;
#define STAMP_DECL
#define STAMP(i)
#define CSTAMP(i)
#define STAMP_OUT(role, slot)

struct Params {
    const char* x; const char* wqkv; const char* wo; const char* logA; const char* aim; const char* cre; const char* cim;
    const char* logdt; const char* dskip; const char* gw; char* out; char* ws;
};

constexpr int LDS_BYTES = 163840, FL = 163584, LDS_TAG = FL + 64, LDS_DUMMY = FL + 128, LDS_JUNK = 161280;
enum { F_SW = 0, F_LANDED = 4, F_W1 = 20, F_B = 28, F_H = 24, F_KVP = 36, F_KVR = 40, F_Q0 = 44, F_QD = 48, F_D = 52, F_C1 = 56, F_W0 = 60 };
constexpr int WO8 = 132096, P2_ACC = 73728, P2_AP = 304, P2_B = 141312, P2_G = 150528;

DEVI float bf2f(unsigned h) { return __uint_as_float(h << 16); }
DEVI float bfhi(unsigned h) { return __uint_as_float(h & 0xffff0000u); }
DEVI unsigned pk_bf16(float a, float b) { return __builtin_bit_cast(unsigned, __builtin_convertvector(f32x2{a, b}, bf16x2)); }
DEVI unsigned pk_f16(f32x2 v) { return __builtin_bit_cast(unsigned, __builtin_convertvector(v, f16x2)); }
DEVI float rbf(float a) { return bf2f(pk_bf16(a, a)); }
DEVI f32x4 mfma16(u32x4 a, u32x4 b, f32x4 c) {
    return __builtin_amdgcn_mfma_f32_16x16x32_bf16(__builtin_bit_cast(bf16x8, a), __builtin_bit_cast(bf16x8, b), c, 0, 0, 0);
}
DEVI void mfma16i(f32x4& c, u32x4 a, u32x4 b) { asm volatile("v_mfma_f32_16x16x32_bf16 %0, %1, %2, %0" : "+v"(c) : "v"(a), "v"(b)); }
#define MFMA_DONE() do { __builtin_amdgcn_sched_barrier(0); asm volatile("s_nop 7\n\ts_nop 1" ::: "memory"); __builtin_amdgcn_sched_barrier(0); } while (0)
DEVI u32x4 lds_r128(lptr p) { return *(LAS u32x4*)p; }
DEVI u32x2 lds_r64(lptr p) { return *(LAS u32x2*)p; }
DEVI float lds_rf(lptr p) { return *(LAS float*)p; }
DEVI void lds_w128(lptr p, u32x4 v) { *(LAS u32x4*)p = v; }
DEVI void lds_w64(lptr p, u32x2 v) { *(LAS u32x2*)p = v; }
DEVI void lds_w32(lptr p, unsigned v) { *(LAS unsigned*)p = v; }
DEVI void lds_w16(lptr p, unsigned v) { *(LAS u16*)p = (u16)v; }
DEVI u32x2 lds_tr(lptr p) { return __builtin_bit_cast(u32x2, __builtin_amdgcn_ds_read_tr16_b64_v4i16((LAS s16x4*)p)); }
DEVI u32x4 lds_trfrag(lptr p, int off2) { u32x2 a = lds_tr(p), b = lds_tr(p + off2); return u32x4{a.x, a.y, b.x, b.y}; }
template <int OFF> DEVI void glds16o(const char* sbase, unsigned voff) {
    asm volatile("global_load_lds_dwordx4 %0, %1 offset:%2" :: "v"(voff), "s"(sbase), "i"(OFF) : "memory");
}
DEVI void glds16(const char* sbase, unsigned voff, unsigned l) {
    asm volatile("s_mov_b32 m0, %2\n\ts_nop 0\n\tglobal_load_lds_dwordx4 %0, %1" :: "v"(voff), "s"(sbase), "s"(l) : "memory");
}
#define SET_M0(v) asm volatile("s_mov_b32 m0, %0\n\ts_nop 0" :: "s"(v) : "memory")
#define W_ADDTID(v, off) asm volatile("ds_write_addtid_b32 %0 offset:%1" :: "v"(v), "i"(off) : "memory")
#define CFENCE() asm volatile("" ::: "memory")
#define SCHED() __builtin_amdgcn_sched_barrier(0)
#define WAIT_VM(n) asm volatile("s_waitcnt vmcnt(" #n ")" ::: "memory")
#define WAIT_LGKM(n) __builtin_amdgcn_s_waitcnt(0xC07F | ((n) << 8))
#define WAIT_LGKM0() asm volatile("s_waitcnt lgkmcnt(0)" ::: "memory")
#define BARRIER() do { SCHED(); asm volatile("s_waitcnt lgkmcnt(0)\n\ts_barrier" ::: "memory"); SCHED(); } while (0)
#define RAW_BARRIER() do { SCHED(); asm volatile("s_barrier" ::: "memory"); SCHED(); } while (0)
#define RLX_AGENT __ATOMIC_RELAXED, __HIP_MEMORY_SCOPE_AGENT
constexpr unsigned SPIN_CAP = 1u << 16;
typedef __amdgpu_buffer_rsrc_t rsrc_t;
DEVI u32x4 ld_sc1(rsrc_t r, unsigned off) { return __builtin_amdgcn_raw_buffer_load_b128(r, off, 0, 16); }
DEVI void st_sc1(rsrc_t r, unsigned off, u32x4 v) { __builtin_amdgcn_raw_buffer_store_b128(v, r, off, 0, 16); }

DEVI u32x4 pack8(f32x4 a, f32x4 b) { return u32x4{pk_bf16(a[0], a[1]), pk_bf16(a[2], a[3]), pk_bf16(b[0], b[1]), pk_bf16(b[2], b[3])}; }
DEVI float phi(float t) {
    const float e = __builtin_amdgcn_fmed3f(__builtin_amdgcn_exp2f(t * 1.44269504089f), 0.f, 1.f);
    return __int_as_float(max(__float_as_int(t), 0x358637bd)) + e;
}
DEVI f32x4 phi4(f32x4 v) {
    f32x2 a, b;
    const f32x2 c = {1.44269504089f, 1.44269504089f};
    asm("v_pk_mul_f32 %0, %2, %4\n\tv_pk_mul_f32 %1, %3, %4" : "=&v"(a), "=&v"(b) : "v"(f32x2{v[0], v[1]}), "v"(f32x2{v[2], v[3]}), "s"(c));
    f32x4 r;
#pragma unroll
    for (int i = 0; i < 4; ++i)
        r[i] = __int_as_float(max(__float_as_int(v[i]), 0x358637bd)) + __builtin_amdgcn_fmed3f(__builtin_amdgcn_exp2f(i < 2 ? a[i] : b[i - 2]), 0.f, 1.f);
    return r;
}
DEVI unsigned umax4(u32x4 a) { return max(max(a.x, a.y), max(a.z, a.w)); }
DEVI u32x4 not_empty(u32x4 a) { return u32x4{min(a.x, EMPTY - 1), min(a.y, EMPTY - 1), min(a.z, EMPTY - 1), min(a.w, EMPTY - 1)}; }
DEVI void tag_post(const Params& P, u64 tag) { __hip_atomic_store((GAS u64*)(P.ws + WS_ARR) + blockIdx.x, tag, RLX_AGENT); }
#define PIN_TAG() do { asm volatile("" : "+s"(tag)); tag += 1ull; } while (0)
template <int SL = PSLEEP>
DEVI void poll_addr(const Params& P, unsigned off, u64 tag) {
    const GAS u64* f = (const GAS u64*)(P.ws + off);
#pragma unroll 1
    for (unsigned spins = 0; spins < SPIN_CAP; ++spins) {
        if (__all(__hip_atomic_load(f, RLX_AGENT) == tag)) break;
        __builtin_amdgcn_s_sleep(SL);
    }
}
template <int SL = PSLEEP>
DEVI void poll_flags(const Params& P, unsigned off, int n, u64 tag, int lane) { poll_addr<SL>(P, off + (lane < n ? lane : 0) * 128, tag); }
DEVI unsigned flag_get(lptr smem, int f) { return __builtin_amdgcn_readfirstlane(__hip_atomic_load((LAS unsigned*)(smem + FL + f), __ATOMIC_RELAXED, __HIP_MEMORY_SCOPE_WORKGROUP)); }
template <int SL = FSLEEP>
DEVI void flag_wait(lptr smem, int f, unsigned v) {
#pragma unroll 1
    for (unsigned spins = 0; spins < (SPIN_CAP << 4); ++spins) { if (flag_get(smem, f) >= v) break; if (SL) __builtin_amdgcn_s_sleep(SL); }
    CFENCE();
}
DEVI void flag_add(int f) { asm volatile("s_mov_b64 exec, 1\n\tds_add_u32 %0, %1\n\ts_mov_b64 exec, -1" :: "v"(FL + f), "v"(1u) : "memory"); }
#define FLAG_ADD(f) asm volatile("s_mov_b64 exec, 1\n\tds_add_u32 %0, %1 offset:%2\n\ts_mov_b64 exec, -1" :: "v"(flp), "v"(one), "i"(f) : "memory")
DEVI void flag_set(int f, unsigned v) { asm volatile("s_mov_b64 exec, 1\n\tds_write_b32 %0, %1\n\ts_mov_b64 exec, -1" :: "v"(FL + f), "v"(v) : "memory"); }
#define NEEDP(seen, ptr, f, v) do { unsigned c_; asm volatile( \
        "v_cmp_gt_u32 vcc, %4, %1\n\ts_cbranch_vccnz 0f\n1:\n\t.subsection 1\n" \
        "0:\n\ts_mov_b32 %0, 0x100000\n2:\n\ts_sleep " NSLEEP "\n\tds_read_b32 %1, %2 offset:%3\n\ts_sub_u32 %0, %0, 1\n\ts_waitcnt lgkmcnt(0)\n\ts_cbranch_scc1 1b\n\t" \
        "v_cmp_gt_u32 vcc, %4, %1\n\ts_cbranch_vccnz 2b\n\ts_branch 1b\n\t.subsection 0" \
        : "=&s"(c_), "+v"(seen) : "v"(ptr), "i"(f), "i"(v) : "memory", "scc", "vcc"); } while (0)
#define NEED(seen, f, v) NEEDP(seen, flp, f, v)
DEVI void touch(const char* sbase, unsigned voff) {
    asm volatile("s_mov_b32 m0, %2\n\ts_nop 0\n\tglobal_load_lds_dword %0, %1" :: "v"(voff), "s"(sbase), "s"(LDS_JUNK) : "memory");
}
DEVI const char* group_addr(const Params& P, int xcd, int n, int lane) {
    if (n < 126) {
        const int c0 = n / 18, r = n - c0 * 18, c = c0 + 2, l = r - 8;
        return r < 8 ? P.x + (unsigned)(((xcd >> 1) * 512 + r * 64 + lane) * 1152 + c * 128)
                     : P.wqkv + (unsigned)((c * 64 + lane) * 3456 + (l < 5 ? 1152 : 2304 - 640) + (xcd & 1) * 512 + l * 128);
    }
    const int c = (n - 126) / 5, l = n - 126 - c * 5;
    return P.wqkv + (unsigned)((c * 64 + lane) * 3456 + (xcd & 1) * 512 + l * 128);
}
DEVI void touch_group(const Params& P, int xcd, int n, int lane) {
    if (n < 126) {
        const int c0 = n / 18, r = n - c0 * 18, c = c0 + 2;
        if (r < 8) touch(P.x, (unsigned)(((xcd >> 1) * 512 + r * 64 + lane) * 1152 + c * 128));
        else { const int l = r - 8; touch(P.wqkv, (unsigned)((c * 64 + lane) * 3456 + (l < 5 ? 1152 : 2304 - 640) + (xcd & 1) * 512 + l * 128)); }
    } else if (n < 171) {
        const int c = (n - 126) / 5, l = n - 126 - c * 5;
        touch(P.wqkv, (unsigned)((c * 64 + lane) * 3456 + (xcd & 1) * 512 + l * 128));
    } else {
        const int l = (n - 171) / 9, c = n - 171 - l * 9;
        touch(P.wo, (unsigned)((c * 64 + lane) * 1152 + l * 128));
    }
}
template <unsigned CH>
DEVI void wo_issue(const Params& P, int cg, int first, int stride, int lane) {
#pragma unroll 1
    for (int m = first; m < 9; m += stride) {
        const int q = 64 * m + lane, rho = q / 9, pc = q - rho * 9;
        const int k = (rho & 32) + 8 * ((rho >> 3) & 3) + 4 * (rho & 1) + ((rho >> 1) & 3);
        const unsigned qo = (unsigned)(k * 1152 + cg * 144 + pc * 16);
#pragma unroll
        for (int c = 0; c < 9; ++c) if ((CH >> c) & 1) glds16(P.wo + c * (64 * 1152), qo, (unsigned)((c < 6 ? c * 9216 : WO8 + (c - 6) * 9216) + m * 1024));
    }
}

constexpr int SP = 3088;
constexpr int XT_OFF = 133632, XT_BYTES = 4608, XPAD = 256;
constexpr int TT_OFF = 104448, TC = 1216, PT_OFF = 55296, PC_OFF = 2560, ACX_OFF = 55296;

#define CM2(o1, p1, a1, b1, o2, p2, a2, b2) do { asm( \
    "v_pk_mul_f32 %0, %2, %3 op_sel:[1,1] op_sel_hi:[1,0] neg_lo:[0,1]\n\tv_pk_mul_f32 %1, %4, %5 op_sel:[1,1] op_sel_hi:[1,0] neg_lo:[0,1]\n\t" \
    "v_pk_fma_f32 %0, %2, %3, %0 op_sel_hi:[0,1,1]\n\tv_pk_fma_f32 %1, %4, %5, %1 op_sel_hi:[0,1,1]" \
    : "=&v"(o1), "=&v"(o2) : "v"(a1), "v"(b1), "v"(a2), "v"(b2)); \
    asm("v_cvt_pk_f16_f32 %0, %2, %3\n\tv_cvt_pk_f16_f32 %1, %4, %5" : "=&v"(p1), "=&v"(p2) : "v"((o1).x), "v"((o1).y), "v"((o2).x), "v"((o2).y)); } while (0)
#define HM2(p1, a1, p2, a2, b) asm( \
    "v_pk_mul_f16 %0, %2, %4 op_sel_hi:[0,1]\n\tv_pk_mul_f16 %1, %3, %4 op_sel_hi:[0,1]\n\t" \
    "v_pk_fma_f16 %0, %2, %4, %0 op_sel:[1,1,0] op_sel_hi:[1,0,1] neg_lo:[0,1,0]\n\tv_pk_fma_f16 %1, %3, %4, %1 op_sel:[1,1,0] op_sel_hi:[1,0,1] neg_lo:[0,1,0]" \
    : "=&v"(p1), "=&v"(p2) : "v"(a1), "v"(a2), "v"(b))
DEVI f32x2 cexp_p(float a2, float f, float p) {
    float m = __builtin_amdgcn_exp2f(a2 * p);
    float r = __builtin_amdgcn_fractf(f * p);
    return f32x2{m * __builtin_amdgcn_cosf(r), m * __builtin_amdgcn_sinf(r)};
}
DEVI u32x4 mask_frag(unsigned bits, int g, unsigned val) {
    unsigned by = bits >> (8 * g);
    u32x4 r;
#pragma unroll
    for (int i = 0; i < 4; ++i) { unsigned t = (by >> (2 * i)) & 3u; r[i] = ((t | (t << 15)) & 0x10001u) * val; }
    return r;
}
struct Tab2 { unsigned v[8][64][4]; };
constexpr Tab2 make_tab2() {
    Tab2 t{};
    for (int l = 0; l < 64; ++l) {
        const int r = l & 15, g = l >> 4, j = l & 31, h = l >> 5;
        const unsigned bits[6] = {7u << (r + 11), 31u << (r + 6), 7u << (r + 3), 127u << (r + 5), 511u << (r + 4), 3u << (r + 12)};
        for (int m = 0; m < 6; ++m)
            for (int i = 0; i < 4; ++i) {
                const unsigned q = (bits[m] >> (8 * g + 2 * i)) & 3u, val = m < 5 ? 0x3f80u : 0xffffu;
                t.v[m][l][i] = (q & 1 ? val : 0u) | (q & 2 ? val << 16 : 0u);
            }
        for (int c = 0; c < 4; ++c) {
            const int v2 = 79 - (32 + (j >> 1)) - c + 4, v3 = 127 - (64 + 32 * h + (j >> 1)) - c + 4;
            t.v[6][l][c] = PT_OFF + (j & 1) * 1280 + c * 320 + (v2 >> 2) * 16 + h * 8 + (v2 & 3) * 2 - 64;
            t.v[7][l][c] = PT_OFF + PC_OFF + c * 320 + (v3 >> 2) * 16 + (j & 1) * 8 + (v3 & 3) * 2 - 64;
        }
    }
    return t;
}
__device__ const Tab2 g_tab2 = make_tab2();
DEVI float wave_max(float v) {
    v = fmaxf(v, __int_as_float(__builtin_amdgcn_update_dpp(0, __float_as_int(v), 0xB1, 0xf, 0xf, true)));
    v = fmaxf(v, __int_as_float(__builtin_amdgcn_update_dpp(0, __float_as_int(v), 0x4E, 0xf, 0xf, true)));
    v = fmaxf(v, __int_as_float(__builtin_amdgcn_update_dpp(0, __float_as_int(v), 0x124, 0xf, 0xf, true)));
    v = fmaxf(v, __int_as_float(__builtin_amdgcn_update_dpp(0, __float_as_int(v), 0x128, 0xf, 0xf, true)));
    const int i = __float_as_int(v);
    const int a = max(__builtin_amdgcn_readlane(i, 0), __builtin_amdgcn_readlane(i, 16)), b = max(__builtin_amdgcn_readlane(i, 32), __builtin_amdgcn_readlane(i, 48));
    return __int_as_float(max(a, b));
}

DEVI void ssm_role(const Params& P, lptr smem, int xcd, int li, int gid, int cg2, u64& tag, rsrc_t wsr) {
    const int gl = gid / 11, gi = gid - gl * 11, ch0 = gl * 64 + (gi < 10 ? gi * 6 : 58);
    const int tid = threadIdx.x, lane = tid & 63, w = __builtin_amdgcn_readfirstlane(tid >> 6);
    const int ch = w >> 1, half = (w ^ (w >> 2)) & 1, d = ch0 + ch;
    const bool own = gi < 10 || ch >= 2;
    const int lo16 = lane & 15, g = lane >> 4;
    STAMP_DECL; STAMP(1);
    __builtin_amdgcn_s_setprio(3);
    const unsigned po = (unsigned)(d * 256 + lane * 4);
    const unsigned r_la = *(const GAS unsigned*)(P.logA + po), r_ai = *(const GAS unsigned*)(P.aim + po);
    const unsigned r_cr = *(const GAS unsigned*)(P.cre + po), r_ci = *(const GAS unsigned*)(P.cim + po);
    const unsigned r_dt = *(const GAS u16*)(P.logdt + d * 2), r_ds = *(const GAS u16*)(P.dskip + d * 2);
    u32x4 ct[8];
    if (half) {
        const char* tp = (const char*)&g_tab2;
        asm volatile("" : "+s"(tp));
#pragma unroll
        for (int i = 0; i < 8; ++i) ct[i] = *(const GAS u32x4*)(tp + (unsigned)(i * 1024 + lane * 16));
    }
    const char* tga = (w < NTW && li < 12) ? group_addr(P, xcd, w * 12 + li, lane) : P.logA + po;
    RAW_BARRIER();
    const unsigned junk = *(const GAS unsigned*)tga;
    SCHED();
    const int tt = (tid + (li & 7) * 64) & 511;
    u32x3 xr[4];
    const unsigned xoff = (unsigned)(tt * 4608 + ch0 * 2);
    const unsigned boff = own ? WS_B + (unsigned)(d * 4096 + half * 2048 + lane * 16) : WS_END;
#define GATHER(i) do { SCHED(); if (w < 8) xr[i] = *(const GAS u32x3*)(P.x + xoff + (i) * 1152); SCHED(); } while (0)
#define PRIO(i) do { SCHED(); __builtin_amdgcn_s_setprio(i); SCHED(); } while (0)
    SCHED();
    lptr xt = smem + XT_OFF + ch * XT_BYTES, tb = smem + TT_OFF + ch * 4864, pt = smem + PT_OFF + ch * 3840;
    const int j32 = lane & 31, h = lane >> 5;
    if (!half) { lds_w64(xt + lane * 8, u32x2{0u, 0u}); lds_w32(smem + FL + 32, 0u); }
    int arow;
    {
        const u64 map0 = 0x0a09080803020100ull, map1 = 0x0c0b0a0a07060504ull, map2 = 0x1413110e12100f0dull;
        const u64 mm = j32 < 8 ? map0 : (j32 < 16 ? map1 : map2);
        arow = (int)((mm >> (8 * (j32 & 7))) & 0xff);
    }
    lptr ap = smem + arow * SP + w * 256 + h * 16, bp = smem + j32 * SP + w * 256 + h * 16;
    const int cpy = (3 - lo16) & 3;
    lptr xf7 = xt + (XPAD - 16 + 16 * lo16 + 8 * g) * 2 - 64 * 7;
    lptr tf = tb + cpy * TC + 2 * (527 - lo16 + 8 * g - cpy) - 64 * 16;
    lptr xc = smem + (w ^ 1) * 256 + lane * 4, xb = smem + XT_OFF + (XPAD + tt * 4) * 2;
    asm volatile("" : "+v"(ap), "+v"(bp), "+v"(xf7), "+v"(tf), "+v"(xc), "+v"(xb));
    const float dt = rbf(__expf(bf2f(r_dt)));
    const float cm = wave_max(fmaxf(fmaxf(fabsf(bf2f(r_cr)), fabsf(bfhi(r_cr))), fmaxf(fabsf(bf2f(r_ci)), fabsf(bfhi(r_ci)))));
    unsigned ce = __float_as_uint(cm) >> 23; ce = ce < 16u ? 16u : (ce > 240u ? 240u : ce);
    const float cs = __uint_as_float((263u - ce) << 23), ics = __uint_as_float((ce - 9u) << 23);
    f32x16 kacc;
#pragma unroll
    for (int i = 0; i < 16; ++i) kacc[i] = 0.f;
    STAMP(2);
    {
        const unsigned sh = half * 16;
        asm volatile("" :: "v"(r_la), "v"(r_ai), "v"(r_ds), "v"(ct[7]));
        const float la = bf2f(r_la >> sh), ai = bf2f(r_ai >> sh), cr = bf2f(r_cr >> sh) * cs, ci = bf2f(r_ci >> sh) * cs;
        const float a2 = (-rbf(__expf(la)) * dt) * 1.44269504089f, f = (ai * dt) * 0.15915494309f;
#define PUT(row, v) W_ADDTID(v, ((row) & 15) * SP)
        const f32x2 w1 = cexp_p(a2, -f, 1.f), W = cexp_p(a2, -f, 256.f);
        f32x2 u[8], v[4], c[9], e4, e8, t2;
        unsigned U[8], V[4], p, q;
        u[1] = cexp_p(a2, f, 8.f); v[1] = cexp_p(a2, f, 64.f);
        c[0] = f32x2{cr, -ci};
        U[1] = pk_f16(u[1]); V[1] = pk_f16(v[1]);
        GATHER(0);
        SET_M0(w * 256);
        PUT(0, pk_f16(c[0]));
        CM2(c[1], p, c[0], w1, u[2], U[2], u[1], u[1]); PUT(1, p);
        CM2(c[2], p, c[1], w1, u[3], U[3], u[2], u[1]); PUT(2, p);
        CM2(c[3], p, c[2], w1, u[4], U[4], u[2], u[2]); PUT(3, p);
        CM2(c[4], p, c[3], w1, u[5], U[5], u[4], u[1]); PUT(4, p);
        CM2(c[5], p, c[4], w1, u[6], U[6], u[4], u[2]); PUT(5, p);
        CM2(c[6], p, c[5], w1, u[7], U[7], u[4], u[3]); PUT(6, p);
        CM2(c[7], p, c[6], w1, v[2], V[2], v[1], v[1]); PUT(7, p);
        CM2(c[8], p, c[7], w1, v[3], V[3], v[2], v[1]);
        PRIO(2);
        GATHER(1);
        CM2(e8, p, c[8], W, e4, q, c[4], W); PUT(12, p); PUT(10, q);
        CM2(c[8], p, e8, W, c[4], q, e4, W); PUT(14, p); PUT(13, q);
        CM2(t2, p, c[0], W, e4, q, c[4], W); PUT(8, p); PUT(15, q);
        CM2(t2, p, c[2], W, e4, q, c[6], W); PUT(9, p); PUT(11, q);
        SET_M0(w * 256 + 16 * SP);
        CM2(e8, p, c[8], W, t2, q, W, W); PUT(17, p);
        CM2(e4, p, c[8], t2, c[0], q, e8, t2); PUT(16, p); PUT(18, q);
        CM2(c[8], p, e4, t2, e8, q, c[0], t2); PUT(19, p); PUT(20, q);
        PRIO(1);
        GATHER(2);
        u32x4 af[8];
#pragma unroll
        for (int s = 0; s < 8; ++s) af[s] = lds_r128(ap + 32 * s);
        CFENCE();
        PUT(16, V[2]); PUT(24, V[3]);
#pragma unroll
        for (int b = 1; b < 8; ++b) { HM2(p, V[2], q, V[3], U[b]); PUT(16 + b, p); PUT(24 + b, q); }
        SET_M0(w * 256);
        PRIO(0);
        GATHER(3);
        PUT(0, 0x3c00u); PUT(8, V[1]);
#pragma unroll
        for (int b = 1; b < 8; b += 2) { HM2(p, U[b], q, U[b == 7 ? 7 : b + 1], V[1]); PUT(8 + b, p); if (b < 7) PUT(9 + b, q); PUT(b, U[b]); if (b < 7) PUT(b + 1, U[b + 1]); }
#undef PUT
        u32x4 bfr[8];
#pragma unroll
        for (int s = 0; s < 8; ++s) bfr[s] = lds_r128(bp + 32 * s);
        SCHED();
        SCHED();
#pragma unroll
        for (int s = 0; s < 8; ++s)
            kacc = __builtin_amdgcn_mfma_f32_32x32x16_f16(__builtin_bit_cast(f16x8, af[s]), __builtin_bit_cast(f16x8, bfr[s]), kacc, 0, 0, 0);
    }
    STAMP(3);
    const float ks = 2.f * dt * ics;
#pragma unroll
    for (int i = 0; i < 12; ++i) kacc[i] *= ks;
    if (half) {
#pragma unroll
        for (int i = 0; i < 8; ++i) W_ADDTID(kacc[i], i * SP);
    } else {
#pragma unroll
        for (int i = 0; i < 4; ++i) W_ADDTID(kacc[8 + i], i * SP);
    }
    PIN_TAG();
    STAMP(4);
    BARRIER();
    STAMP(5);
    if (tid == 704) tag_post(P, tag);
    auto cleared = [&]() {
        if (w < 4) {
            WAIT_VM(0);
            if (lane == 0 && __hip_atomic_fetch_add((LAS unsigned*)(smem + FL + 32), 1u, __ATOMIC_RELAXED, __HIP_MEMORY_SCOPE_WORKGROUP) == 3u)
                __hip_atomic_store((GAS u64*)(P.ws + WS_BFL + gid * 128), tag, RLX_AGENT);
        }
    };
    auto x_in = [&]() {
        asm volatile("" :: "v"(junk));
        if (w < 8) {
#pragma unroll
            for (int k = 0; k < 3; ++k) {
                u32x2 lo = {__builtin_amdgcn_perm(xr[1][k], xr[0][k], 0x05040100u), __builtin_amdgcn_perm(xr[3][k], xr[2][k], 0x05040100u)};
                u32x2 hi = {__builtin_amdgcn_perm(xr[1][k], xr[0][k], 0x07060302u), __builtin_amdgcn_perm(xr[3][k], xr[2][k], 0x07060302u)};
                lds_w64(xb + (2 * k) * XT_BYTES, lo); lds_w64(xb + (2 * k + 1) * XT_BYTES, hi);
            }
        }
        if (w < 4) {
#pragma unroll
            for (int i = 0; i < 6; ++i)
                if (i >= 2 || gi < 10) __builtin_amdgcn_raw_buffer_store_b128(u32x4{EMPTY, EMPTY, EMPTY, EMPTY}, wsr, (unsigned)(tid * 16), WS_B + (ch0 + i) * 4096, 16);
        }
        BARRIER();
        if (w >= 8) wo_issue<0x03f>(P, cg2, w - 8, 4, lane);
    };
    auto TF = [&](int off) { u32x2 a = lds_r64(tf + off), b = lds_r64(tf + off + 8); return u32x4{a.x, a.y, b.x, b.y}; };
    f32x4 acc[8];
#pragma unroll
    for (int i = 0; i < 8; ++i) acc[i] = f32x4{0.f, 0.f, 0.f, 0.f};
    lptr axr;
    if (!half) {
        __builtin_amdgcn_s_setprio(2);
#pragma unroll
        for (int i = 0; i < 8; ++i) kacc[i] += lds_rf(xc + i * SP);
        unsigned dd[6];
        dd[0] = pk_bf16(kacc[7], kacc[6]); dd[1] = pk_bf16(kacc[5], kacc[4]); dd[2] = pk_bf16(kacc[3], kacc[2]);
        dd[3] = pk_bf16(kacc[1], kacc[0] + (lane == 0 ? bf2f(r_ds) : 0.f));
        dd[4] = __builtin_amdgcn_update_dpp(0, dd[0], 0x138, 0xf, 0xf, true);
        dd[5] = __builtin_amdgcn_update_dpp(0, dd[1], 0x138, 0xf, 0xf, true);
        unsigned mm[5];
#pragma unroll
        for (int i = 0; i < 5; ++i) mm[i] = __builtin_amdgcn_alignbit(dd[i + 1], dd[i], 16);
        lptr tw = tb + 2 * (536 - 8 * lane);
        lds_w128(tw, u32x4{dd[0], dd[1], dd[2], dd[3]}); lds_w128(tw + TC, u32x4{mm[0], mm[1], mm[2], mm[3]});
        lds_w128(tw + 2 * TC, u32x4{dd[1], dd[2], dd[3], dd[4]}); lds_w128(tw + 3 * TC, u32x4{mm[1], mm[2], mm[3], mm[4]});
        {
            const unsigned n0 = __builtin_amdgcn_readlane(dd[0], 63), n1 = __builtin_amdgcn_readlane(dd[1], 63);
            const unsigned kk = (n0 & 0xffffu) * 0x10001u, kh = n0 << 16, m4 = (n0 >> 16) | (n1 << 16);
            const int c = lane & 3, bl = lane >> 2;
            u32x4 e;
            e.x = 0u; e.y = lane == 14 ? kh : (lane == 15 ? kk : 0u); e.z = lane == 12 ? kh : (bl == 3 ? kk : 0u);
            e.w = lane == 14 ? n0 : (lane == 15 ? m4 : (bl == 3 ? kk : 0u));
            if (bl < 6) lds_w128(tb + c * TC + bl * 16 + (bl >> 2) * 1024, e);
        }
        x_in();
        u32x4 X[8];
#pragma unroll
        for (int i = 0; i < 8; ++i) X[i] = lds_r128(xf7 + 64 * 6 + 512 * i);
        u32x4 k0 = TF(1024 - 64), k8 = TF(512 - 64);
#pragma unroll
        for (int i = 0; i < 8; ++i) asm volatile("" : "+v"(acc[i]));
        __builtin_amdgcn_s_setprio(0);
        STAMP(6);
#pragma unroll
        for (int c = 1; c < 8; ++c) {
            u32x4 k0n, k8n;
            if (c < 7) { k0n = TF(1024 - 64 * (c + 1)); k8n = TF(512 - 64 * (c + 1)); }
#pragma unroll
            for (int i = 0; i < 8; ++i) {
                SCHED();
                mfma16i(acc[i], k0, X[i]);
                if (i < 7) mfma16i(acc[i + 1], k8, X[i]);
                SCHED();
                if (c < 7) X[i] = lds_r128(xf7 + 64 * (6 - c) + 512 * i);
            }
            k0 = k0n; k8 = k8n;
            if (c == 5) { SCHED(); cleared(); SCHED(); }
        }
        MFMA_DONE();
        SET_M0(ACX_OFF - 40960 + ch * 8192);
#pragma unroll
        for (int i = 0; i < 16; ++i) W_ADDTID(acc[4 + (i >> 2)][i & 3], 40960 + i * 256);
        axr = smem + ACX_OFF + ch * 8192 + 4096 + lane * 4;
    } else {
        u32x4 X[8];
#pragma unroll
        for (int i = 0; i < 4; ++i) if (i < 3 || lane < 48) lds_w128(pt + i * 1024 + lane * 16, u32x4{0u, 0u, 0u, 0u});
        CFENCE();
        const unsigned p89 = pk_bf16(kacc[8] + lds_rf(xc), kacc[9] + lds_rf(xc + SP)), pab = pk_bf16(kacc[10] + lds_rf(xc + 2 * SP), kacc[11] + lds_rf(xc + 3 * SP));
#pragma unroll
        for (int c = 0; c < 4; ++c) {
            lptr q2 = smem + (ct[6][c] + ch * 3840), q3 = smem + (ct[7][c] + ch * 3840);
            lds_w16(q2 + 64, p89); lds_w16(q2, p89 >> 16);
            lds_w16(q3 + 64, pab); lds_w16(q3, pab >> 16);
        }
        CFENCE();
        const u32x4 FX = ct[5] & (((unsigned)__builtin_amdgcn_readlane(p89, 63) >> 16) * 0x10001u);
        lptr pf2 = pt + cpy * 320 + ((79 - 64 - lo16 + 4 * g - cpy + 4) >> 2) * 16;
        u32x4 TA[3], TB[3], TCc[4];
#pragma unroll
        for (int i = 0; i < 3; ++i) { TA[i] = lds_r128(pf2 + 64 * (2 - i)); TB[i] = lds_r128(pf2 + 1280 + 64 * (2 - i)); }
#pragma unroll
        for (int i = 0; i < 4; ++i) TCc[i] = lds_r128(pf2 + PC_OFF + 64 * (3 - i));
        x_in();
#pragma unroll
        for (int i = 0; i < 8; ++i) X[i] = lds_r128(xf7 + 64 * 7 + 512 * i);
        STAMP(6);
        const f32x4 z4 = {0.f, 0.f, 0.f, 0.f};
#pragma unroll
        for (int st = 0; st < 6; ++st) {
            const u32x4 Xc = lds_r128(xf7 + 64 * 7 + 512 * st - 16);
            const u32x4 pa = pack8(mfma16(X[st], ct[0], z4), mfma16(X[st], ct[1], z4));
            const u32x4 pb = pack8(mfma16(X[st], ct[2], z4), mfma16(Xc, ct[1], z4));
#pragma unroll
            for (int dl = 2; dl <= 4; ++dl) if (st + dl < 8) {
                acc[st + dl] = mfma16(pa, TA[dl - 2], acc[st + dl]);
                acc[st + dl] = mfma16(pb, TB[dl - 2], acc[st + dl]);
            }
            if (st < 4) {
                const u32x4 pc = pack8(mfma16(X[st], ct[3], z4), mfma16(Xc, ct[4], z4));
#pragma unroll
                for (int dl = 4; dl <= 7; ++dl) if (st + dl < 8) acc[st + dl] = mfma16(pc, TCc[dl - 4], acc[st + dl]);
            }
        }
        SCHED(); cleared(); SCHED();
#pragma unroll
        for (int i = 4; i < 8; ++i) acc[i] = mfma16(FX, X[i - 4], acc[i]);
        const u32x4 k0 = TF(1024), k8 = TF(512), k16 = TF(0);
#pragma unroll
        for (int i = 0; i < 8; ++i) acc[i] = mfma16(k0, X[i], acc[i]);
#pragma unroll
        for (int i = 1; i < 8; ++i) acc[i] = mfma16(k8, X[i - 1], acc[i]);
#pragma unroll
        for (int i = 2; i < 8; ++i) acc[i] = mfma16(k16, X[i - 2], acc[i]);
        MFMA_DONE();
        SET_M0(ACX_OFF - 40960 + ch * 8192 + 4096);
#pragma unroll
        for (int i = 0; i < 16; ++i) W_ADDTID(acc[i >> 2][i & 3], 40960 + i * 256);
#pragma unroll
        for (int i = 0; i < 4; ++i) acc[i] = acc[4 + i];
        axr = smem + ACX_OFF + ch * 8192 + lane * 4;
    }
    STAMP(7);
    if (w == 0 && lane < 8) lds_w32(smem + FL + lane * 4, 0u);
    BARRIER();
    STAMP(8);
    if (w >= 8) wo_issue<0x1c0>(P, cg2, w - 8, 4, lane);
#pragma unroll
    for (int i = 0; i < 16; ++i) acc[i >> 2][i & 3] += lds_rf(axr + i * 256);
#pragma unroll
    for (int i = 0; i < 2; ++i) {
        const f32x4 ya = acc[2 * i], yb = acc[2 * i + 1];
        const auto sx = __builtin_amdgcn_permlane16_swap(pk_bf16(ya[0], ya[1]), pk_bf16(yb[0], yb[1]), false, false);
        const auto sy = __builtin_amdgcn_permlane16_swap(pk_bf16(ya[2], ya[3]), pk_bf16(yb[2], yb[3]), false, false);
        st_sc1(wsr, boff + i * 1024, not_empty(u32x4{sx[0], sy[0], sx[1], sy[1]}));
    }
    WAIT_VM(2);
    BARRIER();
    STAMP(9);
    if (w < 2) STAMP_OUT(2 + 2 * w, blockIdx.x + 256 * w);
}

constexpr int SLOT = 34816, XCH = 16384, KVT_OFF = 151296, KVP_OFF = 27648;
DEVI constexpr int slot_off(int s) { return (s & 3) == 0 ? SLOT : ((s & 3) == 1 ? 0 : (s & 3) * SLOT); }
DEVI constexpr int wq_off(int t) { return t == 0 ? 0 : 49152 + t * 27648; }

constexpr int quad_src(int quad) {
    const int pp = quad >= 192 ? 1 : 0, qi = quad - 192 * pp;
    const bool isz = qi >= 180;
    const int el = isz ? 0 : qi / 5, qd = qi - el * 5, hi = el >= 18 ? 1 : 0;
    const int dql = isz ? (qi < 189 ? qi - 180 : 0) : qd + 4 * hi;
    const int et = isz ? 0 : (el < 4 ? 0 : (el < 20 ? 1 : 2)), col = isz ? 0 : (el < 4 ? 12 + el : (el < 20 ? el - 4 : el - 20));
    const int tile = dql == 8 ? 5 : (et == 0 ? (dql >> 2) : (et == 1 ? 2 + (dql >> 2) : 4));
    return KVP_OFF + pp * 6144 + tile * 1024 + ((dql == 8 ? et : (dql & 3)) * 16 + col) * 4;
}
constexpr int quad_dst(int quad) {
    const int pp = quad >= 192 ? 1 : 0, qi = quad - 192 * pp;
    const bool isz = qi >= 180;
    const int el = isz ? 0 : qi / 5, qd = qi - el * 5, hi = el >= 18 ? 1 : 0;
    const int dql = isz ? (qi < 189 ? qi - 180 : 0) : qd + 4 * hi;
    const int dq = 9 * pp + dql, ks = dq >> 3, e = 36 * pp + el, ee = e >> 4;
    const int fr = ee == 0 ? 0 : (ee < 3 ? 2 * ee - 1 + ks : 2 * ee - 2 + ks);
    const int a = KVT_OFF + (isz ? (9 + ks) * 1024 + (dq & 3) * 256 + (2 * pp + (dql > 4 ? 1 : 0)) * 16 : fr * 1024 + ((dq & 3) * 16 + (e & 15)) * 16) + ((dq >> 2) & 1) * 8;
    return qi >= 189 ? LDS_DUMMY : (a | (isz ? (dql == 4 ? 0x300003 : 0x100003) : ((!hi && qd == 4) ? 1 : ((hi && qd == 0) ? 2 : 3))));
}
struct Tab { int v[2048]; };
constexpr Tab make_tab() {
    Tab t{};
    for (int c = 0; c < 192; ++c) {
        const int l = c & 63, q = 2 * ((c & 192) + (l >> 5) * 16 + (l & 15)) + ((l >> 4) & 1);
        t.v[4 * c] = quad_src(2 * c); t.v[4 * c + 1] = quad_src(2 * c + 1); t.v[4 * c + 2] = quad_dst(q); t.v[4 * c + 3] = quad_dst(q + 64);
    }
    for (int q = 0; q < 576; ++q) {
        const int rho = q / 9, pc = q % 9, k = (rho & 32) + 8 * ((rho >> 3) & 3) + 4 * (rho & 1) + ((rho >> 1) & 3), m = q >> 6;
        t.v[768 + q] = k * 3456 + pc * 16 - (m & 1) * 1024;
        t.v[1344 + q] = k * 1152 + pc * 16 - (m & 3) * 1024;
    }
    return t;
}
__device__ const Tab g_tab = make_tab();
constexpr int IDX = P2_B, GW_OFF = 139264;

#define GET_TAG() do { const u32x2 t_ = lds_r64(smem + LDS_TAG); tag = (u64)(unsigned)__builtin_amdgcn_readfirstlane(t_.x) | ((u64)(unsigned)__builtin_amdgcn_readfirstlane(t_.y) << 32); } while (0)
DEVI void gemm_kv(const Params& P, lptr smem, int rb, int hg, int kw, int cg2, u64& tag, rsrc_t wsr) {
    const int lane = threadIdx.x & 63, lo16 = lane & 15, g = lane >> 4;
    const int p = kw & 1, ct = (kw < 3 ? kw : 0) * 64 + lane;
    STAMP_DECL; STAMP(1);
    __builtin_amdgcn_s_setprio(2);
    const int xrow = ((kw >> 1) * 64 + lo16) * 128, sw = (g ^ ((lo16 >> 1) & 7)) << 4;
    lptr xa[2] = {smem + xrow + sw, smem + xrow + (sw ^ 64)};
    const int wrow = (4 * g + (lo16 >> 2)) * 288;
    lptr wa = smem + XCH + wrow + (9 * p + (lane & 3)) * 8, wa2 = smem + XCH + wrow + (9 * p + ((lane & 3) == 3 ? 18 : 8)) * 8;
    lptr flp = smem + FL, ixp = smem + IDX + ct * 16, irp = smem + IDX + 3072 + 2304 + lane * 4;
    unsigned one = 1u;
    u32x4 sel[2];
    sel[0] = mask_frag(lo16 < 8 ? 1u << lo16 : 0u, 0, 0x3f80u); sel[1] = mask_frag(lo16 >= 8 ? 1u << (lo16 - 8) : 0u, 0, 0x3f80u);
    unsigned sto = WS_PART + (unsigned)((hg * 16 + rb) * 3072 + ct * 16);
    unsigned off = WS_PART + (unsigned)((hg * 16 + g) * 3072 + kw * 1024 + lo16 * 16);
    asm volatile("" : "+v"(flp), "+v"(one), "+v"(ixp), "+v"(irp), "+v"(sel[0]), "+v"(sel[1]), "+v"(sto), "+v"(off));
    const f32x4 z4 = {0.f, 0.f, 0.f, 0.f};
    f32x4 acc[4][5];
#pragma unroll
    for (int i = 0; i < 4; ++i)
#pragma unroll
        for (int j = 0; j < 5; ++j) { const float e = j < 2 ? 1e-6f : (j == 2 && lo16 < 12 ? 1e-6f : 0.f); acc[i][j] = f32x4{e, e, e, e}; asm volatile("" : "+v"(acc[i][j])); }
    auto rel = [&](int s) { return (s & 2) ? slot_off(s) - 2 * SLOT : slot_off(s); };
    auto rebase = [&](int dlt) {
        xa[0] += dlt; xa[1] += dlt; wa += dlt; wa2 += dlt;
        asm volatile("" : "+v"(xa[0]), "+v"(xa[1]), "+v"(wa), "+v"(wa2));
    };
    constexpr int NB = 6;
    u32x2 wl[NB], wh[NB];
    u32x4 xf[2][4];
    auto rdw = [&](int f, int t) {
        const int n = f / 5, j = f % 5;
        const int o = rel(n >> 1) + (n & 1) * 9216 + t * 4608;
        const u32x2 v = j == 2 ? lds_tr(wa2 + o) : lds_tr(wa + o + (j == 0 ? 0 : (j == 1 ? 32 : (j == 3 ? 152 : 184))));
        if (t) wh[f % NB] = v; else wl[f % NB] = v;
    };
    auto rdx = [&](int n, int r) { xf[n & 1][r] = lds_r128(xa[n & 1] + rel(n >> 1) + r * 2048); };
    rebase(0);
    BARRIER();
    STAMP(2);
    rdw(0, 0); rdw(0, 1);
#pragma unroll
    for (int r = 0; r < 4; ++r) rdx(0, r);
#pragma unroll
    for (int f = 1; f < NB - 1; ++f) { rdw(f, 0); rdw(f, 1); }
    auto kstep = [&](const int n) __attribute__((always_inline)) {
        const int s = n >> 1;
#pragma unroll
        for (int m = 0; m < 20; ++m) {
            const int j = m >> 2, r = m & 3, f = 5 * n + j, fn = f + NB - 1;
            SCHED();
            const u32x4 wv = {wl[f % NB].x, wl[f % NB].y, wh[f % NB].x, wh[f % NB].y};
            mfma16i(acc[r][j], xf[n & 1][r], wv);
            SCHED();
            if (r < 2 && fn < 90) rdw(fn, r);
            if (r == 2 && j < 4 && n < 17) rdx(n + 1, j);
            if (m == 15 && n < 17) WAIT_LGKM(12);
            if (m == 18 && !(n & 1)) {
                if (s < 8 && (s & 1)) rebase((s & 2) ? -2 * SLOT : 2 * SLOT);
                if (s == 8) WAIT_LGKM0();
                RAW_BARRIER();
                if (s == 0) STAMP(3);
                if (s == 4) STAMP(4);
            }
            if (m == 19 && n < 17) WAIT_LGKM(2);
        }
    };
    kstep(0); kstep(1); kstep(2); kstep(3); kstep(4); kstep(5); kstep(6); kstep(7); kstep(8);
    kstep(9); kstep(10); kstep(11); kstep(12); kstep(13); kstep(14); kstep(15); kstep(16); kstep(17);
    __builtin_amdgcn_s_setprio(3);
    GET_TAG();
    const u32x4 ix = lds_r128(ixp);
    unsigned oo[9];
#pragma unroll
    for (int i = 0; i < 9; ++i) oo[i] = *(LAS unsigned*)(irp + i * 256);
    MFMA_DONE();
    STAMP(5);
    {
        f32x4 r[6], t0 = z4, t1 = z4, t2 = z4;
#pragma unroll
        for (int i = 0; i < 5; ++i) r[i] = z4;
#pragma unroll
        for (int u = 0; u < 2; ++u) {
            u32x4 pk[3], vb[3];
#pragma unroll
            for (int j = 0; j < 3; ++j) pk[j] = pack8(phi4(acc[2 * u][j]), phi4(acc[2 * u + 1][j]));
            vb[0] = pack8(acc[2 * u][2], acc[2 * u + 1][2]);
            if (lo16 < 12) vb[0] = u32x4{0x3f803f80u, 0x3f803f80u, 0x3f803f80u, 0x3f803f80u};
            vb[1] = pack8(acc[2 * u][3], acc[2 * u + 1][3]); vb[2] = pack8(acc[2 * u][4], acc[2 * u + 1][4]);
            if (u == 0) { SCHED();
            if (kw == 3 && lane == 0) __hip_atomic_store((GAS u64*)(P.ws + WS_KH + (hg * 16 + rb) * 128), tag, RLX_AGENT);
            SCHED(); }
            t0 = mfma16(pk[2], vb[0], t0); t1 = mfma16(pk[2], vb[1], t1); t2 = mfma16(pk[2], vb[2], t2);
            r[0] = mfma16(pk[0], vb[0], r[0]); r[1] = mfma16(pk[1], vb[0], r[1]); r[2] = mfma16(pk[0], vb[1], r[2]);
            r[3] = mfma16(pk[1], vb[1], r[3]); r[4] = mfma16(pk[1], vb[2], r[4]);
        }
        r[5] = g == 0 ? t0 : (g == 1 ? t1 : t2);
        MFMA_DONE();
        SET_M0(KVP_OFF + kw * 6144);
#pragma unroll
        for (int t = 5; t >= 0; --t) {
            W_ADDTID(r[t][0], t * 1024); W_ADDTID(r[t][1], t * 1024 + 256);
            W_ADDTID(r[t][2], t * 1024 + 512); W_ADDTID(r[t][3], t * 1024 + 768);
        }
        FLAG_ADD(F_KVP);
    }
    STAMP(6);
    if (kw < 3) {
        const int dst[2] = {(int)ix.z, (int)ix.w};
        flag_wait<0>(smem, F_KVP, 4);
        STAMP(7);
        float pa[2][2][4];
#pragma unroll
        for (int i = 0; i < 2; ++i)
#pragma unroll
            for (int r = 0; r < 2; ++r)
#pragma unroll
                for (int k = 0; k < 4; ++k) pa[i][r][k] = lds_rf(smem + ix[i] + r * 12288 + k * 256);
        SCHED();
        u32x4 pv;
#pragma unroll
        for (int i = 0; i < 2; ++i)
#pragma unroll
            for (int k = 0; k < 2; ++k)
                pv[2 * i + k] = pk_bf16(pa[i][0][2 * k] + pa[i][1][2 * k], pa[i][0][2 * k + 1] + pa[i][1][2 * k + 1]);
        st_sc1(wsr, sto, not_empty(pv));
        SCHED();
        FLAG_ADD(F_KVR);
        STAMP(8);
        WAIT_VM(0);
        flag_wait<1>(smem, F_C1, 1);
        STAMP(9);
        f32x4 sum[2];
#pragma unroll 1
        for (unsigned spins = 0; spins < SPIN_CAP; ++spins) {
            u32x4 a[4][4];
#pragma unroll
            for (int i = 0; i < 4; ++i)
#pragma unroll
                for (int t = 0; t < 4; ++t) a[t][i] = __builtin_amdgcn_raw_buffer_load_b128(wsr, off + t * 256, ((i + rb) & 3) * 12288, 16);
            SCHED();
            FLAG_ADD(F_SW);
            sum[0] = sum[1] = f32x4{0.f, 0.f, 0.f, 0.f};
#pragma unroll
            for (int i = 0; i < 4; ++i)
#pragma unroll
                for (int t = 0; t < 4; ++t) sum[t >> 1] = mfma16(sel[t & 1], a[t][i], sum[t >> 1]);
            if (__all(sum[0][0] == sum[0][0] && sum[1][0] == sum[1][0])) break;
            __builtin_amdgcn_s_sleep(RSLEEP);
            if (spins < 8) { CFENCE(); continue; }
            unsigned m = 0;
#pragma unroll
            for (int t = 0; t < 4; ++t)
#pragma unroll
                for (int i = 0; i < 4; ++i) m = max(m, umax4(a[t][i]));
            if (__all(m != EMPTY)) break;
            CFENCE();
        }
#pragma unroll
        for (int i = 0; i < 2; ++i) {
            const unsigned lo = (dst[i] & 1) ? pk_bf16(sum[i][0], sum[i][1]) : 0u, hi = (dst[i] & 2) ? pk_bf16(sum[i][2], sum[i][3]) : 0u;
            lptr dp = smem + (dst[i] & 0xffffc);
            if (dst[i] & 0x100000) {
                const int hs = (dst[i] & 0x200000) ? 20 : 4;
#pragma unroll
                for (int r = 0; r < 4; ++r) { lds_w32(dp + 64 * r, lo); lds_w32(dp + 64 * r + hs, hi); }
            } else lds_w64(dp, u32x2{lo, hi});
        }
        CFENCE();
        FLAG_ADD(F_D);
        STAMP(10);
    } else {
        auto wo_chunks = [&](int c0, int c1) {
#pragma unroll 1
            for (int c = c0; c < c1; ++c) {
                const char* ws = P.wo + cg2 * 144 + c * (64 * 1152);
                const unsigned l = c < 8 ? c * 9216 : WO8;
                SET_M0(l); glds16o<0>(ws, oo[0]); glds16o<1024>(ws, oo[1]); glds16o<2048>(ws, oo[2]); glds16o<3072>(ws, oo[3]);
                SET_M0(l + 4096); glds16o<0>(ws, oo[4]); glds16o<1024>(ws, oo[5]); glds16o<2048>(ws, oo[6]); glds16o<3072>(ws, oo[7]);
                SET_M0(l + 8192); glds16o<0>(ws, oo[8]);
            }
        };
        poll_flags<KSLEEP>(P, WS_KH + hg * 2048, 16, tag, lane);
        FLAG_ADD(F_C1);
        flag_wait(smem, F_KVR, 3);
        flag_wait(smem, F_SW, 3);
        wo_chunks(3, 9);
        flag_wait(smem, F_Q0, 8);
        wo_chunks(0, 3);
        WAIT_VM(27);
        FLAG_ADD(F_W1);
        WAIT_VM(0);
        FLAG_ADD(F_W0);
    }
    __builtin_amdgcn_s_setprio(0);
    if (kw == 0) STAMP_OUT(1, blockIdx.x);
}

DEVI void gemm_q(const Params& P, lptr smem, int rb, int hg, int t8, u64& tag, rsrc_t wsr) {
    const int lane = threadIdx.x & 63, lw = t8 & 3;
    const bool ld = t8 < 4;
    STAMP_DECL; STAMP(1);
    __builtin_amdgcn_s_setprio(3);
    unsigned xo[4], wo_[5], qo[3];
    const int rx = hg & 3, rw = rb & 3;
    unsigned mx[4], mw[4];
    const unsigned xbase = (unsigned)((rb * 128 + 8 * lw + (lane >> 3)) * 1152 + (((lane & 7) ^ ((lane >> 4) + 4 * (lw & 1))) << 4));
#pragma unroll
    for (int i = 0; i < 4; ++i) { mx[i] = (((i + rx) & 3) * 4 + lw) * 1024; mw[i] = XCH + (((i + rw) & 3) * 4 + lw) * 1024; xo[i] = xbase + ((i + rx) & 3) * 36864; }
    const unsigned lb = lw * 4096;
    auto issue_x = [&](int s, int i0 = 0, int i1 = 4) {
#pragma unroll
        for (int i = i0; i < i1; ++i) glds16(P.x + s * 128, xo[i], slot_off(s) + mx[i]);
    };
    auto issue_w = [&](int s, int i0 = 0, int i1 = 4) {
        const char* ws = P.wqkv + s * (64 * 3456);
        if (i1 == 4 && (lw >> 1) == (s & 1)) glds16(ws, wo_[4], (unsigned)(slot_off(s) + XCH + 16384 + (lw & 1) * 1024));
#pragma unroll
        for (int i = i0; i < i1; ++i) glds16(ws, wo_[i], slot_off(s) + mw[i]);
    };
    auto issue_q = [&](int t, int i) {
        const char* ws = P.wqkv + hg * 144 + (3 * t + i) * (64 * 3456);
        if (lw == ((i + t) & 3)) glds16(ws, qo[2], (unsigned)(wq_off(t) + i * 9216 + 8192));
        SET_M0(wq_off(t) + i * 9216 + (lb >> 1));
        glds16o<0>(ws, qo[0]); glds16o<1024>(ws, qo[1]);
    };
    auto w_off = [&](int i) {
        const unsigned q = 64 * (i < 4 ? ((i + rw) & 3) * 4 + lw : 16 + (lw & 1)) + lane, rho = (q * 3641u) >> 16, pc = q - rho * 18;
        const unsigned k = (rho & 35u) + 8 * ((rho >> 2) & 3) + 4 * ((rho >> 4) & 1);
        wo_[i] = k * 3456 + (1152 + hg * 144) + pc * 16 + (pc < 9 ? 0u : 1008u);
    };
    if (ld) {
        issue_x(0, 0, 1); issue_x(1, 0, 1);
        SCHED();
        w_off(0);
        issue_w(0, 0, 1); issue_w(1, 0, 1);
        SCHED();
#pragma unroll
        for (int i = 1; i < 5; ++i) w_off(i);
        if (lw == 1) { glds16(P.gw, (unsigned)(lane * 16), GW_OFF); if (lane < 8) glds16(P.gw + 1024, (unsigned)(lane * 16), GW_OFF + 1024); }
        issue_x(0, 1, 4); issue_w(0, 1, 4); issue_w(1, 1, 4); issue_x(1, 1, 4);
        SET_M0(IDX + (lb >> 1));
        glds16o<0>((const char*)&g_tab + (lb >> 1), (unsigned)(lane * 16)); glds16o<1024>((const char*)&g_tab + (lb >> 1), (unsigned)(lane * 16));
    } else {
        if (lw == 0 && lane < 16) lds_w32(smem + FL + lane * 4, 0u);
#pragma unroll
        for (int i = 0; i < 3; ++i) lds_w128(smem + KVT_OFF + i * 4096 + lw * 1024 + lane * 16, u32x4{0u, 0u, 0u, 0u});
    }
    const int lo16 = lane & 15, g = lane >> 4;
    const int sw = (g ^ ((lo16 >> 1) & 7)) << 4;
    lptr xa[2] = {smem + (16 * t8 + lo16) * 128 + sw, smem + (16 * t8 + lo16) * 128 + (sw ^ 64)}, xb[2] = {xa[0] + 2 * SLOT, xa[1] + 2 * SLOT};
    lptr flp = smem + FL, iqp = smem + IDX + 3072 + lane * 4, gwp = smem + GW_OFF + g * 16;
    const int qrow = (2 * (4 * g + (lo16 >> 2))) * 144;
    lptr qw = smem + qrow + (lane & 3) * 8, qw4 = smem + qrow + (16 + (lane & 1)) * 8;
    lptr qwh = qw + wq_off(1), qw4h = qw4 + wq_off(1), kfp = smem + KVT_OFF + lane * 16;
    unsigned one = 1u, ob0 = WS_O + (unsigned)(((rb * 8 + t8) * 72 + hg * 9 + 2 * (g & 1) + (g >> 1)) * 256 + lo16 * 16);
    asm volatile("" : "+v"(flp), "+v"(one), "+v"(iqp), "+v"(gwp), "+v"(xa[0]), "+v"(xa[1]), "+v"(xb[0]), "+v"(xb[1]));
    asm volatile("" : "+v"(qw), "+v"(qw4), "+v"(qwh), "+v"(qw4h), "+v"(kfp), "+v"(ob0));
    asm volatile(".subsection 2\n.Lkf_code_end:\n\t.fill 256, 4, 0xbf800000\n\t.subsection 0");
    const f32x4 z4 = {0.f, 0.f, 0.f, 0.f};
    f32x4 gacc = z4, qa[5];
#pragma unroll
    for (int j = 0; j < 5; ++j) { qa[j] = f32x4{1e-6f, 1e-6f, 1e-6f, 1e-6f}; asm volatile("" : "+v"(qa[j])); }
    u32x4 xk[18];
    u64 pf = 0;
    WAIT_VM(8); BARRIER();
    if (ld) { issue_w(2); issue_x(2); if (LA3) { issue_w(3); issue_x(3); } }
#pragma unroll
    for (int s = 0; s < 9; ++s) {
#pragma unroll
        for (int k = 0; k < 2; ++k) xk[2 * s + k] = lds_r128(((s & 2) ? xb[k] - 2 * SLOT : xa[k]) + slot_off(s));
        if (t8 == hg) {
            gacc = mfma16(lds_r128(gwp + s * 128), xk[2 * s], gacc);
            gacc = mfma16(lds_r128(gwp + s * 128 + 64), xk[2 * s + 1], gacc);
        }
        if (t8 == 4) {
            if (s == 1) { tag = *(const __attribute__((address_space(4))) u64*)(P.ws + WS_CTL); PIN_TAG(); if (lane == 0) lds_w64(smem + LDS_TAG, u32x2{(unsigned)tag, (unsigned)(tag >> 32)}); }
            if (s == 4) asm volatile("global_load_dwordx2 %0, %1, %2 sc1" : "=v"(pf) : "v"((lane < 5 ? lane : 0) * 128), "s"(P.ws + WS_PFL) : "memory");
            if (s == 7) {
                asm volatile("s_waitcnt vmcnt(0)" : "+v"(pf) :: "memory");
                if (!__all(pf == tag)) poll_flags(P, WS_PFL, 5, tag, lane);
            }
        }
        if (LA3) { if (s < 6) WAIT_VM(17); else if (s == 6) WAIT_VM(14); else WAIT_VM(12); }
        else if (s < 7) WAIT_VM(8); else WAIT_VM(6);
        BARRIER();
        if (ld) {
            const int sn = s + 3 + LA3;
            if (sn < 9) { issue_w(sn); issue_x(sn); } else if (sn < 12) { issue_q(sn - 9, 0); issue_q(sn - 9, 1); issue_q(sn - 9, 2); }
            if (s == 3) { qo[0] = *(LAS unsigned*)(iqp + lw * 512); qo[1] = *(LAS unsigned*)(iqp + lw * 512 + 256); qo[2] = *(LAS unsigned*)(iqp + 2048); CFENCE(); }
        }
    }
    __builtin_amdgcn_s_setprio(0);
    STAMP(5);
    if (!ld) {
        const unsigned co = WS_O + (unsigned)(((rb * 8 + 2 * lw) * 72 + hg * 9) * 256 + lane * 16);
#pragma unroll
        for (int i = 0; i < 5; ++i)
            if (i < 4 || lane < 32) st_sc1(wsr, i < 4 ? co + (i >> 1) * 18432 + (i & 1) * 1024 : co + 2048 + (lane >> 4) * (18432 - 256), u32x4{EMPTY, EMPTY, EMPTY, EMPTY});
    }
    GET_TAG();
    if (t8 == 4 && lane == 0) tag_post(P, tag);
    if (t8 == hg) {
        const float gate = rbf(__builtin_amdgcn_rcpf(1.f + __expf(-rbf(gacc[0]))));
        if (lane < 16) __hip_atomic_store((GAS float*)(P.ws + WS_GATE) + (rb * 128 + hg * 16 + lane), gate, RLX_AGENT);
    }
    auto peek = [&](int f) { return __hip_atomic_load((LAS unsigned*)(flp + f), __ATOMIC_RELAXED, __HIP_MEMORY_SCOPE_WORKGROUP); };
    auto rdq = [&](int ks, int j) {
        const int o = (ks < 6 ? ks : ks - 6) * 4608;
        return j < 4 ? lds_trfrag((ks < 6 ? qw : qwh) + o + 32 * j, 144) : lds_trfrag((ks < 6 ? qw4 : qw4h) + o, 144);
    };
    u32x4 wq[2][5];
#pragma unroll
    for (int i = 0; i < 2; ++i)
#pragma unroll
        for (int j = 0; j < 5; ++j) wq[i][j] = rdq(i, j);
    unsigned seenq = 0;
#pragma unroll
    for (int ks = 0; ks < 18; ++ks) {
        if (ks == 4) {
            SCHED();
            WAIT_VM(0); FLAG_ADD(F_Q0);
            seenq = peek(F_Q0); NEED(seenq, F_Q0, 8);
        }
        if (ks == QH) { SCHED(); seenq = peek(F_KVR); NEED(seenq, F_KVR, 3); STAMP(6); }
        if (ks == 16) { SCHED(); FLAG_ADD(F_QD); }
#pragma unroll
        for (int j = 0; j < 5; ++j) {
            SCHED();
            if (j == 0 && ks < 17) WAIT_LGKM(10);
            mfma16i(qa[j], wq[ks & 1][j], xk[ks]);
            SCHED();
            if (ks + 2 < 18) wq[ks & 1][j] = rdq(ks + 2, j);
        }
    }
    __builtin_amdgcn_s_setprio(2);
    MFMA_DONE();
    u32x4 b0 = pack8(phi4(qa[0]), phi4(qa[1])), b1 = pack8(phi4(qa[2]), phi4(qa[3])), b2 = pack8(phi4(qa[4]), z4);
    asm volatile("" : "+v"(b0), "+v"(b1), "+v"(b2));
    STAMP(7);
    flag_wait<1>(smem, F_D, 3);
    STAMP(8);
    {
        u32x4 kf[12];
#pragma unroll
        for (int i = 0; i < 12; ++i) { kf[i] = lds_r128(kfp + (i < 3 ? 9 + i : i - 3) * 1024); SCHED(); }
        SCHED();
        const f32x4 den = mfma16(kf[2], b2, mfma16(kf[1], b1, mfma16(kf[0], b0, f32x4{1e-6f, 1e-6f, 1e-6f, 1e-6f})));
        f32x4 o[5];
        o[0] = mfma16(kf[3], b0, z4);
        o[1] = mfma16(kf[5], b1, mfma16(kf[4], b0, z4));
        o[2] = mfma16(kf[7], b1, mfma16(kf[6], b0, z4));
        o[3] = mfma16(kf[9], b2, mfma16(kf[8], b1, z4));
        o[4] = mfma16(kf[11], b2, mfma16(kf[10], b1, z4));
        f32x4 r;
#pragma unroll
        for (int i = 0; i < 4; ++i) r[i] = __builtin_amdgcn_rcpf(den[i]);
        const float r1a = g == 0 ? r[0] : r[1], r2 = g == 0 ? r[1] : r[2], r3a = g <= 1 ? r[2] : r[3], r3b = g == 0 ? r[2] : r[3];
        u32x2 ob[5];
        ob[0] = u32x2{pk_bf16(o[0][0] * r[0], o[0][1] * r[0]), pk_bf16(o[0][2] * r[0], o[0][3] * r[0])};
        ob[1] = u32x2{pk_bf16(o[1][0] * r1a, o[1][1] * r1a), pk_bf16(o[1][2] * r[1], o[1][3] * r[1])};
        ob[2] = u32x2{pk_bf16(o[2][0] * r2, o[2][1] * r2), pk_bf16(o[2][2] * r2, o[2][3] * r2)};
        ob[3] = u32x2{pk_bf16(o[3][0] * r3a, o[3][1] * r3a), pk_bf16(o[3][2] * r3b, o[3][3] * r3b)};
        ob[4] = u32x2{pk_bf16(o[4][0] * r[3], o[4][1] * r[3]), pk_bf16(o[4][2] * r[3], o[4][3] * r[3])};
        SCHED();
        if (t8 == 7 && lane == 0) __hip_atomic_store((GAS u64*)(P.ws + WS_HINT + (rb * 8 + hg) * 128), tag, RLX_AGENT);
        SCHED();
#pragma unroll
        for (int t = 0; t < 3; ++t) {
            const u32x2 a = ob[2 * t], b = ob[t < 2 ? 2 * t + 1 : 4];
            const auto sx = __builtin_amdgcn_permlane16_swap(a.x, b.x, false, false), sy = __builtin_amdgcn_permlane16_swap(a.y, b.y, false, false);
            if (t < 2 || g == 0) st_sc1(wsr, ob0 + 1024 * t, not_empty(u32x4{sx[0], sy[0], sx[1], sy[1]}));
        }
    }
    __builtin_amdgcn_s_setprio(0);
    STAMP(9);
    if (t8 == 0) STAMP_OUT(5, blockIdx.x + 256);
    if (t8 == 7) STAMP_OUT(7, blockIdx.x + 768);
}

DEVI void phase2(const Params& P, lptr smem, int rt64, int cg, bool gemm, u64 tag, rsrc_t wsr) {
    const int lane = threadIdx.x & 63, w0 = __builtin_amdgcn_readfirstlane(threadIdx.x >> 6);
    const int w = gemm ? (w0 + 8) % 12 : w0, tid = w * 64 + lane;
    const int lo16 = lane & 15, g = lane >> 4;
    const int rt = w & 3, kt = w >> 2;
    STAMP_DECL; STAMP(1);
    const int pb = gemm ? P2_B : 55296;
    if (gemm) flag_wait<1>(smem, w < 4 ? F_W0 : F_W1, 1);
    STAMP(2);
    const int qrow = (2 * (4 * g + (lo16 >> 2))) * 144;
    lptr qw = smem + qrow + (lane & 3) * 8, qw4 = smem + qrow + (16 + (lane & 1)) * 8;
    int wofs[3], oofs[3];
#pragma unroll
    for (int u = 0; u < 3; ++u) {
        const int c = kt * 3 + (u + cg) % 3;
        wofs[u] = c < (gemm ? 8 : 6) ? c * 9216 : WO8 + (gemm ? 0 : (c - 6) * 9216); oofs[u] = c * 2048;
    }
    auto rdo = [&](int i, int j) { return j < 4 ? lds_trfrag(qw + wofs[i >> 1] + (i & 1) * 4608 + 32 * j, 144) : lds_trfrag(qw4 + wofs[i >> 1] + (i & 1) * 4608, 144); };
    const unsigned ooff = WS_O + (unsigned)((rt64 * 4 + rt) * 18432 + lane * 16);
    auto ldo = [&](int i) { return __builtin_amdgcn_raw_buffer_load_b128(wsr, ooff + (i & 1) * 1024, oofs[i >> 1], 16); };
    u32x4 wf[6][5];
#pragma unroll
    for (int i = 0; i < 6; ++i)
#pragma unroll
        for (int j = 0; j < 5; ++j) wf[i][j] = rdo(i, j);
    const int r0 = tid < 576 ? tid / 9 : 0, c0 = tid - r0 * 9;
    unsigned boff = tid < 576 ? WS_B + (unsigned)((cg * 72 + (tid >> 3)) * 4096 + (rt64 >> 3) * 1024 + (((tid >> 2) & 1) * 32 + ((rt64 >> 2) & 1) * 16 + (rt64 & 3) * 4 + (tid & 3)) * 16) : WS_END;
    unsigned goff = WS_GATE + (unsigned)((rt64 * 64 + r0) * 4);
    unsigned qd = gemm ? __hip_atomic_load((LAS unsigned*)(smem + FL + F_QD), __ATOMIC_RELAXED, __HIP_MEMORY_SCOPE_WORKGROUP) : 8u;
    asm volatile("" : "+v"(boff), "+v"(goff));
    STAMP(3);
    if (w == 3) {
        const int a0 = cg * 72, g0 = (a0 >> 6) * 11 + (a0 & 63) / 6, a1 = a0 + 71, g1 = (a1 >> 6) * 11 + (a1 & 63) / 6;
        poll_addr<OSLEEP>(P, lane < 8 ? WS_HINT + ((rt64 >> 1) * 8 + lane) * 128 : WS_BFL + min(g0 + lane - 8, g1) * 128, tag);
        flag_set(F_H, 1);
    } else flag_wait<1>(smem, F_H, 1);
    STAMP(4);
    u32x4 bv; unsigned gv;
    f32x4 acc[5];
    {
        u32x4 of[6];
#pragma unroll
        for (int i = 0; i < 6; ++i) of[i] = ldo(i);
        bv = ld_sc1(wsr, boff); gv = __builtin_amdgcn_raw_buffer_load_b32(wsr, goff, 0, 16);
        SCHED();
#pragma unroll
        for (int j = 0; j < 5; ++j) acc[j] = f32x4{0.f, 0.f, 0.f, 0.f};
#pragma unroll
        for (int i = 0; i < 6; ++i)
#pragma unroll
            for (int j = 0; j < 5; ++j) acc[j] = mfma16(wf[i][j], of[i], acc[j]);
    }
    if (!__all(acc[0][0] == acc[0][0])) {
#pragma unroll
        for (int j = 0; j < 5; ++j) acc[j] = f32x4{0.f, 0.f, 0.f, 0.f};
        u32x4 of[6];
#pragma unroll 1
        for (unsigned spins = 0; spins < SPIN_CAP; ++spins) {
            CFENCE();
            unsigned m = 0;
#pragma unroll
            for (int i = 0; i < 6; ++i) of[i] = ldo(i);
#pragma unroll
            for (int i = 0; i < 6; ++i) m = max(m, umax4(of[i]));
            if (__all(m != EMPTY)) break;
        }
#pragma unroll
        for (int i = 0; i < 6; ++i)
#pragma unroll
            for (int j = 0; j < 5; ++j) acc[j] = mfma16(rdo(i, j), of[i], acc[j]);
    }
    if ((unsigned)__builtin_amdgcn_readfirstlane(qd) < 8u) flag_wait(smem, F_QD, 8);
    lptr ap = smem + P2_ACC + kt * (64 * P2_AP) + (rt * 16 + lo16) * P2_AP + g * 16;
#pragma unroll
    for (int j = 0; j < 5; ++j) if (j < 4 || g < 2) lds_w128(ap + j * 64, __builtin_bit_cast(u32x4, acc[j]));
    if (w < 9) {
#pragma unroll 1
        for (unsigned spins = 0; spins < SPIN_CAP; ++spins) {
            if (__all(umax4(bv) != EMPTY)) break;
            __builtin_amdgcn_s_sleep(2);
            CFENCE();
            bv = ld_sc1(wsr, boff);
        }
        lptr bp = smem + pb + ((tid & 3) * 16 + ((tid >> 2) & 1) * 8) * 144 + (tid >> 3) * 2;
#pragma unroll
        for (int q = 0; q < 4; ++q) { lds_w16(bp + (2 * q) * 144, bv[q]); lds_w16(bp + (2 * q + 1) * 144, bv[q] >> 16); }
    }
    STAMP(5);
    BARRIER();
    STAMP(6);
    if (w < 9) {
        lptr a = smem + P2_ACC + r0 * P2_AP + c0 * 32;
        const f32x4 s0 = __builtin_bit_cast(f32x4, lds_r128(a)) + __builtin_bit_cast(f32x4, lds_r128(a + 64 * P2_AP)) + __builtin_bit_cast(f32x4, lds_r128(a + 128 * P2_AP));
        const f32x4 s1 = __builtin_bit_cast(f32x4, lds_r128(a + 16)) + __builtin_bit_cast(f32x4, lds_r128(a + 64 * P2_AP + 16)) + __builtin_bit_cast(f32x4, lds_r128(a + 128 * P2_AP + 16));
        const u32x4 bb = lds_r128(smem + pb + tid * 16);
        const float gt = __uint_as_float(gv), og = rbf(1.f - gt);
        u32x4 o;
#pragma unroll
        for (int i = 0; i < 4; ++i) {
            const float a0 = i < 2 ? s0[2 * i] : s1[2 * i - 4], a1 = i < 2 ? s0[2 * i + 1] : s1[2 * i - 3];
            o[i] = pk_bf16(gt * a0 + og * bf2f(bb[i]), gt * a1 + og * bfhi(bb[i]));
        }
        __builtin_amdgcn_raw_buffer_store_b128(o, __builtin_amdgcn_make_buffer_rsrc(P.out, 0, SEQ * DM * 2, 0x00020000), (unsigned)((rt64 * 64 + r0) * 1152 + cg * 144 + c0 * 16), 0, OUTAUX);
    }
    STAMP(7);
    if (w == 4) STAMP_OUT(blockIdx.x < 128 ? 3 : 6, blockIdx.x + 512);
}

DEVI void helper_role(const Params& P, lptr smem, int xcd, int li, u64& tag, rsrc_t wsr) {
    const int tid = threadIdx.x, lane = tid & 63, w = __builtin_amdgcn_readfirstlane(tid >> 6);
    STAMP_DECL; STAMP(1);
    if (li >= 13) {
#pragma unroll 1
        for (int n = 12 * NTW + (li - 13) + 3 * w; n < 171; n += 36) touch_group(P, xcd, n, lane);
    }
    if (li == 12) {
#pragma unroll
        for (int i = 0; i < 7; ++i) {
            const int u = (i * 5 + xcd - 3) * 12 + w;
            if (u < 384) st_sc1(wsr, WS_PART + (unsigned)(u * 1024 + lane * 16), u32x4{EMPTY, EMPTY, EMPTY, EMPTY});
        }
        WAIT_VM(0);
    }
    PIN_TAG();
    BARRIER();
    if (tid == 0) {
        if (li == 12) __hip_atomic_store((GAS u64*)(P.ws + WS_PFL + (xcd - 3) * 128), tag, RLX_AGENT);
        tag_post(P, tag);
    }
    if (li >= 13) {
#pragma unroll 1
        for (int n = 171 + (li - 13) + 3 * w; n < 234; n += 36) touch_group(P, xcd, n, lane);
    }
    STAMP(2);
    if (w == 11) {
        STAMP_OUT(8, blockIdx.x);
        if (blockIdx.x == 255) {
            const GAS u64* f = (const GAS u64*)(P.ws + WS_ARR) + lane;
#pragma unroll 1
            for (unsigned spins = 0; spins < SPIN_CAP; ++spins) {
                const bool ok = __hip_atomic_load(f, RLX_AGENT) == tag && __hip_atomic_load(f + 64, RLX_AGENT) == tag &&
                                __hip_atomic_load(f + 128, RLX_AGENT) == tag && __hip_atomic_load(f + 192, RLX_AGENT) == tag;
                if (__all(ok)) break;
                __builtin_amdgcn_s_sleep(8);
            }
            if (lane == 0) __hip_atomic_store((GAS u64*)(P.ws + WS_CTL), tag, RLX_AGENT);
        }
    }
}

__global__ __launch_bounds__(768, 3) void k_fused(Params P) {
    __shared__ __attribute__((aligned(16))) char smem_[LDS_BYTES];
    lptr smem = (lptr)smem_;
    const int b = blockIdx.x, xcd = b & 7, idx = b >> 3;
    const int lane = threadIdx.x & 63, w = __builtin_amdgcn_readfirstlane(threadIdx.x >> 6);
    const rsrc_t wsr = __builtin_amdgcn_make_buffer_rsrc(P.ws, 0, (int)WS_END, 0x00020000);
    const int rt64 = xcd * 4 + (idx & 3), cg2 = idx >> 2;
    if (idx >= CPF && w < 6) {
        const char* pc; unsigned len;
        asm volatile("s_getpc_b64 %0\n.Lkf_code_begin:\n\ts_mov_b32 %1, .Lkf_code_end-.Lkf_code_begin" : "=s"(pc), "=s"(len));
        touch(pc, min((unsigned)(threadIdx.x * 128), len));
    }
    u64 tag;
    if (__builtin_expect(idx < 16, 1)) {
        const int rbl = idx & 3, hgl = idx >> 2, rb = (xcd >> 1) * 4 + rbl, hg = (xcd & 1) * 4 + hgl;
        if (__builtin_expect((w & 12) != 4, 1)) gemm_q(P, smem, rb, hg, w < 4 ? w : w - 4, tag, wsr);
        else gemm_kv(P, smem, rb, hg, w - 4, cg2, tag, wsr);
    } else {
        tag = *(const __attribute__((address_space(4))) u64*)(P.ws + WS_CTL);
        const int li = idx - 16, gid = li < 11 ? xcd * 11 + li : 88 + (li - 11) * 8 + xcd;
        if (gid < 99) ssm_role(P, smem, xcd, li, gid, cg2, tag, wsr);
        else {
            helper_role(P, smem, xcd, li, tag, wsr);
            if (w < 9) wo_issue<0x1ff>(P, cg2, w, 9, lane);
            if (w == 10 && lane < 16) lds_w32(smem + FL + lane * 4, 0u);
            WAIT_VM(0);
            BARRIER();
        }
    }
    phase2(P, smem, rt64, cg2, idx < 16, tag, wsr);
}

extern "C" void kernel_launch(void* const* d_in, const int* in_sizes, int n_in, void* d_out, int out_size, void* d_ws, size_t ws_size,
                              hipStream_t stream) {
    Params P;
    P.x = (const char*)d_in[0]; P.wqkv = (const char*)d_in[1]; P.wo = (const char*)d_in[2];
    P.logA = (const char*)d_in[4]; P.aim = (const char*)d_in[5]; P.cre = (const char*)d_in[6]; P.cim = (const char*)d_in[7];
    P.logdt = (const char*)d_in[8]; P.dskip = (const char*)d_in[9]; P.gw = (const char*)d_in[10];
    P.out = (char*)d_out; P.ws = (char*)d_ws;
    k_fused<<<256, 768, 0, stream>>>(P);
}
```

```cpp
#include <hip/hip_runtime.h>
#include <stdint.h>

#define GAS __attribute__((address_space(1)))
#define LAS __attribute__((address_space(3)))
#define DEVI __device__ __forceinline__
typedef unsigned short u16;
typedef unsigned long long u64;
typedef __attribute__((ext_vector_type(2))) unsigned u32x2;
typedef __attribute__((ext_vector_type(3))) unsigned u32x3;
typedef __attribute__((ext_vector_type(4))) unsigned u32x4;
typedef __attribute__((ext_vector_type(4))) short s16x4;
typedef __attribute__((ext_vector_type(2))) float f32x2;
typedef __attribute__((ext_vector_type(4))) float f32x4;
typedef __attribute__((ext_vector_type(16))) float f32x16;
typedef __attribute__((ext_vector_type(2))) __bf16 bf16x2;
typedef __attribute__((ext_vector_type(8))) __bf16 bf16x8;
typedef __attribute__((ext_vector_type(2))) _Float16 f16x2;
typedef __attribute__((ext_vector_type(8))) _Float16 f16x8;
typedef LAS char* lptr;

#define PREF 1
#define ROT 0
#define FSLEEP 2
#define NSLEEP "3"
#define LDRIFT 0
#define PSLEEP 6
#define CSLEEP 1
#define OSLEEP 2
#define RSLEEP 3
#define HOLD 1
#define GROT ((w + li) & 3)
#define NTW 8
#define OUTAUX 17
#define TEARLY 1
#define HDELAY 0
#define CPF 16
#define LA3 0
#define TD1 0
#define TD2 16
#define TW2 3
#define HD 30
#define QH 4
constexpr int SEQ = 2048, DM = 576;

constexpr unsigned WS_CTL = 0;
constexpr unsigned WS_ARR = 256;
constexpr unsigned WS_BFL = 4096;
constexpr unsigned WS_HINT = 20480;
constexpr unsigned WS_GATE = 36864;
constexpr unsigned WS_PFL = 49152;
constexpr unsigned WS_PART = 65536;
constexpr unsigned WS_O = WS_PART + 8 * 16 * 3072;
constexpr unsigned WS_B = WS_O + SEQ * DM * 2;
constexpr unsigned WS_END = WS_B + SEQ * DM * 2;
constexpr unsigned EMPTY = 0xffffffffu;

constexpr size_t WS_STAMP = 16u << 20;
#define STAMP_DECL
#define STAMP(i)
#define CSTAMP(i)
#define STAMP_OUT(role, slot)

struct Params {
    const char* x; const char* wqkv; const char* wo; const char* logA; const char* aim; const char* cre; const char* cim;
    const char* logdt; const char* dskip; const char* gw; char* out; char* ws;
};

constexpr int LDS_BYTES = 163840, FL = 163584, LDS_TAG = FL + 64, LDS_DUMMY = FL + 128, LDS_JUNK = 161280;
enum { F_SW = 0, F_LANDED = 4, F_W1 = 20, F_B = 28, F_H = 24, F_KVP = 36, F_KVR = 40, F_Q0 = 44, F_QD = 48, F_D = 52, F_C1 = 56, F_W0 = 60 };
constexpr int WO8 = 132096, P2_ACC = 73728, P2_AP = 304, P2_B = 141312, P2_G = 150528;

DEVI float bf2f(unsigned h) { return __uint_as_float(h << 16); }
DEVI float bfhi(unsigned h) { return __uint_as_float(h & 0xffff0000u); }
DEVI unsigned pk_bf16(float a, float b) { return __builtin_bit_cast(unsigned, __builtin_convertvector(f32x2{a, b}, bf16x2)); }
DEVI unsigned pk_f16(f32x2 v) { return __builtin_bit_cast(unsigned, __builtin_convertvector(v, f16x2)); }
DEVI float rbf(float a) { return bf2f(pk_bf16(a, a)); }
DEVI f32x4 mfma16(u32x4 a, u32x4 b, f32x4 c) {
    return __builtin_amdgcn_mfma_f32_16x16x32_bf16(__builtin_bit_cast(bf16x8, a), __builtin_bit_cast(bf16x8, b), c, 0, 0, 0);
}
DEVI void mfma16i(f32x4& c, u32x4 a, u32x4 b) { asm volatile("v_mfma_f32_16x16x32_bf16 %0, %1, %2, %0" : "+v"(c) : "v"(a), "v"(b)); }
#define MFMA_DONE() do { __builtin_amdgcn_sched_barrier(0); asm volatile("s_nop 7\n\ts_nop 1" ::: "memory"); __builtin_amdgcn_sched_barrier(0); } while (0)
DEVI u32x4 lds_r128(lptr p) { return *(LAS u32x4*)p; }
DEVI u32x2 lds_r64(lptr p) { return *(LAS u32x2*)p; }
DEVI float lds_rf(lptr p) { return *(LAS float*)p; }
DEVI void lds_w128(lptr p, u32x4 v) { *(LAS u32x4*)p = v; }
DEVI void lds_w64(lptr p, u32x2 v) { *(LAS u32x2*)p = v; }
DEVI void lds_w32(lptr p, unsigned v) { *(LAS unsigned*)p = v; }
DEVI void lds_w16(lptr p, unsigned v) { *(LAS u16*)p = (u16)v; }
DEVI u32x2 lds_tr(lptr p) { return __builtin_bit_cast(u32x2, __builtin_amdgcn_ds_read_tr16_b64_v4i16((LAS s16x4*)p)); }
DEVI u32x4 lds_trfrag(lptr p, int off2) { u32x2 a = lds_tr(p), b = lds_tr(p + off2); return u32x4{a.x, a.y, b.x, b.y}; }
template <int OFF> DEVI void glds16o(const char* sbase, unsigned voff) {
    asm volatile("global_load_lds_dwordx4 %0, %1 offset:%2" :: "v"(voff), "s"(sbase), "i"(OFF) : "memory");
}
DEVI void glds16(const char* sbase, unsigned voff, unsigned l) {
    asm volatile("s_mov_b32 m0, %2\n\ts_nop 0\n\tglobal_load_lds_dwordx4 %0, %1" :: "v"(voff), "s"(sbase), "s"(l) : "memory");
}
#define SET_M0(v) asm volatile("s_mov_b32 m0, %0\n\ts_nop 0" :: "s"(v) : "memory")
#define W_ADDTID(v, off) asm volatile("ds_write_addtid_b32 %0 offset:%1" :: "v"(v), "i"(off) : "memory")
#define CFENCE() asm volatile("" ::: "memory")
#define SCHED() __builtin_amdgcn_sched_barrier(0)
#define WAIT_VM(n) asm volatile("s_waitcnt vmcnt(" #n ")" ::: "memory")
#define WAIT_LGKM(n) __builtin_amdgcn_s_waitcnt(0xC07F | ((n) << 8))
#define WAIT_LGKM0() asm volatile("s_waitcnt lgkmcnt(0)" ::: "memory")
#define BARRIER() do { SCHED(); asm volatile("s_waitcnt lgkmcnt(0)\n\ts_barrier" ::: "memory"); SCHED(); } while (0)
#define RAW_BARRIER() do { SCHED(); asm volatile("s_barrier" ::: "memory"); SCHED(); } while (0)
#define RLX_AGENT __ATOMIC_RELAXED, __HIP_MEMORY_SCOPE_AGENT
constexpr unsigned SPIN_CAP = 1u << 16;
typedef __amdgpu_buffer_rsrc_t rsrc_t;
DEVI u32x4 ld_sc1(rsrc_t r, unsigned off) { return __builtin_amdgcn_raw_buffer_load_b128(r, off, 0, 16); }
DEVI void st_sc1(rsrc_t r, unsigned off, u32x4 v) { __builtin_amdgcn_raw_buffer_store_b128(v, r, off, 0, 16); }

DEVI u32x4 pack8(f32x4 a, f32x4 b) { return u32x4{pk_bf16(a[0], a[1]), pk_bf16(a[2], a[3]), pk_bf16(b[0], b[1]), pk_bf16(b[2], b[3])}; }
DEVI float phi(float t) {
    const float e = __builtin_amdgcn_fmed3f(__builtin_amdgcn_exp2f(t * 1.44269504089f), 0.f, 1.f);
    return __int_as_float(max(__float_as_int(t), 0x358637bd)) + e;
}
DEVI f32x4 phi4(f32x4 v) {
    f32x2 a, b;
    const f32x2 c = {1.44269504089f, 1.44269504089f};
    asm("v_pk_mul_f32 %0, %2, %4\n\tv_pk_mul_f32 %1, %3, %4" : "=&v"(a), "=&v"(b) : "v"(f32x2{v[0], v[1]}), "v"(f32x2{v[2], v[3]}), "s"(c));
    f32x4 r;
#pragma unroll
    for (int i = 0; i < 4; ++i)
        r[i] = __int_as_float(max(__float_as_int(v[i]), 0x358637bd)) + __builtin_amdgcn_fmed3f(__builtin_amdgcn_exp2f(i < 2 ? a[i] : b[i - 2]), 0.f, 1.f);
    return r;
}
DEVI unsigned umax4(u32x4 a) { return max(max(a.x, a.y), max(a.z, a.w)); }
DEVI u32x4 not_empty(u32x4 a) { return a; }
DEVI void tag_post(const Params& P, u64 tag) { __hip_atomic_store((GAS u64*)(P.ws + WS_ARR) + blockIdx.x, tag, RLX_AGENT); }
#define PIN_TAG() do { asm volatile("" : "+s"(tag)); tag += 1ull; } while (0)
template <int SL = PSLEEP>
DEVI void poll_addr(const Params& P, unsigned off, u64 tag) {
    const GAS u64* f = (const GAS u64*)(P.ws + off);
#pragma unroll 1
    for (unsigned spins = 0; spins < SPIN_CAP; ++spins) {
        if (__all(__hip_atomic_load(f, RLX_AGENT) == tag)) break;
        __builtin_amdgcn_s_sleep(SL);
    }
}
template <int SL = PSLEEP>
DEVI void poll_flags(const Params& P, unsigned off, int n, u64 tag, int lane) { poll_addr<SL>(P, off + (lane < n ? lane : 0) * 128, tag); }
DEVI unsigned flag_get(lptr smem, int f) { return __builtin_amdgcn_readfirstlane(__hip_atomic_load((LAS unsigned*)(smem + FL + f), __ATOMIC_RELAXED, __HIP_MEMORY_SCOPE_WORKGROUP)); }
template <int SL = FSLEEP>
DEVI void flag_wait(lptr smem, int f, unsigned v) {
#pragma unroll 1
    for (unsigned spins = 0; spins < (SPIN_CAP << 4); ++spins) { if (flag_get(smem, f) >= v) break; if (SL) __builtin_amdgcn_s_sleep(SL); }
    CFENCE();
}
DEVI void flag_add(int f) { asm volatile("s_mov_b64 exec, 1\n\tds_add_u32 %0, %1\n\ts_mov_b64 exec, -1" :: "v"(FL + f), "v"(1u) : "memory"); }
#define FLAG_ADD(f) asm volatile("s_mov_b64 exec, 1\n\tds_add_u32 %0, %1 offset:%2\n\ts_mov_b64 exec, -1" :: "v"(flp), "v"(one), "i"(f) : "memory")
DEVI void flag_set(int f, unsigned v) { asm volatile("s_mov_b64 exec, 1\n\tds_write_b32 %0, %1\n\ts_mov_b64 exec, -1" :: "v"(FL + f), "v"(v) : "memory"); }
#define NEEDP(seen, ptr, f, v) do { unsigned c_; asm volatile( \
        "v_cmp_gt_u32 vcc, %4, %1\n\ts_cbranch_vccnz 0f\n1:\n\t.subsection 1\n" \
        "0:\n\ts_mov_b32 %0, 0x100000\n2:\n\ts_sleep " NSLEEP "\n\tds_read_b32 %1, %2 offset:%3\n\ts_sub_u32 %0, %0, 1\n\ts_waitcnt lgkmcnt(0)\n\ts_cbranch_scc1 1b\n\t" \
        "v_cmp_gt_u32 vcc, %4, %1\n\ts_cbranch_vccnz 2b\n\ts_branch 1b\n\t.subsection 0" \
        : "=&s"(c_), "+v"(seen) : "v"(ptr), "i"(f), "i"(v) : "memory", "scc", "vcc"); } while (0)
#define NEED(seen, f, v) NEEDP(seen, flp, f, v)
DEVI void touch(const char* sbase, unsigned voff) {
    asm volatile("s_mov_b32 m0, %2\n\ts_nop 0\n\tglobal_load_lds_dword %0, %1" :: "v"(voff), "s"(sbase), "s"(LDS_JUNK) : "memory");
}
DEVI const char* group_addr(const Params& P, int xcd, int n, int lane) {
    if (n < 126) {
        const int c0 = n / 18, r = n - c0 * 18, c = c0 + 2, l = r - 8;
        return r < 8 ? P.x + (unsigned)(((xcd >> 1) * 512 + r * 64 + lane) * 1152 + c * 128)
                     : P.wqkv + (unsigned)((c * 64 + lane) * 3456 + (l < 5 ? 1152 : 2304 - 640) + (xcd & 1) * 512 + l * 128);
    }
    const int c = (n - 126) / 5, l = n - 126 - c * 5;
    return P.wqkv + (unsigned)((c * 64 + lane) * 3456 + (xcd & 1) * 512 + l * 128);
}
DEVI void touch_group(const Params& P, int xcd, int n, int lane) {
    if (n < 126) {
        const int c0 = n / 18, r = n - c0 * 18, c = c0 + 2;
        if (r < 8) touch(P.x, (unsigned)(((xcd >> 1) * 512 + r * 64 + lane) * 1152 + c * 128));
        else { const int l = r - 8; touch(P.wqkv, (unsigned)((c * 64 + lane) * 3456 + (l < 5 ? 1152 : 2304 - 640) + (xcd & 1) * 512 + l * 128)); }
    } else if (n < 171) {
        const int c = (n - 126) / 5, l = n - 126 - c * 5;
        touch(P.wqkv, (unsigned)((c * 64 + lane) * 3456 + (xcd & 1) * 512 + l * 128));
    } else {
        const int l = (n - 171) / 9, c = n - 171 - l * 9;
        touch(P.wo, (unsigned)((c * 64 + lane) * 1152 + l * 128));
    }
}
template <unsigned CH>
DEVI void wo_issue(const Params& P, int cg, int first, int stride, int lane) {
#pragma unroll 1
    for (int m = first; m < 9; m += stride) {
        const int q = 64 * m + lane, rho = q / 9, pc = q - rho * 9;
        const int k = (rho & 32) + 8 * ((rho >> 3) & 3) + 4 * (rho & 1) + ((rho >> 1) & 3);
        const unsigned qo = (unsigned)(k * 1152 + cg * 144 + pc * 16);
#pragma unroll
        for (int c = 0; c < 9; ++c) if ((CH >> c) & 1) glds16(P.wo + c * (64 * 1152), qo, (unsigned)((c < 6 ? c * 9216 : WO8 + (c - 6) * 9216) + m * 1024));
    }
}

constexpr int SP = 3088;
constexpr int XT_OFF = 133632, XT_BYTES = 4608, XPAD = 256;
constexpr int TT_OFF = 104448, TC = 1216, PT_OFF = 55296, PC_OFF = 2560, ACX_OFF = 55296;

#define CM2(o1, p1, a1, b1, o2, p2, a2, b2) do { asm( \
    "v_pk_mul_f32 %0, %2, %3 op_sel:[1,1] op_sel_hi:[1,0] neg_lo:[0,1]\n\tv_pk_mul_f32 %1, %4, %5 op_sel:[1,1] op_sel_hi:[1,0] neg_lo:[0,1]\n\t" \
    "v_pk_fma_f32 %0, %2, %3, %0 op_sel_hi:[0,1,1]\n\tv_pk_fma_f32 %1, %4, %5, %1 op_sel_hi:[0,1,1]" \
    : "=&v"(o1), "=&v"(o2) : "v"(a1), "v"(b1), "v"(a2), "v"(b2)); \
    asm("v_cvt_pk_f16_f32 %0, %2, %3\n\tv_cvt_pk_f16_f32 %1, %4, %5" : "=&v"(p1), "=&v"(p2) : "v"((o1).x), "v"((o1).y), "v"((o2).x), "v"((o2).y)); } while (0)
#define HM2(p1, a1, p2, a2, b) asm( \
    "v_pk_mul_f16 %0, %2, %4 op_sel_hi:[0,1]\n\tv_pk_mul_f16 %1, %3, %4 op_sel_hi:[0,1]\n\t" \
    "v_pk_fma_f16 %0, %2, %4, %0 op_sel:[1,1,0] op_sel_hi:[1,0,1] neg_lo:[0,1,0]\n\tv_pk_fma_f16 %1, %3, %4, %1 op_sel:[1,1,0] op_sel_hi:[1,0,1] neg_lo:[0,1,0]" \
    : "=&v"(p1), "=&v"(p2) : "v"(a1), "v"(a2), "v"(b))
DEVI f32x2 cexp_p(float a2, float f, float p) {
    float m = __builtin_amdgcn_exp2f(a2 * p);
    float r = __builtin_amdgcn_fractf(f * p);
    return f32x2{m * __builtin_amdgcn_cosf(r), m * __builtin_amdgcn_sinf(r)};
}
DEVI u32x4 mask_frag(unsigned bits, int g, unsigned val) {
    unsigned by = bits >> (8 * g);
    u32x4 r;
#pragma unroll
    for (int i = 0; i < 4; ++i) { unsigned t = (by >> (2 * i)) & 3u; r[i] = ((t | (t << 15)) & 0x10001u) * val; }
    return r;
}
struct Tab2 { unsigned v[8][64][4]; };
constexpr Tab2 make_tab2() {
    Tab2 t{};
    for (int l = 0; l < 64; ++l) {
        const int r = l & 15, g = l >> 4, j = l & 31, h = l >> 5;
        const unsigned bits[6] = {7u << (r + 11), 31u << (r + 6), 7u << (r + 3), 127u << (r + 5), 511u << (r + 4), 3u << (r + 12)};
        for (int m = 0; m < 6; ++m)
            for (int i = 0; i < 4; ++i) {
                const unsigned q = (bits[m] >> (8 * g + 2 * i)) & 3u, val = m < 5 ? 0x3f80u : 0xffffu;
                t.v[m][l][i] = (q & 1 ? val : 0u) | (q & 2 ? val << 16 : 0u);
            }
        for (int c = 0; c < 4; ++c) {
            const int v2 = 79 - (32 + (j >> 1)) - c + 4, v3 = 127 - (64 + 32 * h + (j >> 1)) - c + 4;
            t.v[6][l][c] = PT_OFF + (j & 1) * 1280 + c * 320 + (v2 >> 2) * 16 + h * 8 + (v2 & 3) * 2 - 64;
            t.v[7][l][c] = PT_OFF + PC_OFF + c * 320 + (v3 >> 2) * 16 + (j & 1) * 8 + (v3 & 3) * 2 - 64;
        }
    }
    return t;
}
__device__ const Tab2 g_tab2 = make_tab2();
DEVI float wave_max(float v) {
    v = fmaxf(v, __int_as_float(__builtin_amdgcn_update_dpp(0, __float_as_int(v), 0xB1, 0xf, 0xf, true)));
    v = fmaxf(v, __int_as_float(__builtin_amdgcn_update_dpp(0, __float_as_int(v), 0x4E, 0xf, 0xf, true)));
    v = fmaxf(v, __int_as_float(__builtin_amdgcn_update_dpp(0, __float_as_int(v), 0x124, 0xf, 0xf, true)));
    v = fmaxf(v, __int_as_float(__builtin_amdgcn_update_dpp(0, __float_as_int(v), 0x128, 0xf, 0xf, true)));
    const int i = __float_as_int(v);
    const int a = max(__builtin_amdgcn_readlane(i, 0), __builtin_amdgcn_readlane(i, 16)), b = max(__builtin_amdgcn_readlane(i, 32), __builtin_amdgcn_readlane(i, 48));
    return __int_as_float(max(a, b));
}

DEVI void ssm_role(const Params& P, lptr smem, int xcd, int li, int gid, int cg2, u64& tag, rsrc_t wsr) {
    const int gl = gid / 11, gi = gid - gl * 11, ch0 = gl * 64 + (gi < 10 ? gi * 6 : 58);
    const int tid = threadIdx.x, lane = tid & 63, w = __builtin_amdgcn_readfirstlane(tid >> 6);
    const int ch = w >> 1, half = (w ^ (w >> 2)) & 1, d = ch0 + ch;
    const bool own = gi < 10 || ch >= 2;
    const int lo16 = lane & 15, g = lane >> 4;
    STAMP_DECL; STAMP(1);
    __builtin_amdgcn_s_setprio(3);
    const unsigned po = (unsigned)(d * 256 + lane * 4);
    const unsigned r_la = *(const GAS unsigned*)(P.logA + po), r_ai = *(const GAS unsigned*)(P.aim + po);
    const unsigned r_cr = *(const GAS unsigned*)(P.cre + po), r_ci = *(const GAS unsigned*)(P.cim + po);
    const unsigned r_dt = *(const GAS u16*)(P.logdt + d * 2), r_ds = *(const GAS u16*)(P.dskip + d * 2);
    u32x4 ct[8];
    if (half) {
        const char* tp = (const char*)&g_tab2;
        asm volatile("" : "+s"(tp));
#pragma unroll
        for (int i = 0; i < 8; ++i) ct[i] = *(const GAS u32x4*)(tp + (unsigned)(i * 1024 + lane * 16));
    }
    const char* tga = (w < NTW && li < 12) ? group_addr(P, xcd, w * 12 + li, lane) : P.logA + po;
    RAW_BARRIER();
    if (TD1) __builtin_amdgcn_s_sleep(TD1);
    if (TD2 && w >= TW2) __builtin_amdgcn_s_sleep(TD2);
    const unsigned junk = *(const GAS unsigned*)tga;
    SCHED();
    const int tt = (tid + (li & 7) * 64) & 511;
    u32x3 xr[4];
    const unsigned xoff = (unsigned)(tt * 4608 + ch0 * 2);
    const unsigned boff = own ? WS_B + (unsigned)(d * 4096 + half * 2048 + lane * 16) : WS_END;
#define GATHER(i) do { SCHED(); if (w < 8) xr[i] = *(const GAS u32x3*)(P.x + xoff + (i) * 1152); SCHED(); } while (0)
#define PRIO(i) do { SCHED(); __builtin_amdgcn_s_setprio(i); SCHED(); } while (0)
    SCHED();
    lptr xt = smem + XT_OFF + ch * XT_BYTES, tb = smem + TT_OFF + ch * 4864, pt = smem + PT_OFF + ch * 3840;
    const int j32 = lane & 31, h = lane >> 5;
    if (!half) { lds_w64(xt + lane * 8, u32x2{0u, 0u}); lds_w32(smem + FL + 32, 0u); }
    int arow;
    {
        const u64 map0 = 0x0a09080803020100ull, map1 = 0x0c0b0a0a07060504ull, map2 = 0x1413110e12100f0dull;
        const u64 mm = j32 < 8 ? map0 : (j32 < 16 ? map1 : map2);
        arow = (int)((mm >> (8 * (j32 & 7))) & 0xff);
    }
    lptr ap = smem + arow * SP + w * 256 + h * 16, bp = smem + j32 * SP + w * 256 + h * 16;
    const int cpy = (3 - lo16) & 3;
    lptr xf7 = xt + (XPAD - 16 + 16 * lo16 + 8 * g) * 2 - 64 * 7;
    lptr tf = tb + cpy * TC + 2 * (527 - lo16 + 8 * g - cpy) - 64 * 16;
    lptr xc = smem + (w ^ 1) * 256 + lane * 4, xb = smem + XT_OFF + (XPAD + tt * 4) * 2;
    asm volatile("" : "+v"(ap), "+v"(bp), "+v"(xf7), "+v"(tf), "+v"(xc), "+v"(xb));
    const float dt = rbf(__expf(bf2f(r_dt)));
    const float cm = wave_max(fmaxf(fmaxf(fabsf(bf2f(r_cr)), fabsf(bfhi(r_cr))), fmaxf(fabsf(bf2f(r_ci)), fabsf(bfhi(r_ci)))));
    unsigned ce = __float_as_uint(cm) >> 23; ce = ce < 16u ? 16u : (ce > 240u ? 240u : ce);
    const float cs = __uint_as_float((263u - ce) << 23), ics = __uint_as_float((ce - 9u) << 23);
    f32x16 kacc;
#pragma unroll
    for (int i = 0; i < 16; ++i) kacc[i] = 0.f;
    STAMP(2);
    {
        const unsigned sh = half * 16;
        asm volatile("" :: "v"(r_la), "v"(r_ai), "v"(r_ds), "v"(ct[7]));
        const float la = bf2f(r_la >> sh), ai = bf2f(r_ai >> sh), cr = bf2f(r_cr >> sh) * cs, ci = bf2f(r_ci >> sh) * cs;
        const float a2 = (-rbf(__expf(la)) * dt) * 1.44269504089f, f = (ai * dt) * 0.15915494309f;
#define PUT(row, v) W_ADDTID(v, ((row) & 15) * SP)
        const f32x2 w1 = cexp_p(a2, -f, 1.f), W = cexp_p(a2, -f, 256.f);
        f32x2 u[8], v[4], c[9], e4, e8, t2;
        unsigned U[8], V[4], p, q;
        u[1] = cexp_p(a2, f, 8.f); v[1] = cexp_p(a2, f, 64.f);
        c[0] = f32x2{cr, -ci};
        U[1] = pk_f16(u[1]); V[1] = pk_f16(v[1]);
        GATHER(0);
        SET_M0(w * 256);
        PUT(0, pk_f16(c[0]));
        CM2(c[1], p, c[0], w1, u[2], U[2], u[1], u[1]); PUT(1, p);
        CM2(c[2], p, c[1], w1, u[3], U[3], u[2], u[1]); PUT(2, p);
        CM2(c[3], p, c[2], w1, u[4], U[4], u[2], u[2]); PUT(3, p);
        CM2(c[4], p, c[3], w1, u[5], U[5], u[4], u[1]); PUT(4, p);
        CM2(c[5], p, c[4], w1, u[6], U[6], u[4], u[2]); PUT(5, p);
        CM2(c[6], p, c[5], w1, u[7], U[7], u[4], u[3]); PUT(6, p);
        CM2(c[7], p, c[6], w1, v[2], V[2], v[1], v[1]); PUT(7, p);
        CM2(c[8], p, c[7], w1, v[3], V[3], v[2], v[1]);
        PRIO(2);
        GATHER(1);
        CM2(e8, p, c[8], W, e4, q, c[4], W); PUT(12, p); PUT(10, q);
        CM2(c[8], p, e8, W, c[4], q, e4, W); PUT(14, p); PUT(13, q);
        CM2(t2, p, c[0], W, e4, q, c[4], W); PUT(8, p); PUT(15, q);
        CM2(t2, p, c[2], W, e4, q, c[6], W); PUT(9, p); PUT(11, q);
        SET_M0(w * 256 + 16 * SP);
        CM2(e8, p, c[8], W, t2, q, W, W); PUT(17, p);
        CM2(e4, p, c[8], t2, c[0], q, e8, t2); PUT(16, p); PUT(18, q);
        CM2(c[8], p, e4, t2, e8, q, c[0], t2); PUT(19, p); PUT(20, q);
        PRIO(1);
        GATHER(2);
        u32x4 af[8];
#pragma unroll
        for (int s = 0; s < 8; ++s) af[s] = lds_r128(ap + 32 * s);
        CFENCE();
        PUT(16, V[2]); PUT(24, V[3]);
#pragma unroll
        for (int b = 1; b < 8; ++b) { HM2(p, V[2], q, V[3], U[b]); PUT(16 + b, p); PUT(24 + b, q); }
        SET_M0(w * 256);
        PRIO(0);
        GATHER(3);
        PUT(0, 0x3c00u); PUT(8, V[1]);
#pragma unroll
        for (int b = 1; b < 8; b += 2) { HM2(p, U[b], q, U[b == 7 ? 7 : b + 1], V[1]); PUT(8 + b, p); if (b < 7) PUT(9 + b, q); PUT(b, U[b]); if (b < 7) PUT(b + 1, U[b + 1]); }
#undef PUT
        u32x4 bfr[8];
#pragma unroll
        for (int s = 0; s < 8; ++s) bfr[s] = lds_r128(bp + 32 * s);
        SCHED();
        SCHED();
#pragma unroll
        for (int s = 0; s < 8; ++s)
            kacc = __builtin_amdgcn_mfma_f32_32x32x16_f16(__builtin_bit_cast(f16x8, af[s]), __builtin_bit_cast(f16x8, bfr[s]), kacc, 0, 0, 0);
    }
    STAMP(3);
    const float ks = 2.f * dt * ics;
#pragma unroll
    for (int i = 0; i < 12; ++i) kacc[i] *= ks;
    if (half) {
#pragma unroll
        for (int i = 0; i < 8; ++i) W_ADDTID(kacc[i], i * SP);
    } else {
#pragma unroll
        for (int i = 0; i < 4; ++i) W_ADDTID(kacc[8 + i], i * SP);
    }
    PIN_TAG();
    STAMP(4);
    BARRIER();
    STAMP(5);
    if (tid == 704) tag_post(P, tag);
    auto cleared = [&]() {
        if (w < 4) {
            WAIT_VM(0);
            if (lane == 0 && __hip_atomic_fetch_add((LAS unsigned*)(smem + FL + 32), 1u, __ATOMIC_RELAXED, __HIP_MEMORY_SCOPE_WORKGROUP) == 3u)
                __hip_atomic_store((GAS u64*)(P.ws + WS_BFL + gid * 128), tag, RLX_AGENT);
        }
    };
    auto x_in = [&]() {
        asm volatile("" :: "v"(junk));
        if (w < 8) {
#pragma unroll
            for (int k = 0; k < 3; ++k) {
                u32x2 lo = {__builtin_amdgcn_perm(xr[1][k], xr[0][k], 0x05040100u), __builtin_amdgcn_perm(xr[3][k], xr[2][k], 0x05040100u)};
                u32x2 hi = {__builtin_amdgcn_perm(xr[1][k], xr[0][k], 0x07060302u), __builtin_amdgcn_perm(xr[3][k], xr[2][k], 0x07060302u)};
                lds_w64(xb + (2 * k) * XT_BYTES, lo); lds_w64(xb + (2 * k + 1) * XT_BYTES, hi);
            }
        }
        if (w < 4) {
#pragma unroll
            for (int i = 0; i < 6; ++i)
                if (i >= 2 || gi < 10) __builtin_amdgcn_raw_buffer_store_b128(u32x4{EMPTY, EMPTY, EMPTY, EMPTY}, wsr, (unsigned)(tid * 16), WS_B + (ch0 + i) * 4096, 16);
        }
        BARRIER();
        if (w >= 8) wo_issue<0x03f>(P, cg2, w - 8, 4, lane);
    };
    auto TF = [&](int off) { u32x2 a = lds_r64(tf + off), b = lds_r64(tf + off + 8); return u32x4{a.x, a.y, b.x, b.y}; };
    f32x4 acc[8];
#pragma unroll
    for (int i = 0; i < 8; ++i) acc[i] = f32x4{0.f, 0.f, 0.f, 0.f};
    lptr axr;
    if (!half) {
        __builtin_amdgcn_s_setprio(2);
#pragma unroll
        for (int i = 0; i < 8; ++i) kacc[i] += lds_rf(xc + i * SP);
        unsigned dd[6];
        dd[0] = pk_bf16(kacc[7], kacc[6]); dd[1] = pk_bf16(kacc[5], kacc[4]); dd[2] = pk_bf16(kacc[3], kacc[2]);
        dd[3] = pk_bf16(kacc[1], kacc[0] + (lane == 0 ? bf2f(r_ds) : 0.f));
        dd[4] = __builtin_amdgcn_update_dpp(0, dd[0], 0x138, 0xf, 0xf, true);
        dd[5] = __builtin_amdgcn_update_dpp(0, dd[1], 0x138, 0xf, 0xf, true);
        unsigned mm[5];
#pragma unroll
        for (int i = 0; i < 5; ++i) mm[i] = __builtin_amdgcn_alignbit(dd[i + 1], dd[i], 16);
        lptr tw = tb + 2 * (536 - 8 * lane);
        lds_w128(tw, u32x4{dd[0], dd[1], dd[2], dd[3]}); lds_w128(tw + TC, u32x4{mm[0], mm[1], mm[2], mm[3]});
        lds_w128(tw + 2 * TC, u32x4{dd[1], dd[2], dd[3], dd[4]}); lds_w128(tw + 3 * TC, u32x4{mm[1], mm[2], mm[3], mm[4]});
        {
            const unsigned n0 = __builtin_amdgcn_readlane(dd[0], 63), n1 = __builtin_amdgcn_readlane(dd[1], 63);
            const unsigned kk = (n0 & 0xffffu) * 0x10001u, kh = n0 << 16, m4 = (n0 >> 16) | (n1 << 16);
            const int c = lane & 3, bl = lane >> 2;
            u32x4 e;
            e.x = 0u; e.y = lane == 14 ? kh : (lane == 15 ? kk : 0u); e.z = lane == 12 ? kh : (bl == 3 ? kk : 0u);
            e.w = lane == 14 ? n0 : (lane == 15 ? m4 : (bl == 3 ? kk : 0u));
            if (bl < 6) lds_w128(tb + c * TC + bl * 16 + (bl >> 2) * 1024, e);
        }
        x_in();
        u32x4 X[8];
#pragma unroll
        for (int i = 0; i < 8; ++i) X[i] = lds_r128(xf7 + 64 * 6 + 512 * i);
        u32x4 k0 = TF(1024 - 64), k8 = TF(512 - 64);
#pragma unroll
        for (int i = 0; i < 8; ++i) asm volatile("" : "+v"(acc[i]));
        __builtin_amdgcn_s_setprio(0);
        STAMP(6);
#pragma unroll
        for (int c = 1; c < 8; ++c) {
            u32x4 k0n, k8n;
            if (c < 7) { k0n = TF(1024 - 64 * (c + 1)); k8n = TF(512 - 64 * (c + 1)); }
#pragma unroll
            for (int i = 0; i < 8; ++i) {
                SCHED();
                mfma16i(acc[i], k0, X[i]);
                if (i < 7) mfma16i(acc[i + 1], k8, X[i]);
                SCHED();
                if (c < 7) X[i] = lds_r128(xf7 + 64 * (6 - c) + 512 * i);
            }
            k0 = k0n; k8 = k8n;
            if (c == 5) { SCHED(); cleared(); SCHED(); }
        }
        MFMA_DONE();
        SET_M0(ACX_OFF - 40960 + ch * 8192);
#pragma unroll
        for (int i = 0; i < 16; ++i) W_ADDTID(acc[4 + (i >> 2)][i & 3], 40960 + i * 256);
        axr = smem + ACX_OFF + ch * 8192 + 4096 + lane * 4;
    } else {
        u32x4 X[8];
#pragma unroll
        for (int i = 0; i < 4; ++i) if (i < 3 || lane < 48) lds_w128(pt + i * 1024 + lane * 16, u32x4{0u, 0u, 0u, 0u});
        CFENCE();
        const unsigned p89 = pk_bf16(kacc[8] + lds_rf(xc), kacc[9] + lds_rf(xc + SP)), pab = pk_bf16(kacc[10] + lds_rf(xc + 2 * SP), kacc[11] + lds_rf(xc + 3 * SP));
#pragma unroll
        for (int c = 0; c < 4; ++c) {
            lptr q2 = smem + (ct[6][c] + ch * 3840), q3 = smem + (ct[7][c] + ch * 3840);
            lds_w16(q2 + 64, p89); lds_w16(q2, p89 >> 16);
            lds_w16(q3 + 64, pab); lds_w16(q3, pab >> 16);
        }
        CFENCE();
        const u32x4 FX = ct[5] & (((unsigned)__builtin_amdgcn_readlane(p89, 63) >> 16) * 0x10001u);
        lptr pf2 = pt + cpy * 320 + ((79 - 64 - lo16 + 4 * g - cpy + 4) >> 2) * 16;
        u32x4 TA[3], TB[3], TCc[4];
#pragma unroll
        for (int i = 0; i < 3; ++i) { TA[i] = lds_r128(pf2 + 64 * (2 - i)); TB[i] = lds_r128(pf2 + 1280 + 64 * (2 - i)); }
#pragma unroll
        for (int i = 0; i < 4; ++i) TCc[i] = lds_r128(pf2 + PC_OFF + 64 * (3 - i));
        x_in();
#pragma unroll
        for (int i = 0; i < 8; ++i) X[i] = lds_r128(xf7 + 64 * 7 + 512 * i);
        STAMP(6);
        const f32x4 z4 = {0.f, 0.f, 0.f, 0.f};
#pragma unroll
        for (int st = 0; st < 6; ++st) {
            const u32x4 Xc = lds_r128(xf7 + 64 * 7 + 512 * st - 16);
            const u32x4 pa = pack8(mfma16(X[st], ct[0], z4), mfma16(X[st], ct[1], z4));
            const u32x4 pb = pack8(mfma16(X[st], ct[2], z4), mfma16(Xc, ct[1], z4));
#pragma unroll
            for (int dl = 2; dl <= 4; ++dl) if (st + dl < 8) {
                acc[st + dl] = mfma16(pa, TA[dl - 2], acc[st + dl]);
                acc[st + dl] = mfma16(pb, TB[dl - 2], acc[st + dl]);
            }
            if (st < 4) {
                const u32x4 pc = pack8(mfma16(X[st], ct[3], z4), mfma16(Xc, ct[4], z4));
#pragma unroll
                for (int dl = 4; dl <= 7; ++dl) if (st + dl < 8) acc[st + dl] = mfma16(pc, TCc[dl - 4], acc[st + dl]);
            }
        }
        SCHED(); cleared(); SCHED();
#pragma unroll
        for (int i = 4; i < 8; ++i) acc[i] = mfma16(FX, X[i - 4], acc[i]);
        const u32x4 k0 = TF(1024), k8 = TF(512), k16 = TF(0);
#pragma unroll
        for (int i = 0; i < 8; ++i) acc[i] = mfma16(k0, X[i], acc[i]);
#pragma unroll
        for (int i = 1; i < 8; ++i) acc[i] = mfma16(k8, X[i - 1], acc[i]);
#pragma unroll
        for (int i = 2; i < 8; ++i) acc[i] = mfma16(k16, X[i - 2], acc[i]);
        MFMA_DONE();
        SET_M0(ACX_OFF - 40960 + ch * 8192 + 4096);
#pragma unroll
        for (int i = 0; i < 16; ++i) W_ADDTID(acc[i >> 2][i & 3], 40960 + i * 256);
#pragma unroll
        for (int i = 0; i < 4; ++i) acc[i] = acc[4 + i];
        axr = smem + ACX_OFF + ch * 8192 + lane * 4;
    }
    STAMP(7);
    if (w == 0 && lane < 8) lds_w32(smem + FL + lane * 4, 0u);
    if (w >= 8) WAIT_VM(0);
    BARRIER();
    STAMP(8);
    if (w >= 8) wo_issue<0x1c0>(P, cg2, w - 8, 4, lane);
#pragma unroll
    for (int i = 0; i < 16; ++i) acc[i >> 2][i & 3] += lds_rf(axr + i * 256);
#pragma unroll
    for (int i = 0; i < 2; ++i) {
        const f32x4 ya = acc[2 * i], yb = acc[2 * i + 1];
        const auto sx = __builtin_amdgcn_permlane16_swap(pk_bf16(ya[0], ya[1]), pk_bf16(yb[0], yb[1]), false, false);
        const auto sy = __builtin_amdgcn_permlane16_swap(pk_bf16(ya[2], ya[3]), pk_bf16(yb[2], yb[3]), false, false);
        st_sc1(wsr, boff + i * 1024, not_empty(u32x4{sx[0], sy[0], sx[1], sy[1]}));
    }
    BARRIER();
    if (w >= 8) { WAIT_VM(2); flag_add(F_W1); flag_wait<1>(smem, F_W1, 4); }
    STAMP(9);
    if (w < 2) STAMP_OUT(2 + 2 * w, blockIdx.x + 256 * w);
}

constexpr int SLOT = 34816, XCH = 16384, KVT_OFF = 151296, KVP_OFF = 27648;
DEVI constexpr int slot_off(int s) { return (s & 3) == 0 ? SLOT : ((s & 3) == 1 ? 0 : (s & 3) * SLOT); }
DEVI constexpr int wq_off(int t) { return t == 0 ? 0 : 49152 + t * 27648; }

constexpr int quad_src(int quad) {
    const int pp = quad >= 192 ? 1 : 0, qi = quad - 192 * pp;
    const bool isz = qi >= 180;
    const int el = isz ? 0 : qi / 5, qd = qi - el * 5, hi = el >= 18 ? 1 : 0;
    const int dql = isz ? (qi < 189 ? qi - 180 : 0) : qd + 4 * hi;
    const int et = isz ? 0 : (el < 4 ? 0 : (el < 20 ? 1 : 2)), col = isz ? 0 : (el < 4 ? 12 + el : (el < 20 ? el - 4 : el - 20));
    const int tile = dql == 8 ? 5 : (et == 0 ? (dql >> 2) : (et == 1 ? 2 + (dql >> 2) : 4));
    return KVP_OFF + pp * 6144 + tile * 1024 + ((dql == 8 ? et : (dql & 3)) * 16 + col) * 4;
}
constexpr int quad_dst(int quad, int hf) {
    const int pp = quad >= 192 ? 1 : 0, qi = quad - 192 * pp;
    const bool isz = qi >= 180;
    const int el = isz ? 0 : qi / 5, qd = qi - el * 5, hi = el >= 18 ? 1 : 0;
    const int dql = isz ? (qi < 189 ? qi - 180 : 0) : qd + 4 * hi;
    const int dq = 9 * pp + dql, ks = dq >> 3, e = 36 * pp + el, ee = e >> 4;
    const int fr = ee == 0 ? 0 : (ee < 3 ? 2 * ee - 1 + ks : 2 * ee - 2 + ks);
    const int hz = 2 * pp + ((dql > 4 || (dql == 4 && hf)) ? 1 : 0);
    const bool dead = isz ? qi >= 189 : ((!hi && qd == 4 && hf) || (hi && qd == 0 && !hf));
    return dead ? LDS_DUMMY : KVT_OFF + (isz ? (9 + ks) * 1024 + (dq & 3) * 256 + hz * 16 : fr * 1024 + ((dq & 3) * 16 + (e & 15)) * 16) + ((dq >> 2) & 1) * 8 + hf * 4;
}
struct Tab { int v[2048]; };
constexpr Tab make_tab() {
    Tab t{};
    for (int c = 0; c < 192; ++c) {
        const int l = c & 63, q = 2 * ((c & 192) + (l >> 5) * 16 + (l & 15)) + ((l >> 4) & 1);
        t.v[4 * c] = quad_src(2 * c); t.v[4 * c + 1] = quad_src(2 * c + 1);
        t.v[4 * c + 2] = (quad_dst(q, 0) - KVT_OFF) | ((quad_dst(q, 1) - KVT_OFF) << 16); t.v[4 * c + 3] = (quad_dst(q + 64, 0) - KVT_OFF) | ((quad_dst(q + 64, 1) - KVT_OFF) << 16);
    }
    for (int q = 0; q < 576; ++q) {
        const int rho = q / 9, pc = q % 9, k = (rho & 32) + 8 * ((rho >> 3) & 3) + 4 * (rho & 1) + ((rho >> 1) & 3), m = q >> 6;
        t.v[768 + q] = k * 3456 + pc * 16 - (m & 1) * 1024;
        t.v[1344 + q] = k * 1152 + pc * 16 - (m & 3) * 1024;
    }
    return t;
}
__device__ const Tab g_tab = make_tab();
constexpr int IDX = P2_B, GW_OFF = 139264;

#define GET_TAG() do { const u32x2 t_ = lds_r64(smem + LDS_TAG); tag = (u64)(unsigned)__builtin_amdgcn_readfirstlane(t_.x) | ((u64)(unsigned)__builtin_amdgcn_readfirstlane(t_.y) << 32); } while (0)
DEVI void gemm_kv(const Params& P, lptr smem, int rb, int hg, int kw, int cg2, u64& tag, rsrc_t wsr) {
    const int lane = threadIdx.x & 63, lo16 = lane & 15, g = lane >> 4;
    const int p = kw & 1, ct = (kw < 3 ? kw : 0) * 64 + lane;
    STAMP_DECL; STAMP(1);
    __builtin_amdgcn_s_setprio(2);
    const int xrow = ((kw >> 1) * 64 + lo16) * 128, sw = (g ^ ((lo16 >> 1) & 7)) << 4;
    lptr xa[2] = {smem + xrow + sw, smem + xrow + (sw ^ 64)};
    const int wrow = (4 * g + (lo16 >> 2)) * 288;
    lptr wa = smem + XCH + wrow + (9 * p + (lane & 3)) * 8, wa2 = smem + XCH + wrow + (9 * p + ((lane & 3) == 3 ? 18 : 8)) * 8;
    lptr flp = smem + FL, ixp = smem + IDX + ct * 16, irp = smem + IDX + 3072 + 2304 + lane * 4;
    unsigned one = 1u;
    u32x4 sel[2];
    sel[0] = mask_frag(lo16 < 8 ? 1u << lo16 : 0u, 0, 0x3f80u); sel[1] = mask_frag(lo16 >= 8 ? 1u << (lo16 - 8) : 0u, 0, 0x3f80u);
    unsigned sto = WS_PART + (unsigned)((hg * 16 + rb) * 3072 + ct * 16);
    unsigned off = WS_PART + (unsigned)((hg * 16 + g) * 3072 + kw * 1024 + lo16 * 16);
    asm volatile("" : "+v"(flp), "+v"(one), "+v"(ixp), "+v"(irp), "+v"(sel[0]), "+v"(sel[1]), "+v"(sto), "+v"(off));
    const f32x4 z4 = {0.f, 0.f, 0.f, 0.f};
    f32x4 acc[4][5];
#pragma unroll
    for (int i = 0; i < 4; ++i)
#pragma unroll
        for (int j = 0; j < 5; ++j) { const float e = j < 2 ? 1e-6f : (j == 2 && lo16 < 12 ? 1e-6f : 0.f); acc[i][j] = f32x4{e, e, e, e}; asm volatile("" : "+v"(acc[i][j])); }
    auto rel = [&](int s) { return (s & 2) ? slot_off(s) - 2 * SLOT : slot_off(s); };
    auto rebase = [&](int dlt) {
        xa[0] += dlt; xa[1] += dlt; wa += dlt; wa2 += dlt;
        asm volatile("" : "+v"(xa[0]), "+v"(xa[1]), "+v"(wa), "+v"(wa2));
    };
    constexpr int NB = 6;
    u32x2 wl[NB], wh[NB];
    u32x4 xf[2][4];
    auto rdw = [&](int f, int t) {
        const int n = f / 5, j = f % 5;
        const int o = rel(n >> 1) + (n & 1) * 9216 + t * 4608;
        const u32x2 v = j == 2 ? lds_tr(wa2 + o) : lds_tr(wa + o + (j == 0 ? 0 : (j == 1 ? 32 : (j == 3 ? 152 : 184))));
        if (t) wh[f % NB] = v; else wl[f % NB] = v;
    };
    auto rdx = [&](int n, int r) { xf[n & 1][r] = lds_r128(xa[n & 1] + rel(n >> 1) + r * 2048); };
    rebase(0);
    BARRIER();
    STAMP(2);
    rdw(0, 0); rdw(0, 1);
#pragma unroll
    for (int r = 0; r < 4; ++r) rdx(0, r);
#pragma unroll
    for (int f = 1; f < NB - 1; ++f) { rdw(f, 0); rdw(f, 1); }
    auto kstep = [&](const int n) __attribute__((always_inline)) {
        const int s = n >> 1;
#pragma unroll
        for (int m = 0; m < 20; ++m) {
            const int j = m >> 2, r = m & 3, f = 5 * n + j, fn = f + NB - 1;
            SCHED();
            const u32x4 wv = {wl[f % NB].x, wl[f % NB].y, wh[f % NB].x, wh[f % NB].y};
            mfma16i(acc[r][j], xf[n & 1][r], wv);
            SCHED();
            if (r < 2 && fn < 90) rdw(fn, r);
            if (r == 2 && j < 4 && n < 17) rdx(n + 1, j);
            if (m == 15 && n < 17) WAIT_LGKM(12);
            if (m == 18 && !(n & 1)) {
                if (s < 8 && (s & 1)) rebase((s & 2) ? -2 * SLOT : 2 * SLOT);
                if (s == 8) WAIT_LGKM0();
                RAW_BARRIER();
                if (s == 0) STAMP(3);
                if (s == 4) STAMP(4);
            }
            if (m == 19 && n < 17) WAIT_LGKM(2);
        }
    };
    kstep(0); kstep(1); kstep(2); kstep(3); kstep(4); kstep(5); kstep(6); kstep(7); kstep(8);
    kstep(9); kstep(10); kstep(11); kstep(12); kstep(13); kstep(14); kstep(15); kstep(16); kstep(17);
    __builtin_amdgcn_s_setprio(3);
    const u32x4 ix = lds_r128(ixp);
    unsigned oo[9];
#pragma unroll
    for (int i = 0; i < 9; ++i) oo[i] = *(LAS unsigned*)(irp + i * 256);
    MFMA_DONE();
    STAMP(5);
    {
        f32x4 r[6], t0 = z4, t1 = z4, t2 = z4;
#pragma unroll
        for (int i = 0; i < 5; ++i) r[i] = z4;
#pragma unroll
        for (int u = 0; u < 2; ++u) {
            u32x4 pk[3], vb[3];
#pragma unroll
            for (int j = 0; j < 3; ++j) pk[j] = pack8(phi4(acc[2 * u][j]), phi4(acc[2 * u + 1][j]));
            vb[0] = pack8(acc[2 * u][2], acc[2 * u + 1][2]);
            if (lo16 < 12) vb[0] = u32x4{0x3f803f80u, 0x3f803f80u, 0x3f803f80u, 0x3f803f80u};
            vb[1] = pack8(acc[2 * u][3], acc[2 * u + 1][3]); vb[2] = pack8(acc[2 * u][4], acc[2 * u + 1][4]);
            t0 = mfma16(pk[2], vb[0], t0); t1 = mfma16(pk[2], vb[1], t1); t2 = mfma16(pk[2], vb[2], t2);
            r[0] = mfma16(pk[0], vb[0], r[0]); r[1] = mfma16(pk[1], vb[0], r[1]); r[2] = mfma16(pk[0], vb[1], r[2]);
            r[3] = mfma16(pk[1], vb[1], r[3]); r[4] = mfma16(pk[1], vb[2], r[4]);
        }
        r[5] = g == 0 ? t0 : (g == 1 ? t1 : t2);
        MFMA_DONE();
        SET_M0(KVP_OFF + kw * 6144);
#pragma unroll
        for (int t = 5; t >= 0; --t) {
            W_ADDTID(r[t][0], t * 1024); W_ADDTID(r[t][1], t * 1024 + 256);
            W_ADDTID(r[t][2], t * 1024 + 512); W_ADDTID(r[t][3], t * 1024 + 768);
        }
        FLAG_ADD(F_KVP);
    }
    STAMP(6);
    if (kw < 3) {
        lptr dst[4] = {smem + KVT_OFF + (ix.z & 0xffffu), smem + KVT_OFF + (ix.z >> 16), smem + KVT_OFF + (ix.w & 0xffffu), smem + KVT_OFF + (ix.w >> 16)};
        asm volatile("" : "+v"(dst[0]), "+v"(dst[1]), "+v"(dst[2]), "+v"(dst[3]));
        flag_wait<0>(smem, F_KVP, 4);
        STAMP(7);
        float pa[2][2][4];
#pragma unroll
        for (int i = 0; i < 2; ++i)
#pragma unroll
            for (int r = 0; r < 2; ++r)
#pragma unroll
                for (int k = 0; k < 4; ++k) pa[i][r][k] = lds_rf(smem + ix[i] + r * 12288 + k * 256);
        SCHED();
        u32x4 pv;
#pragma unroll
        for (int i = 0; i < 2; ++i)
#pragma unroll
            for (int k = 0; k < 2; ++k)
                pv[2 * i + k] = pk_bf16(pa[i][0][2 * k] + pa[i][1][2 * k], pa[i][0][2 * k + 1] + pa[i][1][2 * k + 1]);
        st_sc1(wsr, sto, not_empty(pv));
        SCHED();
        FLAG_ADD(F_KVR);
        STAMP(8);
        WAIT_VM(0);
        STAMP(9);
        f32x4 gs[4][2];
        auto group = [&](int i, const u32x4 (&a)[4]) {
            gs[i][0] = mfma16(sel[1], a[1], mfma16(sel[0], a[0], z4));
            gs[i][1] = mfma16(sel[1], a[3], mfma16(sel[0], a[2], z4));
        };
        {
            u32x4 a[4][4];
#pragma unroll
            for (int i = 0; i < 4; ++i)
#pragma unroll
                for (int t = 0; t < 4; ++t) a[i][t] = __builtin_amdgcn_raw_buffer_load_b128(wsr, off + t * 256, ((i + rb) & 3) * 12288, 16);
            SCHED();
            FLAG_ADD(F_SW);
#pragma unroll
            for (int i = 0; i < 4; ++i) group(i, a[i]);
        }
#pragma unroll
        for (int i = 0; i < 4; ++i) {
#pragma unroll 1
            for (unsigned spins = 0; spins < SPIN_CAP; ++spins) {
                if (!__any(__builtin_isunordered(gs[i][0][0], gs[i][1][0]))) break;
                if (spins) __builtin_amdgcn_s_sleep(RSLEEP);
                CFENCE();
                u32x4 a[4];
#pragma unroll
                for (int t = 0; t < 4; ++t) a[t] = __builtin_amdgcn_raw_buffer_load_b128(wsr, off + t * 256, ((i + rb) & 3) * 12288, 16);
                group(i, a);
                if (spins >= 32 && __all(max(max(umax4(a[0]), umax4(a[1])), max(umax4(a[2]), umax4(a[3]))) != EMPTY)) break;
            }
        }
        f32x4 sum[2];
        sum[0] = (gs[0][0] + gs[1][0]) + (gs[2][0] + gs[3][0]); sum[1] = (gs[0][1] + gs[1][1]) + (gs[2][1] + gs[3][1]);
#pragma unroll
        for (int i = 0; i < 4; ++i) lds_w32(dst[i], pk_bf16(sum[i >> 1][2 * (i & 1)], sum[i >> 1][2 * (i & 1) + 1]));
        CFENCE();
        FLAG_ADD(F_D);
        STAMP(10);
    } else {
        auto wo_chunks = [&](int c0, int c1) {
#pragma unroll 1
            for (int c = c0; c < c1; ++c) {
                const char* ws = P.wo + cg2 * 144 + c * (64 * 1152);
                const unsigned l = c < 8 ? c * 9216 : WO8;
                SET_M0(l); glds16o<0>(ws, oo[0]); glds16o<1024>(ws, oo[1]); glds16o<2048>(ws, oo[2]); glds16o<3072>(ws, oo[3]);
                SET_M0(l + 4096); glds16o<0>(ws, oo[4]); glds16o<1024>(ws, oo[5]); glds16o<2048>(ws, oo[6]); glds16o<3072>(ws, oo[7]);
                SET_M0(l + 8192); glds16o<0>(ws, oo[8]);
            }
        };
        flag_wait(smem, F_KVR, 3);
        wo_chunks(3, 9);
        WAIT_VM(0);
        FLAG_ADD(F_D);
        flag_wait(smem, F_SW, 3);
        flag_wait(smem, F_Q0, 8);
        wo_chunks(0, 3);
        WAIT_VM(0);
        FLAG_ADD(F_W0);
    }
    GET_TAG();
    __builtin_amdgcn_s_setprio(0);
    if (kw == 0) STAMP_OUT(1, blockIdx.x);
}

DEVI void gemm_q(const Params& P, lptr smem, int rb, int hg, int t8, u64& tag, rsrc_t wsr) {
    const int lane = threadIdx.x & 63, lw = t8 & 3;
    const bool ld = t8 < 4;
    STAMP_DECL; STAMP(1);
    __builtin_amdgcn_s_setprio(3);
    unsigned xo[4], wo_[5], qo[3];
    const int rx = hg & 3, rw = rb & 3;
    unsigned mx[4], mw[4];
    const unsigned xbase = (unsigned)((rb * 128 + 8 * lw + (lane >> 3)) * 1152 + (((lane & 7) ^ ((lane >> 4) + 4 * (lw & 1))) << 4));
#pragma unroll
    for (int i = 0; i < 4; ++i) { mx[i] = (((i + rx) & 3) * 4 + lw) * 1024; mw[i] = XCH + (((i + rw) & 3) * 4 + lw) * 1024; xo[i] = xbase + ((i + rx) & 3) * 36864; }
    const unsigned lb = lw * 4096;
    auto issue_x = [&](int s, int i0 = 0, int i1 = 4) {
#pragma unroll
        for (int i = i0; i < i1; ++i) glds16(P.x + s * 128, xo[i], slot_off(s) + mx[i]);
    };
    auto issue_w = [&](int s, int i0 = 0, int i1 = 4) {
        const char* ws = P.wqkv + s * (64 * 3456);
        if (i1 == 4 && (lw >> 1) == (s & 1)) glds16(ws, wo_[4], (unsigned)(slot_off(s) + XCH + 16384 + (lw & 1) * 1024));
#pragma unroll
        for (int i = i0; i < i1; ++i) glds16(ws, wo_[i], slot_off(s) + mw[i]);
    };
    auto issue_q = [&](int t, int i) {
        const char* ws = P.wqkv + hg * 144 + (3 * t + i) * (64 * 3456);
        if (lw == ((i + t) & 3)) glds16(ws, qo[2], (unsigned)(wq_off(t) + i * 9216 + 8192));
        SET_M0(wq_off(t) + i * 9216 + (lb >> 1));
        glds16o<0>(ws, qo[0]); glds16o<1024>(ws, qo[1]);
    };
    auto w_off = [&](int i) {
        const unsigned q = 64 * (i < 4 ? ((i + rw) & 3) * 4 + lw : 16 + (lw & 1)) + lane, rho = (q * 3641u) >> 16, pc = q - rho * 18;
        const unsigned k = (rho & 35u) + 8 * ((rho >> 2) & 3) + 4 * ((rho >> 4) & 1);
        wo_[i] = k * 3456 + (1152 + hg * 144) + pc * 16 + (pc < 9 ? 0u : 1008u);
    };
    if (ld) {
        issue_x(0, 0, 1); issue_x(1, 0, 1);
        SCHED();
        w_off(0);
        issue_w(0, 0, 1); issue_w(1, 0, 1);
        SCHED();
#pragma unroll
        for (int i = 1; i < 5; ++i) w_off(i);
        if (lw == 1) { glds16(P.gw, (unsigned)(lane * 16), GW_OFF); if (lane < 8) glds16(P.gw + 1024, (unsigned)(lane * 16), GW_OFF + 1024); }
        issue_x(0, 1, 4); issue_w(0, 1, 4); issue_w(1, 1, 4); issue_x(1, 1, 4);
        SET_M0(IDX + (lb >> 1));
        glds16o<0>((const char*)&g_tab + (lb >> 1), (unsigned)(lane * 16)); glds16o<1024>((const char*)&g_tab + (lb >> 1), (unsigned)(lane * 16));
    } else {
        if (lw == 0 && lane < 16) lds_w32(smem + FL + lane * 4, 0u);
#pragma unroll
        for (int i = 0; i < 3; ++i) lds_w128(smem + KVT_OFF + i * 4096 + lw * 1024 + lane * 16, u32x4{0u, 0u, 0u, 0u});
    }
    const int lo16 = lane & 15, g = lane >> 4;
    const int sw = (g ^ ((lo16 >> 1) & 7)) << 4;
    lptr xa[2] = {smem + (16 * t8 + lo16) * 128 + sw, smem + (16 * t8 + lo16) * 128 + (sw ^ 64)}, xb[2] = {xa[0] + 2 * SLOT, xa[1] + 2 * SLOT};
    lptr flp = smem + FL, iqp = smem + IDX + 3072 + lane * 4, gwp = smem + GW_OFF + g * 16;
    const int qrow = (2 * (4 * g + (lo16 >> 2))) * 144;
    lptr qw = smem + qrow + (lane & 3) * 8, qw4 = smem + qrow + (16 + (lane & 1)) * 8 + (lane & 2) * 2304;
    lptr qwh = qw + wq_off(1), qw4h = qw4 + wq_off(1), kfp = smem + KVT_OFF + lane * 16, kzp = smem + KVT_OFF + 9216 + (lane & 51) * 16;
    unsigned one = 1u, ob0 = WS_O + (unsigned)(((rb * 8 + t8) * 72 + hg * 9 + 2 * (g & 1) + (g >> 1)) * 256 + lo16 * 16);
    asm volatile("" : "+v"(flp), "+v"(one), "+v"(iqp), "+v"(gwp), "+v"(xa[0]), "+v"(xa[1]), "+v"(xb[0]), "+v"(xb[1]));
    asm volatile("" : "+v"(qw), "+v"(qw4), "+v"(qwh), "+v"(qw4h), "+v"(kfp), "+v"(kzp), "+v"(ob0));
    asm volatile(".subsection 2\n.Lkf_code_end:\n\t.fill 256, 4, 0xbf800000\n\t.subsection 0");
    const f32x4 z4 = {0.f, 0.f, 0.f, 0.f};
    f32x4 gacc = z4, qa[6];
#pragma unroll
    for (int j = 0; j < 6; ++j) { const float e = j < 5 ? 1e-6f : 0.f; qa[j] = f32x4{e, e, e, e}; asm volatile("" : "+v"(qa[j])); }
    u32x4 xk[18];
    u64 pf = 0;
    WAIT_VM(8); BARRIER();
    if (ld) { issue_w(2); issue_x(2); if (LA3) { issue_w(3); issue_x(3); } }
#pragma unroll
    for (int s = 0; s < 9; ++s) {
#pragma unroll
        for (int k = 0; k < 2; ++k) xk[2 * s + k] = lds_r128(((s & 2) ? xb[k] - 2 * SLOT : xa[k]) + slot_off(s));
        if (t8 == hg) {
            gacc = mfma16(lds_r128(gwp + s * 128), xk[2 * s], gacc);
            gacc = mfma16(lds_r128(gwp + s * 128 + 64), xk[2 * s + 1], gacc);
        }
        if (t8 == 4) {
            if (s == 1) { tag = *(const __attribute__((address_space(4))) u64*)(P.ws + WS_CTL); PIN_TAG(); if (lane == 0) lds_w64(smem + LDS_TAG, u32x2{(unsigned)tag, (unsigned)(tag >> 32)}); }
            if (s == 4) asm volatile("global_load_dwordx2 %0, %1, %2 sc1" : "=v"(pf) : "v"((lane < 5 ? lane : 0) * 128), "s"(P.ws + WS_PFL) : "memory");
            if (s == 7) {
                asm volatile("s_waitcnt vmcnt(0)" : "+v"(pf) :: "memory");
                if (!__all(pf == tag)) poll_flags(P, WS_PFL, 5, tag, lane);
            }
        }
        if (LA3) { if (s < 6) WAIT_VM(17); else if (s == 6) WAIT_VM(14); else WAIT_VM(12); }
        else if (s < 7) WAIT_VM(8); else WAIT_VM(6);
        BARRIER();
        if (ld) {
            const int sn = s + 3 + LA3;
            if (sn < 9) { issue_w(sn); issue_x(sn); } else if (sn < 12) { issue_q(sn - 9, 0); issue_q(sn - 9, 1); issue_q(sn - 9, 2); }
            if (s == 3) { qo[0] = *(LAS unsigned*)(iqp + lw * 512); qo[1] = *(LAS unsigned*)(iqp + lw * 512 + 256); qo[2] = *(LAS unsigned*)(iqp + 2048); CFENCE(); }
        }
    }
    __builtin_amdgcn_s_setprio(0);
    STAMP(5);
    if (!ld) {
        const unsigned co = WS_O + (unsigned)(((rb * 8 + 2 * lw) * 72 + hg * 9) * 256 + lane * 16);
#pragma unroll
        for (int i = 0; i < 5; ++i)
            if (i < 4 || lane < 32) st_sc1(wsr, i < 4 ? co + (i >> 1) * 18432 + (i & 1) * 1024 : co + 2048 + (lane >> 4) * (18432 - 256), u32x4{EMPTY, EMPTY, EMPTY, EMPTY});
    }
    GET_TAG();
    if (t8 == 4 && lane == 0) tag_post(P, tag);
    if (t8 == hg) {
        const float gate = rbf(__builtin_amdgcn_rcpf(1.f + __expf(-rbf(gacc[0]))));
        if (lane < 16) __hip_atomic_store((GAS float*)(P.ws + WS_GATE) + (rb * 128 + hg * 16 + lane), gate, RLX_AGENT);
    }
    auto peek = [&](int f) { return __hip_atomic_load((LAS unsigned*)(flp + f), __ATOMIC_RELAXED, __HIP_MEMORY_SCOPE_WORKGROUP); };
    auto rdq = [&](int ks, int j) {
        const int o = (ks < 6 ? ks : ks - 6) * 4608;
        return j < 4 ? lds_trfrag((ks < 6 ? qw : qwh) + o + 32 * j, 144) : lds_trfrag((ks < 6 ? qw4 : qw4h) + o, 144);
    };
    u32x4 wq[2][4], w4[2];
#pragma unroll
    for (int i = 0; i < 2; ++i) {
#pragma unroll
        for (int j = 0; j < 4; ++j) wq[i][j] = rdq(i, j);
        if (i == 0) w4[0] = rdq(0, 4);
    }
    unsigned seenq = 0;
#pragma unroll
    for (int ks = 0; ks < 18; ++ks) {
        if (ks == 4) {
            SCHED();
            WAIT_VM(0); FLAG_ADD(F_Q0);
            seenq = peek(F_Q0); NEED(seenq, F_Q0, 8);
        }
        if (ks == QH) { SCHED(); seenq = peek(F_KVR); NEED(seenq, F_KVR, 3); STAMP(6); }
        if (ks == 16) { SCHED(); FLAG_ADD(F_QD); }
#pragma unroll
        for (int j = 0; j < 5; ++j) {
            SCHED();
            if (j == 0 && ks < 17) { if (ks & 1) WAIT_LGKM(10); else WAIT_LGKM(8); }
            if (j < 4) mfma16i(qa[j], wq[ks & 1][j], xk[ks]); else mfma16i(qa[4 + (ks & 1)], w4[(ks >> 1) & 1], xk[ks]);
            SCHED();
            if (ks + 2 < 18) { if (j < 4) wq[ks & 1][j] = rdq(ks + 2, j); else if (!(ks & 1)) w4[((ks >> 1) + 1) & 1] = rdq(ks + 2, 4); }
        }
    }
    __builtin_amdgcn_s_setprio(2);
    MFMA_DONE();
#pragma unroll
    for (int i = 0; i < 4; ++i) {
        const auto sw2 = __builtin_amdgcn_permlane32_swap(__float_as_uint(qa[5][i]), __float_as_uint(qa[5][i]), false, false);
        qa[4][i] += __uint_as_float(sw2[1]);
    }
    u32x4 b0 = pack8(phi4(qa[0]), phi4(qa[1])), b1 = pack8(phi4(qa[2]), phi4(qa[3])), b2 = pack8(phi4(qa[4]), z4);
    asm volatile("" : "+v"(b0), "+v"(b1), "+v"(b2));
    STAMP(7);
    flag_wait<1>(smem, F_D, 4);
    STAMP(8);
    {
        u32x4 kf[12];
#pragma unroll
        for (int i = 0; i < 12; ++i) { kf[i] = lds_r128(i < 3 ? kzp + i * 1024 : kfp + (i - 3) * 1024); SCHED(); }
        SCHED();
        const f32x4 den = mfma16(kf[2], b2, mfma16(kf[1], b1, mfma16(kf[0], b0, f32x4{1e-6f, 1e-6f, 1e-6f, 1e-6f})));
        f32x4 o[5];
        o[0] = mfma16(kf[3], b0, z4);
        o[1] = mfma16(kf[5], b1, mfma16(kf[4], b0, z4));
        o[2] = mfma16(kf[7], b1, mfma16(kf[6], b0, z4));
        o[3] = mfma16(kf[9], b2, mfma16(kf[8], b1, z4));
        o[4] = mfma16(kf[11], b2, mfma16(kf[10], b1, z4));
        f32x4 r;
#pragma unroll
        for (int i = 0; i < 4; ++i) r[i] = __builtin_amdgcn_rcpf(den[i]);
        const float r1a = g == 0 ? r[0] : r[1], r2 = g == 0 ? r[1] : r[2], r3a = g <= 1 ? r[2] : r[3], r3b = g == 0 ? r[2] : r[3];
        u32x2 ob[5];
        ob[0] = u32x2{pk_bf16(o[0][0] * r[0], o[0][1] * r[0]), pk_bf16(o[0][2] * r[0], o[0][3] * r[0])};
        ob[1] = u32x2{pk_bf16(o[1][0] * r1a, o[1][1] * r1a), pk_bf16(o[1][2] * r[1], o[1][3] * r[1])};
        ob[2] = u32x2{pk_bf16(o[2][0] * r2, o[2][1] * r2), pk_bf16(o[2][2] * r2, o[2][3] * r2)};
        ob[3] = u32x2{pk_bf16(o[3][0] * r3a, o[3][1] * r3a), pk_bf16(o[3][2] * r3b, o[3][3] * r3b)};
        ob[4] = u32x2{pk_bf16(o[4][0] * r[3], o[4][1] * r[3]), pk_bf16(o[4][2] * r[3], o[4][3] * r[3])};
        SCHED();
        if (t8 == 7 && lane == 0) __hip_atomic_store((GAS u64*)(P.ws + WS_HINT + (rb * 8 + hg) * 128), tag, RLX_AGENT);
        SCHED();
#pragma unroll
        for (int t = 0; t < 3; ++t) {
            const u32x2 a = ob[2 * t], b = ob[t < 2 ? 2 * t + 1 : 4];
            const auto sx = __builtin_amdgcn_permlane16_swap(a.x, b.x, false, false), sy = __builtin_amdgcn_permlane16_swap(a.y, b.y, false, false);
            if (t < 2 || g == 0) st_sc1(wsr, ob0 + 1024 * t, not_empty(u32x4{sx[0], sy[0], sx[1], sy[1]}));
        }
    }
    __builtin_amdgcn_s_setprio(0);
    STAMP(9);
    if (t8 == 0) STAMP_OUT(5, blockIdx.x + 256);
    if (t8 == 7) STAMP_OUT(7, blockIdx.x + 768);
}

DEVI void phase2(const Params& P, lptr smem, int rt64, int cg, bool gemm, u64 tag, rsrc_t wsr) {
    const int lane = threadIdx.x & 63, w0 = __builtin_amdgcn_readfirstlane(threadIdx.x >> 6);
    const int w = gemm ? (w0 + 8) % 12 : w0, tid = w * 64 + lane;
    const int lo16 = lane & 15, g = lane >> 4;
    const int rt = w & 3, kt = w >> 2;
    STAMP_DECL; STAMP(1);
    const int pb = gemm ? P2_B : 55296;
    if (gemm && w < 4) flag_wait(smem, F_W0, 1);
    STAMP(2);
    const int qrow = (2 * (4 * g + (lo16 >> 2))) * 144;
    lptr qw = smem + qrow + (lane & 3) * 8, qw4 = smem + qrow + (16 + (lane & 1)) * 8;
    int wofs[3], oofs[3];
#pragma unroll
    for (int u = 0; u < 3; ++u) {
        const int c = kt * 3 + (u + cg) % 3;
        wofs[u] = c < (gemm ? 8 : 6) ? c * 9216 : WO8 + (gemm ? 0 : (c - 6) * 9216); oofs[u] = c * 2048;
    }
    auto rdo = [&](int i, int j) { return j < 4 ? lds_trfrag(qw + wofs[i >> 1] + (i & 1) * 4608 + 32 * j, 144) : lds_trfrag(qw4 + wofs[i >> 1] + (i & 1) * 4608, 144); };
    const unsigned ooff = WS_O + (unsigned)((rt64 * 4 + rt) * 18432 + lane * 16);
    auto ldo = [&](int i) { return __builtin_amdgcn_raw_buffer_load_b128(wsr, ooff + (i & 1) * 1024, oofs[i >> 1], 16); };
    u32x4 wf[6][5];
#pragma unroll
    for (int i = 0; i < 6; ++i)
#pragma unroll
        for (int j = 0; j < 5; ++j) wf[i][j] = rdo(i, j);
    const int r0 = tid < 576 ? tid / 9 : 0, c0 = tid - r0 * 9;
    unsigned boff = tid < 576 ? WS_B + (unsigned)((cg * 72 + (tid >> 3)) * 4096 + (rt64 >> 3) * 1024 + (((tid >> 2) & 1) * 32 + ((rt64 >> 2) & 1) * 16 + (rt64 & 3) * 4 + (tid & 3)) * 16) : WS_END;
    unsigned goff = WS_GATE + (unsigned)((rt64 * 64 + r0) * 4);
    unsigned qd = gemm ? __hip_atomic_load((LAS unsigned*)(smem + FL + F_QD), __ATOMIC_RELAXED, __HIP_MEMORY_SCOPE_WORKGROUP) : 8u;
    asm volatile("" : "+v"(boff), "+v"(goff));
    STAMP(3);
    if (w == 3) {
        const int a0 = cg * 72, g0 = (a0 >> 6) * 11 + (a0 & 63) / 6, a1 = a0 + 71, g1 = (a1 >> 6) * 11 + (a1 & 63) / 6;
        poll_addr<OSLEEP>(P, lane < 8 ? WS_HINT + ((rt64 >> 1) * 8 + lane) * 128 : WS_BFL + min(g0 + lane - 8, g1) * 128, tag);
        flag_set(F_H, 1);
    } else flag_wait<1>(smem, F_H, 1);
    STAMP(4);
    u32x4 bv; unsigned gv;
    f32x4 acc[5];
    {
        u32x4 of[6];
#pragma unroll
        for (int i = 0; i < 6; ++i) of[i] = ldo(i);
        bv = ld_sc1(wsr, boff); gv = __builtin_amdgcn_raw_buffer_load_b32(wsr, goff, 0, 16);
        SCHED();
#pragma unroll
        for (int j = 0; j < 5; ++j) acc[j] = f32x4{0.f, 0.f, 0.f, 0.f};
#pragma unroll
        for (int i = 0; i < 6; ++i)
#pragma unroll
            for (int j = 0; j < 5; ++j) acc[j] = mfma16(wf[i][j], of[i], acc[j]);
    }
    if (!__all(acc[0][0] == acc[0][0])) {
#pragma unroll
        for (int j = 0; j < 5; ++j) acc[j] = f32x4{0.f, 0.f, 0.f, 0.f};
        u32x4 of[6];
#pragma unroll 1
        for (unsigned spins = 0; spins < SPIN_CAP; ++spins) {
            CFENCE();
            unsigned m = 0;
#pragma unroll
            for (int i = 0; i < 6; ++i) of[i] = ldo(i);
#pragma unroll
            for (int i = 0; i < 6; ++i) m = max(m, umax4(of[i]));
            if (__all(m != EMPTY)) break;
        }
#pragma unroll
        for (int i = 0; i < 6; ++i)
#pragma unroll
            for (int j = 0; j < 5; ++j) acc[j] = mfma16(rdo(i, j), of[i], acc[j]);
    }
    if ((unsigned)__builtin_amdgcn_readfirstlane(qd) < 8u) flag_wait(smem, F_QD, 8);
    lptr ap = smem + P2_ACC + kt * (64 * P2_AP) + (rt * 16 + lo16) * P2_AP + g * 16;
#pragma unroll
    for (int j = 0; j < 5; ++j) if (j < 4 || g < 2) lds_w128(ap + j * 64, __builtin_bit_cast(u32x4, acc[j]));
    if (w < 9) {
#pragma unroll 1
        for (unsigned spins = 0; spins < SPIN_CAP; ++spins) {
            if (__all(umax4(bv) != EMPTY)) break;
            __builtin_amdgcn_s_sleep(2);
            CFENCE();
            bv = ld_sc1(wsr, boff);
        }
        lptr bp = smem + pb + ((tid & 3) * 16 + ((tid >> 2) & 1) * 8) * 144 + (tid >> 3) * 2;
#pragma unroll
        for (int q = 0; q < 4; ++q) { lds_w16(bp + (2 * q) * 144, bv[q]); lds_w16(bp + (2 * q + 1) * 144, bv[q] >> 16); }
    }
    STAMP(5);
    BARRIER();
    STAMP(6);
    if (w < 9) {
        lptr a = smem + P2_ACC + r0 * P2_AP + c0 * 32;
        const f32x4 s0 = __builtin_bit_cast(f32x4, lds_r128(a)) + __builtin_bit_cast(f32x4, lds_r128(a + 64 * P2_AP)) + __builtin_bit_cast(f32x4, lds_r128(a + 128 * P2_AP));
        const f32x4 s1 = __builtin_bit_cast(f32x4, lds_r128(a + 16)) + __builtin_bit_cast(f32x4, lds_r128(a + 64 * P2_AP + 16)) + __builtin_bit_cast(f32x4, lds_r128(a + 128 * P2_AP + 16));
        const u32x4 bb = lds_r128(smem + pb + tid * 16);
        const float gt = __uint_as_float(gv), og = rbf(1.f - gt);
        u32x4 o;
#pragma unroll
        for (int i = 0; i < 4; ++i) {
            const float a0 = i < 2 ? s0[2 * i] : s1[2 * i - 4], a1 = i < 2 ? s0[2 * i + 1] : s1[2 * i - 3];
            o[i] = pk_bf16(gt * a0 + og * bf2f(bb[i]), gt * a1 + og * bfhi(bb[i]));
        }
        __builtin_amdgcn_raw_buffer_store_b128(o, __builtin_amdgcn_make_buffer_rsrc(P.out, 0, SEQ * DM * 2, 0x00020000), (unsigned)((rt64 * 64 + r0) * 1152 + cg * 144 + c0 * 16), 0, OUTAUX);
    }
    STAMP(7);
    if (w == 4) STAMP_OUT(blockIdx.x < 128 ? 3 : 6, blockIdx.x + 512);
}

DEVI void helper_role(const Params& P, lptr smem, int xcd, int li, u64& tag, rsrc_t wsr) {
    const int tid = threadIdx.x, lane = tid & 63, w = __builtin_amdgcn_readfirstlane(tid >> 6);
    STAMP_DECL; STAMP(1);
    if (li >= 13) {
        if (HD) __builtin_amdgcn_s_sleep(HD);
#pragma unroll 1
        for (int n = 12 * NTW + (li - 13) + 3 * w; n < 171; n += 36) touch_group(P, xcd, n, lane);
    }
    if (li == 12) {
#pragma unroll
        for (int i = 0; i < 7; ++i) {
            const int u = (i * 5 + xcd - 3) * 12 + w;
            if (u < 384) st_sc1(wsr, WS_PART + (unsigned)(u * 1024 + lane * 16), u32x4{EMPTY, EMPTY, EMPTY, EMPTY});
        }
        WAIT_VM(0);
    }
    PIN_TAG();
    BARRIER();
    if (tid == 0) {
        if (li == 12) __hip_atomic_store((GAS u64*)(P.ws + WS_PFL + (xcd - 3) * 128), tag, RLX_AGENT);
        tag_post(P, tag);
    }
    if (li >= 13) {
#pragma unroll 1
        for (int n = 171 + (li - 13) + 3 * w; n < 234; n += 36) touch_group(P, xcd, n, lane);
    }
    STAMP(2);
    if (w == 11) {
        STAMP_OUT(8, blockIdx.x);
        if (blockIdx.x == 255) {
            const GAS u64* f = (const GAS u64*)(P.ws + WS_ARR) + lane;
#pragma unroll 1
            for (unsigned spins = 0; spins < SPIN_CAP; ++spins) {
                const bool ok = __hip_atomic_load(f, RLX_AGENT) == tag && __hip_atomic_load(f + 64, RLX_AGENT) == tag &&
                                __hip_atomic_load(f + 128, RLX_AGENT) == tag && __hip_atomic_load(f + 192, RLX_AGENT) == tag;
                if (__all(ok)) break;
                __builtin_amdgcn_s_sleep(8);
            }
            if (lane == 0) __hip_atomic_store((GAS u64*)(P.ws + WS_CTL), tag, RLX_AGENT);
        }
    }
}

__global__ __launch_bounds__(768, 3) void k_fused(Params P) {
    __shared__ __attribute__((aligned(16))) char smem_[LDS_BYTES];
    lptr smem = (lptr)smem_;
    const int b = blockIdx.x, xcd = b & 7, idx = b >> 3;
    const int lane = threadIdx.x & 63, w = __builtin_amdgcn_readfirstlane(threadIdx.x >> 6);
    const rsrc_t wsr = __builtin_amdgcn_make_buffer_rsrc(P.ws, 0, (int)WS_END, 0x00020000);
    const int rt64 = xcd * 4 + (idx & 3), cg2 = idx >> 2;
    if (idx >= CPF && w < 6) {
        const char* pc; unsigned len;
        asm volatile("s_getpc_b64 %0\n.Lkf_code_begin:\n\ts_mov_b32 %1, .Lkf_code_end-.Lkf_code_begin" : "=s"(pc), "=s"(len));
        touch(pc, min((unsigned)(threadIdx.x * 128), len));
    }
    u64 tag;
    if (__builtin_expect(idx < 16, 1)) {
        const int rbl = idx & 3, hgl = idx >> 2, rb = (xcd >> 1) * 4 + rbl, hg = (xcd & 1) * 4 + hgl;
        if (__builtin_expect((w & 12) != 4, 1)) gemm_q(P, smem, rb, hg, w < 4 ? w : w - 4, tag, wsr);
        else gemm_kv(P, smem, rb, hg, w - 4, cg2, tag, wsr);
    } else {
        tag = *(const __attribute__((address_space(4))) u64*)(P.ws + WS_CTL);
        const int li = idx - 16, gid = li < 11 ? xcd * 11 + li : 88 + (li - 11) * 8 + xcd;
        if (gid < 99) ssm_role(P, smem, xcd, li, gid, cg2, tag, wsr);
        else {
            helper_role(P, smem, xcd, li, tag, wsr);
            if (w < 9) wo_issue<0x1ff>(P, cg2, w, 9, lane);
            if (w == 10 && lane < 16) lds_w32(smem + FL + lane * 4, 0u);
            WAIT_VM(0);
            BARRIER();
        }
    }
    phase2(P, smem, rt64, cg2, idx < 16, tag, wsr);
}

extern "C" void kernel_launch(void* const* d_in, const int* in_sizes, int n_in, void* d_out, int out_size, void* d_ws, size_t ws_size,
                              hipStream_t stream) {
    Params P;
    P.x = (const char*)d_in[0]; P.wqkv = (const char*)d_in[1]; P.wo = (const char*)d_in[2];
    P.logA = (const char*)d_in[4]; P.aim = (const char*)d_in[5]; P.cre = (const char*)d_in[6]; P.cim = (const char*)d_in[7];
    P.logdt = (const char*)d_in[8]; P.dskip = (const char*)d_in[9]; P.gw = (const char*)d_in[10];
    P.out = (char*)d_out; P.ws = (char*)d_ws;
    k_fused<<<256, 768, 0, stream>>>(P);
}
```

```cpp
#include <hip/hip_runtime.h>
#include <stdint.h>

#define GAS __attribute__((address_space(1)))
#define LAS __attribute__((address_space(3)))
#define DEVI __device__ __forceinline__
typedef unsigned short u16;
typedef unsigned long long u64;
typedef __attribute__((ext_vector_type(2))) unsigned u32x2;
typedef __attribute__((ext_vector_type(3))) unsigned u32x3;
typedef __attribute__((ext_vector_type(4))) unsigned u32x4;
typedef __attribute__((ext_vector_type(4))) short s16x4;
typedef __attribute__((ext_vector_type(2))) float f32x2;
typedef __attribute__((ext_vector_type(4))) float f32x4;
typedef __attribute__((ext_vector_type(16))) float f32x16;
typedef __attribute__((ext_vector_type(2))) __bf16 bf16x2;
typedef __attribute__((ext_vector_type(8))) __bf16 bf16x8;
typedef __attribute__((ext_vector_type(2))) _Float16 f16x2;
typedef __attribute__((ext_vector_type(8))) _Float16 f16x8;
typedef LAS char* lptr;

#define PREF 1
#define ROT 0
#define FSLEEP 2
#define NSLEEP "3"
#define LDRIFT 0
#define PSLEEP 6
#define CSLEEP 1
#define OSLEEP 2
#define RSLEEP 3
#define HOLD 1
#define GROT ((w + li) & 3)
#define NTW 8
#define OUTAUX 17
#define TEARLY 1
#define HDELAY 0
#define CPF 16
#define LA3 0
#define TD1 0
#define TD2 0
#define TW2 3
#define HD 0
#define QH 4
#define WOA 0x00f
#define WOT 48
constexpr int SEQ = 2048, DM = 576;

constexpr unsigned WS_CTL = 0;
constexpr unsigned WS_ARR = 256;
constexpr unsigned WS_BFL = 4096;
constexpr unsigned WS_HINT = 20480;
constexpr unsigned WS_GATE = 36864;
constexpr unsigned WS_PFL = 49152;
constexpr unsigned WS_PART = 65536;
constexpr unsigned WS_O = WS_PART + 8 * 16 * 3072;
constexpr unsigned WS_B = WS_O + SEQ * DM * 2;
constexpr unsigned WS_END = WS_B + SEQ * DM * 2;
constexpr unsigned EMPTY = 0xffffffffu;

constexpr size_t WS_STAMP = 16u << 20;
#define STAMP_DECL
#define STAMP(i)
#define CSTAMP(i)
#define STAMP_OUT(role, slot)

struct Params {
    const char* x; const char* wqkv; const char* wo; const char* logA; const char* aim; const char* cre; const char* cim;
    const char* logdt; const char* dskip; const char* gw; char* out; char* ws;
};

constexpr int LDS_BYTES = 163840, FL = 163584, LDS_TAG = FL + 64, LDS_DUMMY = FL + 128, LDS_JUNK = 161280;
enum { F_SW = 0, F_LANDED = 4, F_W1 = 20, F_B = 28, F_H = 24, F_KVP = 36, F_KVR = 40, F_Q0 = 44, F_QD = 48, F_D = 52, F_C1 = 56, F_W0 = 60 };
constexpr int WO8 = 132096, P2_ACC = 73728, P2_AP = 304, P2_B = 141312, P2_G = 150528;

DEVI float bf2f(unsigned h) { return __uint_as_float(h << 16); }
DEVI float bfhi(unsigned h) { return __uint_as_float(h & 0xffff0000u); }
DEVI unsigned pk_bf16(float a, float b) { return __builtin_bit_cast(unsigned, __builtin_convertvector(f32x2{a, b}, bf16x2)); }
DEVI unsigned pk_f16(f32x2 v) { return __builtin_bit_cast(unsigned, __builtin_convertvector(v, f16x2)); }
DEVI float rbf(float a) { return bf2f(pk_bf16(a, a)); }
DEVI f32x4 mfma16(u32x4 a, u32x4 b, f32x4 c) {
    return __builtin_amdgcn_mfma_f32_16x16x32_bf16(__builtin_bit_cast(bf16x8, a), __builtin_bit_cast(bf16x8, b), c, 0, 0, 0);
}
DEVI void mfma16i(f32x4& c, u32x4 a, u32x4 b) { asm volatile("v_mfma_f32_16x16x32_bf16 %0, %1, %2, %0" : "+v"(c) : "v"(a), "v"(b)); }
#define MFMA_DONE() do { __builtin_amdgcn_sched_barrier(0); asm volatile("s_nop 7\n\ts_nop 1" ::: "memory"); __builtin_amdgcn_sched_barrier(0); } while (0)
DEVI u32x4 lds_r128(lptr p) { return *(LAS u32x4*)p; }
DEVI u32x2 lds_r64(lptr p) { return *(LAS u32x2*)p; }
DEVI float lds_rf(lptr p) { return *(LAS float*)p; }
DEVI void lds_w128(lptr p, u32x4 v) { *(LAS u32x4*)p = v; }
DEVI void lds_w64(lptr p, u32x2 v) { *(LAS u32x2*)p = v; }
DEVI void lds_w32(lptr p, unsigned v) { *(LAS unsigned*)p = v; }
DEVI void lds_w16(lptr p, unsigned v) { *(LAS u16*)p = (u16)v; }
DEVI u32x2 lds_tr(lptr p) { return __builtin_bit_cast(u32x2, __builtin_amdgcn_ds_read_tr16_b64_v4i16((LAS s16x4*)p)); }
DEVI u32x4 lds_trfrag(lptr p, int off2) { u32x2 a = lds_tr(p), b = lds_tr(p + off2); return u32x4{a.x, a.y, b.x, b.y}; }
template <int OFF> DEVI void glds16o(const char* sbase, unsigned voff) {
    asm volatile("global_load_lds_dwordx4 %0, %1 offset:%2" :: "v"(voff), "s"(sbase), "i"(OFF) : "memory");
}
DEVI void glds16(const char* sbase, unsigned voff, unsigned l) {
    asm volatile("s_mov_b32 m0, %2\n\ts_nop 0\n\tglobal_load_lds_dwordx4 %0, %1" :: "v"(voff), "s"(sbase), "s"(l) : "memory");
}
#define SET_M0(v) asm volatile("s_mov_b32 m0, %0\n\ts_nop 0" :: "s"(v) : "memory")
#define W_ADDTID(v, off) asm volatile("ds_write_addtid_b32 %0 offset:%1" :: "v"(v), "i"(off) : "memory")
#define CFENCE() asm volatile("" ::: "memory")
#define SCHED() __builtin_amdgcn_sched_barrier(0)
#define WAIT_VM(n) asm volatile("s_waitcnt vmcnt(" #n ")" ::: "memory")
#define WAIT_LGKM(n) __builtin_amdgcn_s_waitcnt(0xC07F | ((n) << 8))
#define WAIT_LGKM0() asm volatile("s_waitcnt lgkmcnt(0)" ::: "memory")
#define BARRIER() do { SCHED(); asm volatile("s_waitcnt lgkmcnt(0)\n\ts_barrier" ::: "memory"); SCHED(); } while (0)
#define RAW_BARRIER() do { SCHED(); asm volatile("s_barrier" ::: "memory"); SCHED(); } while (0)
#define RLX_AGENT __ATOMIC_RELAXED, __HIP_MEMORY_SCOPE_AGENT
constexpr unsigned SPIN_CAP = 1u << 16;
typedef __amdgpu_buffer_rsrc_t rsrc_t;
DEVI u32x4 ld_sc1(rsrc_t r, unsigned off) { return __builtin_amdgcn_raw_buffer_load_b128(r, off, 0, 16); }
DEVI void st_sc1(rsrc_t r, unsigned off, u32x4 v) { __builtin_amdgcn_raw_buffer_store_b128(v, r, off, 0, 16); }

DEVI u32x4 pack8(f32x4 a, f32x4 b) { return u32x4{pk_bf16(a[0], a[1]), pk_bf16(a[2], a[3]), pk_bf16(b[0], b[1]), pk_bf16(b[2], b[3])}; }
DEVI float phi(float t) {
    const float e = __builtin_amdgcn_fmed3f(__builtin_amdgcn_exp2f(t * 1.44269504089f), 0.f, 1.f);
    return __int_as_float(max(__float_as_int(t), 0x358637bd)) + e;
}
DEVI f32x4 phi4(f32x4 v) {
    f32x2 a, b;
    const f32x2 c = {1.44269504089f, 1.44269504089f};
    asm("v_pk_mul_f32 %0, %2, %4\n\tv_pk_mul_f32 %1, %3, %4" : "=&v"(a), "=&v"(b) : "v"(f32x2{v[0], v[1]}), "v"(f32x2{v[2], v[3]}), "s"(c));
    f32x4 r;
#pragma unroll
    for (int i = 0; i < 4; ++i)
        r[i] = __int_as_float(max(__float_as_int(v[i]), 0x358637bd)) + __builtin_amdgcn_fmed3f(__builtin_amdgcn_exp2f(i < 2 ? a[i] : b[i - 2]), 0.f, 1.f);
    return r;
}
DEVI unsigned umax4(u32x4 a) { return max(max(a.x, a.y), max(a.z, a.w)); }
DEVI u32x4 not_empty(u32x4 a) { return a; }
DEVI void tag_post(const Params& P, u64 tag) { __hip_atomic_store((GAS u64*)(P.ws + WS_ARR) + blockIdx.x, tag, RLX_AGENT); }
#define PIN_TAG() do { asm volatile("" : "+s"(tag)); tag += 1ull; } while (0)
template <int SL = PSLEEP>
DEVI void poll_addr(const Params& P, unsigned off, u64 tag) {
    const GAS u64* f = (const GAS u64*)(P.ws + off);
#pragma unroll 1
    for (unsigned spins = 0; spins < SPIN_CAP; ++spins) {
        if (__all(__hip_atomic_load(f, RLX_AGENT) == tag)) break;
        __builtin_amdgcn_s_sleep(SL);
    }
}
template <int SL = PSLEEP>
DEVI void poll_flags(const Params& P, unsigned off, int n, u64 tag, int lane) { poll_addr<SL>(P, off + (lane < n ? lane : 0) * 128, tag); }
DEVI unsigned flag_get(lptr smem, int f) { return __builtin_amdgcn_readfirstlane(__hip_atomic_load((LAS unsigned*)(smem + FL + f), __ATOMIC_RELAXED, __HIP_MEMORY_SCOPE_WORKGROUP)); }
template <int SL = FSLEEP>
DEVI void flag_wait(lptr smem, int f, unsigned v) {
#pragma unroll 1
    for (unsigned spins = 0; spins < (SPIN_CAP << 4); ++spins) { if (flag_get(smem, f) >= v) break; if (SL) __builtin_amdgcn_s_sleep(SL); }
    CFENCE();
}
DEVI void flag_add(int f) { asm volatile("s_mov_b64 exec, 1\n\tds_add_u32 %0, %1\n\ts_mov_b64 exec, -1" :: "v"(FL + f), "v"(1u) : "memory"); }
#define FLAG_ADD(f) asm volatile("s_mov_b64 exec, 1\n\tds_add_u32 %0, %1 offset:%2\n\ts_mov_b64 exec, -1" :: "v"(flp), "v"(one), "i"(f) : "memory")
DEVI void flag_set(int f, unsigned v) { asm volatile("s_mov_b64 exec, 1\n\tds_write_b32 %0, %1\n\ts_mov_b64 exec, -1" :: "v"(FL + f), "v"(v) : "memory"); }
#define NEEDP(seen, ptr, f, v) do { unsigned c_; asm volatile( \
        "v_cmp_gt_u32 vcc, %4, %1\n\ts_cbranch_vccnz 0f\n1:\n\t.subsection 1\n" \
        "0:\n\ts_mov_b32 %0, 0x100000\n2:\n\ts_sleep " NSLEEP "\n\tds_read_b32 %1, %2 offset:%3\n\ts_sub_u32 %0, %0, 1\n\ts_waitcnt lgkmcnt(0)\n\ts_cbranch_scc1 1b\n\t" \
        "v_cmp_gt_u32 vcc, %4, %1\n\ts_cbranch_vccnz 2b\n\ts_branch 1b\n\t.subsection 0" \
        : "=&s"(c_), "+v"(seen) : "v"(ptr), "i"(f), "i"(v) : "memory", "scc", "vcc"); } while (0)
#define NEED(seen, f, v) NEEDP(seen, flp, f, v)
DEVI void touch(const char* sbase, unsigned voff) {
    asm volatile("s_mov_b32 m0, %2\n\ts_nop 0\n\tglobal_load_lds_dword %0, %1" :: "v"(voff), "s"(sbase), "s"(LDS_JUNK) : "memory");
}
DEVI const char* group_addr(const Params& P, int xcd, int n, int lane) {
    if (n < 126) {
        const int c0 = n / 18, r = n - c0 * 18, c = c0 + 2, l = r - 8;
        return r < 8 ? P.x + (unsigned)(((xcd >> 1) * 512 + r * 64 + lane) * 1152 + c * 128)
                     : P.wqkv + (unsigned)((c * 64 + lane) * 3456 + (l < 5 ? 1152 : 2304 - 640) + (xcd & 1) * 512 + l * 128);
    }
    const int c = (n - 126) / 5, l = n - 126 - c * 5;
    return P.wqkv + (unsigned)((c * 64 + lane) * 3456 + (xcd & 1) * 512 + l * 128);
}
DEVI void touch_group(const Params& P, int xcd, int n, int lane) {
    if (n < 126) {
        const int c0 = n / 18, r = n - c0 * 18, c = c0 + 2;
        if (r < 8) touch(P.x, (unsigned)(((xcd >> 1) * 512 + r * 64 + lane) * 1152 + c * 128));
        else { const int l = r - 8; touch(P.wqkv, (unsigned)((c * 64 + lane) * 3456 + (l < 5 ? 1152 : 2304 - 640) + (xcd & 1) * 512 + l * 128)); }
    } else if (n < 171) {
        const int c = (n - 126) / 5, l = n - 126 - c * 5;
        touch(P.wqkv, (unsigned)((c * 64 + lane) * 3456 + (xcd & 1) * 512 + l * 128));
    } else {
        const int l = (n - 171) / 9, c = n - 171 - l * 9;
        touch(P.wo, (unsigned)((c * 64 + lane) * 1152 + l * 128));
    }
}
template <unsigned CH>
DEVI void wo_issue(const Params& P, int cg, int first, int stride, int lane) {
#pragma unroll 1
    for (int m = first; m < 9; m += stride) {
        const int q = 64 * m + lane, rho = q / 9, pc = q - rho * 9;
        const int k = (rho & 32) + 8 * ((rho >> 3) & 3) + 4 * (rho & 1) + ((rho >> 1) & 3);
        const unsigned qo = (unsigned)(k * 1152 + cg * 144 + pc * 16);
#pragma unroll
        for (int c = 0; c < 9; ++c) if ((CH >> c) & 1) glds16(P.wo + c * (64 * 1152), qo, (unsigned)(c * 9216 + m * 1024));
    }
}

constexpr int SP = 3088;
constexpr int XT_OFF = 135936, XT_BYTES = 4608, XPAD = 256;
constexpr int TT_OFF = 107520, TC = 1184, PT_OFF = 82944, PC_OFF = 2560;

#define CM2(o1, p1, a1, b1, o2, p2, a2, b2) do { asm( \
    "v_pk_mul_f32 %0, %2, %3 op_sel:[1,1] op_sel_hi:[1,0] neg_lo:[0,1]\n\tv_pk_mul_f32 %1, %4, %5 op_sel:[1,1] op_sel_hi:[1,0] neg_lo:[0,1]\n\t" \
    "v_pk_fma_f32 %0, %2, %3, %0 op_sel_hi:[0,1,1]\n\tv_pk_fma_f32 %1, %4, %5, %1 op_sel_hi:[0,1,1]" \
    : "=&v"(o1), "=&v"(o2) : "v"(a1), "v"(b1), "v"(a2), "v"(b2)); \
    asm("v_cvt_pk_f16_f32 %0, %2, %3\n\tv_cvt_pk_f16_f32 %1, %4, %5" : "=&v"(p1), "=&v"(p2) : "v"((o1).x), "v"((o1).y), "v"((o2).x), "v"((o2).y)); } while (0)
#define HM2(p1, a1, p2, a2, b) asm( \
    "v_pk_mul_f16 %0, %2, %4 op_sel_hi:[0,1]\n\tv_pk_mul_f16 %1, %3, %4 op_sel_hi:[0,1]\n\t" \
    "v_pk_fma_f16 %0, %2, %4, %0 op_sel:[1,1,0] op_sel_hi:[1,0,1] neg_lo:[0,1,0]\n\tv_pk_fma_f16 %1, %3, %4, %1 op_sel:[1,1,0] op_sel_hi:[1,0,1] neg_lo:[0,1,0]" \
    : "=&v"(p1), "=&v"(p2) : "v"(a1), "v"(a2), "v"(b))
DEVI f32x2 cexp_p(float a2, float f, float p) {
    float m = __builtin_amdgcn_exp2f(a2 * p);
    float r = __builtin_amdgcn_fractf(f * p);
    return f32x2{m * __builtin_amdgcn_cosf(r), m * __builtin_amdgcn_sinf(r)};
}
DEVI u32x4 mask_frag(unsigned bits, int g, unsigned val) {
    unsigned by = bits >> (8 * g);
    u32x4 r;
#pragma unroll
    for (int i = 0; i < 4; ++i) { unsigned t = (by >> (2 * i)) & 3u; r[i] = ((t | (t << 15)) & 0x10001u) * val; }
    return r;
}
struct Tab2 { unsigned v[8][64][4]; };
constexpr Tab2 make_tab2() {
    Tab2 t{};
    for (int l = 0; l < 64; ++l) {
        const int r = l & 15, g = l >> 4, j = l & 31, h = l >> 5;
        const unsigned bits[6] = {7u << (r + 11), 31u << (r + 6), 7u << (r + 3), 127u << (r + 5), 511u << (r + 4), 3u << (r + 12)};
        for (int m = 0; m < 6; ++m)
            for (int i = 0; i < 4; ++i) {
                const unsigned q = (bits[m] >> (8 * g + 2 * i)) & 3u, val = m < 5 ? 0x3f80u : 0xffffu;
                t.v[m][l][i] = (q & 1 ? val : 0u) | (q & 2 ? val << 16 : 0u);
            }
        for (int c = 0; c < 4; ++c) {
            const int v2 = 79 - (32 + (j >> 1)) - c + 4, v3 = 127 - (64 + 32 * h + (j >> 1)) - c + 4;
            t.v[6][l][c] = PT_OFF + (j & 1) * 1280 + c * 320 + (v2 >> 2) * 16 + h * 8 + (v2 & 3) * 2 - 64;
            t.v[7][l][c] = PT_OFF + PC_OFF + c * 320 + (v3 >> 2) * 16 + (j & 1) * 8 + (v3 & 3) * 2 - 64;
        }
    }
    return t;
}
__device__ const Tab2 g_tab2 = make_tab2();
DEVI float wave_max(float v) {
    v = fmaxf(v, __int_as_float(__builtin_amdgcn_update_dpp(0, __float_as_int(v), 0xB1, 0xf, 0xf, true)));
    v = fmaxf(v, __int_as_float(__builtin_amdgcn_update_dpp(0, __float_as_int(v), 0x4E, 0xf, 0xf, true)));
    v = fmaxf(v, __int_as_float(__builtin_amdgcn_update_dpp(0, __float_as_int(v), 0x124, 0xf, 0xf, true)));
    v = fmaxf(v, __int_as_float(__builtin_amdgcn_update_dpp(0, __float_as_int(v), 0x128, 0xf, 0xf, true)));
    const int i = __float_as_int(v);
    const int a = max(__builtin_amdgcn_readlane(i, 0), __builtin_amdgcn_readlane(i, 16)), b = max(__builtin_amdgcn_readlane(i, 32), __builtin_amdgcn_readlane(i, 48));
    return __int_as_float(max(a, b));
}

DEVI void ssm_role(const Params& P, lptr smem, int xcd, int li, int gid, int cg2, u64& tag, rsrc_t wsr) {
    const int gl = gid / 11, gi = gid - gl * 11, ch0 = gl * 64 + (gi < 10 ? gi * 6 : 58);
    const int tid = threadIdx.x, lane = tid & 63, w = __builtin_amdgcn_readfirstlane(tid >> 6);
    const int ch = w >> 1, half = (w ^ (w >> 2)) & 1, d = ch0 + ch;
    const bool own = gi < 10 || ch >= 2;
    const int lo16 = lane & 15, g = lane >> 4;
    STAMP_DECL; STAMP(1);
    __builtin_amdgcn_s_setprio(3);
    if (half) {
        const char* tp = (const char*)&g_tab2;
        SET_M0(TT_OFF + ch * 8192);
        glds16o<0>(tp, (unsigned)(lane * 16)); glds16o<1024>(tp, (unsigned)(lane * 16)); glds16o<2048>(tp, (unsigned)(lane * 16)); glds16o<3072>(tp, (unsigned)(lane * 16));
        SET_M0(TT_OFF + ch * 8192 + 4096);
        tp += 4096;
        glds16o<0>(tp, (unsigned)(lane * 16)); glds16o<1024>(tp, (unsigned)(lane * 16)); glds16o<2048>(tp, (unsigned)(lane * 16)); glds16o<3072>(tp, (unsigned)(lane * 16));
    }
    const unsigned po = (unsigned)(d * 256 + lane * 4);
    unsigned r_la, r_ai, r_cr, r_ci, r_dt, r_ds, junk = 0, junk2 = 0;
    asm volatile("global_load_dword %0, %6, %8\n\tglobal_load_dword %1, %6, %9\n\tglobal_load_dword %2, %6, %10\n\tglobal_load_dword %3, %6, %11\n\t"
                 "global_load_ushort %4, %7, %12\n\tglobal_load_ushort %5, %7, %13"
                 : "=&v"(r_cr), "=&v"(r_ci), "=&v"(r_la), "=&v"(r_ai), "=&v"(r_dt), "=&v"(r_ds)
                 : "v"(po), "v"(d * 2), "s"(P.cre), "s"(P.cim), "s"(P.logA), "s"(P.aim), "s"(P.logdt), "s"(P.dskip) : "memory");
#define TOUCH(dst, addr) asm volatile("global_load_dword %0, %1, off" : "+v"(dst) : "v"(addr) : "memory")
    const bool tch = w < NTW && li < 12;
    const char* tga = group_addr(P, xcd, tch ? w * 12 + li : 0, lane);
    RAW_BARRIER();
    if (tch && w < TW2) TOUCH(junk, tga);
    SCHED();
    const int tt = (tid + (li & 7) * 64) & 511;
    u32x3 xr[4] = {};
    const unsigned xoff = (unsigned)(tt * 4608 + ch0 * 2);
    const unsigned boff = own ? WS_B + (unsigned)(d * 4096 + half * 2048 + lane * 16) : WS_END;
#define GATHER(i) do { SCHED(); if (w < 8) asm volatile("global_load_dwordx3 %0, %1, %2 offset:%3" : "+v"(xr[i]) : "v"(xoff), "s"(P.x), "i"((i) * 1152) : "memory"); SCHED(); } while (0)
#define PRIO(i) do { SCHED(); __builtin_amdgcn_s_setprio(i); SCHED(); } while (0)
    SCHED();
    lptr xt = smem + XT_OFF + ch * XT_BYTES, tb = smem + TT_OFF + ch * 4 * TC, pt = smem + PT_OFF + ch * 4096;
    const int j32 = lane & 31, h = lane >> 5;
    if (!half) { lds_w32(smem + FL + 32, 0u); lds_w32(smem + FL + 36 + ch * 4, 0u); }
    int arow;
    {
        const u64 map0 = 0x0a09080803020100ull, map1 = 0x0c0b0a0a07060504ull, map2 = 0x1413110e12100f0dull;
        const u64 mm = j32 < 8 ? map0 : (j32 < 16 ? map1 : map2);
        arow = (int)((mm >> (8 * (j32 & 7))) & 0xff);
    }
    lptr ap = smem + arow * SP + w * 256 + h * 16, bp = smem + j32 * SP + w * 256 + h * 16;
    const int cpy = (3 - lo16) & 3;
    lptr xf7 = xt + (XPAD - 16 + 16 * lo16 + 8 * g) * 2 - 64 * 7;
    lptr tf = tb + cpy * TC + 2 * (527 - lo16 + 8 * g - cpy) - 64 * 16;
    lptr xc = smem + (w ^ 1) * 256 + lane * 4, xb = smem + XT_OFF + (XPAD + tt * 4) * 2;
    asm volatile("" : "+v"(ap), "+v"(bp), "+v"(xf7), "+v"(tf), "+v"(xc), "+v"(xb));
    if (tch && w < TW2) WAIT_VM(1); else WAIT_VM(0);
    asm volatile("" : "+v"(r_la), "+v"(r_ai), "+v"(r_cr), "+v"(r_ci), "+v"(r_dt), "+v"(r_ds));
    if (tch && w >= TW2) TOUCH(junk, tga);
    const float dt = rbf(__expf(bf2f(r_dt)));
    const float cm = wave_max(fmaxf(fmaxf(fabsf(bf2f(r_cr)), fabsf(bfhi(r_cr))), fmaxf(fabsf(bf2f(r_ci)), fabsf(bfhi(r_ci)))));
    unsigned ce = __float_as_uint(cm) >> 23; ce = ce < 16u ? 16u : (ce > 240u ? 240u : ce);
    const float cs = __uint_as_float((263u - ce) << 23), ics = __uint_as_float((ce - 9u) << 23);
    f32x16 kacc;
#pragma unroll
    for (int i = 0; i < 16; ++i) kacc[i] = 0.f;
    STAMP(2);
    {
        const unsigned sh = half * 16;
        const float la = bf2f(r_la >> sh), ai = bf2f(r_ai >> sh), cr = bf2f(r_cr >> sh) * cs, ci = bf2f(r_ci >> sh) * cs;
        const float a2 = (-rbf(__expf(la)) * dt) * 1.44269504089f, f = (ai * dt) * 0.15915494309f;
#define PUT(row, v) W_ADDTID(v, ((row) & 15) * SP)
        const f32x2 w1 = cexp_p(a2, -f, 1.f), W = cexp_p(a2, -f, 256.f);
        f32x2 u[8], v[4], c[9], e4, e8, t2;
        unsigned U[8], V[4], p, q;
        u[1] = cexp_p(a2, f, 8.f); v[1] = cexp_p(a2, f, 64.f);
        c[0] = f32x2{cr, -ci};
        U[1] = pk_f16(u[1]); V[1] = pk_f16(v[1]);
        GATHER(0);
        SET_M0(w * 256);
        PUT(0, pk_f16(c[0]));
        CM2(c[1], p, c[0], w1, u[2], U[2], u[1], u[1]); PUT(1, p);
        CM2(c[2], p, c[1], w1, u[3], U[3], u[2], u[1]); PUT(2, p);
        CM2(c[3], p, c[2], w1, u[4], U[4], u[2], u[2]); PUT(3, p);
        CM2(c[4], p, c[3], w1, u[5], U[5], u[4], u[1]); PUT(4, p);
        CM2(c[5], p, c[4], w1, u[6], U[6], u[4], u[2]); PUT(5, p);
        CM2(c[6], p, c[5], w1, u[7], U[7], u[4], u[3]); PUT(6, p);
        CM2(c[7], p, c[6], w1, v[2], V[2], v[1], v[1]); PUT(7, p);
        CM2(c[8], p, c[7], w1, v[3], V[3], v[2], v[1]);
        PRIO(2);
        GATHER(1);
        CM2(e8, p, c[8], W, e4, q, c[4], W); PUT(12, p); PUT(10, q);
        CM2(c[8], p, e8, W, c[4], q, e4, W); PUT(14, p); PUT(13, q);
        CM2(t2, p, c[0], W, e4, q, c[4], W); PUT(8, p); PUT(15, q);
        CM2(t2, p, c[2], W, e4, q, c[6], W); PUT(9, p); PUT(11, q);
        SET_M0(w * 256 + 16 * SP);
        CM2(e8, p, c[8], W, t2, q, W, W); PUT(17, p);
        CM2(e4, p, c[8], t2, c[0], q, e8, t2); PUT(16, p); PUT(18, q);
        CM2(c[8], p, e4, t2, e8, q, c[0], t2); PUT(19, p); PUT(20, q);
        PRIO(1);
        GATHER(2);
        u32x4 af[8];
#pragma unroll
        for (int s = 0; s < 8; ++s) af[s] = lds_r128(ap + 32 * s);
        CFENCE();
        PUT(16, V[2]); PUT(24, V[3]);
#pragma unroll
        for (int b = 1; b < 8; ++b) { HM2(p, V[2], q, V[3], U[b]); PUT(16 + b, p); PUT(24 + b, q); }
        SET_M0(w * 256);
        PRIO(0);
        GATHER(3);
        PUT(0, 0x3c00u); PUT(8, V[1]);
#pragma unroll
        for (int b = 1; b < 8; b += 2) { HM2(p, U[b], q, U[b == 7 ? 7 : b + 1], V[1]); PUT(8 + b, p); if (b < 7) PUT(9 + b, q); PUT(b, U[b]); if (b < 7) PUT(b + 1, U[b + 1]); }
#undef PUT
        u32x4 bfr[8];
#pragma unroll
        for (int s = 0; s < 8; ++s) bfr[s] = lds_r128(bp + 32 * s);
        SCHED();
        SCHED();
#pragma unroll
        for (int s = 0; s < 8; ++s)
            kacc = __builtin_amdgcn_mfma_f32_32x32x16_f16(__builtin_bit_cast(f16x8, af[s]), __builtin_bit_cast(f16x8, bfr[s]), kacc, 0, 0, 0);
    }
    STAMP(3);
    const float ks = 2.f * dt * ics;
#pragma unroll
    for (int i = 0; i < 12; ++i) kacc[i] *= ks;
    if (half) {
#pragma unroll
        for (int i = 0; i < 8; ++i) W_ADDTID(kacc[i], i * SP);
    } else {
#pragma unroll
        for (int i = 0; i < 4; ++i) W_ADDTID(kacc[8 + i], i * SP);
    }
    if (w >= 8 && li < 12) {
        const int n = (11 - w) * 12 + li, l = n / 9, c = n - l * 9;
        TOUCH(junk2, P.wo + (unsigned)((c * 64 + lane) * 1152 + (8 - l) * 128));
    }
    u32x4 ct[8];
#pragma unroll
    for (int i = 0; i < 8; ++i) ct[i] = lds_r128(smem + TT_OFF + ch * 8192 + i * 1024 + lane * 16);
    PIN_TAG();
    STAMP(4);
    BARRIER();
    STAMP(5);
    if (tid == 704) tag_post(P, tag);
    auto cleared = [&]() {
        if (w < 4) {
            WAIT_VM(0);
            if (lane == 0 && __hip_atomic_fetch_add((LAS unsigned*)(smem + FL + 32), 1u, __ATOMIC_RELAXED, __HIP_MEMORY_SCOPE_WORKGROUP) == 3u)
                __hip_atomic_store((GAS u64*)(P.ws + WS_BFL + gid * 128), tag, RLX_AGENT);
        }
    };
    auto x_in = [&]() {
        if (w < 8) {
            WAIT_VM(0); SCHED();
            asm volatile("" : "+v"(xr[0]), "+v"(xr[1]), "+v"(xr[2]), "+v"(xr[3]), "+v"(junk) :: "memory");
#pragma unroll
            for (int k = 0; k < 3; ++k) {
                u32x2 lo = {__builtin_amdgcn_perm(xr[1][k], xr[0][k], 0x05040100u), __builtin_amdgcn_perm(xr[3][k], xr[2][k], 0x05040100u)};
                u32x2 hi = {__builtin_amdgcn_perm(xr[1][k], xr[0][k], 0x07060302u), __builtin_amdgcn_perm(xr[3][k], xr[2][k], 0x07060302u)};
                lds_w64(xb + (2 * k) * XT_BYTES, lo); lds_w64(xb + (2 * k + 1) * XT_BYTES, hi);
            }
        }
        if (w < 4) {
#pragma unroll
            for (int i = 0; i < 6; ++i)
                if (i >= 2 || gi < 10) __builtin_amdgcn_raw_buffer_store_b128(u32x4{EMPTY, EMPTY, EMPTY, EMPTY}, wsr, (unsigned)(tid * 16), WS_B + (ch0 + i) * 4096, 16);
        }
        BARRIER();
        if (w >= 8) wo_issue<WOA>(P, cg2, w - 8, 4, lane);
    };
    auto TF = [&](int off) { u32x2 a = lds_r64(tf + off), b = lds_r64(tf + off + 8); return u32x4{a.x, a.y, b.x, b.y}; };
    f32x4 acc[8];
#pragma unroll
    for (int i = 0; i < 8; ++i) acc[i] = f32x4{0.f, 0.f, 0.f, 0.f};
    lptr axr;
    if (!half) {
        __builtin_amdgcn_s_setprio(2);
        lds_w64(xt + lane * 8, u32x2{0u, 0u});
#pragma unroll
        for (int i = 0; i < 8; ++i) kacc[i] += lds_rf(xc + i * SP);
        unsigned dd[6];
        dd[0] = pk_bf16(kacc[7], kacc[6]); dd[1] = pk_bf16(kacc[5], kacc[4]); dd[2] = pk_bf16(kacc[3], kacc[2]);
        dd[3] = pk_bf16(kacc[1], kacc[0] + (lane == 0 ? bf2f(r_ds) : 0.f));
        dd[4] = __builtin_amdgcn_update_dpp(0, dd[0], 0x138, 0xf, 0xf, true);
        dd[5] = __builtin_amdgcn_update_dpp(0, dd[1], 0x138, 0xf, 0xf, true);
        unsigned mm[5];
#pragma unroll
        for (int i = 0; i < 5; ++i) mm[i] = __builtin_amdgcn_alignbit(dd[i + 1], dd[i], 16);
        lptr tw = tb + 2 * (536 - 8 * lane);
        lds_w128(tw, u32x4{dd[0], dd[1], dd[2], dd[3]}); lds_w128(tw + TC, u32x4{mm[0], mm[1], mm[2], mm[3]});
        lds_w128(tw + 2 * TC, u32x4{dd[1], dd[2], dd[3], dd[4]}); lds_w128(tw + 3 * TC, u32x4{mm[1], mm[2], mm[3], mm[4]});
        {
            const unsigned n0 = __builtin_amdgcn_readlane(dd[0], 63), n1 = __builtin_amdgcn_readlane(dd[1], 63);
            const unsigned kk = (n0 & 0xffffu) * 0x10001u, kh = n0 << 16, m4 = (n0 >> 16) | (n1 << 16);
            const int c = lane & 3, bl = lane >> 2;
            u32x4 e;
            e.x = 0u; e.y = lane == 14 ? kh : (lane == 15 ? kk : 0u); e.z = lane == 12 ? kh : (bl == 3 ? kk : 0u);
            e.w = lane == 14 ? n0 : (lane == 15 ? m4 : (bl == 3 ? kk : 0u));
            if (bl < 6) lds_w128(tb + c * TC + bl * 16 + (bl >> 2) * 1024, e);
        }
        x_in();
        u32x4 X[8];
#pragma unroll
        for (int i = 0; i < 8; ++i) X[i] = lds_r128(xf7 + 64 * 6 + 512 * i);
        u32x4 k0 = TF(1024 - 64), k8 = TF(512 - 64);
#pragma unroll
        for (int i = 0; i < 8; ++i) asm volatile("" : "+v"(acc[i]));
        __builtin_amdgcn_s_setprio(0);
        STAMP(6);
#pragma unroll
        for (int c = 1; c < 8; ++c) {
            u32x4 k0n, k8n;
            if (c < 7) { k0n = TF(1024 - 64 * (c + 1)); k8n = TF(512 - 64 * (c + 1)); }
#pragma unroll
            for (int i = 0; i < 8; ++i) {
                SCHED();
                mfma16i(acc[i], k0, X[i]);
                if (i < 7) mfma16i(acc[i + 1], k8, X[i]);
                SCHED();
                if (c < 7) X[i] = lds_r128(xf7 + 64 * (6 - c) + 512 * i);
            }
            k0 = k0n; k8 = k8n;
            if (c == 5) { SCHED(); cleared(); SCHED(); }
        }
        MFMA_DONE();
        if (w < 4) wo_issue<0x1ff & ~WOA>(P, cg2, w >> 1, 2, lane);
        flag_wait(smem, 36 + ch * 4, 1);
#pragma unroll
        for (int i = 0; i < 4; ++i) lds_w128(xt + i * 1024 + lane * 16, __builtin_bit_cast(u32x4, acc[4 + i]));
        axr = pt + lane * 16;
    } else {
        u32x4 X[8];
#pragma unroll
        for (int i = 0; i < 4; ++i) if (i < 3 || lane < 48) lds_w128(pt + i * 1024 + lane * 16, u32x4{0u, 0u, 0u, 0u});
        CFENCE();
        const unsigned p89 = pk_bf16(kacc[8] + lds_rf(xc), kacc[9] + lds_rf(xc + SP)), pab = pk_bf16(kacc[10] + lds_rf(xc + 2 * SP), kacc[11] + lds_rf(xc + 3 * SP));
#pragma unroll
        for (int c = 0; c < 4; ++c) {
            lptr q2 = smem + (ct[6][c] + ch * 4096), q3 = smem + (ct[7][c] + ch * 4096);
            lds_w16(q2 + 64, p89); lds_w16(q2, p89 >> 16);
            lds_w16(q3 + 64, pab); lds_w16(q3, pab >> 16);
        }
        CFENCE();
        const u32x4 FX = ct[5] & (((unsigned)__builtin_amdgcn_readlane(p89, 63) >> 16) * 0x10001u);
        lptr pf2 = pt + cpy * 320 + ((79 - 64 - lo16 + 4 * g - cpy + 4) >> 2) * 16;
        x_in();
#pragma unroll
        for (int i = 0; i < 8; ++i) X[i] = lds_r128(xf7 + 64 * 7 + 512 * i);
        u32x4 TA[3], TB[3], TCc[4];
#pragma unroll
        for (int i = 0; i < 3; ++i) { TA[i] = lds_r128(pf2 + 64 * (2 - i)); TB[i] = lds_r128(pf2 + 1280 + 64 * (2 - i)); }
#pragma unroll
        for (int i = 0; i < 4; ++i) TCc[i] = lds_r128(pf2 + PC_OFF + 64 * (3 - i));
        STAMP(6);
        const f32x4 z4 = {0.f, 0.f, 0.f, 0.f};
#pragma unroll
        for (int st = 0; st < 6; ++st) {
            const u32x4 Xc = lds_r128(xf7 + 64 * 7 + 512 * st - 16);
            const u32x4 pa = pack8(mfma16(X[st], ct[0], z4), mfma16(X[st], ct[1], z4));
            const u32x4 pb = pack8(mfma16(X[st], ct[2], z4), mfma16(Xc, ct[1], z4));
#pragma unroll
            for (int dl = 2; dl <= 4; ++dl) if (st + dl < 8) {
                acc[st + dl] = mfma16(pa, TA[dl - 2], acc[st + dl]);
                acc[st + dl] = mfma16(pb, TB[dl - 2], acc[st + dl]);
            }
            if (st < 4) {
                const u32x4 pc = pack8(mfma16(X[st], ct[3], z4), mfma16(Xc, ct[4], z4));
#pragma unroll
                for (int dl = 4; dl <= 7; ++dl) if (st + dl < 8) acc[st + dl] = mfma16(pc, TCc[dl - 4], acc[st + dl]);
            }
        }
        SCHED(); lds_w32(smem + FL + 36 + ch * 4, 1u); cleared(); SCHED();
#pragma unroll
        for (int i = 4; i < 8; ++i) acc[i] = mfma16(FX, X[i - 4], acc[i]);
        const u32x4 k0 = TF(1024), k8 = TF(512), k16 = TF(0);
#pragma unroll
        for (int i = 0; i < 8; ++i) acc[i] = mfma16(k0, X[i], acc[i]);
#pragma unroll
        for (int i = 1; i < 8; ++i) acc[i] = mfma16(k8, X[i - 1], acc[i]);
#pragma unroll
        for (int i = 2; i < 8; ++i) acc[i] = mfma16(k16, X[i - 2], acc[i]);
        MFMA_DONE();
#pragma unroll
        for (int i = 0; i < 4; ++i) { lds_w128(pt + i * 1024 + lane * 16, __builtin_bit_cast(u32x4, acc[i])); acc[i] = acc[4 + i]; }
        axr = xt + lane * 16;
    }
    STAMP(7);
    if (w == 0 && lane < 8) lds_w32(smem + FL + lane * 4, 0u);
    WAIT_VM(0); SCHED();
    asm volatile("" :: "v"(junk2));
    BARRIER();
    STAMP(8);
#pragma unroll
    for (int i = 0; i < 4; ++i) acc[i] += __builtin_bit_cast(f32x4, lds_r128(axr + i * 1024));
#pragma unroll
    for (int i = 0; i < 2; ++i) {
        const f32x4 ya = acc[2 * i], yb = acc[2 * i + 1];
        const auto sx = __builtin_amdgcn_permlane16_swap(pk_bf16(ya[0], ya[1]), pk_bf16(yb[0], yb[1]), false, false);
        const auto sy = __builtin_amdgcn_permlane16_swap(pk_bf16(ya[2], ya[3]), pk_bf16(yb[2], yb[3]), false, false);
        st_sc1(wsr, boff + i * 1024, not_empty(u32x4{sx[0], sy[0], sx[1], sy[1]}));
    }
    BARRIER();
    STAMP(9);
    if (w < 2) STAMP_OUT(2 + 2 * w, blockIdx.x + 256 * w);
}

constexpr int SLOT = 34816, XCH = 16384, KVT_OFF = 151296, KVP_OFF = 27648;
DEVI constexpr int slot_off(int s) { return (s & 3) == 0 ? SLOT : ((s & 3) == 1 ? 0 : (s & 3) * SLOT); }
DEVI constexpr int wq_off(int t) { return t == 0 ? 0 : 49152 + t * 27648; }

constexpr int quad_src(int quad) {
    const int pp = quad >= 192 ? 1 : 0, qi = quad - 192 * pp;
    const bool isz = qi >= 180;
    const int el = isz ? 0 : qi / 5, qd = qi - el * 5, hi = el >= 18 ? 1 : 0;
    const int dql = isz ? (qi < 189 ? qi - 180 : 0) : qd + 4 * hi;
    const int et = isz ? 0 : (el < 4 ? 0 : (el < 20 ? 1 : 2)), col = isz ? 0 : (el < 4 ? 12 + el : (el < 20 ? el - 4 : el - 20));
    const int tile = dql == 8 ? 5 : (et == 0 ? (dql >> 2) : (et == 1 ? 2 + (dql >> 2) : 4));
    return KVP_OFF + pp * 6144 + tile * 1024 + ((dql == 8 ? et : (dql & 3)) * 16 + col) * 4;
}
constexpr int quad_dst(int quad, int hf) {
    const int pp = quad >= 192 ? 1 : 0, qi = quad - 192 * pp;
    const bool isz = qi >= 180;
    const int el = isz ? 0 : qi / 5, qd = qi - el * 5, hi = el >= 18 ? 1 : 0;
    const int dql = isz ? (qi < 189 ? qi - 180 : 0) : qd + 4 * hi;
    const int dq = 9 * pp + dql, ks = dq >> 3, e = 36 * pp + el, ee = e >> 4;
    const int fr = ee == 0 ? 0 : (ee < 3 ? 2 * ee - 1 + ks : 2 * ee - 2 + ks);
    const int hz = 2 * pp + ((dql > 4 || (dql == 4 && hf)) ? 1 : 0);
    const bool dead = isz ? qi >= 189 : ((!hi && qd == 4 && hf) || (hi && qd == 0 && !hf));
    return dead ? LDS_DUMMY : KVT_OFF + (isz ? (9 + ks) * 1024 + (dq & 3) * 256 + hz * 16 : fr * 1024 + ((dq & 3) * 16 + (e & 15)) * 16) + ((dq >> 2) & 1) * 8 + hf * 4;
}
struct Tab { int v[2048]; };
constexpr Tab make_tab() {
    Tab t{};
    for (int c = 0; c < 192; ++c) {
        const int l = c & 63, q = 2 * ((c & 192) + (l >> 5) * 16 + (l & 15)) + ((l >> 4) & 1);
        t.v[4 * c] = quad_src(2 * c); t.v[4 * c + 1] = quad_src(2 * c + 1);
        t.v[4 * c + 2] = (quad_dst(q, 0) - KVT_OFF) | ((quad_dst(q, 1) - KVT_OFF) << 16); t.v[4 * c + 3] = (quad_dst(q + 64, 0) - KVT_OFF) | ((quad_dst(q + 64, 1) - KVT_OFF) << 16);
    }
    for (int q = 0; q < 576; ++q) {
        const int rho = q / 9, pc = q % 9, k = (rho & 32) + 8 * ((rho >> 3) & 3) + 4 * (rho & 1) + ((rho >> 1) & 3), m = q >> 6;
        t.v[768 + q] = k * 3456 + pc * 16 - (m & 1) * 1024;
        t.v[1344 + q] = k * 1152 + pc * 16 - (m & 3) * 1024;
    }
    return t;
}
__device__ const Tab g_tab = make_tab();
constexpr int IDX = P2_B, GW_OFF = 139264;

#define GET_TAG() do { const u32x2 t_ = lds_r64(smem + LDS_TAG); tag = (u64)(unsigned)__builtin_amdgcn_readfirstlane(t_.x) | ((u64)(unsigned)__builtin_amdgcn_readfirstlane(t_.y) << 32); } while (0)
DEVI void gemm_kv(const Params& P, lptr smem, int rb, int hg, int kw, int cg2, u64& tag, rsrc_t wsr) {
    const int lane = threadIdx.x & 63, lo16 = lane & 15, g = lane >> 4;
    const int p = kw & 1, ct = (kw < 3 ? kw : 0) * 64 + lane;
    STAMP_DECL; STAMP(1);
    __builtin_amdgcn_s_setprio(2);
    const int xrow = ((kw >> 1) * 64 + lo16) * 128, sw = (g ^ ((lo16 >> 1) & 7)) << 4;
    lptr xa[2] = {smem + xrow + sw, smem + xrow + (sw ^ 64)};
    const int wrow = (4 * g + (lo16 >> 2)) * 288;
    lptr wa = smem + XCH + wrow + (9 * p + (lane & 3)) * 8, wa2 = smem + XCH + wrow + (9 * p + ((lane & 3) == 3 ? 18 : 8)) * 8;
    lptr flp = smem + FL, ixp = smem + IDX + ct * 16, irp = smem + IDX + 3072 + 2304 + lane * 4;
    unsigned one = 1u;
    u32x4 sel[2];
    sel[0] = mask_frag(lo16 < 8 ? 1u << lo16 : 0u, 0, 0x3f80u); sel[1] = mask_frag(lo16 >= 8 ? 1u << (lo16 - 8) : 0u, 0, 0x3f80u);
    unsigned sto = WS_PART + (unsigned)((hg * 16 + rb) * 3072 + ct * 16);
    unsigned off = WS_PART + (unsigned)((hg * 16 + g) * 3072 + kw * 1024 + lo16 * 16);
    asm volatile("" : "+v"(flp), "+v"(one), "+v"(ixp), "+v"(irp), "+v"(sel[0]), "+v"(sel[1]), "+v"(sto), "+v"(off));
    const f32x4 z4 = {0.f, 0.f, 0.f, 0.f};
    f32x4 acc[4][5];
#pragma unroll
    for (int i = 0; i < 4; ++i)
#pragma unroll
        for (int j = 0; j < 5; ++j) { const float e = j < 2 ? 1e-6f : (j == 2 && lo16 < 12 ? 1e-6f : 0.f); acc[i][j] = f32x4{e, e, e, e}; asm volatile("" : "+v"(acc[i][j])); }
    auto rel = [&](int s) { return (s & 2) ? slot_off(s) - 2 * SLOT : slot_off(s); };
    auto rebase = [&](int dlt) {
        xa[0] += dlt; xa[1] += dlt; wa += dlt; wa2 += dlt;
        asm volatile("" : "+v"(xa[0]), "+v"(xa[1]), "+v"(wa), "+v"(wa2));
    };
    constexpr int NB = 6;
    u32x2 wl[NB], wh[NB];
    u32x4 xf[2][4];
    auto rdw = [&](int f, int t) {
        const int n = f / 5, j = f % 5;
        const int o = rel(n >> 1) + (n & 1) * 9216 + t * 4608;
        const u32x2 v = j == 2 ? lds_tr(wa2 + o) : lds_tr(wa + o + (j == 0 ? 0 : (j == 1 ? 32 : (j == 3 ? 152 : 184))));
        if (t) wh[f % NB] = v; else wl[f % NB] = v;
    };
    auto rdx = [&](int n, int r) { xf[n & 1][r] = lds_r128(xa[n & 1] + rel(n >> 1) + r * 2048); };
    rebase(0);
    BARRIER();
    STAMP(2);
    rdw(0, 0); rdw(0, 1);
#pragma unroll
    for (int r = 0; r < 4; ++r) rdx(0, r);
#pragma unroll
    for (int f = 1; f < NB - 1; ++f) { rdw(f, 0); rdw(f, 1); }
    auto kstep = [&](const int n) __attribute__((always_inline)) {
        const int s = n >> 1;
#pragma unroll
        for (int m = 0; m < 20; ++m) {
            const int j = m >> 2, r = m & 3, f = 5 * n + j, fn = f + NB - 1;
            SCHED();
            const u32x4 wv = {wl[f % NB].x, wl[f % NB].y, wh[f % NB].x, wh[f % NB].y};
            mfma16i(acc[r][j], xf[n & 1][r], wv);
            SCHED();
            if (r < 2 && fn < 90) rdw(fn, r);
            if (r == 2 && j < 4 && n < 17) rdx(n + 1, j);
            if (m == 15 && n < 17) WAIT_LGKM(12);
            if (m == 18 && !(n & 1)) {
                if (s < 8 && (s & 1)) rebase((s & 2) ? -2 * SLOT : 2 * SLOT);
                if (s == 8) WAIT_LGKM0();
                RAW_BARRIER();
                if (s == 0) STAMP(3);
                if (s == 4) STAMP(4);
            }
            if (m == 19 && n < 17) WAIT_LGKM(2);
        }
    };
    kstep(0); kstep(1); kstep(2); kstep(3); kstep(4); kstep(5); kstep(6); kstep(7); kstep(8);
    kstep(9); kstep(10); kstep(11); kstep(12); kstep(13); kstep(14); kstep(15); kstep(16); kstep(17);
    __builtin_amdgcn_s_setprio(3);
    const u32x4 ix = lds_r128(ixp);
    unsigned oo[9];
#pragma unroll
    for (int i = 0; i < 9; ++i) oo[i] = *(LAS unsigned*)(irp + i * 256);
    MFMA_DONE();
    STAMP(5);
    {
        f32x4 r[6], t0 = z4, t1 = z4, t2 = z4;
#pragma unroll
        for (int i = 0; i < 5; ++i) r[i] = z4;
#pragma unroll
        for (int u = 0; u < 2; ++u) {
            u32x4 pk[3], vb[3];
#pragma unroll
            for (int j = 0; j < 3; ++j) pk[j] = pack8(phi4(acc[2 * u][j]), phi4(acc[2 * u + 1][j]));
            vb[0] = pack8(acc[2 * u][2], acc[2 * u + 1][2]);
            if (lo16 < 12) vb[0] = u32x4{0x3f803f80u, 0x3f803f80u, 0x3f803f80u, 0x3f803f80u};
            vb[1] = pack8(acc[2 * u][3], acc[2 * u + 1][3]); vb[2] = pack8(acc[2 * u][4], acc[2 * u + 1][4]);
            t0 = mfma16(pk[2], vb[0], t0); t1 = mfma16(pk[2], vb[1], t1); t2 = mfma16(pk[2], vb[2], t2);
            r[0] = mfma16(pk[0], vb[0], r[0]); r[1] = mfma16(pk[1], vb[0], r[1]); r[2] = mfma16(pk[0], vb[1], r[2]);
            r[3] = mfma16(pk[1], vb[1], r[3]); r[4] = mfma16(pk[1], vb[2], r[4]);
        }
        r[5] = g == 0 ? t0 : (g == 1 ? t1 : t2);
        MFMA_DONE();
        SET_M0(KVP_OFF + kw * 6144);
#pragma unroll
        for (int t = 5; t >= 0; --t) {
            W_ADDTID(r[t][0], t * 1024); W_ADDTID(r[t][1], t * 1024 + 256);
            W_ADDTID(r[t][2], t * 1024 + 512); W_ADDTID(r[t][3], t * 1024 + 768);
        }
        FLAG_ADD(F_KVP);
    }
    STAMP(6);
    if (kw < 3) {
        lptr dst[4] = {smem + KVT_OFF + (ix.z & 0xffffu), smem + KVT_OFF + (ix.z >> 16), smem + KVT_OFF + (ix.w & 0xffffu), smem + KVT_OFF + (ix.w >> 16)};
        asm volatile("" : "+v"(dst[0]), "+v"(dst[1]), "+v"(dst[2]), "+v"(dst[3]));
        flag_wait<0>(smem, F_KVP, 4);
        STAMP(7);
        float pa[2][2][4];
#pragma unroll
        for (int i = 0; i < 2; ++i)
#pragma unroll
            for (int r = 0; r < 2; ++r)
#pragma unroll
                for (int k = 0; k < 4; ++k) pa[i][r][k] = lds_rf(smem + ix[i] + r * 12288 + k * 256);
        SCHED();
        u32x4 pv;
#pragma unroll
        for (int i = 0; i < 2; ++i)
#pragma unroll
            for (int k = 0; k < 2; ++k)
                pv[2 * i + k] = pk_bf16(pa[i][0][2 * k] + pa[i][1][2 * k], pa[i][0][2 * k + 1] + pa[i][1][2 * k + 1]);
        st_sc1(wsr, sto, not_empty(pv));
        SCHED();
        FLAG_ADD(F_KVR);
        STAMP(8);
        WAIT_VM(0);
        STAMP(9);
        f32x4 gs[4][2];
        auto group = [&](int i, const u32x4 (&a)[4]) {
            gs[i][0] = mfma16(sel[1], a[1], mfma16(sel[0], a[0], z4));
            gs[i][1] = mfma16(sel[1], a[3], mfma16(sel[0], a[2], z4));
        };
        {
            u32x4 a[4][4];
#pragma unroll
            for (int i = 0; i < 4; ++i)
#pragma unroll
                for (int t = 0; t < 4; ++t) a[i][t] = __builtin_amdgcn_raw_buffer_load_b128(wsr, off + t * 256, ((i + rb) & 3) * 12288, 16);
            SCHED();
            FLAG_ADD(F_SW);
#pragma unroll
            for (int i = 0; i < 4; ++i) group(i, a[i]);
        }
#pragma unroll
        for (int i = 0; i < 4; ++i) {
#pragma unroll 1
            for (unsigned spins = 0; spins < SPIN_CAP; ++spins) {
                if (!__any(__builtin_isunordered(gs[i][0][0], gs[i][1][0]))) break;
                if (spins) __builtin_amdgcn_s_sleep(RSLEEP);
                CFENCE();
                u32x4 a[4];
#pragma unroll
                for (int t = 0; t < 4; ++t) a[t] = __builtin_amdgcn_raw_buffer_load_b128(wsr, off + t * 256, ((i + rb) & 3) * 12288, 16);
                group(i, a);
                if (spins >= 32 && __all(max(max(umax4(a[0]), umax4(a[1])), max(umax4(a[2]), umax4(a[3]))) != EMPTY)) break;
            }
        }
        f32x4 sum[2];
        sum[0] = (gs[0][0] + gs[1][0]) + (gs[2][0] + gs[3][0]); sum[1] = (gs[0][1] + gs[1][1]) + (gs[2][1] + gs[3][1]);
#pragma unroll
        for (int i = 0; i < 4; ++i) lds_w32(dst[i], pk_bf16(sum[i >> 1][2 * (i & 1)], sum[i >> 1][2 * (i & 1) + 1]));
        CFENCE();
        FLAG_ADD(F_D);
        STAMP(10);
    } else {
        auto wo_chunks = [&](int c0, int c1) {
#pragma unroll 1
            for (int c = c0; c < c1; ++c) {
                const char* ws = P.wo + cg2 * 144 + c * (64 * 1152);
                const unsigned l = c < 8 ? c * 9216 : WO8;
                SET_M0(l); glds16o<0>(ws, oo[0]); glds16o<1024>(ws, oo[1]); glds16o<2048>(ws, oo[2]); glds16o<3072>(ws, oo[3]);
                SET_M0(l + 4096); glds16o<0>(ws, oo[4]); glds16o<1024>(ws, oo[5]); glds16o<2048>(ws, oo[6]); glds16o<3072>(ws, oo[7]);
                SET_M0(l + 8192); glds16o<0>(ws, oo[8]);
            }
        };
        flag_wait(smem, F_KVR, 3);
        wo_chunks(3, 9);
        WAIT_VM(0);
        FLAG_ADD(F_D);
        flag_wait(smem, F_SW, 3);
        flag_wait(smem, F_Q0, 8);
        wo_chunks(0, 3);
        WAIT_VM(0);
        FLAG_ADD(F_W0);
    }
    GET_TAG();
    __builtin_amdgcn_s_setprio(0);
    if (kw == 0) STAMP_OUT(1, blockIdx.x);
}

DEVI void gemm_q(const Params& P, lptr smem, int rb, int hg, int t8, u64& tag, rsrc_t wsr) {
    const int lane = threadIdx.x & 63, lw = t8 & 3;
    const bool ld = t8 < 4;
    STAMP_DECL; STAMP(1);
    __builtin_amdgcn_s_setprio(3);
    unsigned xo[4], wo_[5], qo[3];
    const int rx = hg & 3, rw = rb & 3;
    unsigned mx[4], mw[4];
    const unsigned xbase = (unsigned)((rb * 128 + 8 * lw + (lane >> 3)) * 1152 + (((lane & 7) ^ ((lane >> 4) + 4 * (lw & 1))) << 4));
#pragma unroll
    for (int i = 0; i < 4; ++i) { mx[i] = (((i + rx) & 3) * 4 + lw) * 1024; mw[i] = XCH + (((i + rw) & 3) * 4 + lw) * 1024; xo[i] = xbase + ((i + rx) & 3) * 36864; }
    const unsigned lb = lw * 4096;
    auto issue_x = [&](int s, int i0 = 0, int i1 = 4) {
#pragma unroll
        for (int i = i0; i < i1; ++i) glds16(P.x + s * 128, xo[i], slot_off(s) + mx[i]);
    };
    auto issue_w = [&](int s, int i0 = 0, int i1 = 4) {
        const char* ws = P.wqkv + s * (64 * 3456);
        if (i1 == 4 && (lw >> 1) == (s & 1)) glds16(ws, wo_[4], (unsigned)(slot_off(s) + XCH + 16384 + (lw & 1) * 1024));
#pragma unroll
        for (int i = i0; i < i1; ++i) glds16(ws, wo_[i], slot_off(s) + mw[i]);
    };
    auto issue_q = [&](int t, int i) {
        const char* ws = P.wqkv + hg * 144 + (3 * t + i) * (64 * 3456);
        if (lw == ((i + t) & 3)) glds16(ws, qo[2], (unsigned)(wq_off(t) + i * 9216 + 8192));
        SET_M0(wq_off(t) + i * 9216 + (lb >> 1));
        glds16o<0>(ws, qo[0]); glds16o<1024>(ws, qo[1]);
    };
    auto w_off = [&](int i) {
        const unsigned q = 64 * (i < 4 ? ((i + rw) & 3) * 4 + lw : 16 + (lw & 1)) + lane, rho = (q * 3641u) >> 16, pc = q - rho * 18;
        const unsigned k = (rho & 35u) + 8 * ((rho >> 2) & 3) + 4 * ((rho >> 4) & 1);
        wo_[i] = k * 3456 + (1152 + hg * 144) + pc * 16 + (pc < 9 ? 0u : 1008u);
    };
    if (ld) {
        issue_x(0, 0, 1); issue_x(1, 0, 1);
        SCHED();
        w_off(0);
        issue_w(0, 0, 1); issue_w(1, 0, 1);
        SCHED();
#pragma unroll
        for (int i = 1; i < 5; ++i) w_off(i);
        if (lw == 1) { glds16(P.gw, (unsigned)(lane * 16), GW_OFF); if (lane < 8) glds16(P.gw + 1024, (unsigned)(lane * 16), GW_OFF + 1024); }
        issue_x(0, 1, 4); issue_w(0, 1, 4); issue_w(1, 1, 4); issue_x(1, 1, 4);
        SET_M0(IDX + (lb >> 1));
        glds16o<0>((const char*)&g_tab + (lb >> 1), (unsigned)(lane * 16)); glds16o<1024>((const char*)&g_tab + (lb >> 1), (unsigned)(lane * 16));
    } else {
        if (lw == 0 && lane < 16) lds_w32(smem + FL + lane * 4, 0u);
#pragma unroll
        for (int i = 0; i < 3; ++i) lds_w128(smem + KVT_OFF + i * 4096 + lw * 1024 + lane * 16, u32x4{0u, 0u, 0u, 0u});
    }
    const int lo16 = lane & 15, g = lane >> 4;
    const int sw = (g ^ ((lo16 >> 1) & 7)) << 4;
    lptr xa[2] = {smem + (16 * t8 + lo16) * 128 + sw, smem + (16 * t8 + lo16) * 128 + (sw ^ 64)}, xb[2] = {xa[0] + 2 * SLOT, xa[1] + 2 * SLOT};
    lptr flp = smem + FL, iqp = smem + IDX + 3072 + lane * 4, gwp = smem + GW_OFF + g * 16;
    const int qrow = (2 * (4 * g + (lo16 >> 2))) * 144;
    lptr qw = smem + qrow + (lane & 3) * 8, qw4 = smem + qrow + (16 + (lane & 1)) * 8 + (lane & 2) * 2304;
    lptr qwh = qw + wq_off(1), qw4h = qw4 + wq_off(1), kfp = smem + KVT_OFF + lane * 16, kzp = smem + KVT_OFF + 9216 + (lane & 51) * 16;
    unsigned one = 1u, ob0 = WS_O + (unsigned)(((rb * 8 + t8) * 72 + hg * 9 + 2 * (g & 1) + (g >> 1)) * 256 + lo16 * 16);
    asm volatile("" : "+v"(flp), "+v"(one), "+v"(iqp), "+v"(gwp), "+v"(xa[0]), "+v"(xa[1]), "+v"(xb[0]), "+v"(xb[1]));
    asm volatile("" : "+v"(qw), "+v"(qw4), "+v"(qwh), "+v"(qw4h), "+v"(kfp), "+v"(kzp), "+v"(ob0));
    asm volatile(".subsection 2\n.Lkf_code_end:\n\t.fill 256, 4, 0xbf800000\n\t.subsection 0");
    const f32x4 z4 = {0.f, 0.f, 0.f, 0.f};
    f32x4 gacc = z4, qa[6];
#pragma unroll
    for (int j = 0; j < 6; ++j) { const float e = j < 5 ? 1e-6f : 0.f; qa[j] = f32x4{e, e, e, e}; asm volatile("" : "+v"(qa[j])); }
    u32x4 xk[18];
    u64 pf = 0;
    WAIT_VM(8); BARRIER();
    if (ld) { issue_w(2); issue_x(2); if (LA3) { issue_w(3); issue_x(3); } }
#pragma unroll
    for (int s = 0; s < 9; ++s) {
#pragma unroll
        for (int k = 0; k < 2; ++k) xk[2 * s + k] = lds_r128(((s & 2) ? xb[k] - 2 * SLOT : xa[k]) + slot_off(s));
        if (t8 == hg) {
            gacc = mfma16(lds_r128(gwp + s * 128), xk[2 * s], gacc);
            gacc = mfma16(lds_r128(gwp + s * 128 + 64), xk[2 * s + 1], gacc);
        }
        if (t8 == 4) {
            if (s == 1) { tag = *(const __attribute__((address_space(4))) u64*)(P.ws + WS_CTL); PIN_TAG(); if (lane == 0) lds_w64(smem + LDS_TAG, u32x2{(unsigned)tag, (unsigned)(tag >> 32)}); }
            if (s == 4) asm volatile("global_load_dwordx2 %0, %1, %2 sc1" : "=v"(pf) : "v"((lane < 5 ? lane : 0) * 128), "s"(P.ws + WS_PFL) : "memory");
            if (s == 7) {
                asm volatile("s_waitcnt vmcnt(0)" : "+v"(pf) :: "memory");
                if (!__all(pf == tag)) poll_flags(P, WS_PFL, 5, tag, lane);
            }
        }
        if (LA3) { if (s < 6) WAIT_VM(17); else if (s == 6) WAIT_VM(14); else WAIT_VM(12); }
        else if (s < 7) WAIT_VM(8); else WAIT_VM(6);
        BARRIER();
        if (ld) {
            const int sn = s + 3 + LA3;
            if (sn < 9) { issue_w(sn); issue_x(sn); } else if (sn < 12) { issue_q(sn - 9, 0); issue_q(sn - 9, 1); issue_q(sn - 9, 2); }
            if (s == 3) { qo[0] = *(LAS unsigned*)(iqp + lw * 512); qo[1] = *(LAS unsigned*)(iqp + lw * 512 + 256); qo[2] = *(LAS unsigned*)(iqp + 2048); CFENCE(); }
        }
    }
    __builtin_amdgcn_s_setprio(0);
    STAMP(5);
    if (!ld) {
        const unsigned co = WS_O + (unsigned)(((rb * 8 + 2 * lw) * 72 + hg * 9) * 256 + lane * 16);
#pragma unroll
        for (int i = 0; i < 5; ++i)
            if (i < 4 || lane < 32) st_sc1(wsr, i < 4 ? co + (i >> 1) * 18432 + (i & 1) * 1024 : co + 2048 + (lane >> 4) * (18432 - 256), u32x4{EMPTY, EMPTY, EMPTY, EMPTY});
    }
    GET_TAG();
    if (t8 == 4 && lane == 0) tag_post(P, tag);
    if (t8 == hg) {
        const float gate = rbf(__builtin_amdgcn_rcpf(1.f + __expf(-rbf(gacc[0]))));
        if (lane < 16) __hip_atomic_store((GAS float*)(P.ws + WS_GATE) + (rb * 128 + hg * 16 + lane), gate, RLX_AGENT);
    }
    auto peek = [&](int f) { return __hip_atomic_load((LAS unsigned*)(flp + f), __ATOMIC_RELAXED, __HIP_MEMORY_SCOPE_WORKGROUP); };
    auto rdq = [&](int ks, int j) {
        const int o = (ks < 6 ? ks : ks - 6) * 4608;
        return j < 4 ? lds_trfrag((ks < 6 ? qw : qwh) + o + 32 * j, 144) : lds_trfrag((ks < 6 ? qw4 : qw4h) + o, 144);
    };
    u32x4 wq[2][4], w4[2];
#pragma unroll
    for (int i = 0; i < 2; ++i) {
#pragma unroll
        for (int j = 0; j < 4; ++j) wq[i][j] = rdq(i, j);
        if (i == 0) w4[0] = rdq(0, 4);
    }
    unsigned seenq = 0;
#pragma unroll
    for (int ks = 0; ks < 18; ++ks) {
        if (ks == 4) {
            SCHED();
            WAIT_VM(0); FLAG_ADD(F_Q0);
            seenq = peek(F_Q0); NEED(seenq, F_Q0, 8);
        }
        if (ks == QH) { SCHED(); seenq = peek(F_KVR); NEED(seenq, F_KVR, 3); STAMP(6); }
        if (ks == 16) { SCHED(); FLAG_ADD(F_QD); }
#pragma unroll
        for (int j = 0; j < 5; ++j) {
            SCHED();
            if (j == 0 && ks < 17) { if (ks & 1) WAIT_LGKM(10); else WAIT_LGKM(8); }
            if (j < 4) mfma16i(qa[j], wq[ks & 1][j], xk[ks]); else mfma16i(qa[4 + (ks & 1)], w4[(ks >> 1) & 1], xk[ks]);
            SCHED();
            if (ks + 2 < 18) { if (j < 4) wq[ks & 1][j] = rdq(ks + 2, j); else if (!(ks & 1)) w4[((ks >> 1) + 1) & 1] = rdq(ks + 2, 4); }
        }
    }
    __builtin_amdgcn_s_setprio(2);
    MFMA_DONE();
#pragma unroll
    for (int i = 0; i < 4; ++i) {
        const auto sw2 = __builtin_amdgcn_permlane32_swap(__float_as_uint(qa[5][i]), __float_as_uint(qa[5][i]), false, false);
        qa[4][i] += __uint_as_float(sw2[1]);
    }
    u32x4 b0 = pack8(phi4(qa[0]), phi4(qa[1])), b1 = pack8(phi4(qa[2]), phi4(qa[3])), b2 = pack8(phi4(qa[4]), z4);
    asm volatile("" : "+v"(b0), "+v"(b1), "+v"(b2));
    STAMP(7);
    flag_wait<1>(smem, F_D, 4);
    STAMP(8);
    {
        u32x4 kf[12];
#pragma unroll
        for (int i = 0; i < 12; ++i) { kf[i] = lds_r128(i < 3 ? kzp + i * 1024 : kfp + (i - 3) * 1024); SCHED(); }
        SCHED();
        const f32x4 den = mfma16(kf[2], b2, mfma16(kf[1], b1, mfma16(kf[0], b0, f32x4{1e-6f, 1e-6f, 1e-6f, 1e-6f})));
        f32x4 o[5];
        o[0] = mfma16(kf[3], b0, z4);
        o[1] = mfma16(kf[5], b1, mfma16(kf[4], b0, z4));
        o[2] = mfma16(kf[7], b1, mfma16(kf[6], b0, z4));
        o[3] = mfma16(kf[9], b2, mfma16(kf[8], b1, z4));
        o[4] = mfma16(kf[11], b2, mfma16(kf[10], b1, z4));
        f32x4 r;
#pragma unroll
        for (int i = 0; i < 4; ++i) r[i] = __builtin_amdgcn_rcpf(den[i]);
        const float r1a = g == 0 ? r[0] : r[1], r2 = g == 0 ? r[1] : r[2], r3a = g <= 1 ? r[2] : r[3], r3b = g == 0 ? r[2] : r[3];
        u32x2 ob[5];
        ob[0] = u32x2{pk_bf16(o[0][0] * r[0], o[0][1] * r[0]), pk_bf16(o[0][2] * r[0], o[0][3] * r[0])};
        ob[1] = u32x2{pk_bf16(o[1][0] * r1a, o[1][1] * r1a), pk_bf16(o[1][2] * r[1], o[1][3] * r[1])};
        ob[2] = u32x2{pk_bf16(o[2][0] * r2, o[2][1] * r2), pk_bf16(o[2][2] * r2, o[2][3] * r2)};
        ob[3] = u32x2{pk_bf16(o[3][0] * r3a, o[3][1] * r3a), pk_bf16(o[3][2] * r3b, o[3][3] * r3b)};
        ob[4] = u32x2{pk_bf16(o[4][0] * r[3], o[4][1] * r[3]), pk_bf16(o[4][2] * r[3], o[4][3] * r[3])};
        SCHED();
        if (t8 == 7 && lane == 0) __hip_atomic_store((GAS u64*)(P.ws + WS_HINT + (rb * 8 + hg) * 128), tag, RLX_AGENT);
        SCHED();
#pragma unroll
        for (int t = 0; t < 3; ++t) {
            const u32x2 a = ob[2 * t], b = ob[t < 2 ? 2 * t + 1 : 4];
            const auto sx = __builtin_amdgcn_permlane16_swap(a.x, b.x, false, false), sy = __builtin_amdgcn_permlane16_swap(a.y, b.y, false, false);
            if (t < 2 || g == 0) st_sc1(wsr, ob0 + 1024 * t, not_empty(u32x4{sx[0], sy[0], sx[1], sy[1]}));
        }
    }
    __builtin_amdgcn_s_setprio(0);
    STAMP(9);
    if (t8 == 0) STAMP_OUT(5, blockIdx.x + 256);
    if (t8 == 7) STAMP_OUT(7, blockIdx.x + 768);
}

DEVI void phase2(const Params& P, lptr smem, int rt64, int cg, bool gemm, u64 tag, rsrc_t wsr) {
    const int lane = threadIdx.x & 63, w0 = __builtin_amdgcn_readfirstlane(threadIdx.x >> 6);
    const int w = gemm ? (w0 + 8) % 12 : w0, tid = threadIdx.x, bt = tid - 192;
    const int lo16 = lane & 15, g = lane >> 4;
    const int rt = w & 3, kt = w >> 2;
    STAMP_DECL; STAMP(1);
    constexpr int pb = P2_B;
    const int pa = gemm ? P2_ACC : 82944;
    if (gemm && w < 4) flag_wait(smem, F_W0, 1);
    STAMP(2);
    const int qrow = (2 * (4 * g + (lo16 >> 2))) * 144;
    lptr qw = smem + qrow + (lane & 3) * 8, qw4 = smem + qrow + (16 + (lane & 1)) * 8;
    int wofs[3], oofs[3];
#pragma unroll
    for (int u = 0; u < 3; ++u) {
        const int c = kt * 3 + (u + cg) % 3;
        wofs[u] = (c < 8 || !gemm) ? c * 9216 : WO8; oofs[u] = c * 2048;
    }
    auto rdo = [&](int i, int j) { return j < 4 ? lds_trfrag(qw + wofs[i >> 1] + (i & 1) * 4608 + 32 * j, 144) : lds_trfrag(qw4 + wofs[i >> 1] + (i & 1) * 4608, 144); };
    const unsigned ooff = WS_O + (unsigned)((rt64 * 4 + rt) * 18432 + lane * 16);
    auto ldo = [&](int i) { return __builtin_amdgcn_raw_buffer_load_b128(wsr, ooff + (i & 1) * 1024, oofs[i >> 1], 16); };
    u32x4 wf[6][5];
#pragma unroll
    for (int i = 0; i < 6; ++i)
#pragma unroll
        for (int j = 0; j < 5; ++j) wf[i][j] = rdo(i, j);
    const int r0 = tid < 576 ? tid / 9 : 0, c0 = tid - r0 * 9;
    unsigned boff = WS_B + (unsigned)((cg * 72 + (bt >> 3)) * 4096 + (rt64 >> 3) * 1024 + (((bt >> 2) & 1) * 32 + ((rt64 >> 2) & 1) * 16 + (rt64 & 3) * 4 + (bt & 3)) * 16);
    lptr bp = smem + pb + ((bt & 3) * 2 + ((bt >> 2) & 1)) * 1168 + (bt >> 3) * 2;
    unsigned goff = WS_GATE + (unsigned)((rt64 * 64 + r0) * 4);
    lptr ap = smem + pa + kt * (64 * P2_AP) + (rt * 16 + lo16) * P2_AP + (g & 1) * 144 + (g >> 1) * 16;
    lptr fa = smem + pa + r0 * P2_AP + c0 * 16, fb = smem + pb + (tid + (r0 >> 3)) * 16;
    unsigned fo = (unsigned)((rt64 * 64 + r0) * 1152 + cg * 144 + c0 * 16);
    asm volatile("" : "+v"(boff), "+v"(goff), "+v"(bp), "+v"(ap), "+v"(fa), "+v"(fb), "+v"(fo));
    STAMP(3);
    if (w == 3) {
        const int a0 = cg * 72, g0 = (a0 >> 6) * 11 + (a0 & 63) / 6, a1 = a0 + 71, g1 = (a1 >> 6) * 11 + (a1 & 63) / 6;
        poll_addr<OSLEEP>(P, lane < 8 ? WS_HINT + ((rt64 >> 1) * 8 + lane) * 128 : WS_BFL + min(g0 + lane - 8, g1) * 128, tag);
        if (gemm) flag_wait<1>(smem, F_QD, 8);
        flag_set(F_H, 1);
    } else flag_wait<1>(smem, F_H, 1);
    STAMP(4);
    u32x4 bv; unsigned gv;
    auto b_put = [&]() {
#pragma unroll
        for (int q = 0; q < 4; ++q) { lds_w16(bp + (2 * q) * 144, bv[q]); lds_w16(bp + (2 * q + 1) * 144, bv[q] >> 16); }
    };
    bool b_todo = false;
    if (w0 >= 3) bv = ld_sc1(wsr, boff);
    f32x4 acc[5];
    {
        u32x4 of[6];
#pragma unroll
        for (int i = 0; i < 6; ++i) of[i] = ldo(i);
        gv = __builtin_amdgcn_raw_buffer_load_b32(wsr, goff, 0, 16);
        SCHED();
        if (w0 >= 3) { if (__all(umax4(bv) != EMPTY)) b_put(); else b_todo = true; }
        SCHED();
#pragma unroll
        for (int j = 0; j < 5; ++j) {
            acc[j] = f32x4{0.f, 0.f, 0.f, 0.f};
#pragma unroll
            for (int i = 0; i < 6; ++i) {
                acc[j] = mfma16(wf[i][j], of[i], acc[j]);
                if (i == 3 && j > 0) { SCHED(); lds_w128(ap + (j - 1) * 32, __builtin_bit_cast(u32x4, acc[j - 1])); SCHED(); }
            }
        }
        if (g < 2) lds_w128(ap + 128, __builtin_bit_cast(u32x4, acc[4]));
    }
    if (!__all(acc[4][0] == acc[4][0])) {
#pragma unroll
        for (int j = 0; j < 5; ++j) acc[j] = f32x4{0.f, 0.f, 0.f, 0.f};
        u32x4 of[6];
#pragma unroll 1
        for (unsigned spins = 0; spins < SPIN_CAP; ++spins) {
            CFENCE();
            unsigned m = 0;
#pragma unroll
            for (int i = 0; i < 6; ++i) of[i] = ldo(i);
#pragma unroll
            for (int i = 0; i < 6; ++i) m = max(m, umax4(of[i]));
            if (__all(m != EMPTY)) break;
        }
#pragma unroll
        for (int i = 0; i < 6; ++i)
#pragma unroll
            for (int j = 0; j < 5; ++j) acc[j] = mfma16(rdo(i, j), of[i], acc[j]);
#pragma unroll
        for (int j = 0; j < 5; ++j) if (j < 4 || g < 2) lds_w128(ap + j * 32, __builtin_bit_cast(u32x4, acc[j]));
    }
    if (b_todo) {
#pragma unroll 1
        for (unsigned spins = 0; spins < SPIN_CAP; ++spins) {
            CFENCE();
            bv = ld_sc1(wsr, boff);
            if (__all(umax4(bv) != EMPTY)) break;
            __builtin_amdgcn_s_sleep(2);
        }
        b_put();
    }
    const float gt = __uint_as_float(gv);
    float og = rbf(1.f - gt);
    asm volatile("" : "+v"(og));
    STAMP(5);
    BARRIER();
    STAMP(6);
    if (w0 < 9) {
        lptr a = fa;
        const f32x4 s0 = __builtin_bit_cast(f32x4, lds_r128(a)) + __builtin_bit_cast(f32x4, lds_r128(a + 64 * P2_AP)) + __builtin_bit_cast(f32x4, lds_r128(a + 128 * P2_AP));
        const f32x4 s1 = __builtin_bit_cast(f32x4, lds_r128(a + 144)) + __builtin_bit_cast(f32x4, lds_r128(a + 64 * P2_AP + 144)) + __builtin_bit_cast(f32x4, lds_r128(a + 128 * P2_AP + 144));
        const u32x4 bb = lds_r128(fb);
        u32x4 o;
#pragma unroll
        for (int i = 0; i < 4; ++i) {
            const float a0 = i < 2 ? s0[2 * i] : s1[2 * i - 4], a1 = i < 2 ? s0[2 * i + 1] : s1[2 * i - 3];
            o[i] = pk_bf16(gt * a0 + og * bf2f(bb[i]), gt * a1 + og * bfhi(bb[i]));
        }
        __builtin_amdgcn_raw_buffer_store_b128(o, __builtin_amdgcn_make_buffer_rsrc(P.out, 0, SEQ * DM * 2, 0x00020000), fo, 0, OUTAUX);
    }
    STAMP(7);
    if (w == 4) STAMP_OUT(blockIdx.x < 128 ? 3 : 6, blockIdx.x + 512);
}

DEVI void helper_role(const Params& P, lptr smem, int xcd, int li, u64& tag, rsrc_t wsr) {
    const int tid = threadIdx.x, lane = tid & 63, w = __builtin_amdgcn_readfirstlane(tid >> 6);
    STAMP_DECL; STAMP(1);
    if (li >= 13) {
        if (HD) __builtin_amdgcn_s_sleep(HD);
#pragma unroll 1
        for (int n = 12 * NTW + (li - 13) + 3 * w; n < 171; n += 36) touch_group(P, xcd, n, lane);
    }
    if (li == 12) {
#pragma unroll
        for (int i = 0; i < 7; ++i) {
            const int u = (i * 5 + xcd - 3) * 12 + w;
            if (u < 384) st_sc1(wsr, WS_PART + (unsigned)(u * 1024 + lane * 16), u32x4{EMPTY, EMPTY, EMPTY, EMPTY});
        }
        WAIT_VM(0);
    }
    PIN_TAG();
    BARRIER();
    if (tid == 0) {
        if (li == 12) __hip_atomic_store((GAS u64*)(P.ws + WS_PFL + (xcd - 3) * 128), tag, RLX_AGENT);
        tag_post(P, tag);
    }
    if (li >= 13) {
#pragma unroll 1
        for (int n = 171 + (li - 13) + 3 * w; n < 216; n += 36) touch_group(P, xcd, n, lane);
    }
    STAMP(2);
    if (w == 11) {
        STAMP_OUT(8, blockIdx.x);
        if (blockIdx.x == 255) {
            const GAS u64* f = (const GAS u64*)(P.ws + WS_ARR) + lane;
#pragma unroll 1
            for (unsigned spins = 0; spins < SPIN_CAP; ++spins) {
                const bool ok = __hip_atomic_load(f, RLX_AGENT) == tag && __hip_atomic_load(f + 64, RLX_AGENT) == tag &&
                                __hip_atomic_load(f + 128, RLX_AGENT) == tag && __hip_atomic_load(f + 192, RLX_AGENT) == tag;
                if (__all(ok)) break;
                __builtin_amdgcn_s_sleep(8);
            }
            if (lane == 0) __hip_atomic_store((GAS u64*)(P.ws + WS_CTL), tag, RLX_AGENT);
        }
    }
}

__global__ __launch_bounds__(768, 3) void k_fused(Params P) {
    __shared__ __attribute__((aligned(16))) char smem_[LDS_BYTES];
    lptr smem = (lptr)smem_;
    const int b = blockIdx.x, xcd = b & 7, idx = b >> 3;
    const int lane = threadIdx.x & 63, w = __builtin_amdgcn_readfirstlane(threadIdx.x >> 6);
    const rsrc_t wsr = __builtin_amdgcn_make_buffer_rsrc(P.ws, 0, (int)WS_END, 0x00020000);
    const int rt64 = xcd * 4 + (idx & 3), cg2 = idx >> 2;
    if (idx >= CPF && w < 6) {
        const char* pc; unsigned len;
        asm volatile("s_getpc_b64 %0\n.Lkf_code_begin:\n\ts_mov_b32 %1, .Lkf_code_end-.Lkf_code_begin" : "=s"(pc), "=s"(len));
        touch(pc, min((unsigned)(threadIdx.x * 128), len));
    }
    u64 tag;
    if (__builtin_expect(idx < 16, 1)) {
        const int rbl = idx & 3, hgl = idx >> 2, rb = (xcd >> 1) * 4 + rbl, hg = (xcd & 1) * 4 + hgl;
        if (__builtin_expect((w & 12) != 4, 1)) gemm_q(P, smem, rb, hg, w < 4 ? w : w - 4, tag, wsr);
        else gemm_kv(P, smem, rb, hg, w - 4, cg2, tag, wsr);
    } else {
        tag = *(const __attribute__((address_space(4))) u64*)(P.ws + WS_CTL);
        const int li = idx - 16, gid = li < 11 ? xcd * 11 + li : 88 + (li - 11) * 8 + xcd;
        if (gid < 99) ssm_role(P, smem, xcd, li, gid, cg2, tag, wsr);
        else {
            helper_role(P, smem, xcd, li, tag, wsr);
            if (w < 9) { wo_issue<0x1ff>(P, cg2, w, 9, lane); WAIT_VM(0); }
            if (w == 10 && lane < 16) lds_w32(smem + FL + lane * 4, 0u);
            BARRIER();
        }
    }
    phase2(P, smem, rt64, cg2, idx < 16, tag, wsr);
}

extern "C" void kernel_launch(void* const* d_in, const int* in_sizes, int n_in, void* d_out, int out_size, void* d_ws, size_t ws_size,
                              hipStream_t stream) {
    Params P;
    P.x = (const char*)d_in[0]; P.wqkv = (const char*)d_in[1]; P.wo = (const char*)d_in[2];
    P.logA = (const char*)d_in[4]; P.aim = (const char*)d_in[5]; P.cre = (const char*)d_in[6]; P.cim = (const char*)d_in[7];
    P.logdt = (const char*)d_in[8]; P.dskip = (const char*)d_in[9]; P.gw = (const char*)d_in[10];
    P.out = (char*)d_out; P.ws = (char*)d_ws;
    k_fused<<<256, 768, 0, stream>>>(P);
}
```

```cpp
#include <hip/hip_runtime.h>
#include <stdint.h>

#define GAS __attribute__((address_space(1)))
#define LAS __attribute__((address_space(3)))
#define DEVI __device__ __forceinline__
typedef unsigned short u16;
typedef unsigned long long u64;
typedef __attribute__((ext_vector_type(2))) unsigned u32x2;
typedef __attribute__((ext_vector_type(3))) unsigned u32x3;
typedef __attribute__((ext_vector_type(4))) unsigned u32x4;
typedef __attribute__((ext_vector_type(4))) short s16x4;
typedef __attribute__((ext_vector_type(2))) float f32x2;
typedef __attribute__((ext_vector_type(4))) float f32x4;
typedef __attribute__((ext_vector_type(16))) float f32x16;
typedef __attribute__((ext_vector_type(2))) __bf16 bf16x2;
typedef __attribute__((ext_vector_type(8))) __bf16 bf16x8;
typedef __attribute__((ext_vector_type(2))) _Float16 f16x2;
typedef __attribute__((ext_vector_type(8))) _Float16 f16x8;
typedef LAS char* lptr;

#define PREF 1
#define ROT 0
#define FSLEEP 2
#define NSLEEP "3"
#define LDRIFT 0
#define PSLEEP 6
#define CSLEEP 1
#define OSLEEP 2
#define RSLEEP 3
#define HOLD 1
#define GROT ((w + li) & 3)
#define NTW 8
#define OUTAUX 17
#define TEARLY 1
#define HDELAY 0
#define CPF 16
#define LA3 0
#define TD1 0
#define TD2 0
#define TW2 3
#define HD 0
#define QH 4
#define WOA 0x00f
#define WOT 48
constexpr int SEQ = 2048, DM = 576;

constexpr unsigned WS_CTL = 0;
constexpr unsigned WS_ARR = 256;
constexpr unsigned WS_BFL = 4096;
constexpr unsigned WS_HINT = 20480;
constexpr unsigned WS_GATE = 36864;
constexpr unsigned WS_PFL = 49152;
constexpr unsigned WS_PART = 65536;
constexpr unsigned WS_O = WS_PART + 8 * 16 * 3072;
constexpr unsigned WS_B = WS_O + SEQ * DM * 2;
constexpr unsigned WS_END = WS_B + SEQ * DM * 2;
constexpr unsigned EMPTY = 0xffffffffu;

constexpr size_t WS_STAMP = 16u << 20;
#define STAMP_DECL
#define STAMP(i)
#define CSTAMP(i)
#define STAMP_OUT(role, slot)

struct Params {
    const char* x; const char* wqkv; const char* wo; const char* logA; const char* aim; const char* cre; const char* cim;
    const char* logdt; const char* dskip; const char* gw; char* out; char* ws;
};

constexpr int LDS_BYTES = 163840, FL = 163584, LDS_TAG = FL + 64, LDS_DUMMY = FL + 128, LDS_JUNK = 161280;
enum { F_SW = 0, F_LANDED = 4, F_W1 = 20, F_B = 28, F_H = 24, F_KVP = 36, F_KVR = 40, F_Q0 = 44, F_QD = 48, F_D = 52, F_C1 = 56, F_W0 = 60 };
constexpr int WO8 = 132096, P2_ACC = 73728, P2_AP = 304, P2_B = 141312, P2_G = 150528;

DEVI float bf2f(unsigned h) { return __uint_as_float(h << 16); }
DEVI float bfhi(unsigned h) { return __uint_as_float(h & 0xffff0000u); }
DEVI unsigned pk_bf16(float a, float b) { return __builtin_bit_cast(unsigned, __builtin_convertvector(f32x2{a, b}, bf16x2)); }
DEVI unsigned pk_f16(f32x2 v) { return __builtin_bit_cast(unsigned, __builtin_convertvector(v, f16x2)); }
DEVI float rbf(float a) { return bf2f(pk_bf16(a, a)); }
DEVI f32x4 mfma16(u32x4 a, u32x4 b, f32x4 c) {
    return __builtin_amdgcn_mfma_f32_16x16x32_bf16(__builtin_bit_cast(bf16x8, a), __builtin_bit_cast(bf16x8, b), c, 0, 0, 0);
}
DEVI void mfma16i(f32x4& c, u32x4 a, u32x4 b) { asm volatile("v_mfma_f32_16x16x32_bf16 %0, %1, %2, %0" : "+v"(c) : "v"(a), "v"(b)); }
#define MFMA_DONE() do { __builtin_amdgcn_sched_barrier(0); asm volatile("s_nop 7\n\ts_nop 1" ::: "memory"); __builtin_amdgcn_sched_barrier(0); } while (0)
DEVI u32x4 lds_r128(lptr p) { return *(LAS u32x4*)p; }
DEVI u32x2 lds_r64(lptr p) { return *(LAS u32x2*)p; }
DEVI float lds_rf(lptr p) { return *(LAS float*)p; }
DEVI void lds_w128(lptr p, u32x4 v) { *(LAS u32x4*)p = v; }
DEVI void lds_w64(lptr p, u32x2 v) { *(LAS u32x2*)p = v; }
DEVI void lds_w32(lptr p, unsigned v) { *(LAS unsigned*)p = v; }
DEVI void lds_w16(lptr p, unsigned v) { *(LAS u16*)p = (u16)v; }
DEVI u32x2 lds_tr(lptr p) { return __builtin_bit_cast(u32x2, __builtin_amdgcn_ds_read_tr16_b64_v4i16((LAS s16x4*)p)); }
DEVI u32x4 lds_trfrag(lptr p, int off2) { u32x2 a = lds_tr(p), b = lds_tr(p + off2); return u32x4{a.x, a.y, b.x, b.y}; }
template <int OFF> DEVI void glds16o(const char* sbase, unsigned voff) {
    asm volatile("global_load_lds_dwordx4 %0, %1 offset:%2" :: "v"(voff), "s"(sbase), "i"(OFF) : "memory");
}
DEVI void glds16(const char* sbase, unsigned voff, unsigned l) {
    asm volatile("s_mov_b32 m0, %2\n\ts_nop 0\n\tglobal_load_lds_dwordx4 %0, %1" :: "v"(voff), "s"(sbase), "s"(l) : "memory");
}
#define SET_M0(v) asm volatile("s_mov_b32 m0, %0\n\ts_nop 0" :: "s"(v) : "memory")
#define W_ADDTID(v, off) asm volatile("ds_write_addtid_b32 %0 offset:%1" :: "v"(v), "i"(off) : "memory")
#define CFENCE() asm volatile("" ::: "memory")
#define SCHED() __builtin_amdgcn_sched_barrier(0)
#define WAIT_VM(n) asm volatile("s_waitcnt vmcnt(" #n ")" ::: "memory")
#define WAIT_LGKM(n) __builtin_amdgcn_s_waitcnt(0xC07F | ((n) << 8))
#define WAIT_LGKM0() asm volatile("s_waitcnt lgkmcnt(0)" ::: "memory")
#define BARRIER() do { SCHED(); asm volatile("s_waitcnt lgkmcnt(0)\n\ts_barrier" ::: "memory"); SCHED(); } while (0)
#define RAW_BARRIER() do { SCHED(); asm volatile("s_barrier" ::: "memory"); SCHED(); } while (0)
#define RLX_AGENT __ATOMIC_RELAXED, __HIP_MEMORY_SCOPE_AGENT
constexpr unsigned SPIN_CAP = 1u << 16;
typedef __amdgpu_buffer_rsrc_t rsrc_t;
DEVI u32x4 ld_sc1(rsrc_t r, unsigned off) { return __builtin_amdgcn_raw_buffer_load_b128(r, off, 0, 16); }
DEVI void st_sc1(rsrc_t r, unsigned off, u32x4 v) { __builtin_amdgcn_raw_buffer_store_b128(v, r, off, 0, 16); }

DEVI u32x4 pack8(f32x4 a, f32x4 b) { return u32x4{pk_bf16(a[0], a[1]), pk_bf16(a[2], a[3]), pk_bf16(b[0], b[1]), pk_bf16(b[2], b[3])}; }
DEVI float phi(float t) {
    const float e = __builtin_amdgcn_fmed3f(__builtin_amdgcn_exp2f(t * 1.44269504089f), 0.f, 1.f);
    return __int_as_float(max(__float_as_int(t), 0x358637bd)) + e;
}
DEVI f32x4 phi4(f32x4 v) {
    f32x2 a, b;
    const f32x2 c = {1.44269504089f, 1.44269504089f};
    asm("v_pk_mul_f32 %0, %2, %4\n\tv_pk_mul_f32 %1, %3, %4" : "=&v"(a), "=&v"(b) : "v"(f32x2{v[0], v[1]}), "v"(f32x2{v[2], v[3]}), "s"(c));
    f32x4 r;
#pragma unroll
    for (int i = 0; i < 4; ++i)
        r[i] = __int_as_float(max(__float_as_int(v[i]), 0x358637bd)) + __builtin_amdgcn_fmed3f(__builtin_amdgcn_exp2f(i < 2 ? a[i] : b[i - 2]), 0.f, 1.f);
    return r;
}
DEVI unsigned umax4(u32x4 a) { return max(max(a.x, a.y), max(a.z, a.w)); }
DEVI u32x4 not_empty(u32x4 a) { return a; }
DEVI void tag_post(const Params& P, u64 tag) { __hip_atomic_store((GAS u64*)(P.ws + WS_ARR) + blockIdx.x, tag, RLX_AGENT); }
#define PIN_TAG() do { asm volatile("" : "+s"(tag)); tag += 1ull; } while (0)
template <int SL = PSLEEP>
DEVI void poll_addr(const Params& P, unsigned off, u64 tag) {
    const GAS u64* f = (const GAS u64*)(P.ws + off);
#pragma unroll 1
    for (unsigned spins = 0; spins < SPIN_CAP; ++spins) {
        if (__all(__hip_atomic_load(f, RLX_AGENT) == tag)) break;
        __builtin_amdgcn_s_sleep(SL);
    }
}
template <int SL = PSLEEP>
DEVI void poll_flags(const Params& P, unsigned off, int n, u64 tag, int lane) { poll_addr<SL>(P, off + (lane < n ? lane : 0) * 128, tag); }
DEVI unsigned flag_get(lptr smem, int f) { return __builtin_amdgcn_readfirstlane(__hip_atomic_load((LAS unsigned*)(smem + FL + f), __ATOMIC_RELAXED, __HIP_MEMORY_SCOPE_WORKGROUP)); }
template <int SL = FSLEEP>
DEVI void flag_wait(lptr smem, int f, unsigned v) {
#pragma unroll 1
    for (unsigned spins = 0; spins < (SPIN_CAP << 4); ++spins) { if (flag_get(smem, f) >= v) break; if (SL) __builtin_amdgcn_s_sleep(SL); }
    CFENCE();
}
DEVI void flag_add(int f) { asm volatile("s_mov_b64 exec, 1\n\tds_add_u32 %0, %1\n\ts_mov_b64 exec, -1" :: "v"(FL + f), "v"(1u) : "memory"); }
#define FLAG_ADD(f) asm volatile("s_mov_b64 exec, 1\n\tds_add_u32 %0, %1 offset:%2\n\ts_mov_b64 exec, -1" :: "v"(flp), "v"(one), "i"(f) : "memory")
DEVI void flag_set(int f, unsigned v) { asm volatile("s_mov_b64 exec, 1\n\tds_write_b32 %0, %1\n\ts_mov_b64 exec, -1" :: "v"(FL + f), "v"(v) : "memory"); }
#define NEEDP(seen, ptr, f, v) do { unsigned c_; asm volatile( \
        "v_cmp_gt_u32 vcc, %4, %1\n\ts_cbranch_vccnz 0f\n1:\n\t.subsection 1\n" \
        "0:\n\ts_mov_b32 %0, 0x100000\n2:\n\ts_sleep " NSLEEP "\n\tds_read_b32 %1, %2 offset:%3\n\ts_sub_u32 %0, %0, 1\n\ts_waitcnt lgkmcnt(0)\n\ts_cbranch_scc1 1b\n\t" \
        "v_cmp_gt_u32 vcc, %4, %1\n\ts_cbranch_vccnz 2b\n\ts_branch 1b\n\t.subsection 0" \
        : "=&s"(c_), "+v"(seen) : "v"(ptr), "i"(f), "i"(v) : "memory", "scc", "vcc"); } while (0)
#define NEED(seen, f, v) NEEDP(seen, flp, f, v)
DEVI void touch(const char* sbase, unsigned voff) {
    asm volatile("s_mov_b32 m0, %2\n\ts_nop 0\n\tglobal_load_lds_dword %0, %1" :: "v"(voff), "s"(sbase), "s"(LDS_JUNK) : "memory");
}
DEVI const char* group_addr(const Params& P, int xcd, int n, int lane) {
    if (n < 126) {
        const int c0 = n / 18, r = n - c0 * 18, c = c0 + 2, l = r - 8;
        return r < 8 ? P.x + (unsigned)(((xcd >> 1) * 512 + r * 64 + lane) * 1152 + c * 128)
                     : P.wqkv + (unsigned)((c * 64 + lane) * 3456 + (l < 5 ? 1152 : 2304 - 640) + (xcd & 1) * 512 + l * 128);
    }
    const int c = (n - 126) / 5, l = n - 126 - c * 5;
    return P.wqkv + (unsigned)((c * 64 + lane) * 3456 + (xcd & 1) * 512 + l * 128);
}
DEVI void touch_group(const Params& P, int xcd, int n, int lane) {
    if (n < 126) {
        const int c0 = n / 18, r = n - c0 * 18, c = c0 + 2;
        if (r < 8) touch(P.x, (unsigned)(((xcd >> 1) * 512 + r * 64 + lane) * 1152 + c * 128));
        else { const int l = r - 8; touch(P.wqkv, (unsigned)((c * 64 + lane) * 3456 + (l < 5 ? 1152 : 2304 - 640) + (xcd & 1) * 512 + l * 128)); }
    } else if (n < 171) {
        const int c = (n - 126) / 5, l = n - 126 - c * 5;
        touch(P.wqkv, (unsigned)((c * 64 + lane) * 3456 + (xcd & 1) * 512 + l * 128));
    } else {
        const int l = (n - 171) / 9, c = n - 171 - l * 9;
        touch(P.wo, (unsigned)((c * 64 + lane) * 1152 + l * 128));
    }
}
template <unsigned CH>
DEVI void wo_issue(const Params& P, int cg, int first, int stride, int lane) {
#pragma unroll 1
    for (int m = first; m < 9; m += stride) {
        const int q = 64 * m + lane, rho = q / 9, pc = q - rho * 9;
        const int k = (rho & 32) + 8 * ((rho >> 3) & 3) + 4 * (rho & 1) + ((rho >> 1) & 3);
        const unsigned qo = (unsigned)(k * 1152 + cg * 144 + pc * 16);
#pragma unroll
        for (int c = 0; c < 9; ++c) if ((CH >> c) & 1) glds16(P.wo + c * (64 * 1152), qo, (unsigned)(c * 9216 + m * 1024));
    }
}

constexpr int SP = 3088;
constexpr int XT_OFF = 135936, XT_BYTES = 4608, XPAD = 256;
constexpr int TT_OFF = 107520, TC = 1184, PT_OFF = 82944, PC_OFF = 2560;

#define CM2(o1, p1, a1, b1, o2, p2, a2, b2) do { asm( \
    "v_pk_mul_f32 %0, %2, %3 op_sel:[1,1] op_sel_hi:[1,0] neg_lo:[0,1]\n\tv_pk_mul_f32 %1, %4, %5 op_sel:[1,1] op_sel_hi:[1,0] neg_lo:[0,1]\n\t" \
    "v_pk_fma_f32 %0, %2, %3, %0 op_sel_hi:[0,1,1]\n\tv_pk_fma_f32 %1, %4, %5, %1 op_sel_hi:[0,1,1]" \
    : "=&v"(o1), "=&v"(o2) : "v"(a1), "v"(b1), "v"(a2), "v"(b2)); \
    asm("v_cvt_pk_f16_f32 %0, %2, %3\n\tv_cvt_pk_f16_f32 %1, %4, %5" : "=&v"(p1), "=&v"(p2) : "v"((o1).x), "v"((o1).y), "v"((o2).x), "v"((o2).y)); } while (0)
#define HM2(p1, a1, p2, a2, b) asm( \
    "v_pk_mul_f16 %0, %2, %4 op_sel_hi:[0,1]\n\tv_pk_mul_f16 %1, %3, %4 op_sel_hi:[0,1]\n\t" \
    "v_pk_fma_f16 %0, %2, %4, %0 op_sel:[1,1,0] op_sel_hi:[1,0,1] neg_lo:[0,1,0]\n\tv_pk_fma_f16 %1, %3, %4, %1 op_sel:[1,1,0] op_sel_hi:[1,0,1] neg_lo:[0,1,0]" \
    : "=&v"(p1), "=&v"(p2) : "v"(a1), "v"(a2), "v"(b))
DEVI f32x2 cexp_p(float a2, float f, float p) {
    float m = __builtin_amdgcn_exp2f(a2 * p);
    float r = __builtin_amdgcn_fractf(f * p);
    return f32x2{m * __builtin_amdgcn_cosf(r), m * __builtin_amdgcn_sinf(r)};
}
DEVI u32x4 mask_frag(unsigned bits, int g, unsigned val) {
    unsigned by = bits >> (8 * g);
    u32x4 r;
#pragma unroll
    for (int i = 0; i < 4; ++i) { unsigned t = (by >> (2 * i)) & 3u; r[i] = ((t | (t << 15)) & 0x10001u) * val; }
    return r;
}
struct Tab2 { unsigned v[8][64][4]; };
constexpr Tab2 make_tab2() {
    Tab2 t{};
    for (int l = 0; l < 64; ++l) {
        const int r = l & 15, g = l >> 4, j = l & 31, h = l >> 5;
        const unsigned bits[6] = {7u << (r + 11), 31u << (r + 6), 7u << (r + 3), 127u << (r + 5), 511u << (r + 4), 3u << (r + 12)};
        for (int m = 0; m < 6; ++m)
            for (int i = 0; i < 4; ++i) {
                const unsigned q = (bits[m] >> (8 * g + 2 * i)) & 3u, val = m < 5 ? 0x3f80u : 0xffffu;
                t.v[m][l][i] = (q & 1 ? val : 0u) | (q & 2 ? val << 16 : 0u);
            }
        for (int c = 0; c < 4; ++c) {
            const int v2 = 79 - (32 + (j >> 1)) - c + 4, v3 = 127 - (64 + 32 * h + (j >> 1)) - c + 4;
            t.v[6][l][c] = PT_OFF + (j & 1) * 1280 + c * 320 + (v2 >> 2) * 16 + h * 8 + (v2 & 3) * 2 - 64;
            t.v[7][l][c] = PT_OFF + PC_OFF + c * 320 + (v3 >> 2) * 16 + (j & 1) * 8 + (v3 & 3) * 2 - 64;
        }
    }
    return t;
}
__device__ const Tab2 g_tab2 = make_tab2();
DEVI float wave_max(float v) {
    v = fmaxf(v, __int_as_float(__builtin_amdgcn_update_dpp(0, __float_as_int(v), 0xB1, 0xf, 0xf, true)));
    v = fmaxf(v, __int_as_float(__builtin_amdgcn_update_dpp(0, __float_as_int(v), 0x4E, 0xf, 0xf, true)));
    v = fmaxf(v, __int_as_float(__builtin_amdgcn_update_dpp(0, __float_as_int(v), 0x124, 0xf, 0xf, true)));
    v = fmaxf(v, __int_as_float(__builtin_amdgcn_update_dpp(0, __float_as_int(v), 0x128, 0xf, 0xf, true)));
    const int i = __float_as_int(v);
    const int a = max(__builtin_amdgcn_readlane(i, 0), __builtin_amdgcn_readlane(i, 16)), b = max(__builtin_amdgcn_readlane(i, 32), __builtin_amdgcn_readlane(i, 48));
    return __int_as_float(max(a, b));
}

DEVI void ssm_role(const Params& P, lptr smem, int xcd, int li, int gid, int cg2, u64& tag, rsrc_t wsr, u64& tp) {
    const int gl = gid / 11, gi = gid - gl * 11, ch0 = gl * 64 + (gi < 10 ? gi * 6 : 58);
    const int tid = threadIdx.x, lane = tid & 63, w = __builtin_amdgcn_readfirstlane(tid >> 6);
    const int ch = w >> 1, half = (w ^ (w >> 2)) & 1, d = ch0 + ch;
    const bool own = gi < 10 || ch >= 2;
    const int lo16 = lane & 15, g = lane >> 4;
    STAMP_DECL; STAMP(1);
    __builtin_amdgcn_s_setprio(3);
    if (half) {
        const char* tp = (const char*)&g_tab2;
        SET_M0(TT_OFF + ch * 8192);
        glds16o<0>(tp, (unsigned)(lane * 16)); glds16o<1024>(tp, (unsigned)(lane * 16)); glds16o<2048>(tp, (unsigned)(lane * 16)); glds16o<3072>(tp, (unsigned)(lane * 16));
        SET_M0(TT_OFF + ch * 8192 + 4096);
        tp += 4096;
        glds16o<0>(tp, (unsigned)(lane * 16)); glds16o<1024>(tp, (unsigned)(lane * 16)); glds16o<2048>(tp, (unsigned)(lane * 16)); glds16o<3072>(tp, (unsigned)(lane * 16));
    }
    const unsigned po = (unsigned)(d * 256 + lane * 4);
    unsigned r_la, r_ai, r_cr, r_ci, r_dt, r_ds, junk = 0, junk2 = 0;
    asm volatile("global_load_dword %0, %6, %8\n\tglobal_load_dword %1, %6, %9\n\tglobal_load_dword %2, %6, %10\n\tglobal_load_dword %3, %6, %11\n\t"
                 "global_load_ushort %4, %7, %12\n\tglobal_load_ushort %5, %7, %13"
                 : "=&v"(r_cr), "=&v"(r_ci), "=&v"(r_la), "=&v"(r_ai), "=&v"(r_dt), "=&v"(r_ds)
                 : "v"(po), "v"(d * 2), "s"(P.cre), "s"(P.cim), "s"(P.logA), "s"(P.aim), "s"(P.logdt), "s"(P.dskip) : "memory");
#define TOUCH(dst, addr) asm volatile("global_load_dword %0, %1, off" : "+v"(dst) : "v"(addr) : "memory")
    const bool tch = w < NTW && li < 12;
    const char* tga = group_addr(P, xcd, tch ? w * 12 + li : 0, lane);
    RAW_BARRIER();
    if (tch && w < TW2) TOUCH(junk, tga);
    SCHED();
    const int tt = (tid + (li & 7) * 64) & 511;
    u32x3 xr[4] = {};
    const unsigned xoff = (unsigned)(tt * 4608 + ch0 * 2);
    const unsigned boff = own ? WS_B + (unsigned)(d * 4096 + half * 2048 + lane * 16) : WS_END;
#define GATHER(i) do { SCHED(); if (w < 8) asm volatile("global_load_dwordx3 %0, %1, %2 offset:%3" : "+v"(xr[i]) : "v"(xoff), "s"(P.x), "i"((i) * 1152) : "memory"); SCHED(); } while (0)
#define PRIO(i) do { SCHED(); __builtin_amdgcn_s_setprio(i); SCHED(); } while (0)
    SCHED();
    lptr xt = smem + XT_OFF + ch * XT_BYTES, tb = smem + TT_OFF + ch * 4 * TC, pt = smem + PT_OFF + ch * 4096;
    const int j32 = lane & 31, h = lane >> 5;
    if (!half) { lds_w32(smem + FL + 32, 0u); lds_w32(smem + FL + 36 + ch * 4, 0u); }
    int arow;
    {
        const u64 map0 = 0x0a09080803020100ull, map1 = 0x0c0b0a0a07060504ull, map2 = 0x1413110e12100f0dull;
        const u64 mm = j32 < 8 ? map0 : (j32 < 16 ? map1 : map2);
        arow = (int)((mm >> (8 * (j32 & 7))) & 0xff);
    }
    lptr ap = smem + arow * SP + w * 256 + h * 16, bp = smem + j32 * SP + w * 256 + h * 16;
    const int cpy = (3 - lo16) & 3;
    lptr xf7 = xt + (XPAD - 16 + 16 * lo16 + 8 * g) * 2 - 64 * 7;
    lptr tf = tb + cpy * TC + 2 * (527 - lo16 + 8 * g - cpy) - 64 * 16;
    lptr xc = smem + (w ^ 1) * 256 + lane * 4, xb = smem + XT_OFF + (XPAD + tt * 4) * 2;
    asm volatile("" : "+v"(ap), "+v"(bp), "+v"(xf7), "+v"(tf), "+v"(xc), "+v"(xb));
    if (tch && w < TW2) WAIT_VM(1); else WAIT_VM(0);
    asm volatile("" : "+v"(r_la), "+v"(r_ai), "+v"(r_cr), "+v"(r_ci), "+v"(r_dt), "+v"(r_ds));
    if (tch && w >= TW2) TOUCH(junk, tga);
    const float dt = rbf(__expf(bf2f(r_dt)));
    const float cm = wave_max(fmaxf(fmaxf(fabsf(bf2f(r_cr)), fabsf(bfhi(r_cr))), fmaxf(fabsf(bf2f(r_ci)), fabsf(bfhi(r_ci)))));
    unsigned ce = __float_as_uint(cm) >> 23; ce = ce < 16u ? 16u : (ce > 240u ? 240u : ce);
    const float cs = __uint_as_float((263u - ce) << 23), ics = __uint_as_float((ce - 9u) << 23);
    f32x16 kacc;
#pragma unroll
    for (int i = 0; i < 16; ++i) kacc[i] = 0.f;
    STAMP(2);
    {
        const unsigned sh = half * 16;
        const float la = bf2f(r_la >> sh), ai = bf2f(r_ai >> sh), cr = bf2f(r_cr >> sh) * cs, ci = bf2f(r_ci >> sh) * cs;
        const float a2 = (-rbf(__expf(la)) * dt) * 1.44269504089f, f = (ai * dt) * 0.15915494309f;
#define PUT(row, v) W_ADDTID(v, ((row) & 15) * SP)
        const f32x2 w1 = cexp_p(a2, -f, 1.f), W = cexp_p(a2, -f, 256.f);
        f32x2 u[8], v[4], c[9], e4, e8, t2;
        unsigned U[8], V[4], p, q;
        u[1] = cexp_p(a2, f, 8.f); v[1] = cexp_p(a2, f, 64.f);
        c[0] = f32x2{cr, -ci};
        U[1] = pk_f16(u[1]); V[1] = pk_f16(v[1]);
        GATHER(0);
        SET_M0(w * 256);
        PUT(0, pk_f16(c[0]));
        CM2(c[1], p, c[0], w1, u[2], U[2], u[1], u[1]); PUT(1, p);
        CM2(c[2], p, c[1], w1, u[3], U[3], u[2], u[1]); PUT(2, p);
        CM2(c[3], p, c[2], w1, u[4], U[4], u[2], u[2]); PUT(3, p);
        CM2(c[4], p, c[3], w1, u[5], U[5], u[4], u[1]); PUT(4, p);
        CM2(c[5], p, c[4], w1, u[6], U[6], u[4], u[2]); PUT(5, p);
        CM2(c[6], p, c[5], w1, u[7], U[7], u[4], u[3]); PUT(6, p);
        CM2(c[7], p, c[6], w1, v[2], V[2], v[1], v[1]); PUT(7, p);
        CM2(c[8], p, c[7], w1, v[3], V[3], v[2], v[1]);
        PRIO(2);
        GATHER(1);
        CM2(e8, p, c[8], W, e4, q, c[4], W); PUT(12, p); PUT(10, q);
        CM2(c[8], p, e8, W, c[4], q, e4, W); PUT(14, p); PUT(13, q);
        CM2(t2, p, c[0], W, e4, q, c[4], W); PUT(8, p); PUT(15, q);
        CM2(t2, p, c[2], W, e4, q, c[6], W); PUT(9, p); PUT(11, q);
        SET_M0(w * 256 + 16 * SP);
        CM2(e8, p, c[8], W, t2, q, W, W); PUT(17, p);
        CM2(e4, p, c[8], t2, c[0], q, e8, t2); PUT(16, p); PUT(18, q);
        CM2(c[8], p, e4, t2, e8, q, c[0], t2); PUT(19, p); PUT(20, q);
        PRIO(1);
        GATHER(2);
        u32x4 af[8];
#pragma unroll
        for (int s = 0; s < 8; ++s) af[s] = lds_r128(ap + 32 * s);
        CFENCE();
        PUT(16, V[2]); PUT(24, V[3]);
#pragma unroll
        for (int b = 1; b < 8; ++b) { HM2(p, V[2], q, V[3], U[b]); PUT(16 + b, p); PUT(24 + b, q); }
        SET_M0(w * 256);
        PRIO(0);
        GATHER(3);
        PUT(0, 0x3c00u); PUT(8, V[1]);
#pragma unroll
        for (int b = 1; b < 8; b += 2) { HM2(p, U[b], q, U[b == 7 ? 7 : b + 1], V[1]); PUT(8 + b, p); if (b < 7) PUT(9 + b, q); PUT(b, U[b]); if (b < 7) PUT(b + 1, U[b + 1]); }
#undef PUT
        u32x4 bfr[8];
#pragma unroll
        for (int s = 0; s < 8; ++s) bfr[s] = lds_r128(bp + 32 * s);
        SCHED();
        SCHED();
#pragma unroll
        for (int s = 0; s < 8; ++s)
            kacc = __builtin_amdgcn_mfma_f32_32x32x16_f16(__builtin_bit_cast(f16x8, af[s]), __builtin_bit_cast(f16x8, bfr[s]), kacc, 0, 0, 0);
    }
    STAMP(3);
    const float ks = 2.f * dt * ics;
#pragma unroll
    for (int i = 0; i < 12; ++i) kacc[i] *= ks;
    if (half) {
#pragma unroll
        for (int i = 0; i < 8; ++i) W_ADDTID(kacc[i], i * SP);
    } else {
#pragma unroll
        for (int i = 0; i < 4; ++i) W_ADDTID(kacc[8 + i], i * SP);
    }
    if (w >= 8 && li < 12) {
        const int n = (11 - w) * 12 + li, l = n / 9, c = n - l * 9;
        TOUCH(junk2, P.wo + (unsigned)((c * 64 + lane) * 1152 + (8 - l) * 128));
    }
    u32x4 ct[8];
#pragma unroll
    for (int i = 0; i < 8; ++i) ct[i] = lds_r128(smem + TT_OFF + ch * 8192 + i * 1024 + lane * 16);
    PIN_TAG();
    STAMP(4);
    BARRIER();
    STAMP(5);
    if (tid == 704) tag_post(P, tag);
    auto cleared = [&]() {
        if (w < 4) {
            WAIT_VM(0);
            if (lane == 0 && __hip_atomic_fetch_add((LAS unsigned*)(smem + FL + 32), 1u, __ATOMIC_RELAXED, __HIP_MEMORY_SCOPE_WORKGROUP) == 3u)
                __hip_atomic_store((GAS u64*)(P.ws + WS_BFL + gid * 128), tag, RLX_AGENT);
        }
    };
    auto x_in = [&]() {
        if (w < 8) {
            WAIT_VM(0); SCHED();
            asm volatile("" : "+v"(xr[0]), "+v"(xr[1]), "+v"(xr[2]), "+v"(xr[3]), "+v"(junk) :: "memory");
#pragma unroll
            for (int k = 0; k < 3; ++k) {
                u32x2 lo = {__builtin_amdgcn_perm(xr[1][k], xr[0][k], 0x05040100u), __builtin_amdgcn_perm(xr[3][k], xr[2][k], 0x05040100u)};
                u32x2 hi = {__builtin_amdgcn_perm(xr[1][k], xr[0][k], 0x07060302u), __builtin_amdgcn_perm(xr[3][k], xr[2][k], 0x07060302u)};
                lds_w64(xb + (2 * k) * XT_BYTES, lo); lds_w64(xb + (2 * k + 1) * XT_BYTES, hi);
            }
        }
        if (w < 4) {
#pragma unroll
            for (int i = 0; i < 6; ++i)
                if (i >= 2 || gi < 10) __builtin_amdgcn_raw_buffer_store_b128(u32x4{EMPTY, EMPTY, EMPTY, EMPTY}, wsr, (unsigned)(tid * 16), WS_B + (ch0 + i) * 4096, 16);
        }
        BARRIER();
        if (w >= 8) wo_issue<WOA>(P, cg2, w - 8, 4, lane);
    };
    auto TF = [&](int off) { u32x2 a = lds_r64(tf + off), b = lds_r64(tf + off + 8); return u32x4{a.x, a.y, b.x, b.y}; };
    f32x4 acc[8];
#pragma unroll
    for (int i = 0; i < 8; ++i) acc[i] = f32x4{0.f, 0.f, 0.f, 0.f};
    lptr axr;
    if (!half) {
        __builtin_amdgcn_s_setprio(2);
        lds_w64(xt + lane * 8, u32x2{0u, 0u});
#pragma unroll
        for (int i = 0; i < 8; ++i) kacc[i] += lds_rf(xc + i * SP);
        unsigned dd[6];
        dd[0] = pk_bf16(kacc[7], kacc[6]); dd[1] = pk_bf16(kacc[5], kacc[4]); dd[2] = pk_bf16(kacc[3], kacc[2]);
        dd[3] = pk_bf16(kacc[1], kacc[0] + (lane == 0 ? bf2f(r_ds) : 0.f));
        dd[4] = __builtin_amdgcn_update_dpp(0, dd[0], 0x138, 0xf, 0xf, true);
        dd[5] = __builtin_amdgcn_update_dpp(0, dd[1], 0x138, 0xf, 0xf, true);
        unsigned mm[5];
#pragma unroll
        for (int i = 0; i < 5; ++i) mm[i] = __builtin_amdgcn_alignbit(dd[i + 1], dd[i], 16);
        lptr tw = tb + 2 * (536 - 8 * lane);
        lds_w128(tw, u32x4{dd[0], dd[1], dd[2], dd[3]}); lds_w128(tw + TC, u32x4{mm[0], mm[1], mm[2], mm[3]});
        lds_w128(tw + 2 * TC, u32x4{dd[1], dd[2], dd[3], dd[4]}); lds_w128(tw + 3 * TC, u32x4{mm[1], mm[2], mm[3], mm[4]});
        {
            const unsigned n0 = __builtin_amdgcn_readlane(dd[0], 63), n1 = __builtin_amdgcn_readlane(dd[1], 63);
            const unsigned kk = (n0 & 0xffffu) * 0x10001u, kh = n0 << 16, m4 = (n0 >> 16) | (n1 << 16);
            const int c = lane & 3, bl = lane >> 2;
            u32x4 e;
            e.x = 0u; e.y = lane == 14 ? kh : (lane == 15 ? kk : 0u); e.z = lane == 12 ? kh : (bl == 3 ? kk : 0u);
            e.w = lane == 14 ? n0 : (lane == 15 ? m4 : (bl == 3 ? kk : 0u));
            if (bl < 6) lds_w128(tb + c * TC + bl * 16 + (bl >> 2) * 1024, e);
        }
        x_in();
        u32x4 X[8];
#pragma unroll
        for (int i = 0; i < 8; ++i) X[i] = lds_r128(xf7 + 64 * 6 + 512 * i);
        u32x4 k0 = TF(1024 - 64), k8 = TF(512 - 64);
#pragma unroll
        for (int i = 0; i < 8; ++i) asm volatile("" : "+v"(acc[i]));
        __builtin_amdgcn_s_setprio(0);
        STAMP(6);
#pragma unroll
        for (int c = 1; c < 8; ++c) {
            u32x4 k0n, k8n;
            if (c < 7) { k0n = TF(1024 - 64 * (c + 1)); k8n = TF(512 - 64 * (c + 1)); }
#pragma unroll
            for (int i = 0; i < 8; ++i) {
                SCHED();
                mfma16i(acc[i], k0, X[i]);
                if (i < 7) mfma16i(acc[i + 1], k8, X[i]);
                SCHED();
                if (c < 7) X[i] = lds_r128(xf7 + 64 * (6 - c) + 512 * i);
            }
            k0 = k0n; k8 = k8n;
            if (c == 5) { SCHED(); cleared(); SCHED(); }
        }
        MFMA_DONE();
        if (w < 4) wo_issue<0x1ff & ~WOA>(P, cg2, w >> 1, 2, lane);
        flag_wait(smem, 36 + ch * 4, 1);
#pragma unroll
        for (int i = 0; i < 4; ++i) lds_w128(xt + i * 1024 + lane * 16, __builtin_bit_cast(u32x4, acc[4 + i]));
        axr = pt + lane * 16;
    } else {
        u32x4 X[8];
#pragma unroll
        for (int i = 0; i < 4; ++i) if (i < 3 || lane < 48) lds_w128(pt + i * 1024 + lane * 16, u32x4{0u, 0u, 0u, 0u});
        CFENCE();
        const unsigned p89 = pk_bf16(kacc[8] + lds_rf(xc), kacc[9] + lds_rf(xc + SP)), pab = pk_bf16(kacc[10] + lds_rf(xc + 2 * SP), kacc[11] + lds_rf(xc + 3 * SP));
#pragma unroll
        for (int c = 0; c < 4; ++c) {
            lptr q2 = smem + (ct[6][c] + ch * 4096), q3 = smem + (ct[7][c] + ch * 4096);
            lds_w16(q2 + 64, p89); lds_w16(q2, p89 >> 16);
            lds_w16(q3 + 64, pab); lds_w16(q3, pab >> 16);
        }
        CFENCE();
        const u32x4 FX = ct[5] & (((unsigned)__builtin_amdgcn_readlane(p89, 63) >> 16) * 0x10001u);
        lptr pf2 = pt + cpy * 320 + ((79 - 64 - lo16 + 4 * g - cpy + 4) >> 2) * 16;
        x_in();
#pragma unroll
        for (int i = 0; i < 8; ++i) X[i] = lds_r128(xf7 + 64 * 7 + 512 * i);
        u32x4 TA[3], TB[3], TCc[4];
#pragma unroll
        for (int i = 0; i < 3; ++i) { TA[i] = lds_r128(pf2 + 64 * (2 - i)); TB[i] = lds_r128(pf2 + 1280 + 64 * (2 - i)); }
#pragma unroll
        for (int i = 0; i < 4; ++i) TCc[i] = lds_r128(pf2 + PC_OFF + 64 * (3 - i));
        STAMP(6);
        const f32x4 z4 = {0.f, 0.f, 0.f, 0.f};
#pragma unroll
        for (int st = 0; st < 6; ++st) {
            const u32x4 Xc = lds_r128(xf7 + 64 * 7 + 512 * st - 16);
            const u32x4 pa = pack8(mfma16(X[st], ct[0], z4), mfma16(X[st], ct[1], z4));
            const u32x4 pb = pack8(mfma16(X[st], ct[2], z4), mfma16(Xc, ct[1], z4));
#pragma unroll
            for (int dl = 2; dl <= 4; ++dl) if (st + dl < 8) {
                acc[st + dl] = mfma16(pa, TA[dl - 2], acc[st + dl]);
                acc[st + dl] = mfma16(pb, TB[dl - 2], acc[st + dl]);
            }
            if (st < 4) {
                const u32x4 pc = pack8(mfma16(X[st], ct[3], z4), mfma16(Xc, ct[4], z4));
#pragma unroll
                for (int dl = 4; dl <= 7; ++dl) if (st + dl < 8) acc[st + dl] = mfma16(pc, TCc[dl - 4], acc[st + dl]);
            }
        }
        SCHED(); lds_w32(smem + FL + 36 + ch * 4, 1u); cleared(); SCHED();
#pragma unroll
        for (int i = 4; i < 8; ++i) acc[i] = mfma16(FX, X[i - 4], acc[i]);
        const u32x4 k0 = TF(1024), k8 = TF(512), k16 = TF(0);
#pragma unroll
        for (int i = 0; i < 8; ++i) acc[i] = mfma16(k0, X[i], acc[i]);
#pragma unroll
        for (int i = 1; i < 8; ++i) acc[i] = mfma16(k8, X[i - 1], acc[i]);
#pragma unroll
        for (int i = 2; i < 8; ++i) acc[i] = mfma16(k16, X[i - 2], acc[i]);
        MFMA_DONE();
#pragma unroll
        for (int i = 0; i < 4; ++i) { lds_w128(pt + i * 1024 + lane * 16, __builtin_bit_cast(u32x4, acc[i])); acc[i] = acc[4 + i]; }
        axr = xt + lane * 16;
    }
    STAMP(7);
    if (w == 0 && lane < 8) lds_w32(smem + FL + lane * 4, 0u);
    WAIT_VM(0); SCHED();
    asm volatile("" :: "v"(junk2));
    BARRIER();
    STAMP(8);
#pragma unroll
    for (int i = 0; i < 4; ++i) acc[i] += __builtin_bit_cast(f32x4, lds_r128(axr + i * 1024));
#pragma unroll
    for (int i = 0; i < 2; ++i) {
        const f32x4 ya = acc[2 * i], yb = acc[2 * i + 1];
        const auto sx = __builtin_amdgcn_permlane16_swap(pk_bf16(ya[0], ya[1]), pk_bf16(yb[0], yb[1]), false, false);
        const auto sy = __builtin_amdgcn_permlane16_swap(pk_bf16(ya[2], ya[3]), pk_bf16(yb[2], yb[3]), false, false);
        st_sc1(wsr, boff + i * 1024, not_empty(u32x4{sx[0], sy[0], sx[1], sy[1]}));
    }
    BARRIER();
    STAMP(9);
    if (w < 2) STAMP_OUT(2 + 2 * w, blockIdx.x + 256 * w);
}

constexpr int SLOT = 34816, XCH = 16384, KVT_OFF = 151296, KVP_OFF = 27648;
DEVI constexpr int slot_off(int s) { return (s & 3) == 0 ? SLOT : ((s & 3) == 1 ? 0 : (s & 3) * SLOT); }
DEVI constexpr int wq_off(int t) { return t == 0 ? 0 : 49152 + t * 27648; }

constexpr int quad_src(int quad) {
    const int pp = quad >= 192 ? 1 : 0, qi = quad - 192 * pp;
    const bool isz = qi >= 180;
    const int el = isz ? 0 : qi / 5, qd = qi - el * 5, hi = el >= 18 ? 1 : 0;
    const int dql = isz ? (qi < 189 ? qi - 180 : 0) : qd + 4 * hi;
    const int et = isz ? 0 : (el < 4 ? 0 : (el < 20 ? 1 : 2)), col = isz ? 0 : (el < 4 ? 12 + el : (el < 20 ? el - 4 : el - 20));
    const int tile = dql == 8 ? 5 : (et == 0 ? (dql >> 2) : (et == 1 ? 2 + (dql >> 2) : 4));
    return KVP_OFF + pp * 6144 + tile * 1024 + ((dql == 8 ? et : (dql & 3)) * 16 + col) * 4;
}
constexpr int quad_dst(int quad, int hf) {
    const int pp = quad >= 192 ? 1 : 0, qi = quad - 192 * pp;
    const bool isz = qi >= 180;
    const int el = isz ? 0 : qi / 5, qd = qi - el * 5, hi = el >= 18 ? 1 : 0;
    const int dql = isz ? (qi < 189 ? qi - 180 : 0) : qd + 4 * hi;
    const int dq = 9 * pp + dql, ks = dq >> 3, e = 36 * pp + el, ee = e >> 4;
    const int fr = ee == 0 ? 0 : (ee < 3 ? 2 * ee - 1 + ks : 2 * ee - 2 + ks);
    const int hz = 2 * pp + ((dql > 4 || (dql == 4 && hf)) ? 1 : 0);
    const bool dead = isz ? qi >= 189 : ((!hi && qd == 4 && hf) || (hi && qd == 0 && !hf));
    return dead ? LDS_DUMMY : KVT_OFF + (isz ? (9 + ks) * 1024 + (dq & 3) * 256 + hz * 16 : fr * 1024 + ((dq & 3) * 16 + (e & 15)) * 16) + ((dq >> 2) & 1) * 8 + hf * 4;
}
struct Tab { int v[2048]; };
constexpr Tab make_tab() {
    Tab t{};
    for (int c = 0; c < 192; ++c) {
        const int l = c & 63, q = 2 * ((c & 192) + (l >> 5) * 16 + (l & 15)) + ((l >> 4) & 1);
        t.v[4 * c] = quad_src(2 * c); t.v[4 * c + 1] = quad_src(2 * c + 1);
        t.v[4 * c + 2] = (quad_dst(q, 0) - KVT_OFF) | ((quad_dst(q, 1) - KVT_OFF) << 16); t.v[4 * c + 3] = (quad_dst(q + 64, 0) - KVT_OFF) | ((quad_dst(q + 64, 1) - KVT_OFF) << 16);
    }
    for (int q = 0; q < 576; ++q) {
        const int rho = q / 9, pc = q % 9, k = (rho & 32) + 8 * ((rho >> 3) & 3) + 4 * (rho & 1) + ((rho >> 1) & 3), m = q >> 6;
        t.v[768 + q] = k * 3456 + pc * 16 - (m & 1) * 1024;
        t.v[1344 + q] = k * 1152 + pc * 16 - (m & 3) * 1024;
    }
    return t;
}
__device__ const Tab g_tab = make_tab();
constexpr int IDX = P2_B, GW_OFF = 139264;

#define GET_TAG() do { const u32x2 t_ = lds_r64(smem + LDS_TAG); tag = (u64)(unsigned)__builtin_amdgcn_readfirstlane(t_.x) | ((u64)(unsigned)__builtin_amdgcn_readfirstlane(t_.y) << 32); } while (0)
DEVI void gemm_kv(const Params& P, lptr smem, int rb, int hg, int kw, int cg2, u64& tag, rsrc_t wsr, u64& tp) {
    const int lane = threadIdx.x & 63, lo16 = lane & 15, g = lane >> 4;
    const int p = kw & 1, ct = (kw < 3 ? kw : 0) * 64 + lane;
    STAMP_DECL; STAMP(1);
    __builtin_amdgcn_s_setprio(2);
    const int xrow = ((kw >> 1) * 64 + lo16) * 128, sw = (g ^ ((lo16 >> 1) & 7)) << 4;
    lptr xa[2] = {smem + xrow + sw, smem + xrow + (sw ^ 64)};
    const int wrow = (4 * g + (lo16 >> 2)) * 288;
    lptr wa = smem + XCH + wrow + (9 * p + (lane & 3)) * 8, wa2 = smem + XCH + wrow + (9 * p + ((lane & 3) == 3 ? 18 : 8)) * 8;
    lptr flp = smem + FL, ixp = smem + IDX + ct * 16, irp = smem + IDX + 3072 + 2304 + lane * 4;
    unsigned one = 1u;
    u32x4 sel[2];
    sel[0] = mask_frag(lo16 < 8 ? 1u << lo16 : 0u, 0, 0x3f80u); sel[1] = mask_frag(lo16 >= 8 ? 1u << (lo16 - 8) : 0u, 0, 0x3f80u);
    unsigned sto = WS_PART + (unsigned)((hg * 16 + rb) * 3072 + ct * 16);
    unsigned off = WS_PART + (unsigned)((hg * 16 + g) * 3072 + kw * 1024 + lo16 * 16);
    asm volatile("" : "+v"(flp), "+v"(one), "+v"(ixp), "+v"(irp), "+v"(sel[0]), "+v"(sel[1]), "+v"(sto), "+v"(off));
    const f32x4 z4 = {0.f, 0.f, 0.f, 0.f};
    f32x4 acc[4][5];
#pragma unroll
    for (int i = 0; i < 4; ++i)
#pragma unroll
        for (int j = 0; j < 5; ++j) { const float e = j < 2 ? 1e-6f : (j == 2 && lo16 < 12 ? 1e-6f : 0.f); acc[i][j] = f32x4{e, e, e, e}; asm volatile("" : "+v"(acc[i][j])); }
    auto rel = [&](int s) { return (s & 2) ? slot_off(s) - 2 * SLOT : slot_off(s); };
    auto rebase = [&](int dlt) {
        xa[0] += dlt; xa[1] += dlt; wa += dlt; wa2 += dlt;
        asm volatile("" : "+v"(xa[0]), "+v"(xa[1]), "+v"(wa), "+v"(wa2));
    };
    constexpr int NB = 6;
    u32x2 wl[NB], wh[NB];
    u32x4 xf[2][4];
    auto rdw = [&](int f, int t) {
        const int n = f / 5, j = f % 5;
        const int o = rel(n >> 1) + (n & 1) * 9216 + t * 4608;
        const u32x2 v = j == 2 ? lds_tr(wa2 + o) : lds_tr(wa + o + (j == 0 ? 0 : (j == 1 ? 32 : (j == 3 ? 152 : 184))));
        if (t) wh[f % NB] = v; else wl[f % NB] = v;
    };
    auto rdx = [&](int n, int r) { xf[n & 1][r] = lds_r128(xa[n & 1] + rel(n >> 1) + r * 2048); };
    rebase(0);
    BARRIER();
    STAMP(2);
    rdw(0, 0); rdw(0, 1);
#pragma unroll
    for (int r = 0; r < 4; ++r) rdx(0, r);
#pragma unroll
    for (int f = 1; f < NB - 1; ++f) { rdw(f, 0); rdw(f, 1); }
    auto kstep = [&](const int n) __attribute__((always_inline)) {
        const int s = n >> 1;
#pragma unroll
        for (int m = 0; m < 20; ++m) {
            const int j = m >> 2, r = m & 3, f = 5 * n + j, fn = f + NB - 1;
            SCHED();
            const u32x4 wv = {wl[f % NB].x, wl[f % NB].y, wh[f % NB].x, wh[f % NB].y};
            mfma16i(acc[r][j], xf[n & 1][r], wv);
            SCHED();
            if (r < 2 && fn < 90) rdw(fn, r);
            if (r == 2 && j < 4 && n < 17) rdx(n + 1, j);
            if (m == 15 && n < 17) WAIT_LGKM(12);
            if (m == 18 && !(n & 1)) {
                if (s < 8 && (s & 1)) rebase((s & 2) ? -2 * SLOT : 2 * SLOT);
                if (s == 8) WAIT_LGKM0();
                RAW_BARRIER();
                if (s == 0) { asm volatile("s_memrealtime %0\n\ts_waitcnt lgkmcnt(0)" : "=s"(tp) :: "memory"); }
                if (s == 4) STAMP(4);
            }
            if (m == 19 && n < 17) WAIT_LGKM(2);
        }
    };
    kstep(0); kstep(1); kstep(2); kstep(3); kstep(4); kstep(5); kstep(6); kstep(7); kstep(8);
    kstep(9); kstep(10); kstep(11); kstep(12); kstep(13); kstep(14); kstep(15); kstep(16); kstep(17);
    __builtin_amdgcn_s_setprio(3);
    const u32x4 ix = lds_r128(ixp);
    unsigned oo[9];
#pragma unroll
    for (int i = 0; i < 9; ++i) oo[i] = *(LAS unsigned*)(irp + i * 256);
    MFMA_DONE();
    STAMP(5);
    {
        f32x4 r[6], t0 = z4, t1 = z4, t2 = z4;
#pragma unroll
        for (int i = 0; i < 5; ++i) r[i] = z4;
#pragma unroll
        for (int u = 0; u < 2; ++u) {
            u32x4 pk[3], vb[3];
#pragma unroll
            for (int j = 0; j < 3; ++j) pk[j] = pack8(phi4(acc[2 * u][j]), phi4(acc[2 * u + 1][j]));
            vb[0] = pack8(acc[2 * u][2], acc[2 * u + 1][2]);
            if (lo16 < 12) vb[0] = u32x4{0x3f803f80u, 0x3f803f80u, 0x3f803f80u, 0x3f803f80u};
            vb[1] = pack8(acc[2 * u][3], acc[2 * u + 1][3]); vb[2] = pack8(acc[2 * u][4], acc[2 * u + 1][4]);
            t0 = mfma16(pk[2], vb[0], t0); t1 = mfma16(pk[2], vb[1], t1); t2 = mfma16(pk[2], vb[2], t2);
            r[0] = mfma16(pk[0], vb[0], r[0]); r[1] = mfma16(pk[1], vb[0], r[1]); r[2] = mfma16(pk[0], vb[1], r[2]);
            r[3] = mfma16(pk[1], vb[1], r[3]); r[4] = mfma16(pk[1], vb[2], r[4]);
        }
        r[5] = g == 0 ? t0 : (g == 1 ? t1 : t2);
        MFMA_DONE();
        SET_M0(KVP_OFF + kw * 6144);
#pragma unroll
        for (int t = 5; t >= 0; --t) {
            W_ADDTID(r[t][0], t * 1024); W_ADDTID(r[t][1], t * 1024 + 256);
            W_ADDTID(r[t][2], t * 1024 + 512); W_ADDTID(r[t][3], t * 1024 + 768);
        }
        FLAG_ADD(F_KVP);
    }
    STAMP(6);
    if (kw < 3) {
        lptr dst[4] = {smem + KVT_OFF + (ix.z & 0xffffu), smem + KVT_OFF + (ix.z >> 16), smem + KVT_OFF + (ix.w & 0xffffu), smem + KVT_OFF + (ix.w >> 16)};
        asm volatile("" : "+v"(dst[0]), "+v"(dst[1]), "+v"(dst[2]), "+v"(dst[3]));
        flag_wait<0>(smem, F_KVP, 4);
        STAMP(7);
        float pa[2][2][4];
#pragma unroll
        for (int i = 0; i < 2; ++i)
#pragma unroll
            for (int r = 0; r < 2; ++r)
#pragma unroll
                for (int k = 0; k < 4; ++k) pa[i][r][k] = lds_rf(smem + ix[i] + r * 12288 + k * 256);
        SCHED();
        u32x4 pv;
#pragma unroll
        for (int i = 0; i < 2; ++i)
#pragma unroll
            for (int k = 0; k < 2; ++k)
                pv[2 * i + k] = pk_bf16(pa[i][0][2 * k] + pa[i][1][2 * k], pa[i][0][2 * k + 1] + pa[i][1][2 * k + 1]);
        st_sc1(wsr, sto, not_empty(pv));
        SCHED();
        FLAG_ADD(F_KVR);
        STAMP(8);
        WAIT_VM(0);
        STAMP(9);
        f32x4 gs[4][2];
        auto group = [&](int i, const u32x4 (&a)[4]) {
            gs[i][0] = mfma16(sel[1], a[1], mfma16(sel[0], a[0], z4));
            gs[i][1] = mfma16(sel[1], a[3], mfma16(sel[0], a[2], z4));
        };
        {
            u32x4 a[4][4];
#pragma unroll
            for (int i = 0; i < 4; ++i)
#pragma unroll
                for (int t = 0; t < 4; ++t) a[i][t] = __builtin_amdgcn_raw_buffer_load_b128(wsr, off + t * 256, ((i + rb) & 3) * 12288, 16);
            SCHED();
            FLAG_ADD(F_SW);
#pragma unroll
            for (int i = 0; i < 4; ++i) group(i, a[i]);
        }
#pragma unroll
        for (int i = 0; i < 4; ++i) {
#pragma unroll 1
            for (unsigned spins = 0; spins < SPIN_CAP; ++spins) {
                if (!__any(__builtin_isunordered(gs[i][0][0], gs[i][1][0]))) break;
                if (spins) __builtin_amdgcn_s_sleep(RSLEEP);
                CFENCE();
                u32x4 a[4];
#pragma unroll
                for (int t = 0; t < 4; ++t) a[t] = __builtin_amdgcn_raw_buffer_load_b128(wsr, off + t * 256, ((i + rb) & 3) * 12288, 16);
                group(i, a);
                if (spins >= 32 && __all(max(max(umax4(a[0]), umax4(a[1])), max(umax4(a[2]), umax4(a[3]))) != EMPTY)) break;
            }
        }
        f32x4 sum[2];
        sum[0] = (gs[0][0] + gs[1][0]) + (gs[2][0] + gs[3][0]); sum[1] = (gs[0][1] + gs[1][1]) + (gs[2][1] + gs[3][1]);
#pragma unroll
        for (int i = 0; i < 4; ++i) lds_w32(dst[i], pk_bf16(sum[i >> 1][2 * (i & 1)], sum[i >> 1][2 * (i & 1) + 1]));
        CFENCE();
        FLAG_ADD(F_D);
        STAMP(10);
    } else {
        auto wo_chunks = [&](int c0, int c1) {
#pragma unroll 1
            for (int c = c0; c < c1; ++c) {
                const char* ws = P.wo + cg2 * 144 + c * (64 * 1152);
                const unsigned l = c < 8 ? c * 9216 : WO8;
                SET_M0(l); glds16o<0>(ws, oo[0]); glds16o<1024>(ws, oo[1]); glds16o<2048>(ws, oo[2]); glds16o<3072>(ws, oo[3]);
                SET_M0(l + 4096); glds16o<0>(ws, oo[4]); glds16o<1024>(ws, oo[5]); glds16o<2048>(ws, oo[6]); glds16o<3072>(ws, oo[7]);
                SET_M0(l + 8192); glds16o<0>(ws, oo[8]);
            }
        };
        flag_wait(smem, F_KVR, 3);
        wo_chunks(3, 9);
        WAIT_VM(0);
        FLAG_ADD(F_D);
        flag_wait(smem, F_SW, 3);
        flag_wait(smem, F_Q0, 8);
        wo_chunks(0, 3);
        WAIT_VM(0);
        FLAG_ADD(F_W0);
    }
    GET_TAG();
    __builtin_amdgcn_s_setprio(0);
    if (kw == 0) STAMP_OUT(1, blockIdx.x);
}

DEVI void gemm_q(const Params& P, lptr smem, int rb, int hg, int t8, u64& tag, rsrc_t wsr, u64& tp) {
    const int lane = threadIdx.x & 63, lw = t8 & 3;
    const bool ld = t8 < 4;
    STAMP_DECL; STAMP(1);
    __builtin_amdgcn_s_setprio(3);
    unsigned xo[4], wo_[5], qo[3];
    const int rx = hg & 3, rw = rb & 3;
    unsigned mx[4], mw[4];
    const unsigned xbase = (unsigned)((rb * 128 + 8 * lw + (lane >> 3)) * 1152 + (((lane & 7) ^ ((lane >> 4) + 4 * (lw & 1))) << 4));
#pragma unroll
    for (int i = 0; i < 4; ++i) { mx[i] = (((i + rx) & 3) * 4 + lw) * 1024; mw[i] = XCH + (((i + rw) & 3) * 4 + lw) * 1024; xo[i] = xbase + ((i + rx) & 3) * 36864; }
    const unsigned lb = lw * 4096;
    auto issue_x = [&](int s, int i0 = 0, int i1 = 4) {
#pragma unroll
        for (int i = i0; i < i1; ++i) glds16(P.x + s * 128, xo[i], slot_off(s) + mx[i]);
    };
    auto issue_w = [&](int s, int i0 = 0, int i1 = 4) {
        const char* ws = P.wqkv + s * (64 * 3456);
        if (i1 == 4 && (lw >> 1) == (s & 1)) glds16(ws, wo_[4], (unsigned)(slot_off(s) + XCH + 16384 + (lw & 1) * 1024));
#pragma unroll
        for (int i = i0; i < i1; ++i) glds16(ws, wo_[i], slot_off(s) + mw[i]);
    };
    auto issue_q = [&](int t, int i) {
        const char* ws = P.wqkv + hg * 144 + (3 * t + i) * (64 * 3456);
        if (lw == ((i + t) & 3)) glds16(ws, qo[2], (unsigned)(wq_off(t) + i * 9216 + 8192));
        SET_M0(wq_off(t) + i * 9216 + (lb >> 1));
        glds16o<0>(ws, qo[0]); glds16o<1024>(ws, qo[1]);
    };
    auto w_off = [&](int i) {
        const unsigned q = 64 * (i < 4 ? ((i + rw) & 3) * 4 + lw : 16 + (lw & 1)) + lane, rho = (q * 3641u) >> 16, pc = q - rho * 18;
        const unsigned k = (rho & 35u) + 8 * ((rho >> 2) & 3) + 4 * ((rho >> 4) & 1);
        wo_[i] = k * 3456 + (1152 + hg * 144) + pc * 16 + (pc < 9 ? 0u : 1008u);
    };
    if (ld) {
        issue_x(0, 0, 1); issue_x(1, 0, 1);
        SCHED();
        w_off(0);
        issue_w(0, 0, 1); issue_w(1, 0, 1);
        SCHED();
#pragma unroll
        for (int i = 1; i < 5; ++i) w_off(i);
        if (lw == 1) { glds16(P.gw, (unsigned)(lane * 16), GW_OFF); if (lane < 8) glds16(P.gw + 1024, (unsigned)(lane * 16), GW_OFF + 1024); }
        issue_x(0, 1, 4); issue_w(0, 1, 4); issue_w(1, 1, 4); issue_x(1, 1, 4);
        SET_M0(IDX + (lb >> 1));
        glds16o<0>((const char*)&g_tab + (lb >> 1), (unsigned)(lane * 16)); glds16o<1024>((const char*)&g_tab + (lb >> 1), (unsigned)(lane * 16));
    } else {
        if (lw == 0 && lane < 16) lds_w32(smem + FL + lane * 4, 0u);
#pragma unroll
        for (int i = 0; i < 3; ++i) lds_w128(smem + KVT_OFF + i * 4096 + lw * 1024 + lane * 16, u32x4{0u, 0u, 0u, 0u});
    }
    const int lo16 = lane & 15, g = lane >> 4;
    const int sw = (g ^ ((lo16 >> 1) & 7)) << 4;
    lptr xa[2] = {smem + (16 * t8 + lo16) * 128 + sw, smem + (16 * t8 + lo16) * 128 + (sw ^ 64)}, xb[2] = {xa[0] + 2 * SLOT, xa[1] + 2 * SLOT};
    lptr flp = smem + FL, iqp = smem + IDX + 3072 + lane * 4, gwp = smem + GW_OFF + g * 16;
    const int qrow = (2 * (4 * g + (lo16 >> 2))) * 144;
    lptr qw = smem + qrow + (lane & 3) * 8, qw4 = smem + qrow + (16 + (lane & 1)) * 8 + (lane & 2) * 2304;
    lptr qwh = qw + wq_off(1), qw4h = qw4 + wq_off(1), kfp = smem + KVT_OFF + lane * 16, kzp = smem + KVT_OFF + 9216 + (lane & 51) * 16;
    unsigned one = 1u, ob0 = WS_O + (unsigned)(((rb * 8 + t8) * 72 + hg * 9 + 2 * (g & 1) + (g >> 1)) * 256 + lo16 * 16);
    asm volatile("" : "+v"(flp), "+v"(one), "+v"(iqp), "+v"(gwp), "+v"(xa[0]), "+v"(xa[1]), "+v"(xb[0]), "+v"(xb[1]));
    asm volatile("" : "+v"(qw), "+v"(qw4), "+v"(qwh), "+v"(qw4h), "+v"(kfp), "+v"(kzp), "+v"(ob0));
    asm volatile(".subsection 2\n.Lkf_code_end:\n\t.fill 256, 4, 0xbf800000\n\t.subsection 0");
    const f32x4 z4 = {0.f, 0.f, 0.f, 0.f};
    f32x4 gacc = z4, qa[6];
#pragma unroll
    for (int j = 0; j < 6; ++j) { const float e = j < 5 ? 1e-6f : 0.f; qa[j] = f32x4{e, e, e, e}; asm volatile("" : "+v"(qa[j])); }
    u32x4 xk[18];
    u64 pf = 0;
    WAIT_VM(8); BARRIER();
    if (ld) { issue_w(2); issue_x(2); if (LA3) { issue_w(3); issue_x(3); } }
#pragma unroll
    for (int s = 0; s < 9; ++s) {
#pragma unroll
        for (int k = 0; k < 2; ++k) xk[2 * s + k] = lds_r128(((s & 2) ? xb[k] - 2 * SLOT : xa[k]) + slot_off(s));
        if (t8 == hg) {
            gacc = mfma16(lds_r128(gwp + s * 128), xk[2 * s], gacc);
            gacc = mfma16(lds_r128(gwp + s * 128 + 64), xk[2 * s + 1], gacc);
        }
        if (t8 == 4) {
            if (s == 1) { tag = *(const __attribute__((address_space(4))) u64*)(P.ws + WS_CTL); PIN_TAG(); if (lane == 0) lds_w64(smem + LDS_TAG, u32x2{(unsigned)tag, (unsigned)(tag >> 32)}); }
            if (s == 4) asm volatile("global_load_dwordx2 %0, %1, %2 sc1" : "=v"(pf) : "v"((lane < 5 ? lane : 0) * 128), "s"(P.ws + WS_PFL) : "memory");
            if (s == 7) {
                asm volatile("s_waitcnt vmcnt(0)" : "+v"(pf) :: "memory");
                if (!__all(pf == tag)) poll_flags(P, WS_PFL, 5, tag, lane);
            }
        }
        if (LA3) { if (s < 6) WAIT_VM(17); else if (s == 6) WAIT_VM(14); else WAIT_VM(12); }
        else if (s < 7) WAIT_VM(8); else WAIT_VM(6);
        BARRIER();
        if (ld) {
            const int sn = s + 3 + LA3;
            if (sn < 9) { issue_w(sn); issue_x(sn); } else if (sn < 12) { issue_q(sn - 9, 0); issue_q(sn - 9, 1); issue_q(sn - 9, 2); }
            if (s == 3) { qo[0] = *(LAS unsigned*)(iqp + lw * 512); qo[1] = *(LAS unsigned*)(iqp + lw * 512 + 256); qo[2] = *(LAS unsigned*)(iqp + 2048); CFENCE(); }
        }
    }
    __builtin_amdgcn_s_setprio(0);
    STAMP(5);
    if (!ld) {
        const unsigned co = WS_O + (unsigned)(((rb * 8 + 2 * lw) * 72 + hg * 9) * 256 + lane * 16);
#pragma unroll
        for (int i = 0; i < 5; ++i)
            if (i < 4 || lane < 32) st_sc1(wsr, i < 4 ? co + (i >> 1) * 18432 + (i & 1) * 1024 : co + 2048 + (lane >> 4) * (18432 - 256), u32x4{EMPTY, EMPTY, EMPTY, EMPTY});
    }
    GET_TAG();
    if (t8 == 4 && lane == 0) tag_post(P, tag);
    if (t8 == hg) {
        const float gate = rbf(__builtin_amdgcn_rcpf(1.f + __expf(-rbf(gacc[0]))));
        if (lane < 16) __hip_atomic_store((GAS float*)(P.ws + WS_GATE) + (rb * 128 + hg * 16 + lane), gate, RLX_AGENT);
    }
    auto peek = [&](int f) { return __hip_atomic_load((LAS unsigned*)(flp + f), __ATOMIC_RELAXED, __HIP_MEMORY_SCOPE_WORKGROUP); };
    auto rdq = [&](int ks, int j) {
        const int o = (ks < 6 ? ks : ks - 6) * 4608;
        return j < 4 ? lds_trfrag((ks < 6 ? qw : qwh) + o + 32 * j, 144) : lds_trfrag((ks < 6 ? qw4 : qw4h) + o, 144);
    };
    u32x4 wq[2][4], w4[2];
#pragma unroll
    for (int i = 0; i < 2; ++i) {
#pragma unroll
        for (int j = 0; j < 4; ++j) wq[i][j] = rdq(i, j);
        if (i == 0) w4[0] = rdq(0, 4);
    }
    unsigned seenq = 0;
#pragma unroll
    for (int ks = 0; ks < 18; ++ks) {
        if (ks == 4) {
            SCHED();
            WAIT_VM(0); FLAG_ADD(F_Q0);
            seenq = peek(F_Q0); NEED(seenq, F_Q0, 8);
        }
        if (ks == QH) { SCHED(); seenq = peek(F_KVR); NEED(seenq, F_KVR, 3); STAMP(6); }
        if (ks == 16) { SCHED(); FLAG_ADD(F_QD); }
#pragma unroll
        for (int j = 0; j < 5; ++j) {
            SCHED();
            if (j == 0 && ks < 17) { if (ks & 1) WAIT_LGKM(10); else WAIT_LGKM(8); }
            if (j < 4) mfma16i(qa[j], wq[ks & 1][j], xk[ks]); else mfma16i(qa[4 + (ks & 1)], w4[(ks >> 1) & 1], xk[ks]);
            SCHED();
            if (ks + 2 < 18) { if (j < 4) wq[ks & 1][j] = rdq(ks + 2, j); else if (!(ks & 1)) w4[((ks >> 1) + 1) & 1] = rdq(ks + 2, 4); }
        }
    }
    __builtin_amdgcn_s_setprio(2);
    MFMA_DONE();
#pragma unroll
    for (int i = 0; i < 4; ++i) {
        const auto sw2 = __builtin_amdgcn_permlane32_swap(__float_as_uint(qa[5][i]), __float_as_uint(qa[5][i]), false, false);
        qa[4][i] += __uint_as_float(sw2[1]);
    }
    u32x4 b0 = pack8(phi4(qa[0]), phi4(qa[1])), b1 = pack8(phi4(qa[2]), phi4(qa[3])), b2 = pack8(phi4(qa[4]), z4);
    asm volatile("" : "+v"(b0), "+v"(b1), "+v"(b2));
    STAMP(7);
    flag_wait<1>(smem, F_D, 4);
        if (t8 == 7 && lane == 0) __hip_atomic_store((GAS u64*)(P.ws + WS_HINT + (rb * 8 + hg) * 128), tag, RLX_AGENT);
    STAMP(8);
    {
        u32x4 kf[12];
#pragma unroll
        for (int i = 0; i < 12; ++i) { kf[i] = lds_r128(i < 3 ? kzp + i * 1024 : kfp + (i - 3) * 1024); SCHED(); }
        SCHED();
        const f32x4 den = mfma16(kf[2], b2, mfma16(kf[1], b1, mfma16(kf[0], b0, f32x4{1e-6f, 1e-6f, 1e-6f, 1e-6f})));
        f32x4 o[5];
        o[0] = mfma16(kf[3], b0, z4);
        o[1] = mfma16(kf[5], b1, mfma16(kf[4], b0, z4));
        o[2] = mfma16(kf[7], b1, mfma16(kf[6], b0, z4));
        o[3] = mfma16(kf[9], b2, mfma16(kf[8], b1, z4));
        o[4] = mfma16(kf[11], b2, mfma16(kf[10], b1, z4));
        f32x4 r;
#pragma unroll
        for (int i = 0; i < 4; ++i) r[i] = __builtin_amdgcn_rcpf(den[i]);
        const float r1a = g == 0 ? r[0] : r[1], r2 = g == 0 ? r[1] : r[2], r3a = g <= 1 ? r[2] : r[3], r3b = g == 0 ? r[2] : r[3];
        u32x2 ob[5];
        ob[0] = u32x2{pk_bf16(o[0][0] * r[0], o[0][1] * r[0]), pk_bf16(o[0][2] * r[0], o[0][3] * r[0])};
        ob[1] = u32x2{pk_bf16(o[1][0] * r1a, o[1][1] * r1a), pk_bf16(o[1][2] * r[1], o[1][3] * r[1])};
        ob[2] = u32x2{pk_bf16(o[2][0] * r2, o[2][1] * r2), pk_bf16(o[2][2] * r2, o[2][3] * r2)};
        ob[3] = u32x2{pk_bf16(o[3][0] * r3a, o[3][1] * r3a), pk_bf16(o[3][2] * r3b, o[3][3] * r3b)};
        ob[4] = u32x2{pk_bf16(o[4][0] * r[3], o[4][1] * r[3]), pk_bf16(o[4][2] * r[3], o[4][3] * r[3])};
        SCHED();
        SCHED();
#pragma unroll
        for (int t = 0; t < 3; ++t) {
            const u32x2 a = ob[2 * t], b = ob[t < 2 ? 2 * t + 1 : 4];
            const auto sx = __builtin_amdgcn_permlane16_swap(a.x, b.x, false, false), sy = __builtin_amdgcn_permlane16_swap(a.y, b.y, false, false);
            if (t < 2 || g == 0) st_sc1(wsr, ob0 + 1024 * t, not_empty(u32x4{sx[0], sy[0], sx[1], sy[1]}));
        }
    }
    __builtin_amdgcn_s_setprio(0);
    STAMP(9);
    if (t8 == 0) STAMP_OUT(5, blockIdx.x + 256);
    if (t8 == 7) STAMP_OUT(7, blockIdx.x + 768);
}

DEVI void phase2(const Params& P, lptr smem, int rt64, int cg, bool gemm, u64 tag, rsrc_t wsr, u64& tp) {
    const int lane = threadIdx.x & 63, w0 = __builtin_amdgcn_readfirstlane(threadIdx.x >> 6);
    const int w = gemm ? (w0 + 8) % 12 : w0, tid = threadIdx.x, bt = tid - 192;
    const int lo16 = lane & 15, g = lane >> 4;
    const int rt = w & 3, kt = w >> 2;
    STAMP_DECL; STAMP(1);
    constexpr int pb = P2_B;
    const int pa = gemm ? P2_ACC : 82944;
    if (gemm && w < 4) flag_wait(smem, F_W0, 1);
    STAMP(2);
    const int qrow = (2 * (4 * g + (lo16 >> 2))) * 144;
    lptr qw = smem + qrow + (lane & 3) * 8, qw4 = smem + qrow + (16 + (lane & 1)) * 8;
    int wofs[3], oofs[3];
#pragma unroll
    for (int u = 0; u < 3; ++u) {
        const int c = kt * 3 + (u + cg) % 3;
        wofs[u] = (c < 8 || !gemm) ? c * 9216 : WO8; oofs[u] = c * 2048;
    }
    auto rdo = [&](int i, int j) { return j < 4 ? lds_trfrag(qw + wofs[i >> 1] + (i & 1) * 4608 + 32 * j, 144) : lds_trfrag(qw4 + wofs[i >> 1] + (i & 1) * 4608, 144); };
    const unsigned ooff = WS_O + (unsigned)((rt64 * 4 + rt) * 18432 + lane * 16);
    auto ldo = [&](int i) { return __builtin_amdgcn_raw_buffer_load_b128(wsr, ooff + (i & 1) * 1024, oofs[i >> 1], 16); };
    u32x4 wf[6][5];
#pragma unroll
    for (int i = 0; i < 6; ++i)
#pragma unroll
        for (int j = 0; j < 5; ++j) wf[i][j] = rdo(i, j);
    const int r0 = tid < 576 ? tid / 9 : 0, c0 = tid - r0 * 9;
    unsigned boff = WS_B + (unsigned)((cg * 72 + (bt >> 3)) * 4096 + (rt64 >> 3) * 1024 + (((bt >> 2) & 1) * 32 + ((rt64 >> 2) & 1) * 16 + (rt64 & 3) * 4 + (bt & 3)) * 16);
    lptr bp = smem + pb + ((bt & 3) * 2 + ((bt >> 2) & 1)) * 1168 + (bt >> 3) * 2;
    unsigned goff = WS_GATE + (unsigned)((rt64 * 64 + r0) * 4);
    lptr ap = smem + pa + kt * (64 * P2_AP) + (rt * 16 + lo16) * P2_AP + (g & 1) * 144 + (g >> 1) * 16;
    lptr fa = smem + pa + r0 * P2_AP + c0 * 16, fb = smem + pb + (tid + (r0 >> 3)) * 16;
    unsigned fo = (unsigned)((rt64 * 64 + r0) * 1152 + cg * 144 + c0 * 16);
    asm volatile("" : "+v"(boff), "+v"(goff), "+v"(bp), "+v"(ap), "+v"(fa), "+v"(fb), "+v"(fo));
    STAMP(3);
    if (w == 3) {
        const int a0 = cg * 72, g0 = (a0 >> 6) * 11 + (a0 & 63) / 6, a1 = a0 + 71, g1 = (a1 >> 6) * 11 + (a1 & 63) / 6;
        poll_addr<OSLEEP>(P, lane < 8 ? WS_HINT + ((rt64 >> 1) * 8 + lane) * 128 : WS_BFL + min(g0 + lane - 8, g1) * 128, tag);
        if (gemm) flag_wait<1>(smem, F_QD, 8);
        flag_set(F_H, 1);
    } else flag_wait<1>(smem, F_H, 1);
    STAMP(4);
    u32x4 bv; unsigned gv;
    auto b_put = [&]() {
#pragma unroll
        for (int q = 0; q < 4; ++q) { lds_w16(bp + (2 * q) * 144, bv[q]); lds_w16(bp + (2 * q + 1) * 144, bv[q] >> 16); }
    };
    bool b_todo = false;
    if (w0 >= 3) bv = ld_sc1(wsr, boff);
    f32x4 acc[5];
    {
        u32x4 of[6];
#pragma unroll
        for (int i = 0; i < 6; ++i) of[i] = ldo(i);
        gv = __builtin_amdgcn_raw_buffer_load_b32(wsr, goff, 0, 16);
        SCHED();
        if (w0 >= 3) { if (__all(umax4(bv) != EMPTY)) b_put(); else b_todo = true; }
        SCHED();
#pragma unroll
        for (int j = 0; j < 5; ++j) {
            acc[j] = f32x4{0.f, 0.f, 0.f, 0.f};
#pragma unroll
            for (int i = 0; i < 6; ++i) {
                acc[j] = mfma16(wf[i][j], of[i], acc[j]);
                if (i == 3 && j > 0) { SCHED(); lds_w128(ap + (j - 1) * 32, __builtin_bit_cast(u32x4, acc[j - 1])); SCHED(); }
            }
        }
        if (g < 2) lds_w128(ap + 128, __builtin_bit_cast(u32x4, acc[4]));
    }
    if (!__all(acc[4][0] == acc[4][0])) {
#pragma unroll
        for (int j = 0; j < 5; ++j) acc[j] = f32x4{0.f, 0.f, 0.f, 0.f};
        u32x4 of[6];
#pragma unroll 1
        for (unsigned spins = 0; spins < SPIN_CAP; ++spins) {
            CFENCE();
            unsigned m = 0;
#pragma unroll
            for (int i = 0; i < 6; ++i) of[i] = ldo(i);
#pragma unroll
            for (int i = 0; i < 6; ++i) m = max(m, umax4(of[i]));
            if (__all(m != EMPTY)) break;
        }
#pragma unroll
        for (int i = 0; i < 6; ++i)
#pragma unroll
            for (int j = 0; j < 5; ++j) acc[j] = mfma16(rdo(i, j), of[i], acc[j]);
#pragma unroll
        for (int j = 0; j < 5; ++j) if (j < 4 || g < 2) lds_w128(ap + j * 32, __builtin_bit_cast(u32x4, acc[j]));
    }
    if (b_todo) {
#pragma unroll 1
        for (unsigned spins = 0; spins < SPIN_CAP; ++spins) {
            CFENCE();
            bv = ld_sc1(wsr, boff);
            if (__all(umax4(bv) != EMPTY)) break;
            __builtin_amdgcn_s_sleep(2);
        }
        b_put();
    }
    const float gt = __uint_as_float(gv);
    float og = rbf(1.f - gt);
    asm volatile("" : "+v"(og));
    STAMP(5);
    BARRIER();
    STAMP(6);
    if (w0 < 9) {
        lptr a = fa;
        const f32x4 s0 = __builtin_bit_cast(f32x4, lds_r128(a)) + __builtin_bit_cast(f32x4, lds_r128(a + 64 * P2_AP)) + __builtin_bit_cast(f32x4, lds_r128(a + 128 * P2_AP));
        const f32x4 s1 = __builtin_bit_cast(f32x4, lds_r128(a + 144)) + __builtin_bit_cast(f32x4, lds_r128(a + 64 * P2_AP + 144)) + __builtin_bit_cast(f32x4, lds_r128(a + 128 * P2_AP + 144));
        const u32x4 bb = lds_r128(fb);
        u32x4 o;
#pragma unroll
        for (int i = 0; i < 4; ++i) {
            const float a0 = i < 2 ? s0[2 * i] : s1[2 * i - 4], a1 = i < 2 ? s0[2 * i + 1] : s1[2 * i - 3];
            o[i] = pk_bf16(gt * a0 + og * bf2f(bb[i]), gt * a1 + og * bfhi(bb[i]));
        }
        __builtin_amdgcn_raw_buffer_store_b128(o, __builtin_amdgcn_make_buffer_rsrc(P.out, 0, SEQ * DM * 2, 0x00020000), fo, 0, OUTAUX);
    }
    STAMP(7);
    if (w == 4) STAMP_OUT(blockIdx.x < 128 ? 3 : 6, blockIdx.x + 512);
}

DEVI void helper_role(const Params& P, lptr smem, int xcd, int li, u64& tag, rsrc_t wsr, u64& tp) {
    const int tid = threadIdx.x, lane = tid & 63, w = __builtin_amdgcn_readfirstlane(tid >> 6);
    STAMP_DECL; STAMP(1);
    if (li >= 13) {
        if (HD) __builtin_amdgcn_s_sleep(HD);
#pragma unroll 1
        for (int n = 12 * NTW + (li - 13) + 3 * w; n < 171; n += 36) touch_group(P, xcd, n, lane);
    }
    if (li == 12) {
#pragma unroll
        for (int i = 0; i < 7; ++i) {
            const int u = (i * 5 + xcd - 3) * 12 + w;
            if (u < 384) st_sc1(wsr, WS_PART + (unsigned)(u * 1024 + lane * 16), u32x4{EMPTY, EMPTY, EMPTY, EMPTY});
        }
        WAIT_VM(0);
    }
    PIN_TAG();
    BARRIER();
    if (tid == 0) {
        if (li == 12) __hip_atomic_store((GAS u64*)(P.ws + WS_PFL + (xcd - 3) * 128), tag, RLX_AGENT);
        tag_post(P, tag);
    }
    if (li >= 13) {
#pragma unroll 1
        for (int n = 171 + (li - 13) + 3 * w; n < 216; n += 36) touch_group(P, xcd, n, lane);
    }
    STAMP(2);
    if (w == 11) {
        STAMP_OUT(8, blockIdx.x);
        if (blockIdx.x == 255) {
            const GAS u64* f = (const GAS u64*)(P.ws + WS_ARR) + lane;
#pragma unroll 1
            for (unsigned spins = 0; spins < SPIN_CAP; ++spins) {
                const bool ok = __hip_atomic_load(f, RLX_AGENT) == tag && __hip_atomic_load(f + 64, RLX_AGENT) == tag &&
                                __hip_atomic_load(f + 128, RLX_AGENT) == tag && __hip_atomic_load(f + 192, RLX_AGENT) == tag;
                if (__all(ok)) break;
                __builtin_amdgcn_s_sleep(8);
            }
            if (lane == 0) __hip_atomic_store((GAS u64*)(P.ws + WS_CTL), tag, RLX_AGENT);
        }
    }
}

__global__ __launch_bounds__(768, 3) void k_fused(Params P) {
    __shared__ __attribute__((aligned(16))) char smem_[LDS_BYTES];
    lptr smem = (lptr)smem_;
    const int b = blockIdx.x, xcd = b & 7, idx = b >> 3;
    const int lane = threadIdx.x & 63, w = __builtin_amdgcn_readfirstlane(threadIdx.x >> 6);
    const rsrc_t wsr = __builtin_amdgcn_make_buffer_rsrc(P.ws, 0, (int)WS_END, 0x00020000);
    const int rt64 = xcd * 4 + (idx & 3), cg2 = idx >> 2;
    if (idx >= CPF && w < 6) {
        const char* pc; unsigned len;
        asm volatile("s_getpc_b64 %0\n.Lkf_code_begin:\n\ts_mov_b32 %1, .Lkf_code_end-.Lkf_code_begin" : "=s"(pc), "=s"(len));
        touch(pc, min((unsigned)(threadIdx.x * 128), len));
    }
    u64 tag; u64 tp = 0;
    if (__builtin_expect(idx < 16, 1)) {
        const int rbl = idx & 3, hgl = idx >> 2, rb = (xcd >> 1) * 4 + rbl, hg = (xcd & 1) * 4 + hgl;
        if (__builtin_expect((w & 12) != 4, 1)) gemm_q(P, smem, rb, hg, w < 4 ? w : w - 4, tag, wsr, tp);
        else gemm_kv(P, smem, rb, hg, w - 4, cg2, tag, wsr, tp);
    } else {
        tag = *(const __attribute__((address_space(4))) u64*)(P.ws + WS_CTL);
        const int li = idx - 16, gid = li < 11 ? xcd * 11 + li : 88 + (li - 11) * 8 + xcd;
        if (gid < 99) ssm_role(P, smem, xcd, li, gid, cg2, tag, wsr, tp);
        else {
            helper_role(P, smem, xcd, li, tag, wsr, tp);
            if (w < 9) { wo_issue<0x1ff>(P, cg2, w, 9, lane); WAIT_VM(0); }
            if (w == 10 && lane < 16) lds_w32(smem + FL + lane * 4, 0u);
            BARRIER();
        }
    }
    phase2(P, smem, rt64, cg2, idx < 16, tag, wsr, tp);
    if (tp) while (__builtin_amdgcn_s_memrealtime() - tp < 5000) __builtin_amdgcn_s_sleep(1);
}

extern "C" void kernel_launch(void* const* d_in, const int* in_sizes, int n_in, void* d_out, int out_size, void* d_ws, size_t ws_size,
                              hipStream_t stream) {
    Params P;
    P.x = (const char*)d_in[0]; P.wqkv = (const char*)d_in[1]; P.wo = (const char*)d_in[2];
    P.logA = (const char*)d_in[4]; P.aim = (const char*)d_in[5]; P.cre = (const char*)d_in[6]; P.cim = (const char*)d_in[7];
    P.logdt = (const char*)d_in[8]; P.dskip = (const char*)d_in[9]; P.gw = (const char*)d_in[10];
    P.out = (char*)d_out; P.ws = (char*)d_ws;
    k_fused<<<256, 768, 0, stream>>>(P);
}
```

```cpp
#include <hip/hip_runtime.h>
#include <stdint.h>

#define GAS __attribute__((address_space(1)))
#define LAS __attribute__((address_space(3)))
#define DEVI __device__ __forceinline__
typedef unsigned short u16;
typedef unsigned long long u64;
typedef __attribute__((ext_vector_type(2))) unsigned u32x2;
typedef __attribute__((ext_vector_type(3))) unsigned u32x3;
typedef __attribute__((ext_vector_type(4))) unsigned u32x4;
typedef __attribute__((ext_vector_type(4))) short s16x4;
typedef __attribute__((ext_vector_type(2))) float f32x2;
typedef __attribute__((ext_vector_type(4))) float f32x4;
typedef __attribute__((ext_vector_type(16))) float f32x16;
typedef __attribute__((ext_vector_type(2))) __bf16 bf16x2;
typedef __attribute__((ext_vector_type(8))) __bf16 bf16x8;
typedef __attribute__((ext_vector_type(2))) _Float16 f16x2;
typedef __attribute__((ext_vector_type(8))) _Float16 f16x8;
typedef LAS char* lptr;

#define PREF 1
#define ROT 0
#define FSLEEP 2
#define NSLEEP "3"
#define LDRIFT 0
#define PSLEEP 6
#define CSLEEP 1
#define OSLEEP 2
#define RSLEEP 3
#define HOLD 1
#define GROT ((w + li) & 3)
#define NTW 8
#define OUTAUX 17
#define TEARLY 1
#define HDELAY 0
#define CPF 16
#define LA3 0
#define TD1 0
#define TD2 0
#define TW2 3
#define HD 0
#define QH 4
#define WOA 0x00f
#define WOT 48
constexpr int SEQ = 2048, DM = 576;

constexpr unsigned WS_CTL = 0;
constexpr unsigned WS_ARR = 256;
constexpr unsigned WS_BFL = 4096;
constexpr unsigned WS_HINT = 20480;
constexpr unsigned WS_GATE = 36864;
constexpr unsigned WS_PFL = 49152;
constexpr unsigned WS_PART = 65536;
constexpr unsigned WS_O = WS_PART + 8 * 16 * 3072;
constexpr unsigned WS_B = WS_O + SEQ * DM * 2;
constexpr unsigned WS_END = WS_B + SEQ * DM * 2;
constexpr unsigned EMPTY = 0xffffffffu;

constexpr size_t WS_STAMP = 16u << 20;
#define STAMP_DECL
#define STAMP(i)
#define CSTAMP(i)
#define STAMP_OUT(role, slot)

struct Params {
    const char* x; const char* wqkv; const char* wo; const char* logA; const char* aim; const char* cre; const char* cim;
    const char* logdt; const char* dskip; const char* gw; char* out; char* ws;
};

constexpr int LDS_BYTES = 163840, FL = 163584, LDS_TAG = FL + 64, LDS_DUMMY = FL + 128, LDS_JUNK = 161280;
enum { F_SW = 0, F_LANDED = 4, F_W1 = 20, F_B = 28, F_H = 24, F_KVP = 36, F_KVR = 40, F_Q0 = 44, F_QD = 48, F_D = 52, F_C1 = 56, F_W0 = 60 };
constexpr int WO8 = 132096, P2_ACC = 73728, P2_AP = 304, P2_B = 141312, P2_G = 150528;

DEVI float bf2f(unsigned h) { return __uint_as_float(h << 16); }
DEVI float bfhi(unsigned h) { return __uint_as_float(h & 0xffff0000u); }
DEVI unsigned pk_bf16(float a, float b) { return __builtin_bit_cast(unsigned, __builtin_convertvector(f32x2{a, b}, bf16x2)); }
DEVI unsigned pk_f16(f32x2 v) { return __builtin_bit_cast(unsigned, __builtin_convertvector(v, f16x2)); }
DEVI float rbf(float a) { return bf2f(pk_bf16(a, a)); }
DEVI f32x4 mfma16(u32x4 a, u32x4 b, f32x4 c) {
    return __builtin_amdgcn_mfma_f32_16x16x32_bf16(__builtin_bit_cast(bf16x8, a), __builtin_bit_cast(bf16x8, b), c, 0, 0, 0);
}
DEVI void mfma16i(f32x4& c, u32x4 a, u32x4 b) { asm volatile("v_mfma_f32_16x16x32_bf16 %0, %1, %2, %0" : "+v"(c) : "v"(a), "v"(b)); }
#define MFMA_DONE() do { __builtin_amdgcn_sched_barrier(0); asm volatile("s_nop 7\n\ts_nop 1" ::: "memory"); __builtin_amdgcn_sched_barrier(0); } while (0)
DEVI u32x4 lds_r128(lptr p) { return *(LAS u32x4*)p; }
DEVI u32x2 lds_r64(lptr p) { return *(LAS u32x2*)p; }
DEVI float lds_rf(lptr p) { return *(LAS float*)p; }
DEVI void lds_w128(lptr p, u32x4 v) { *(LAS u32x4*)p = v; }
DEVI void lds_w64(lptr p, u32x2 v) { *(LAS u32x2*)p = v; }
DEVI void lds_w32(lptr p, unsigned v) { *(LAS unsigned*)p = v; }
DEVI void lds_w16(lptr p, unsigned v) { *(LAS u16*)p = (u16)v; }
DEVI u32x2 lds_tr(lptr p) { return __builtin_bit_cast(u32x2, __builtin_amdgcn_ds_read_tr16_b64_v4i16((LAS s16x4*)p)); }
DEVI u32x4 lds_trfrag(lptr p, int off2) { u32x2 a = lds_tr(p), b = lds_tr(p + off2); return u32x4{a.x, a.y, b.x, b.y}; }
template <int OFF> DEVI void glds16o(const char* sbase, unsigned voff) {
    asm volatile("global_load_lds_dwordx4 %0, %1 offset:%2" :: "v"(voff), "s"(sbase), "i"(OFF) : "memory");
}
DEVI void glds16(const char* sbase, unsigned voff, unsigned l) {
    asm volatile("s_mov_b32 m0, %2\n\ts_nop 0\n\tglobal_load_lds_dwordx4 %0, %1" :: "v"(voff), "s"(sbase), "s"(l) : "memory");
}
#define SET_M0(v) asm volatile("s_mov_b32 m0, %0\n\ts_nop 0" :: "s"(v) : "memory")
#define W_ADDTID(v, off) asm volatile("ds_write_addtid_b32 %0 offset:%1" :: "v"(v), "i"(off) : "memory")
#define CFENCE() asm volatile("" ::: "memory")
#define SCHED() __builtin_amdgcn_sched_barrier(0)
#define WAIT_VM(n) asm volatile("s_waitcnt vmcnt(" #n ")" ::: "memory")
#define WAIT_LGKM(n) __builtin_amdgcn_s_waitcnt(0xC07F | ((n) << 8))
#define WAIT_LGKM0() asm volatile("s_waitcnt lgkmcnt(0)" ::: "memory")
#define BARRIER() do { SCHED(); asm volatile("s_waitcnt lgkmcnt(0)\n\ts_barrier" ::: "memory"); SCHED(); } while (0)
#define RAW_BARRIER() do { SCHED(); asm volatile("s_barrier" ::: "memory"); SCHED(); } while (0)
#define RLX_AGENT __ATOMIC_RELAXED, __HIP_MEMORY_SCOPE_AGENT
constexpr unsigned SPIN_CAP = 1u << 16;
typedef __amdgpu_buffer_rsrc_t rsrc_t;
DEVI u32x4 ld_sc1(rsrc_t r, unsigned off) { return __builtin_amdgcn_raw_buffer_load_b128(r, off, 0, 16); }
DEVI void st_sc1(rsrc_t r, unsigned off, u32x4 v) { __builtin_amdgcn_raw_buffer_store_b128(v, r, off, 0, 16); }

DEVI u32x4 pack8(f32x4 a, f32x4 b) { return u32x4{pk_bf16(a[0], a[1]), pk_bf16(a[2], a[3]), pk_bf16(b[0], b[1]), pk_bf16(b[2], b[3])}; }
DEVI float phi(float t) {
    const float e = __builtin_amdgcn_fmed3f(__builtin_amdgcn_exp2f(t * 1.44269504089f), 0.f, 1.f);
    return __int_as_float(max(__float_as_int(t), 0x358637bd)) + e;
}
DEVI f32x4 phi4(f32x4 v) {
    f32x2 a, b;
    const f32x2 c = {1.44269504089f, 1.44269504089f};
    asm("v_pk_mul_f32 %0, %2, %4\n\tv_pk_mul_f32 %1, %3, %4" : "=&v"(a), "=&v"(b) : "v"(f32x2{v[0], v[1]}), "v"(f32x2{v[2], v[3]}), "s"(c));
    f32x4 r;
#pragma unroll
    for (int i = 0; i < 4; ++i)
        r[i] = __int_as_float(max(__float_as_int(v[i]), 0x358637bd)) + __builtin_amdgcn_fmed3f(__builtin_amdgcn_exp2f(i < 2 ? a[i] : b[i - 2]), 0.f, 1.f);
    return r;
}
DEVI unsigned umax4(u32x4 a) { return max(max(a.x, a.y), max(a.z, a.w)); }
DEVI u32x4 not_empty(u32x4 a) { return a; }
DEVI void tag_post(const Params& P, u64 tag) { __hip_atomic_store((GAS u64*)(P.ws + WS_ARR) + blockIdx.x, tag, RLX_AGENT); }
#define PIN_TAG() do { asm volatile("" : "+s"(tag)); tag += 1ull; } while (0)
template <int SL = PSLEEP>
DEVI void poll_addr(const Params& P, unsigned off, u64 tag) {
    const GAS u64* f = (const GAS u64*)(P.ws + off);
#pragma unroll 1
    for (unsigned spins = 0; spins < SPIN_CAP; ++spins) {
        if (__all(__hip_atomic_load(f, RLX_AGENT) == tag)) break;
        __builtin_amdgcn_s_sleep(SL);
    }
}
template <int SL = PSLEEP>
DEVI void poll_flags(const Params& P, unsigned off, int n, u64 tag, int lane) { poll_addr<SL>(P, off + (lane < n ? lane : 0) * 128, tag); }
DEVI unsigned flag_get(lptr smem, int f) { return __builtin_amdgcn_readfirstlane(__hip_atomic_load((LAS unsigned*)(smem + FL + f), __ATOMIC_RELAXED, __HIP_MEMORY_SCOPE_WORKGROUP)); }
template <int SL = FSLEEP>
DEVI void flag_wait(lptr smem, int f, unsigned v) {
#pragma unroll 1
    for (unsigned spins = 0; spins < (SPIN_CAP << 4); ++spins) { if (flag_get(smem, f) >= v) break; if (SL) __builtin_amdgcn_s_sleep(SL); }
    CFENCE();
}
DEVI void flag_add(int f) { asm volatile("s_mov_b64 exec, 1\n\tds_add_u32 %0, %1\n\ts_mov_b64 exec, -1" :: "v"(FL + f), "v"(1u) : "memory"); }
#define FLAG_ADD(f) asm volatile("s_mov_b64 exec, 1\n\tds_add_u32 %0, %1 offset:%2\n\ts_mov_b64 exec, -1" :: "v"(flp), "v"(one), "i"(f) : "memory")
DEVI void flag_set(int f, unsigned v) { asm volatile("s_mov_b64 exec, 1\n\tds_write_b32 %0, %1\n\ts_mov_b64 exec, -1" :: "v"(FL + f), "v"(v) : "memory"); }
#define NEEDP(seen, ptr, f, v) do { unsigned c_; asm volatile( \
        "v_cmp_gt_u32 vcc, %4, %1\n\ts_cbranch_vccnz 0f\n1:\n\t.subsection 1\n" \
        "0:\n\ts_mov_b32 %0, 0x100000\n2:\n\ts_sleep " NSLEEP "\n\tds_read_b32 %1, %2 offset:%3\n\ts_sub_u32 %0, %0, 1\n\ts_waitcnt lgkmcnt(0)\n\ts_cbranch_scc1 1b\n\t" \
        "v_cmp_gt_u32 vcc, %4, %1\n\ts_cbranch_vccnz 2b\n\ts_branch 1b\n\t.subsection 0" \
        : "=&s"(c_), "+v"(seen) : "v"(ptr), "i"(f), "i"(v) : "memory", "scc", "vcc"); } while (0)
#define NEED(seen, f, v) NEEDP(seen, flp, f, v)
DEVI void touch(const char* sbase, unsigned voff) {
    asm volatile("s_mov_b32 m0, %2\n\ts_nop 0\n\tglobal_load_lds_dword %0, %1" :: "v"(voff), "s"(sbase), "s"(LDS_JUNK) : "memory");
}
DEVI const char* group_addr(const Params& P, int xcd, int n, int lane) {
    if (n < 126) {
        const int c0 = n / 18, r = n - c0 * 18, c = c0 + 2, l = r - 8;
        return r < 8 ? P.x + (unsigned)(((xcd >> 1) * 512 + r * 64 + lane) * 1152 + c * 128)
                     : P.wqkv + (unsigned)((c * 64 + lane) * 3456 + (l < 5 ? 1152 : 2304 - 640) + (xcd & 1) * 512 + l * 128);
    }
    const int c = (n - 126) / 5, l = n - 126 - c * 5;
    return P.wqkv + (unsigned)((c * 64 + lane) * 3456 + (xcd & 1) * 512 + l * 128);
}
DEVI void touch_group(const Params& P, int xcd, int n, int lane) {
    if (n < 126) {
        const int c0 = n / 18, r = n - c0 * 18, c = c0 + 2;
        if (r < 8) touch(P.x, (unsigned)(((xcd >> 1) * 512 + r * 64 + lane) * 1152 + c * 128));
        else { const int l = r - 8; touch(P.wqkv, (unsigned)((c * 64 + lane) * 3456 + (l < 5 ? 1152 : 2304 - 640) + (xcd & 1) * 512 + l * 128)); }
    } else if (n < 171) {
        const int c = (n - 126) / 5, l = n - 126 - c * 5;
        touch(P.wqkv, (unsigned)((c * 64 + lane) * 3456 + (xcd & 1) * 512 + l * 128));
    } else {
        const int l = (n - 171) / 9, c = n - 171 - l * 9;
        touch(P.wo, (unsigned)((c * 64 + lane) * 1152 + l * 128));
    }
}
template <unsigned CH>
DEVI void wo_issue(const Params& P, int cg, int first, int stride, int lane) {
#pragma unroll 1
    for (int m = first; m < 9; m += stride) {
        const int q = 64 * m + lane, rho = q / 9, pc = q - rho * 9;
        const int k = (rho & 32) + 8 * ((rho >> 3) & 3) + 4 * (rho & 1) + ((rho >> 1) & 3);
        const unsigned qo = (unsigned)(k * 1152 + cg * 144 + pc * 16);
#pragma unroll
        for (int c = 0; c < 9; ++c) if ((CH >> c) & 1) glds16(P.wo + c * (64 * 1152), qo, (unsigned)(c * 9216 + m * 1024));
    }
}

constexpr int SP = 3088;
constexpr int XT_OFF = 135936, XT_BYTES = 4608, XPAD = 256;
constexpr int TT_OFF = 107520, TC = 1184, PT_OFF = 82944, PC_OFF = 2560;

#define CM2(o1, p1, a1, b1, o2, p2, a2, b2) do { asm( \
    "v_pk_mul_f32 %0, %2, %3 op_sel:[1,1] op_sel_hi:[1,0] neg_lo:[0,1]\n\tv_pk_mul_f32 %1, %4, %5 op_sel:[1,1] op_sel_hi:[1,0] neg_lo:[0,1]\n\t" \
    "v_pk_fma_f32 %0, %2, %3, %0 op_sel_hi:[0,1,1]\n\tv_pk_fma_f32 %1, %4, %5, %1 op_sel_hi:[0,1,1]" \
    : "=&v"(o1), "=&v"(o2) : "v"(a1), "v"(b1), "v"(a2), "v"(b2)); \
    asm("v_cvt_pk_f16_f32 %0, %2, %3\n\tv_cvt_pk_f16_f32 %1, %4, %5" : "=&v"(p1), "=&v"(p2) : "v"((o1).x), "v"((o1).y), "v"((o2).x), "v"((o2).y)); } while (0)
#define HM2(p1, a1, p2, a2, b) asm( \
    "v_pk_mul_f16 %0, %2, %4 op_sel_hi:[0,1]\n\tv_pk_mul_f16 %1, %3, %4 op_sel_hi:[0,1]\n\t" \
    "v_pk_fma_f16 %0, %2, %4, %0 op_sel:[1,1,0] op_sel_hi:[1,0,1] neg_lo:[0,1,0]\n\tv_pk_fma_f16 %1, %3, %4, %1 op_sel:[1,1,0] op_sel_hi:[1,0,1] neg_lo:[0,1,0]" \
    : "=&v"(p1), "=&v"(p2) : "v"(a1), "v"(a2), "v"(b))
DEVI f32x2 cexp_p(float a2, float f, float p) {
    float m = __builtin_amdgcn_exp2f(a2 * p);
    float r = __builtin_amdgcn_fractf(f * p);
    return f32x2{m * __builtin_amdgcn_cosf(r), m * __builtin_amdgcn_sinf(r)};
}
DEVI u32x4 mask_frag(unsigned bits, int g, unsigned val) {
    unsigned by = bits >> (8 * g);
    u32x4 r;
#pragma unroll
    for (int i = 0; i < 4; ++i) { unsigned t = (by >> (2 * i)) & 3u; r[i] = ((t | (t << 15)) & 0x10001u) * val; }
    return r;
}
struct Tab2 { unsigned v[8][64][4]; };
constexpr Tab2 make_tab2() {
    Tab2 t{};
    for (int l = 0; l < 64; ++l) {
        const int r = l & 15, g = l >> 4, j = l & 31, h = l >> 5;
        const unsigned bits[6] = {7u << (r + 11), 31u << (r + 6), 7u << (r + 3), 127u << (r + 5), 511u << (r + 4), 3u << (r + 12)};
        for (int m = 0; m < 6; ++m)
            for (int i = 0; i < 4; ++i) {
                const unsigned q = (bits[m] >> (8 * g + 2 * i)) & 3u, val = m < 5 ? 0x3f80u : 0xffffu;
                t.v[m][l][i] = (q & 1 ? val : 0u) | (q & 2 ? val << 16 : 0u);
            }
        for (int c = 0; c < 4; ++c) {
            const int v2 = 79 - (32 + (j >> 1)) - c + 4, v3 = 127 - (64 + 32 * h + (j >> 1)) - c + 4;
            t.v[6][l][c] = PT_OFF + (j & 1) * 1280 + c * 320 + (v2 >> 2) * 16 + h * 8 + (v2 & 3) * 2 - 64;
            t.v[7][l][c] = PT_OFF + PC_OFF + c * 320 + (v3 >> 2) * 16 + (j & 1) * 8 + (v3 & 3) * 2 - 64;
        }
    }
    return t;
}
__device__ const Tab2 g_tab2 = make_tab2();
DEVI float wave_max(float v) {
    v = fmaxf(v, __int_as_float(__builtin_amdgcn_update_dpp(0, __float_as_int(v), 0xB1, 0xf, 0xf, true)));
    v = fmaxf(v, __int_as_float(__builtin_amdgcn_update_dpp(0, __float_as_int(v), 0x4E, 0xf, 0xf, true)));
    v = fmaxf(v, __int_as_float(__builtin_amdgcn_update_dpp(0, __float_as_int(v), 0x124, 0xf, 0xf, true)));
    v = fmaxf(v, __int_as_float(__builtin_amdgcn_update_dpp(0, __float_as_int(v), 0x128, 0xf, 0xf, true)));
    const int i = __float_as_int(v);
    const int a = max(__builtin_amdgcn_readlane(i, 0), __builtin_amdgcn_readlane(i, 16)), b = max(__builtin_amdgcn_readlane(i, 32), __builtin_amdgcn_readlane(i, 48));
    return __int_as_float(max(a, b));
}

DEVI void ssm_role(const Params& P, lptr smem, int xcd, int li, int gid, int cg2, u64& tag, rsrc_t wsr) {
    const int gl = gid / 11, gi = gid - gl * 11, ch0 = gl * 64 + (gi < 10 ? gi * 6 : 58);
    const int tid = threadIdx.x, lane = tid & 63, w = __builtin_amdgcn_readfirstlane(tid >> 6);
    const int ch = w >> 1, half = (w ^ (w >> 2)) & 1, d = ch0 + ch;
    const bool own = gi < 10 || ch >= 2;
    const int lo16 = lane & 15, g = lane >> 4;
    STAMP_DECL; STAMP(1);
    __builtin_amdgcn_s_setprio(3);
    if (half) {
        const char* tp = (const char*)&g_tab2;
        SET_M0(TT_OFF + ch * 8192);
        glds16o<0>(tp, (unsigned)(lane * 16)); glds16o<1024>(tp, (unsigned)(lane * 16)); glds16o<2048>(tp, (unsigned)(lane * 16)); glds16o<3072>(tp, (unsigned)(lane * 16));
        SET_M0(TT_OFF + ch * 8192 + 4096);
        tp += 4096;
        glds16o<0>(tp, (unsigned)(lane * 16)); glds16o<1024>(tp, (unsigned)(lane * 16)); glds16o<2048>(tp, (unsigned)(lane * 16)); glds16o<3072>(tp, (unsigned)(lane * 16));
    }
    const unsigned po = (unsigned)(d * 256 + lane * 4);
    unsigned r_la, r_ai, r_cr, r_ci, r_dt, r_ds, junk = 0, junk2 = 0;
    asm volatile("global_load_dword %0, %6, %8\n\tglobal_load_dword %1, %6, %9\n\tglobal_load_dword %2, %6, %10\n\tglobal_load_dword %3, %6, %11\n\t"
                 "global_load_ushort %4, %7, %12\n\tglobal_load_ushort %5, %7, %13"
                 : "=&v"(r_cr), "=&v"(r_ci), "=&v"(r_la), "=&v"(r_ai), "=&v"(r_dt), "=&v"(r_ds)
                 : "v"(po), "v"(d * 2), "s"(P.cre), "s"(P.cim), "s"(P.logA), "s"(P.aim), "s"(P.logdt), "s"(P.dskip) : "memory");
#define TOUCH(dst, addr) asm volatile("global_load_dword %0, %1, off" : "+v"(dst) : "v"(addr) : "memory")
    const bool tch = w < NTW && li < 12;
    const char* tga = group_addr(P, xcd, tch ? w * 12 + li : 0, lane);
    RAW_BARRIER();
    if (tch && w < TW2) TOUCH(junk, tga);
    SCHED();
    const int tt = (tid + (li & 7) * 64) & 511;
    u32x3 xr[4] = {};
    const unsigned xoff = (unsigned)(tt * 4608 + ch0 * 2);
    const unsigned boff = own ? WS_B + (unsigned)(d * 4096 + half * 2048 + lane * 16) : WS_END;
#define GATHER(i) do { SCHED(); if (w < 8) asm volatile("global_load_dwordx3 %0, %1, %2 offset:%3" : "+v"(xr[i]) : "v"(xoff), "s"(P.x), "i"((i) * 1152) : "memory"); SCHED(); } while (0)
#define PRIO(i) do { SCHED(); __builtin_amdgcn_s_setprio(i); SCHED(); } while (0)
    SCHED();
    lptr xt = smem + XT_OFF + ch * XT_BYTES, tb = smem + TT_OFF + ch * 4 * TC, pt = smem + PT_OFF + ch * 4096;
    const int j32 = lane & 31, h = lane >> 5;
    if (!half) { lds_w32(smem + FL + 32, 0u); lds_w32(smem + FL + 36 + ch * 4, 0u); }
    int arow;
    {
        const u64 map0 = 0x0a09080803020100ull, map1 = 0x0c0b0a0a07060504ull, map2 = 0x1413110e12100f0dull;
        const u64 mm = j32 < 8 ? map0 : (j32 < 16 ? map1 : map2);
        arow = (int)((mm >> (8 * (j32 & 7))) & 0xff);
    }
    lptr ap = smem + arow * SP + w * 256 + h * 16, bp = smem + j32 * SP + w * 256 + h * 16;
    const int cpy = (3 - lo16) & 3;
    lptr xf7 = xt + (XPAD - 16 + 16 * lo16 + 8 * g) * 2 - 64 * 7;
    lptr tf = tb + cpy * TC + 2 * (527 - lo16 + 8 * g - cpy) - 64 * 16;
    lptr xc = smem + (w ^ 1) * 256 + lane * 4, xb = smem + XT_OFF + (XPAD + tt * 4) * 2;
    asm volatile("" : "+v"(ap), "+v"(bp), "+v"(xf7), "+v"(tf), "+v"(xc), "+v"(xb));
    if (tch && w < TW2) WAIT_VM(1); else WAIT_VM(0);
    asm volatile("" : "+v"(r_la), "+v"(r_ai), "+v"(r_cr), "+v"(r_ci), "+v"(r_dt), "+v"(r_ds));
    if (tch && w >= TW2) TOUCH(junk, tga);
    const float dt = rbf(__expf(bf2f(r_dt)));
    const float cm = wave_max(fmaxf(fmaxf(fabsf(bf2f(r_cr)), fabsf(bfhi(r_cr))), fmaxf(fabsf(bf2f(r_ci)), fabsf(bfhi(r_ci)))));
    unsigned ce = __float_as_uint(cm) >> 23; ce = ce < 16u ? 16u : (ce > 240u ? 240u : ce);
    const float cs = __uint_as_float((263u - ce) << 23), ics = __uint_as_float((ce - 9u) << 23);
    f32x16 kacc;
#pragma unroll
    for (int i = 0; i < 16; ++i) kacc[i] = 0.f;
    STAMP(2);
    {
        const unsigned sh = half * 16;
        const float la = bf2f(r_la >> sh), ai = bf2f(r_ai >> sh), cr = bf2f(r_cr >> sh) * cs, ci = bf2f(r_ci >> sh) * cs;
        const float a2 = (-rbf(__expf(la)) * dt) * 1.44269504089f, f = (ai * dt) * 0.15915494309f;
#define PUT(row, v) W_ADDTID(v, ((row) & 15) * SP)
        const f32x2 w1 = cexp_p(a2, -f, 1.f), W = cexp_p(a2, -f, 256.f);
        f32x2 u[8], v[4], c[9], e4, e8, t2;
        unsigned U[8], V[4], p, q;
        u[1] = cexp_p(a2, f, 8.f); v[1] = cexp_p(a2, f, 64.f);
        c[0] = f32x2{cr, -ci};
        U[1] = pk_f16(u[1]); V[1] = pk_f16(v[1]);
        GATHER(0);
        SET_M0(w * 256);
        PUT(0, pk_f16(c[0]));
        CM2(c[1], p, c[0], w1, u[2], U[2], u[1], u[1]); PUT(1, p);
        CM2(c[2], p, c[1], w1, u[3], U[3], u[2], u[1]); PUT(2, p);
        CM2(c[3], p, c[2], w1, u[4], U[4], u[2], u[2]); PUT(3, p);
        CM2(c[4], p, c[3], w1, u[5], U[5], u[4], u[1]); PUT(4, p);
        CM2(c[5], p, c[4], w1, u[6], U[6], u[4], u[2]); PUT(5, p);
        CM2(c[6], p, c[5], w1, u[7], U[7], u[4], u[3]); PUT(6, p);
        CM2(c[7], p, c[6], w1, v[2], V[2], v[1], v[1]); PUT(7, p);
        CM2(c[8], p, c[7], w1, v[3], V[3], v[2], v[1]);
        PRIO(2);
        GATHER(1);
        CM2(e8, p, c[8], W, e4, q, c[4], W); PUT(12, p); PUT(10, q);
        CM2(c[8], p, e8, W, c[4], q, e4, W); PUT(14, p); PUT(13, q);
        CM2(t2, p, c[0], W, e4, q, c[4], W); PUT(8, p); PUT(15, q);
        CM2(t2, p, c[2], W, e4, q, c[6], W); PUT(9, p); PUT(11, q);
        SET_M0(w * 256 + 16 * SP);
        CM2(e8, p, c[8], W, t2, q, W, W); PUT(17, p);
        CM2(e4, p, c[8], t2, c[0], q, e8, t2); PUT(16, p); PUT(18, q);
        CM2(c[8], p, e4, t2, e8, q, c[0], t2); PUT(19, p); PUT(20, q);
        PRIO(1);
        GATHER(2);
        u32x4 af[8];
#pragma unroll
        for (int s = 0; s < 8; ++s) af[s] = lds_r128(ap + 32 * s);
        CFENCE();
        PUT(16, V[2]); PUT(24, V[3]);
#pragma unroll
        for (int b = 1; b < 8; ++b) { HM2(p, V[2], q, V[3], U[b]); PUT(16 + b, p); PUT(24 + b, q); }
        SET_M0(w * 256);
        PRIO(0);
        GATHER(3);
        PUT(0, 0x3c00u); PUT(8, V[1]);
#pragma unroll
        for (int b = 1; b < 8; b += 2) { HM2(p, U[b], q, U[b == 7 ? 7 : b + 1], V[1]); PUT(8 + b, p); if (b < 7) PUT(9 + b, q); PUT(b, U[b]); if (b < 7) PUT(b + 1, U[b + 1]); }
#undef PUT
        u32x4 bfr[8];
#pragma unroll
        for (int s = 0; s < 8; ++s) bfr[s] = lds_r128(bp + 32 * s);
        SCHED();
        SCHED();
#pragma unroll
        for (int s = 0; s < 8; ++s)
            kacc = __builtin_amdgcn_mfma_f32_32x32x16_f16(__builtin_bit_cast(f16x8, af[s]), __builtin_bit_cast(f16x8, bfr[s]), kacc, 0, 0, 0);
    }
    STAMP(3);
    const float ks = 2.f * dt * ics;
#pragma unroll
    for (int i = 0; i < 12; ++i) kacc[i] *= ks;
    if (half) {
#pragma unroll
        for (int i = 0; i < 8; ++i) W_ADDTID(kacc[i], i * SP);
    } else {
#pragma unroll
        for (int i = 0; i < 4; ++i) W_ADDTID(kacc[8 + i], i * SP);
    }
    if (w >= 8 && li < 12) {
        const int n = (11 - w) * 12 + li, l = n / 9, c = n - l * 9;
        TOUCH(junk2, P.wo + (unsigned)((c * 64 + lane) * 1152 + (8 - l) * 128));
    }
    u32x4 ct[8];
#pragma unroll
    for (int i = 0; i < 8; ++i) ct[i] = lds_r128(smem + TT_OFF + ch * 8192 + i * 1024 + lane * 16);
    PIN_TAG();
    STAMP(4);
    BARRIER();
    STAMP(5);
    if (tid == 704) tag_post(P, tag);
    auto cleared = [&]() {
        if (w < 4) {
            WAIT_VM(0);
            if (lane == 0 && __hip_atomic_fetch_add((LAS unsigned*)(smem + FL + 32), 1u, __ATOMIC_RELAXED, __HIP_MEMORY_SCOPE_WORKGROUP) == 3u)
                __hip_atomic_store((GAS u64*)(P.ws + WS_BFL + gid * 128), tag, RLX_AGENT);
        }
    };
    auto x_in = [&]() {
        if (w < 8) {
            WAIT_VM(0); SCHED();
            asm volatile("" : "+v"(xr[0]), "+v"(xr[1]), "+v"(xr[2]), "+v"(xr[3]), "+v"(junk) :: "memory");
#pragma unroll
            for (int k = 0; k < 3; ++k) {
                u32x2 lo = {__builtin_amdgcn_perm(xr[1][k], xr[0][k], 0x05040100u), __builtin_amdgcn_perm(xr[3][k], xr[2][k], 0x05040100u)};
                u32x2 hi = {__builtin_amdgcn_perm(xr[1][k], xr[0][k], 0x07060302u), __builtin_amdgcn_perm(xr[3][k], xr[2][k], 0x07060302u)};
                lds_w64(xb + (2 * k) * XT_BYTES, lo); lds_w64(xb + (2 * k + 1) * XT_BYTES, hi);
            }
        }
        if (w < 4) {
#pragma unroll
            for (int i = 0; i < 6; ++i)
                if (i >= 2 || gi < 10) __builtin_amdgcn_raw_buffer_store_b128(u32x4{EMPTY, EMPTY, EMPTY, EMPTY}, wsr, (unsigned)(tid * 16), WS_B + (ch0 + i) * 4096, 16);
        }
        BARRIER();
        if (w >= 8) wo_issue<WOA>(P, cg2, w - 8, 4, lane);
    };
    auto TF = [&](int off) { u32x2 a = lds_r64(tf + off), b = lds_r64(tf + off + 8); return u32x4{a.x, a.y, b.x, b.y}; };
    f32x4 acc[8];
#pragma unroll
    for (int i = 0; i < 8; ++i) acc[i] = f32x4{0.f, 0.f, 0.f, 0.f};
    lptr axr;
    if (!half) {
        __builtin_amdgcn_s_setprio(2);
        lds_w64(xt + lane * 8, u32x2{0u, 0u});
#pragma unroll
        for (int i = 0; i < 8; ++i) kacc[i] += lds_rf(xc + i * SP);
        unsigned dd[6];
        dd[0] = pk_bf16(kacc[7], kacc[6]); dd[1] = pk_bf16(kacc[5], kacc[4]); dd[2] = pk_bf16(kacc[3], kacc[2]);
        dd[3] = pk_bf16(kacc[1], kacc[0] + (lane == 0 ? bf2f(r_ds) : 0.f));
        dd[4] = __builtin_amdgcn_update_dpp(0, dd[0], 0x138, 0xf, 0xf, true);
        dd[5] = __builtin_amdgcn_update_dpp(0, dd[1], 0x138, 0xf, 0xf, true);
        unsigned mm[5];
#pragma unroll
        for (int i = 0; i < 5; ++i) mm[i] = __builtin_amdgcn_alignbit(dd[i + 1], dd[i], 16);
        lptr tw = tb + 2 * (536 - 8 * lane);
        lds_w128(tw, u32x4{dd[0], dd[1], dd[2], dd[3]}); lds_w128(tw + TC, u32x4{mm[0], mm[1], mm[2], mm[3]});
        lds_w128(tw + 2 * TC, u32x4{dd[1], dd[2], dd[3], dd[4]}); lds_w128(tw + 3 * TC, u32x4{mm[1], mm[2], mm[3], mm[4]});
        {
            const unsigned n0 = __builtin_amdgcn_readlane(dd[0], 63), n1 = __builtin_amdgcn_readlane(dd[1], 63);
            const unsigned kk = (n0 & 0xffffu) * 0x10001u, kh = n0 << 16, m4 = (n0 >> 16) | (n1 << 16);
            const int c = lane & 3, bl = lane >> 2;
            u32x4 e;
            e.x = 0u; e.y = lane == 14 ? kh : (lane == 15 ? kk : 0u); e.z = lane == 12 ? kh : (bl == 3 ? kk : 0u);
            e.w = lane == 14 ? n0 : (lane == 15 ? m4 : (bl == 3 ? kk : 0u));
            if (bl < 6) lds_w128(tb + c * TC + bl * 16 + (bl >> 2) * 1024, e);
        }
        x_in();
        u32x4 X[8];
#pragma unroll
        for (int i = 0; i < 8; ++i) X[i] = lds_r128(xf7 + 64 * 6 + 512 * i);
        u32x4 k0 = TF(1024 - 64), k8 = TF(512 - 64);
#pragma unroll
        for (int i = 0; i < 8; ++i) asm volatile("" : "+v"(acc[i]));
        __builtin_amdgcn_s_setprio(0);
        STAMP(6);
#pragma unroll
        for (int c = 1; c < 8; ++c) {
            u32x4 k0n, k8n;
            if (c < 7) { k0n = TF(1024 - 64 * (c + 1)); k8n = TF(512 - 64 * (c + 1)); }
#pragma unroll
            for (int i = 0; i < 8; ++i) {
                SCHED();
                mfma16i(acc[i], k0, X[i]);
                if (i < 7) mfma16i(acc[i + 1], k8, X[i]);
                SCHED();
                if (c < 7) X[i] = lds_r128(xf7 + 64 * (6 - c) + 512 * i);
            }
            k0 = k0n; k8 = k8n;
            if (c == 5) { SCHED(); cleared(); SCHED(); }
        }
        MFMA_DONE();
        if (w < 4) wo_issue<0x1ff & ~WOA>(P, cg2, w >> 1, 2, lane);
        flag_wait(smem, 36 + ch * 4, 1);
#pragma unroll
        for (int i = 0; i < 4; ++i) lds_w128(xt + i * 1024 + lane * 16, __builtin_bit_cast(u32x4, acc[4 + i]));
        axr = pt + lane * 16;
    } else {
        u32x4 X[8];
#pragma unroll
        for (int i = 0; i < 4; ++i) if (i < 3 || lane < 48) lds_w128(pt + i * 1024 + lane * 16, u32x4{0u, 0u, 0u, 0u});
        CFENCE();
        const unsigned p89 = pk_bf16(kacc[8] + lds_rf(xc), kacc[9] + lds_rf(xc + SP)), pab = pk_bf16(kacc[10] + lds_rf(xc + 2 * SP), kacc[11] + lds_rf(xc + 3 * SP));
#pragma unroll
        for (int c = 0; c < 4; ++c) {
            lptr q2 = smem + (ct[6][c] + ch * 4096), q3 = smem + (ct[7][c] + ch * 4096);
            lds_w16(q2 + 64, p89); lds_w16(q2, p89 >> 16);
            lds_w16(q3 + 64, pab); lds_w16(q3, pab >> 16);
        }
        CFENCE();
        const u32x4 FX = ct[5] & (((unsigned)__builtin_amdgcn_readlane(p89, 63) >> 16) * 0x10001u);
        lptr pf2 = pt + cpy * 320 + ((79 - 64 - lo16 + 4 * g - cpy + 4) >> 2) * 16;
        x_in();
#pragma unroll
        for (int i = 0; i < 8; ++i) X[i] = lds_r128(xf7 + 64 * 7 + 512 * i);
        u32x4 TA[3], TB[3], TCc[4];
#pragma unroll
        for (int i = 0; i < 3; ++i) { TA[i] = lds_r128(pf2 + 64 * (2 - i)); TB[i] = lds_r128(pf2 + 1280 + 64 * (2 - i)); }
#pragma unroll
        for (int i = 0; i < 4; ++i) TCc[i] = lds_r128(pf2 + PC_OFF + 64 * (3 - i));
        STAMP(6);
        const f32x4 z4 = {0.f, 0.f, 0.f, 0.f};
#pragma unroll
        for (int st = 0; st < 6; ++st) {
            const u32x4 Xc = lds_r128(xf7 + 64 * 7 + 512 * st - 16);
            const u32x4 pa = pack8(mfma16(X[st], ct[0], z4), mfma16(X[st], ct[1], z4));
            const u32x4 pb = pack8(mfma16(X[st], ct[2], z4), mfma16(Xc, ct[1], z4));
#pragma unroll
            for (int dl = 2; dl <= 4; ++dl) if (st + dl < 8) {
                acc[st + dl] = mfma16(pa, TA[dl - 2], acc[st + dl]);
                acc[st + dl] = mfma16(pb, TB[dl - 2], acc[st + dl]);
            }
            if (st < 4) {
                const u32x4 pc = pack8(mfma16(X[st], ct[3], z4), mfma16(Xc, ct[4], z4));
#pragma unroll
                for (int dl = 4; dl <= 7; ++dl) if (st + dl < 8) acc[st + dl] = mfma16(pc, TCc[dl - 4], acc[st + dl]);
            }
        }
        SCHED(); lds_w32(smem + FL + 36 + ch * 4, 1u); cleared(); SCHED();
#pragma unroll
        for (int i = 4; i < 8; ++i) acc[i] = mfma16(FX, X[i - 4], acc[i]);
        const u32x4 k0 = TF(1024), k8 = TF(512), k16 = TF(0);
#pragma unroll
        for (int i = 0; i < 8; ++i) acc[i] = mfma16(k0, X[i], acc[i]);
#pragma unroll
        for (int i = 1; i < 8; ++i) acc[i] = mfma16(k8, X[i - 1], acc[i]);
#pragma unroll
        for (int i = 2; i < 8; ++i) acc[i] = mfma16(k16, X[i - 2], acc[i]);
        MFMA_DONE();
#pragma unroll
        for (int i = 0; i < 4; ++i) { lds_w128(pt + i * 1024 + lane * 16, __builtin_bit_cast(u32x4, acc[i])); acc[i] = acc[4 + i]; }
        axr = xt + lane * 16;
    }
    STAMP(7);
    if (w == 0 && lane < 8) lds_w32(smem + FL + lane * 4, 0u);
    WAIT_VM(0); SCHED();
    asm volatile("" :: "v"(junk2));
    BARRIER();
    STAMP(8);
#pragma unroll
    for (int i = 0; i < 4; ++i) acc[i] += __builtin_bit_cast(f32x4, lds_r128(axr + i * 1024));
#pragma unroll
    for (int i = 0; i < 2; ++i) {
        const f32x4 ya = acc[2 * i], yb = acc[2 * i + 1];
        const auto sx = __builtin_amdgcn_permlane16_swap(pk_bf16(ya[0], ya[1]), pk_bf16(yb[0], yb[1]), false, false);
        const auto sy = __builtin_amdgcn_permlane16_swap(pk_bf16(ya[2], ya[3]), pk_bf16(yb[2], yb[3]), false, false);
        st_sc1(wsr, boff + i * 1024, not_empty(u32x4{sx[0], sy[0], sx[1], sy[1]}));
    }
    BARRIER();
    STAMP(9);
    if (w < 2) STAMP_OUT(2 + 2 * w, blockIdx.x + 256 * w);
}

constexpr int SLOT = 34816, XCH = 16384, KVT_OFF = 151296, KVP_OFF = 27648;
DEVI constexpr int slot_off(int s) { return (s & 3) == 0 ? SLOT : ((s & 3) == 1 ? 0 : (s & 3) * SLOT); }
DEVI constexpr int wq_off(int t) { return t == 0 ? 0 : 49152 + t * 27648; }

constexpr int quad_src(int quad) {
    const int pp = quad >= 192 ? 1 : 0, qi = quad - 192 * pp;
    const bool isz = qi >= 180;
    const int el = isz ? 0 : qi / 5, qd = qi - el * 5, hi = el >= 18 ? 1 : 0;
    const int dql = isz ? (qi < 189 ? qi - 180 : 0) : qd + 4 * hi;
    const int et = isz ? 0 : (el < 4 ? 0 : (el < 20 ? 1 : 2)), col = isz ? 0 : (el < 4 ? 12 + el : (el < 20 ? el - 4 : el - 20));
    const int tile = dql == 8 ? 5 : (et == 0 ? (dql >> 2) : (et == 1 ? 2 + (dql >> 2) : 4));
    return KVP_OFF + pp * 6144 + tile * 1024 + ((dql == 8 ? et : (dql & 3)) * 16 + col) * 4;
}
constexpr int quad_dst(int quad, int hf) {
    const int pp = quad >= 192 ? 1 : 0, qi = quad - 192 * pp;
    const bool isz = qi >= 180;
    const int el = isz ? 0 : qi / 5, qd = qi - el * 5, hi = el >= 18 ? 1 : 0;
    const int dql = isz ? (qi < 189 ? qi - 180 : 0) : qd + 4 * hi;
    const int dq = 9 * pp + dql, ks = dq >> 3, e = 36 * pp + el, ee = e >> 4;
    const int fr = ee == 0 ? 0 : (ee < 3 ? 2 * ee - 1 + ks : 2 * ee - 2 + ks);
    const int hz = 2 * pp + ((dql > 4 || (dql == 4 && hf)) ? 1 : 0);
    const bool dead = isz ? qi >= 189 : ((!hi && qd == 4 && hf) || (hi && qd == 0 && !hf));
    return dead ? LDS_DUMMY : KVT_OFF + (isz ? (9 + ks) * 1024 + (dq & 3) * 256 + hz * 16 : fr * 1024 + ((dq & 3) * 16 + (e & 15)) * 16) + ((dq >> 2) & 1) * 8 + hf * 4;
}
struct Tab { int v[2048]; };
constexpr Tab make_tab() {
    Tab t{};
    for (int c = 0; c < 192; ++c) {
        const int l = c & 63, q = 2 * ((c & 192) + (l >> 5) * 16 + (l & 15)) + ((l >> 4) & 1);
        t.v[4 * c] = quad_src(2 * c); t.v[4 * c + 1] = quad_src(2 * c + 1);
        t.v[4 * c + 2] = (quad_dst(q, 0) - KVT_OFF) | ((quad_dst(q, 1) - KVT_OFF) << 16); t.v[4 * c + 3] = (quad_dst(q + 64, 0) - KVT_OFF) | ((quad_dst(q + 64, 1) - KVT_OFF) << 16);
    }
    for (int q = 0; q < 576; ++q) {
        const int rho = q / 9, pc = q % 9, k = (rho & 32) + 8 * ((rho >> 3) & 3) + 4 * (rho & 1) + ((rho >> 1) & 3), m = q >> 6;
        t.v[768 + q] = k * 3456 + pc * 16 - (m & 1) * 1024;
        t.v[1344 + q] = k * 1152 + pc * 16 - (m & 3) * 1024;
    }
    return t;
}
__device__ const Tab g_tab = make_tab();
constexpr int IDX = P2_B, GW_OFF = 139264;

#define GET_TAG() do { const u32x2 t_ = lds_r64(smem + LDS_TAG); tag = (u64)(unsigned)__builtin_amdgcn_readfirstlane(t_.x) | ((u64)(unsigned)__builtin_amdgcn_readfirstlane(t_.y) << 32); } while (0)
DEVI void gemm_kv(const Params& P, lptr smem, int rb, int hg, int kw, int cg2, u64& tag, rsrc_t wsr) {
    const int lane = threadIdx.x & 63, lo16 = lane & 15, g = lane >> 4;
    const int p = kw & 1, ct = (kw < 3 ? kw : 0) * 64 + lane;
    STAMP_DECL; STAMP(1);
    __builtin_amdgcn_s_setprio(2);
    const int xrow = ((kw >> 1) * 64 + lo16) * 128, sw = (g ^ ((lo16 >> 1) & 7)) << 4;
    lptr xa[2] = {smem + xrow + sw, smem + xrow + (sw ^ 64)};
    const int wrow = (4 * g + (lo16 >> 2)) * 288;
    lptr wa = smem + XCH + wrow + (9 * p + (lane & 3)) * 8, wa2 = smem + XCH + wrow + (9 * p + ((lane & 3) == 3 ? 18 : 8)) * 8;
    lptr flp = smem + FL, ixp = smem + IDX + ct * 16, irp = smem + IDX + 3072 + 2304 + lane * 4;
    unsigned one = 1u;
    u32x4 sel[2];
    sel[0] = mask_frag(lo16 < 8 ? 1u << lo16 : 0u, 0, 0x3f80u); sel[1] = mask_frag(lo16 >= 8 ? 1u << (lo16 - 8) : 0u, 0, 0x3f80u);
    unsigned sto = WS_PART + (unsigned)((hg * 16 + rb) * 3072 + ct * 16);
    unsigned off = WS_PART + (unsigned)((hg * 16 + g) * 3072 + kw * 1024 + lo16 * 16);
    asm volatile("" : "+v"(flp), "+v"(one), "+v"(ixp), "+v"(irp), "+v"(sel[0]), "+v"(sel[1]), "+v"(sto), "+v"(off));
    const f32x4 z4 = {0.f, 0.f, 0.f, 0.f};
    f32x4 acc[4][5];
#pragma unroll
    for (int i = 0; i < 4; ++i)
#pragma unroll
        for (int j = 0; j < 5; ++j) { const float e = j < 2 ? 1e-6f : (j == 2 && lo16 < 12 ? 1e-6f : 0.f); acc[i][j] = f32x4{e, e, e, e}; asm volatile("" : "+v"(acc[i][j])); }
    auto rel = [&](int s) { return (s & 2) ? slot_off(s) - 2 * SLOT : slot_off(s); };
    auto rebase = [&](int dlt) {
        xa[0] += dlt; xa[1] += dlt; wa += dlt; wa2 += dlt;
        asm volatile("" : "+v"(xa[0]), "+v"(xa[1]), "+v"(wa), "+v"(wa2));
    };
    constexpr int NB = 6;
    u32x2 wl[NB], wh[NB];
    u32x4 xf[2][4];
    auto rdw = [&](int f, int t) {
        const int n = f / 5, j = f % 5;
        const int o = rel(n >> 1) + (n & 1) * 9216 + t * 4608;
        const u32x2 v = j == 2 ? lds_tr(wa2 + o) : lds_tr(wa + o + (j == 0 ? 0 : (j == 1 ? 32 : (j == 3 ? 152 : 184))));
        if (t) wh[f % NB] = v; else wl[f % NB] = v;
    };
    auto rdx = [&](int n, int r) { xf[n & 1][r] = lds_r128(xa[n & 1] + rel(n >> 1) + r * 2048); };
    rebase(0);
    BARRIER();
    STAMP(2);
    rdw(0, 0); rdw(0, 1);
#pragma unroll
    for (int r = 0; r < 4; ++r) rdx(0, r);
#pragma unroll
    for (int f = 1; f < NB - 1; ++f) { rdw(f, 0); rdw(f, 1); }
    auto kstep = [&](const int n) __attribute__((always_inline)) {
        const int s = n >> 1;
#pragma unroll
        for (int m = 0; m < 20; ++m) {
            const int j = m >> 2, r = m & 3, f = 5 * n + j, fn = f + NB - 1;
            SCHED();
            const u32x4 wv = {wl[f % NB].x, wl[f % NB].y, wh[f % NB].x, wh[f % NB].y};
            mfma16i(acc[r][j], xf[n & 1][r], wv);
            SCHED();
            if (r < 2 && fn < 90) rdw(fn, r);
            if (r == 2 && j < 4 && n < 17) rdx(n + 1, j);
            if (m == 15 && n < 17) WAIT_LGKM(12);
            if (m == 18 && !(n & 1)) {
                if (s < 8 && (s & 1)) rebase((s & 2) ? -2 * SLOT : 2 * SLOT);
                if (s == 8) WAIT_LGKM0();
                RAW_BARRIER();
                if (s == 0) STAMP(3);
                if (s == 4) STAMP(4);
            }
            if (m == 19 && n < 17) WAIT_LGKM(2);
        }
    };
    kstep(0); kstep(1); kstep(2); kstep(3); kstep(4); kstep(5); kstep(6); kstep(7); kstep(8);
    kstep(9); kstep(10); kstep(11); kstep(12); kstep(13); kstep(14); kstep(15); kstep(16); kstep(17);
    __builtin_amdgcn_s_setprio(3);
    const u32x4 ix = lds_r128(ixp);
    unsigned oo[9];
#pragma unroll
    for (int i = 0; i < 9; ++i) oo[i] = *(LAS unsigned*)(irp + i * 256);
    MFMA_DONE();
    STAMP(5);
    {
        f32x4 r[6], t0 = z4, t1 = z4, t2 = z4;
#pragma unroll
        for (int i = 0; i < 5; ++i) r[i] = z4;
#pragma unroll
        for (int u = 0; u < 2; ++u) {
            u32x4 pk[3], vb[3];
#pragma unroll
            for (int j = 0; j < 3; ++j) pk[j] = pack8(phi4(acc[2 * u][j]), phi4(acc[2 * u + 1][j]));
            vb[0] = pack8(acc[2 * u][2], acc[2 * u + 1][2]);
            if (lo16 < 12) vb[0] = u32x4{0x3f803f80u, 0x3f803f80u, 0x3f803f80u, 0x3f803f80u};
            vb[1] = pack8(acc[2 * u][3], acc[2 * u + 1][3]); vb[2] = pack8(acc[2 * u][4], acc[2 * u + 1][4]);
            t0 = mfma16(pk[2], vb[0], t0); t1 = mfma16(pk[2], vb[1], t1); t2 = mfma16(pk[2], vb[2], t2);
            r[0] = mfma16(pk[0], vb[0], r[0]); r[1] = mfma16(pk[1], vb[0], r[1]); r[2] = mfma16(pk[0], vb[1], r[2]);
            r[3] = mfma16(pk[1], vb[1], r[3]); r[4] = mfma16(pk[1], vb[2], r[4]);
        }
        r[5] = g == 0 ? t0 : (g == 1 ? t1 : t2);
        MFMA_DONE();
        SET_M0(KVP_OFF + kw * 6144);
#pragma unroll
        for (int t = 5; t >= 0; --t) {
            W_ADDTID(r[t][0], t * 1024); W_ADDTID(r[t][1], t * 1024 + 256);
            W_ADDTID(r[t][2], t * 1024 + 512); W_ADDTID(r[t][3], t * 1024 + 768);
        }
        FLAG_ADD(F_KVP);
    }
    STAMP(6);
    if (kw < 3) {
        lptr dst[4] = {smem + KVT_OFF + (ix.z & 0xffffu), smem + KVT_OFF + (ix.z >> 16), smem + KVT_OFF + (ix.w & 0xffffu), smem + KVT_OFF + (ix.w >> 16)};
        asm volatile("" : "+v"(dst[0]), "+v"(dst[1]), "+v"(dst[2]), "+v"(dst[3]));
        flag_wait<0>(smem, F_KVP, 4);
        STAMP(7);
        float pa[2][2][4];
#pragma unroll
        for (int i = 0; i < 2; ++i)
#pragma unroll
            for (int r = 0; r < 2; ++r)
#pragma unroll
                for (int k = 0; k < 4; ++k) pa[i][r][k] = lds_rf(smem + ix[i] + r * 12288 + k * 256);
        SCHED();
        u32x4 pv;
#pragma unroll
        for (int i = 0; i < 2; ++i)
#pragma unroll
            for (int k = 0; k < 2; ++k)
                pv[2 * i + k] = pk_bf16(pa[i][0][2 * k] + pa[i][1][2 * k], pa[i][0][2 * k + 1] + pa[i][1][2 * k + 1]);
        st_sc1(wsr, sto, not_empty(pv));
        SCHED();
        FLAG_ADD(F_KVR);
        STAMP(8);
        WAIT_VM(0);
        STAMP(9);
        f32x4 gs[4][2];
        auto group = [&](int i, const u32x4 (&a)[4]) {
            gs[i][0] = mfma16(sel[1], a[1], mfma16(sel[0], a[0], z4));
            gs[i][1] = mfma16(sel[1], a[3], mfma16(sel[0], a[2], z4));
        };
        {
            u32x4 a[4][4];
#pragma unroll
            for (int i = 0; i < 4; ++i)
#pragma unroll
                for (int t = 0; t < 4; ++t) a[i][t] = __builtin_amdgcn_raw_buffer_load_b128(wsr, off + t * 256, ((i + rb) & 3) * 12288, 16);
            SCHED();
            FLAG_ADD(F_SW);
#pragma unroll
            for (int i = 0; i < 4; ++i) group(i, a[i]);
        }
#pragma unroll
        for (int i = 0; i < 4; ++i) {
#pragma unroll 1
            for (unsigned spins = 0; spins < SPIN_CAP; ++spins) {
                if (!__any(__builtin_isunordered(gs[i][0][0], gs[i][1][0]))) break;
                if (spins) __builtin_amdgcn_s_sleep(RSLEEP);
                CFENCE();
                u32x4 a[4];
#pragma unroll
                for (int t = 0; t < 4; ++t) a[t] = __builtin_amdgcn_raw_buffer_load_b128(wsr, off + t * 256, ((i + rb) & 3) * 12288, 16);
                group(i, a);
                if (spins >= 32 && __all(max(max(umax4(a[0]), umax4(a[1])), max(umax4(a[2]), umax4(a[3]))) != EMPTY)) break;
            }
        }
        f32x4 sum[2];
        sum[0] = (gs[0][0] + gs[1][0]) + (gs[2][0] + gs[3][0]); sum[1] = (gs[0][1] + gs[1][1]) + (gs[2][1] + gs[3][1]);
#pragma unroll
        for (int i = 0; i < 4; ++i) lds_w32(dst[i], pk_bf16(sum[i >> 1][2 * (i & 1)], sum[i >> 1][2 * (i & 1) + 1]));
        CFENCE();
        FLAG_ADD(F_D);
        STAMP(10);
    } else {
        auto wo_chunks = [&](int c0, int c1) {
#pragma unroll 1
            for (int c = c0; c < c1; ++c) {
                const char* ws = P.wo + cg2 * 144 + c * (64 * 1152);
                const unsigned l = c < 8 ? c * 9216 : WO8;
                SET_M0(l); glds16o<0>(ws, oo[0]); glds16o<1024>(ws, oo[1]); glds16o<2048>(ws, oo[2]); glds16o<3072>(ws, oo[3]);
                SET_M0(l + 4096); glds16o<0>(ws, oo[4]); glds16o<1024>(ws, oo[5]); glds16o<2048>(ws, oo[6]); glds16o<3072>(ws, oo[7]);
                SET_M0(l + 8192); glds16o<0>(ws, oo[8]);
            }
        };
        flag_wait(smem, F_KVR, 3);
        wo_chunks(3, 9);
        WAIT_VM(0);
        FLAG_ADD(F_D);
        flag_wait(smem, F_SW, 3);
        flag_wait(smem, F_Q0, 8);
        wo_chunks(0, 3);
        WAIT_VM(0);
        FLAG_ADD(F_W0);
    }
    GET_TAG();
    __builtin_amdgcn_s_setprio(0);
    if (kw == 0) STAMP_OUT(1, blockIdx.x);
}

DEVI void gemm_q(const Params& P, lptr smem, int rb, int hg, int t8, u64& tag, rsrc_t wsr) {
    const int lane = threadIdx.x & 63, lw = t8 & 3;
    const bool ld = t8 < 4;
    STAMP_DECL; STAMP(1);
    __builtin_amdgcn_s_setprio(3);
    unsigned xo[4], wo_[5], qo[3];
    const int rx = hg & 3, rw = rb & 3;
    unsigned mx[4], mw[4];
    const unsigned xbase = (unsigned)((rb * 128 + 8 * lw + (lane >> 3)) * 1152 + (((lane & 7) ^ ((lane >> 4) + 4 * (lw & 1))) << 4));
#pragma unroll
    for (int i = 0; i < 4; ++i) { mx[i] = (((i + rx) & 3) * 4 + lw) * 1024; mw[i] = XCH + (((i + rw) & 3) * 4 + lw) * 1024; xo[i] = xbase + ((i + rx) & 3) * 36864; }
    const unsigned lb = lw * 4096;
    auto issue_x = [&](int s, int i0 = 0, int i1 = 4) {
#pragma unroll
        for (int i = i0; i < i1; ++i) glds16(P.x + s * 128, xo[i], slot_off(s) + mx[i]);
    };
    auto issue_w = [&](int s, int i0 = 0, int i1 = 4) {
        const char* ws = P.wqkv + s * (64 * 3456);
        if (i1 == 4 && (lw >> 1) == (s & 1)) glds16(ws, wo_[4], (unsigned)(slot_off(s) + XCH + 16384 + (lw & 1) * 1024));
#pragma unroll
        for (int i = i0; i < i1; ++i) glds16(ws, wo_[i], slot_off(s) + mw[i]);
    };
    auto issue_q = [&](int t, int i) {
        const char* ws = P.wqkv + hg * 144 + (3 * t + i) * (64 * 3456);
        if (lw == ((i + t) & 3)) glds16(ws, qo[2], (unsigned)(wq_off(t) + i * 9216 + 8192));
        SET_M0(wq_off(t) + i * 9216 + (lb >> 1));
        glds16o<0>(ws, qo[0]); glds16o<1024>(ws, qo[1]);
    };
    auto w_off = [&](int i) {
        const unsigned q = 64 * (i < 4 ? ((i + rw) & 3) * 4 + lw : 16 + (lw & 1)) + lane, rho = (q * 3641u) >> 16, pc = q - rho * 18;
        const unsigned k = (rho & 35u) + 8 * ((rho >> 2) & 3) + 4 * ((rho >> 4) & 1);
        wo_[i] = k * 3456 + (1152 + hg * 144) + pc * 16 + (pc < 9 ? 0u : 1008u);
    };
    if (ld) {
        issue_x(0, 0, 1); issue_x(1, 0, 1);
        SCHED();
        w_off(0);
        issue_w(0, 0, 1); issue_w(1, 0, 1);
        SCHED();
#pragma unroll
        for (int i = 1; i < 5; ++i) w_off(i);
        if (lw == 1) { glds16(P.gw, (unsigned)(lane * 16), GW_OFF); if (lane < 8) glds16(P.gw + 1024, (unsigned)(lane * 16), GW_OFF + 1024); }
        issue_x(0, 1, 4); issue_w(0, 1, 4); issue_w(1, 1, 4); issue_x(1, 1, 4);
        SET_M0(IDX + (lb >> 1));
        glds16o<0>((const char*)&g_tab + (lb >> 1), (unsigned)(lane * 16)); glds16o<1024>((const char*)&g_tab + (lb >> 1), (unsigned)(lane * 16));
    } else {
        if (lw == 0 && lane < 16) lds_w32(smem + FL + lane * 4, 0u);
#pragma unroll
        for (int i = 0; i < 3; ++i) lds_w128(smem + KVT_OFF + i * 4096 + lw * 1024 + lane * 16, u32x4{0u, 0u, 0u, 0u});
    }
    const int lo16 = lane & 15, g = lane >> 4;
    const int sw = (g ^ ((lo16 >> 1) & 7)) << 4;
    lptr xa[2] = {smem + (16 * t8 + lo16) * 128 + sw, smem + (16 * t8 + lo16) * 128 + (sw ^ 64)}, xb[2] = {xa[0] + 2 * SLOT, xa[1] + 2 * SLOT};
    lptr flp = smem + FL, iqp = smem + IDX + 3072 + lane * 4, gwp = smem + GW_OFF + g * 16;
    const int qrow = (2 * (4 * g + (lo16 >> 2))) * 144;
    lptr qw = smem + qrow + (lane & 3) * 8, qw4 = smem + qrow + (16 + (lane & 1)) * 8 + (lane & 2) * 2304;
    lptr qwh = qw + wq_off(1), qw4h = qw4 + wq_off(1), kfp = smem + KVT_OFF + lane * 16, kzp = smem + KVT_OFF + 9216 + (lane & 51) * 16;
    unsigned one = 1u, ob0 = WS_O + (unsigned)(((rb * 8 + t8) * 72 + hg * 9 + 2 * (g & 1) + (g >> 1)) * 256 + lo16 * 16);
    asm volatile("" : "+v"(flp), "+v"(one), "+v"(iqp), "+v"(gwp), "+v"(xa[0]), "+v"(xa[1]), "+v"(xb[0]), "+v"(xb[1]));
    asm volatile("" : "+v"(qw), "+v"(qw4), "+v"(qwh), "+v"(qw4h), "+v"(kfp), "+v"(kzp), "+v"(ob0));
    asm volatile(".subsection 2\n.Lkf_code_end:\n\t.fill 256, 4, 0xbf800000\n\t.subsection 0");
    const f32x4 z4 = {0.f, 0.f, 0.f, 0.f};
    f32x4 gacc = z4, qa[6];
#pragma unroll
    for (int j = 0; j < 6; ++j) { const float e = j < 5 ? 1e-6f : 0.f; qa[j] = f32x4{e, e, e, e}; asm volatile("" : "+v"(qa[j])); }
    u32x4 xk[18];
    u64 pf = 0;
    WAIT_VM(8); BARRIER();
    if (ld) { issue_w(2); issue_x(2); if (LA3) { issue_w(3); issue_x(3); } }
#pragma unroll
    for (int s = 0; s < 9; ++s) {
#pragma unroll
        for (int k = 0; k < 2; ++k) xk[2 * s + k] = lds_r128(((s & 2) ? xb[k] - 2 * SLOT : xa[k]) + slot_off(s));
        if (t8 == hg) {
            gacc = mfma16(lds_r128(gwp + s * 128), xk[2 * s], gacc);
            gacc = mfma16(lds_r128(gwp + s * 128 + 64), xk[2 * s + 1], gacc);
        }
        if (t8 == 4) {
            if (s == 1) { tag = *(const __attribute__((address_space(4))) u64*)(P.ws + WS_CTL); PIN_TAG(); if (lane == 0) lds_w64(smem + LDS_TAG, u32x2{(unsigned)tag, (unsigned)(tag >> 32)}); }
            if (s == 4) asm volatile("global_load_dwordx2 %0, %1, %2 sc1" : "=v"(pf) : "v"((lane < 5 ? lane : 0) * 128), "s"(P.ws + WS_PFL) : "memory");
            if (s == 7) {
                asm volatile("s_waitcnt vmcnt(0)" : "+v"(pf) :: "memory");
                if (!__all(pf == tag)) poll_flags(P, WS_PFL, 5, tag, lane);
            }
        }
        if (LA3) { if (s < 6) WAIT_VM(17); else if (s == 6) WAIT_VM(14); else WAIT_VM(12); }
        else if (s < 7) WAIT_VM(8); else WAIT_VM(6);
        BARRIER();
        if (ld) {
            const int sn = s + 3 + LA3;
            if (sn < 9) { issue_w(sn); issue_x(sn); } else if (sn < 12) { issue_q(sn - 9, 0); issue_q(sn - 9, 1); issue_q(sn - 9, 2); }
            if (s == 3) { qo[0] = *(LAS unsigned*)(iqp + lw * 512); qo[1] = *(LAS unsigned*)(iqp + lw * 512 + 256); qo[2] = *(LAS unsigned*)(iqp + 2048); CFENCE(); }
        }
    }
    __builtin_amdgcn_s_setprio(0);
    STAMP(5);
    if (!ld) {
        const unsigned co = WS_O + (unsigned)(((rb * 8 + 2 * lw) * 72 + hg * 9) * 256 + lane * 16);
#pragma unroll
        for (int i = 0; i < 5; ++i)
            if (i < 4 || lane < 32) st_sc1(wsr, i < 4 ? co + (i >> 1) * 18432 + (i & 1) * 1024 : co + 2048 + (lane >> 4) * (18432 - 256), u32x4{EMPTY, EMPTY, EMPTY, EMPTY});
    }
    GET_TAG();
    if (t8 == 4 && lane == 0) tag_post(P, tag);
    if (t8 == hg) {
        const float gate = rbf(__builtin_amdgcn_rcpf(1.f + __expf(-rbf(gacc[0]))));
        if (lane < 16) __hip_atomic_store((GAS float*)(P.ws + WS_GATE) + (rb * 128 + hg * 16 + lane), gate, RLX_AGENT);
    }
    auto peek = [&](int f) { return __hip_atomic_load((LAS unsigned*)(flp + f), __ATOMIC_RELAXED, __HIP_MEMORY_SCOPE_WORKGROUP); };
    auto rdq = [&](int ks, int j) {
        const int o = (ks < 6 ? ks : ks - 6) * 4608;
        return j < 4 ? lds_trfrag((ks < 6 ? qw : qwh) + o + 32 * j, 144) : lds_trfrag((ks < 6 ? qw4 : qw4h) + o, 144);
    };
    u32x4 wq[2][4], w4[2];
#pragma unroll
    for (int i = 0; i < 2; ++i) {
#pragma unroll
        for (int j = 0; j < 4; ++j) wq[i][j] = rdq(i, j);
        if (i == 0) w4[0] = rdq(0, 4);
    }
    unsigned seenq = 0;
#pragma unroll
    for (int ks = 0; ks < 18; ++ks) {
        if (ks == 4) {
            SCHED();
            WAIT_VM(0); FLAG_ADD(F_Q0);
            seenq = peek(F_Q0); NEED(seenq, F_Q0, 8);
        }
        if (ks == QH) { SCHED(); seenq = peek(F_KVR); NEED(seenq, F_KVR, 3); STAMP(6); }
        if (ks == 16) { SCHED(); FLAG_ADD(F_QD); }
#pragma unroll
        for (int j = 0; j < 5; ++j) {
            SCHED();
            if (j == 0 && ks < 17) { if (ks & 1) WAIT_LGKM(10); else WAIT_LGKM(8); }
            if (j < 4) mfma16i(qa[j], wq[ks & 1][j], xk[ks]); else mfma16i(qa[4 + (ks & 1)], w4[(ks >> 1) & 1], xk[ks]);
            SCHED();
            if (ks + 2 < 18) { if (j < 4) wq[ks & 1][j] = rdq(ks + 2, j); else if (!(ks & 1)) w4[((ks >> 1) + 1) & 1] = rdq(ks + 2, 4); }
        }
    }
    __builtin_amdgcn_s_setprio(2);
    MFMA_DONE();
#pragma unroll
    for (int i = 0; i < 4; ++i) {
        const auto sw2 = __builtin_amdgcn_permlane32_swap(__float_as_uint(qa[5][i]), __float_as_uint(qa[5][i]), false, false);
        qa[4][i] += __uint_as_float(sw2[1]);
    }
    u32x4 b0 = pack8(phi4(qa[0]), phi4(qa[1])), b1 = pack8(phi4(qa[2]), phi4(qa[3])), b2 = pack8(phi4(qa[4]), z4);
    asm volatile("" : "+v"(b0), "+v"(b1), "+v"(b2));
    STAMP(7);
    flag_wait<1>(smem, F_D, 4);
        if (t8 == 7 && lane == 0) __hip_atomic_store((GAS u64*)(P.ws + WS_HINT + (rb * 8 + hg) * 128), tag, RLX_AGENT);
    STAMP(8);
    {
        u32x4 kf[12];
#pragma unroll
        for (int i = 0; i < 12; ++i) { kf[i] = lds_r128(i < 3 ? kzp + i * 1024 : kfp + (i - 3) * 1024); SCHED(); }
        SCHED();
        const f32x4 den = mfma16(kf[2], b2, mfma16(kf[1], b1, mfma16(kf[0], b0, f32x4{1e-6f, 1e-6f, 1e-6f, 1e-6f})));
        f32x4 o[5];
        o[0] = mfma16(kf[3], b0, z4);
        o[1] = mfma16(kf[5], b1, mfma16(kf[4], b0, z4));
        o[2] = mfma16(kf[7], b1, mfma16(kf[6], b0, z4));
        o[3] = mfma16(kf[9], b2, mfma16(kf[8], b1, z4));
        o[4] = mfma16(kf[11], b2, mfma16(kf[10], b1, z4));
        f32x4 r;
#pragma unroll
        for (int i = 0; i < 4; ++i) r[i] = __builtin_amdgcn_rcpf(den[i]);
        const float r1a = g == 0 ? r[0] : r[1], r2 = g == 0 ? r[1] : r[2], r3a = g <= 1 ? r[2] : r[3], r3b = g == 0 ? r[2] : r[3];
        u32x2 ob[5];
        ob[0] = u32x2{pk_bf16(o[0][0] * r[0], o[0][1] * r[0]), pk_bf16(o[0][2] * r[0], o[0][3] * r[0])};
        ob[1] = u32x2{pk_bf16(o[1][0] * r1a, o[1][1] * r1a), pk_bf16(o[1][2] * r[1], o[1][3] * r[1])};
        ob[2] = u32x2{pk_bf16(o[2][0] * r2, o[2][1] * r2), pk_bf16(o[2][2] * r2, o[2][3] * r2)};
        ob[3] = u32x2{pk_bf16(o[3][0] * r3a, o[3][1] * r3a), pk_bf16(o[3][2] * r3b, o[3][3] * r3b)};
        ob[4] = u32x2{pk_bf16(o[4][0] * r[3], o[4][1] * r[3]), pk_bf16(o[4][2] * r[3], o[4][3] * r[3])};
        SCHED();
        SCHED();
#pragma unroll
        for (int t = 0; t < 3; ++t) {
            const u32x2 a = ob[2 * t], b = ob[t < 2 ? 2 * t + 1 : 4];
            const auto sx = __builtin_amdgcn_permlane16_swap(a.x, b.x, false, false), sy = __builtin_amdgcn_permlane16_swap(a.y, b.y, false, false);
            if (t < 2 || g == 0) st_sc1(wsr, ob0 + 1024 * t, not_empty(u32x4{sx[0], sy[0], sx[1], sy[1]}));
        }
    }
    __builtin_amdgcn_s_setprio(0);
    STAMP(9);
    if (t8 == 0) STAMP_OUT(5, blockIdx.x + 256);
    if (t8 == 7) STAMP_OUT(7, blockIdx.x + 768);
}

DEVI void phase2(const Params& P, lptr smem, int rt64, int cg, bool gemm, u64 tag, rsrc_t wsr) {
    const int lane = threadIdx.x & 63, w0 = __builtin_amdgcn_readfirstlane(threadIdx.x >> 6);
    const int w = gemm ? (w0 + 8) % 12 : w0, tid = threadIdx.x, bt = tid - 192;
    const int lo16 = lane & 15, g = lane >> 4;
    const int rt = w & 3, kt = w >> 2;
    STAMP_DECL; STAMP(1);
    constexpr int pb = P2_B;
    const int pa = gemm ? P2_ACC : 82944;
    if (gemm && w < 4) flag_wait(smem, F_W0, 1);
    STAMP(2);
    const int qrow = (2 * (4 * g + (lo16 >> 2))) * 144;
    lptr qw = smem + qrow + (lane & 3) * 8, qw4 = smem + qrow + (16 + (lane & 1)) * 8;
    int wofs[3], oofs[3];
#pragma unroll
    for (int u = 0; u < 3; ++u) {
        const int c = kt * 3 + (u + cg) % 3;
        wofs[u] = (c < 8 || !gemm) ? c * 9216 : WO8; oofs[u] = c * 2048;
    }
    auto rdo = [&](int i, int j) { return j < 4 ? lds_trfrag(qw + wofs[i >> 1] + (i & 1) * 4608 + 32 * j, 144) : lds_trfrag(qw4 + wofs[i >> 1] + (i & 1) * 4608, 144); };
    const unsigned ooff = WS_O + (unsigned)((rt64 * 4 + rt) * 18432 + lane * 16);
    auto ldo = [&](int i) { return __builtin_amdgcn_raw_buffer_load_b128(wsr, ooff + (i & 1) * 1024, oofs[i >> 1], 16); };
    u32x4 wf[6][5];
#pragma unroll
    for (int i = 0; i < 6; ++i)
#pragma unroll
        for (int j = 0; j < 5; ++j) wf[i][j] = rdo(i, j);
    const int r0 = tid < 576 ? tid / 9 : 0, c0 = tid - r0 * 9;
    unsigned boff = WS_B + (unsigned)((cg * 72 + (bt >> 3)) * 4096 + (rt64 >> 3) * 1024 + (((bt >> 2) & 1) * 32 + ((rt64 >> 2) & 1) * 16 + (rt64 & 3) * 4 + (bt & 3)) * 16);
    lptr bp = smem + pb + ((bt & 3) * 2 + ((bt >> 2) & 1)) * 1168 + (bt >> 3) * 2;
    unsigned goff = WS_GATE + (unsigned)((rt64 * 64 + r0) * 4);
    lptr ap = smem + pa + kt * (64 * P2_AP) + (rt * 16 + lo16) * P2_AP + (g & 1) * 144 + (g >> 1) * 16;
    lptr fa = smem + pa + r0 * P2_AP + c0 * 16, fb = smem + pb + (tid + (r0 >> 3)) * 16;
    unsigned fo = (unsigned)((rt64 * 64 + r0) * 1152 + cg * 144 + c0 * 16);
    asm volatile("" : "+v"(boff), "+v"(goff), "+v"(bp), "+v"(ap), "+v"(fa), "+v"(fb), "+v"(fo));
    STAMP(3);
    if (w == 3) {
        const int a0 = cg * 72, g0 = (a0 >> 6) * 11 + (a0 & 63) / 6, a1 = a0 + 71, g1 = (a1 >> 6) * 11 + (a1 & 63) / 6;
        poll_addr<OSLEEP>(P, lane < 8 ? WS_HINT + ((rt64 >> 1) * 8 + lane) * 128 : WS_BFL + min(g0 + lane - 8, g1) * 128, tag);
        if (gemm) flag_wait<1>(smem, F_QD, 8);
        flag_set(F_H, 1);
    } else flag_wait<1>(smem, F_H, 1);
    STAMP(4);
    u32x4 bv; unsigned gv;
    auto b_put = [&]() {
#pragma unroll
        for (int q = 0; q < 4; ++q) { lds_w16(bp + (2 * q) * 144, bv[q]); lds_w16(bp + (2 * q + 1) * 144, bv[q] >> 16); }
    };
    bool b_todo = false;
    if (w0 >= 3) bv = ld_sc1(wsr, boff);
    f32x4 acc[5];
    {
        u32x4 of[6];
#pragma unroll
        for (int i = 0; i < 6; ++i) of[i] = ldo(i);
        gv = __builtin_amdgcn_raw_buffer_load_b32(wsr, goff, 0, 16);
        SCHED();
        if (w0 >= 3) { if (__all(umax4(bv) != EMPTY)) b_put(); else b_todo = true; }
        SCHED();
#pragma unroll
        for (int j = 0; j < 5; ++j) {
            acc[j] = f32x4{0.f, 0.f, 0.f, 0.f};
#pragma unroll
            for (int i = 0; i < 6; ++i) {
                acc[j] = mfma16(wf[i][j], of[i], acc[j]);
                if (i == 3 && j > 0) { SCHED(); lds_w128(ap + (j - 1) * 32, __builtin_bit_cast(u32x4, acc[j - 1])); SCHED(); }
            }
        }
        if (g < 2) lds_w128(ap + 128, __builtin_bit_cast(u32x4, acc[4]));
    }
    if (!__all(acc[4][0] == acc[4][0])) {
#pragma unroll
        for (int j = 0; j < 5; ++j) acc[j] = f32x4{0.f, 0.f, 0.f, 0.f};
        u32x4 of[6];
#pragma unroll 1
        for (unsigned spins = 0; spins < SPIN_CAP; ++spins) {
            CFENCE();
            unsigned m = 0;
#pragma unroll
            for (int i = 0; i < 6; ++i) of[i] = ldo(i);
#pragma unroll
            for (int i = 0; i < 6; ++i) m = max(m, umax4(of[i]));
            if (__all(m != EMPTY)) break;
        }
#pragma unroll
        for (int i = 0; i < 6; ++i)
#pragma unroll
            for (int j = 0; j < 5; ++j) acc[j] = mfma16(rdo(i, j), of[i], acc[j]);
#pragma unroll
        for (int j = 0; j < 5; ++j) if (j < 4 || g < 2) lds_w128(ap + j * 32, __builtin_bit_cast(u32x4, acc[j]));
    }
    if (b_todo) {
#pragma unroll 1
        for (unsigned spins = 0; spins < SPIN_CAP; ++spins) {
            CFENCE();
            bv = ld_sc1(wsr, boff);
            if (__all(umax4(bv) != EMPTY)) break;
            __builtin_amdgcn_s_sleep(2);
        }
        b_put();
    }
    const float gt = __uint_as_float(gv);
    float og = rbf(1.f - gt);
    asm volatile("" : "+v"(og));
    STAMP(5);
    BARRIER();
    STAMP(6);
    if (w0 < 9) {
        lptr a = fa;
        const f32x4 s0 = __builtin_bit_cast(f32x4, lds_r128(a)) + __builtin_bit_cast(f32x4, lds_r128(a + 64 * P2_AP)) + __builtin_bit_cast(f32x4, lds_r128(a + 128 * P2_AP));
        const f32x4 s1 = __builtin_bit_cast(f32x4, lds_r128(a + 144)) + __builtin_bit_cast(f32x4, lds_r128(a + 64 * P2_AP + 144)) + __builtin_bit_cast(f32x4, lds_r128(a + 128 * P2_AP + 144));
        const u32x4 bb = lds_r128(fb);
        u32x4 o;
#pragma unroll
        for (int i = 0; i < 4; ++i) {
            const float a0 = i < 2 ? s0[2 * i] : s1[2 * i - 4], a1 = i < 2 ? s0[2 * i + 1] : s1[2 * i - 3];
            o[i] = pk_bf16(gt * a0 + og * bf2f(bb[i]), gt * a1 + og * bfhi(bb[i]));
        }
        __builtin_amdgcn_raw_buffer_store_b128(o, __builtin_amdgcn_make_buffer_rsrc(P.out, 0, SEQ * DM * 2, 0x00020000), fo, 0, OUTAUX);
    }
    STAMP(7);
    if (w == 4) STAMP_OUT(blockIdx.x < 128 ? 3 : 6, blockIdx.x + 512);
}

DEVI void helper_role(const Params& P, lptr smem, int xcd, int li, u64& tag, rsrc_t wsr) {
    const int tid = threadIdx.x, lane = tid & 63, w = __builtin_amdgcn_readfirstlane(tid >> 6);
    STAMP_DECL; STAMP(1);
    if (li >= 13) {
        if (HD) __builtin_amdgcn_s_sleep(HD);
#pragma unroll 1
        for (int n = 12 * NTW + (li - 13) + 3 * w; n < 171; n += 36) touch_group(P, xcd, n, lane);
    }
    if (li == 12) {
#pragma unroll
        for (int i = 0; i < 7; ++i) {
            const int u = (i * 5 + xcd - 3) * 12 + w;
            if (u < 384) st_sc1(wsr, WS_PART + (unsigned)(u * 1024 + lane * 16), u32x4{EMPTY, EMPTY, EMPTY, EMPTY});
        }
        WAIT_VM(0);
    }
    PIN_TAG();
    BARRIER();
    if (tid == 0) {
        if (li == 12) __hip_atomic_store((GAS u64*)(P.ws + WS_PFL + (xcd - 3) * 128), tag, RLX_AGENT);
        tag_post(P, tag);
    }
    if (li >= 13) {
#pragma unroll 1
        for (int n = 171 + (li - 13) + 3 * w; n < 216; n += 36) touch_group(P, xcd, n, lane);
    }
    STAMP(2);
    if (w == 11) {
        STAMP_OUT(8, blockIdx.x);
        if (blockIdx.x == 255) {
            const GAS u64* f = (const GAS u64*)(P.ws + WS_ARR) + lane;
#pragma unroll 1
            for (unsigned spins = 0; spins < SPIN_CAP; ++spins) {
                const bool ok = __hip_atomic_load(f, RLX_AGENT) == tag && __hip_atomic_load(f + 64, RLX_AGENT) == tag &&
                                __hip_atomic_load(f + 128, RLX_AGENT) == tag && __hip_atomic_load(f + 192, RLX_AGENT) == tag;
                if (__all(ok)) break;
                __builtin_amdgcn_s_sleep(8);
            }
            if (lane == 0) __hip_atomic_store((GAS u64*)(P.ws + WS_CTL), tag, RLX_AGENT);
        }
    }
}

__global__ __launch_bounds__(768, 3) void k_fused(Params P) {
    __shared__ __attribute__((aligned(16))) char smem_[LDS_BYTES];
    lptr smem = (lptr)smem_;
    const int b = blockIdx.x, xcd = b & 7, idx = b >> 3;
    const int lane = threadIdx.x & 63, w = __builtin_amdgcn_readfirstlane(threadIdx.x >> 6);
    const rsrc_t wsr = __builtin_amdgcn_make_buffer_rsrc(P.ws, 0, (int)WS_END, 0x00020000);
    const int rt64 = xcd * 4 + (idx & 3), cg2 = idx >> 2;
    if (idx >= CPF && w < 6) {
        const char* pc; unsigned len;
        asm volatile("s_getpc_b64 %0\n.Lkf_code_begin:\n\ts_mov_b32 %1, .Lkf_code_end-.Lkf_code_begin" : "=s"(pc), "=s"(len));
        touch(pc, min((unsigned)(threadIdx.x * 128), len));
    }
    u64 tag;
    if (__builtin_expect(idx < 16, 1)) {
        const int rbl = idx & 3, hgl = idx >> 2, rb = (xcd >> 1) * 4 + rbl, hg = (xcd & 1) * 4 + hgl;
        if (__builtin_expect((w & 12) != 4, 1)) gemm_q(P, smem, rb, hg, w < 4 ? w : w - 4, tag, wsr);
        else gemm_kv(P, smem, rb, hg, w - 4, cg2, tag, wsr);
    } else {
        tag = *(const __attribute__((address_space(4))) u64*)(P.ws + WS_CTL);
        const int li = idx - 16, gid = li < 11 ? xcd * 11 + li : 88 + (li - 11) * 8 + xcd;
        if (gid < 99) ssm_role(P, smem, xcd, li, gid, cg2, tag, wsr);
        else {
            helper_role(P, smem, xcd, li, tag, wsr);
            if (w < 9) { wo_issue<0x1ff>(P, cg2, w, 9, lane); WAIT_VM(0); }
            if (w == 10 && lane < 16) lds_w32(smem + FL + lane * 4, 0u);
            BARRIER();
        }
    }
    phase2(P, smem, rt64, cg2, idx < 16, tag, wsr);
}

extern "C" void kernel_launch(void* const* d_in, const int* in_sizes, int n_in, void* d_out, int out_size, void* d_ws, size_t ws_size,
                              hipStream_t stream) {
    Params P;
    P.x = (const char*)d_in[0]; P.wqkv = (const char*)d_in[1]; P.wo = (const char*)d_in[2];
    P.logA = (const char*)d_in[4]; P.aim = (const char*)d_in[5]; P.cre = (const char*)d_in[6]; P.cim = (const char*)d_in[7];
    P.logdt = (const char*)d_in[8]; P.dskip = (const char*)d_in[9]; P.gw = (const char*)d_in[10];
    P.out = (char*)d_out; P.ws = (char*)d_ws;
    k_fused<<<256, 768, 0, stream>>>(P);
}
```

```cpp
#include <hip/hip_runtime.h>
#include <stdint.h>

#define GAS __attribute__((address_space(1)))
#define LAS __attribute__((address_space(3)))
#define DEVI __device__ __forceinline__
typedef unsigned short u16;
typedef unsigned long long u64;
typedef __attribute__((ext_vector_type(2))) unsigned u32x2;
typedef __attribute__((ext_vector_type(3))) unsigned u32x3;
typedef __attribute__((ext_vector_type(4))) unsigned u32x4;
typedef __attribute__((ext_vector_type(4))) short s16x4;
typedef __attribute__((ext_vector_type(2))) float f32x2;
typedef __attribute__((ext_vector_type(4))) float f32x4;
typedef __attribute__((ext_vector_type(16))) float f32x16;
typedef __attribute__((ext_vector_type(2))) __bf16 bf16x2;
typedef __attribute__((ext_vector_type(8))) __bf16 bf16x8;
typedef __attribute__((ext_vector_type(2))) _Float16 f16x2;
typedef __attribute__((ext_vector_type(8))) _Float16 f16x8;
typedef LAS char* lptr;

#define PREF 1
#define ROT 0
#define FSLEEP 2
#define NSLEEP "3"
#define LDRIFT 0
#define PSLEEP 6
#define CSLEEP 1
#define OSLEEP 2
#define RSLEEP 3
#define HOLD 1
#define GROT ((w + li) & 3)
#define NTW 8
#define OUTAUX 17
#define TEARLY 1
#define HDELAY 0
#define CPF 16
#define LA3 0
#define TD1 0
#define TD2 0
#define TW2 3
#define HD 0
#define QH 4
#define WOA 0x00f
#define WOT 48
#define WOSW 1
constexpr int SEQ = 2048, DM = 576;

constexpr unsigned WS_CTL = 0;
constexpr unsigned WS_ARR = 256;
constexpr unsigned WS_BFL = 4096;
constexpr unsigned WS_HINT = 20480;
constexpr unsigned WS_GATE = 36864;
constexpr unsigned WS_PFL = 49152;
constexpr unsigned WS_PART = 65536;
constexpr unsigned WS_O = WS_PART + 8 * 16 * 3072;
constexpr unsigned WS_B = WS_O + SEQ * DM * 2;
constexpr unsigned WS_END = WS_B + SEQ * DM * 2;
constexpr unsigned EMPTY = 0xffffffffu;

constexpr size_t WS_STAMP = 16u << 20;
#define STAMP_DECL
#define STAMP(i)
#define CSTAMP(i)
#define STAMP_OUT(role, slot)

struct Params {
    const char* x; const char* wqkv; const char* wo; const char* logA; const char* aim; const char* cre; const char* cim;
    const char* logdt; const char* dskip; const char* gw; char* out; char* ws;
};

constexpr int LDS_BYTES = 163840, FL = 163584, LDS_TAG = FL + 64, LDS_DUMMY = FL + 128, LDS_JUNK = 161280;
enum { F_SW = 0, F_LANDED = 4, F_W1 = 20, F_B = 28, F_H = 24, F_KVP = 36, F_KVR = 40, F_Q0 = 44, F_QD = 48, F_D = 52, F_C1 = 56, F_W0 = 60 };
constexpr int WO8 = 132096, P2_ACC = 73728, P2_AP = 304, P2_B = 141312, P2_G = 150528;

DEVI float bf2f(unsigned h) { return __uint_as_float(h << 16); }
DEVI float bfhi(unsigned h) { return __uint_as_float(h & 0xffff0000u); }
DEVI unsigned pk_bf16(float a, float b) { return __builtin_bit_cast(unsigned, __builtin_convertvector(f32x2{a, b}, bf16x2)); }
DEVI unsigned pk_f16(f32x2 v) { return __builtin_bit_cast(unsigned, __builtin_convertvector(v, f16x2)); }
DEVI float rbf(float a) { return bf2f(pk_bf16(a, a)); }
DEVI f32x4 mfma16(u32x4 a, u32x4 b, f32x4 c) {
    return __builtin_amdgcn_mfma_f32_16x16x32_bf16(__builtin_bit_cast(bf16x8, a), __builtin_bit_cast(bf16x8, b), c, 0, 0, 0);
}
DEVI void mfma16i(f32x4& c, u32x4 a, u32x4 b) { asm volatile("v_mfma_f32_16x16x32_bf16 %0, %1, %2, %0" : "+v"(c) : "v"(a), "v"(b)); }
#define MFMA_DONE() do { __builtin_amdgcn_sched_barrier(0); asm volatile("s_nop 7\n\ts_nop 1" ::: "memory"); __builtin_amdgcn_sched_barrier(0); } while (0)
DEVI u32x4 lds_r128(lptr p) { return *(LAS u32x4*)p; }
DEVI u32x2 lds_r64(lptr p) { return *(LAS u32x2*)p; }
DEVI float lds_rf(lptr p) { return *(LAS float*)p; }
DEVI void lds_w128(lptr p, u32x4 v) { *(LAS u32x4*)p = v; }
DEVI void lds_w64(lptr p, u32x2 v) { *(LAS u32x2*)p = v; }
DEVI void lds_w32(lptr p, unsigned v) { *(LAS unsigned*)p = v; }
DEVI void lds_w16(lptr p, unsigned v) { *(LAS u16*)p = (u16)v; }
DEVI u32x2 lds_tr(lptr p) { return __builtin_bit_cast(u32x2, __builtin_amdgcn_ds_read_tr16_b64_v4i16((LAS s16x4*)p)); }
DEVI u32x4 lds_trfrag(lptr p, int off2) { u32x2 a = lds_tr(p), b = lds_tr(p + off2); return u32x4{a.x, a.y, b.x, b.y}; }
template <int OFF> DEVI void glds16o(const char* sbase, unsigned voff) {
    asm volatile("global_load_lds_dwordx4 %0, %1 offset:%2" :: "v"(voff), "s"(sbase), "i"(OFF) : "memory");
}
DEVI void glds16(const char* sbase, unsigned voff, unsigned l) {
    asm volatile("s_mov_b32 m0, %2\n\ts_nop 0\n\tglobal_load_lds_dwordx4 %0, %1" :: "v"(voff), "s"(sbase), "s"(l) : "memory");
}
#define SET_M0(v) asm volatile("s_mov_b32 m0, %0\n\ts_nop 0" :: "s"(v) : "memory")
#define W_ADDTID(v, off) asm volatile("ds_write_addtid_b32 %0 offset:%1" :: "v"(v), "i"(off) : "memory")
#define CFENCE() asm volatile("" ::: "memory")
#define SCHED() __builtin_amdgcn_sched_barrier(0)
#define WAIT_VM(n) asm volatile("s_waitcnt vmcnt(" #n ")" ::: "memory")
#define WAIT_LGKM(n) __builtin_amdgcn_s_waitcnt(0xC07F | ((n) << 8))
#define WAIT_LGKM0() asm volatile("s_waitcnt lgkmcnt(0)" ::: "memory")
#define BARRIER() do { SCHED(); asm volatile("s_waitcnt lgkmcnt(0)\n\ts_barrier" ::: "memory"); SCHED(); } while (0)
#define RAW_BARRIER() do { SCHED(); asm volatile("s_barrier" ::: "memory"); SCHED(); } while (0)
#define RLX_AGENT __ATOMIC_RELAXED, __HIP_MEMORY_SCOPE_AGENT
constexpr unsigned SPIN_CAP = 1u << 16;
typedef __amdgpu_buffer_rsrc_t rsrc_t;
DEVI u32x4 ld_sc1(rsrc_t r, unsigned off) { return __builtin_amdgcn_raw_buffer_load_b128(r, off, 0, 16); }
DEVI void st_sc1(rsrc_t r, unsigned off, u32x4 v) { __builtin_amdgcn_raw_buffer_store_b128(v, r, off, 0, 16); }

DEVI u32x4 pack8(f32x4 a, f32x4 b) { return u32x4{pk_bf16(a[0], a[1]), pk_bf16(a[2], a[3]), pk_bf16(b[0], b[1]), pk_bf16(b[2], b[3])}; }
DEVI float phi(float t) {
    const float e = __builtin_amdgcn_fmed3f(__builtin_amdgcn_exp2f(t * 1.44269504089f), 0.f, 1.f);
    return __int_as_float(max(__float_as_int(t), 0x358637bd)) + e;
}
DEVI f32x4 phi4(f32x4 v) {
    f32x2 a, b;
    const f32x2 c = {1.44269504089f, 1.44269504089f};
    asm("v_pk_mul_f32 %0, %2, %4\n\tv_pk_mul_f32 %1, %3, %4" : "=&v"(a), "=&v"(b) : "v"(f32x2{v[0], v[1]}), "v"(f32x2{v[2], v[3]}), "s"(c));
    f32x4 r;
#pragma unroll
    for (int i = 0; i < 4; ++i)
        r[i] = __int_as_float(max(__float_as_int(v[i]), 0x358637bd)) + __builtin_amdgcn_fmed3f(__builtin_amdgcn_exp2f(i < 2 ? a[i] : b[i - 2]), 0.f, 1.f);
    return r;
}
DEVI unsigned umax4(u32x4 a) { return max(max(a.x, a.y), max(a.z, a.w)); }
DEVI u32x4 not_empty(u32x4 a) { return a; }
DEVI void tag_post(const Params& P, u64 tag) { __hip_atomic_store((GAS u64*)(P.ws + WS_ARR) + blockIdx.x, tag, RLX_AGENT); }
#define PIN_TAG() do { asm volatile("" : "+s"(tag)); tag += 1ull; } while (0)
template <int SL = PSLEEP>
DEVI void poll_addr(const Params& P, unsigned off, u64 tag) {
    const GAS u64* f = (const GAS u64*)(P.ws + off);
#pragma unroll 1
    for (unsigned spins = 0; spins < SPIN_CAP; ++spins) {
        if (__all(__hip_atomic_load(f, RLX_AGENT) == tag)) break;
        __builtin_amdgcn_s_sleep(SL);
    }
}
template <int SL = PSLEEP>
DEVI void poll_flags(const Params& P, unsigned off, int n, u64 tag, int lane) { poll_addr<SL>(P, off + (lane < n ? lane : 0) * 128, tag); }
DEVI unsigned flag_get(lptr smem, int f) { return __builtin_amdgcn_readfirstlane(__hip_atomic_load((LAS unsigned*)(smem + FL + f), __ATOMIC_RELAXED, __HIP_MEMORY_SCOPE_WORKGROUP)); }
template <int SL = FSLEEP>
DEVI void flag_wait(lptr smem, int f, unsigned v) {
#pragma unroll 1
    for (unsigned spins = 0; spins < (SPIN_CAP << 4); ++spins) { if (flag_get(smem, f) >= v) break; if (SL) __builtin_amdgcn_s_sleep(SL); }
    CFENCE();
}
DEVI void flag_add(int f) { asm volatile("s_mov_b64 exec, 1\n\tds_add_u32 %0, %1\n\ts_mov_b64 exec, -1" :: "v"(FL + f), "v"(1u) : "memory"); }
#define FLAG_ADD(f) asm volatile("s_mov_b64 exec, 1\n\tds_add_u32 %0, %1 offset:%2\n\ts_mov_b64 exec, -1" :: "v"(flp), "v"(one), "i"(f) : "memory")
DEVI void flag_set(int f, unsigned v) { asm volatile("s_mov_b64 exec, 1\n\tds_write_b32 %0, %1\n\ts_mov_b64 exec, -1" :: "v"(FL + f), "v"(v) : "memory"); }
#define NEEDP(seen, ptr, f, v) do { unsigned c_; asm volatile( \
        "v_cmp_gt_u32 vcc, %4, %1\n\ts_cbranch_vccnz 0f\n1:\n\t.subsection 1\n" \
        "0:\n\ts_mov_b32 %0, 0x100000\n2:\n\ts_sleep " NSLEEP "\n\tds_read_b32 %1, %2 offset:%3\n\ts_sub_u32 %0, %0, 1\n\ts_waitcnt lgkmcnt(0)\n\ts_cbranch_scc1 1b\n\t" \
        "v_cmp_gt_u32 vcc, %4, %1\n\ts_cbranch_vccnz 2b\n\ts_branch 1b\n\t.subsection 0" \
        : "=&s"(c_), "+v"(seen) : "v"(ptr), "i"(f), "i"(v) : "memory", "scc", "vcc"); } while (0)
#define NEED(seen, f, v) NEEDP(seen, flp, f, v)
DEVI void touch(const char* sbase, unsigned voff) {
    asm volatile("s_mov_b32 m0, %2\n\ts_nop 0\n\tglobal_load_lds_dword %0, %1" :: "v"(voff), "s"(sbase), "s"(LDS_JUNK) : "memory");
}
DEVI const char* group_addr(const Params& P, int xcd, int n, int lane) {
    if (n < 126) {
        const int c0 = n / 18, r = n - c0 * 18, c = c0 + 2, l = r - 8;
        return r < 8 ? P.x + (unsigned)(((xcd >> 1) * 512 + r * 64 + lane) * 1152 + c * 128)
                     : P.wqkv + (unsigned)((c * 64 + lane) * 3456 + (l < 5 ? 1152 : 2304 - 640) + (xcd & 1) * 512 + l * 128);
    }
    const int c = (n - 126) / 5, l = n - 126 - c * 5;
    return P.wqkv + (unsigned)((c * 64 + lane) * 3456 + (xcd & 1) * 512 + l * 128);
}
DEVI void touch_group(const Params& P, int xcd, int n, int lane) {
    if (n < 126) {
        const int c0 = n / 18, r = n - c0 * 18, c = c0 + 2;
        if (r < 8) touch(P.x, (unsigned)(((xcd >> 1) * 512 + r * 64 + lane) * 1152 + c * 128));
        else { const int l = r - 8; touch(P.wqkv, (unsigned)((c * 64 + lane) * 3456 + (l < 5 ? 1152 : 2304 - 640) + (xcd & 1) * 512 + l * 128)); }
    } else if (n < 171) {
        const int c = (n - 126) / 5, l = n - 126 - c * 5;
        touch(P.wqkv, (unsigned)((c * 64 + lane) * 3456 + (xcd & 1) * 512 + l * 128));
    } else {
        const int l = (n - 171) / 9, c = n - 171 - l * 9;
        touch(P.wo, (unsigned)((c * 64 + lane) * 1152 + l * 128));
    }
}
template <unsigned CH>
DEVI void wo_issue(const Params& P, int cg, int first, int stride, int lane) {
#pragma unroll 1
    for (int m = first; m < 9; m += stride) {
        const int q = 64 * m + lane, rho = q / 9, pc = q - rho * 9;
        const int k = (rho & 32) + 8 * ((rho >> 3) & 3) + 4 * (rho & 1) + ((rho >> 1) & 3);
        const unsigned qo = (unsigned)(k * 1152 + cg * 144 + pc * 16);
#pragma unroll
        for (int c = 0; c < 9; ++c) if ((CH >> c) & 1) glds16(P.wo + c * (64 * 1152), qo, (unsigned)(c * 9216 + m * 1024));
    }
}

constexpr int SP = 3088;
constexpr int XT_OFF = 135936, XT_BYTES = 4608, XPAD = 256;
constexpr int TT_OFF = 107520, TC = 1184, PT_OFF = 82944, PC_OFF = 2560;

#define CM2(o1, p1, a1, b1, o2, p2, a2, b2) do { asm( \
    "v_pk_mul_f32 %0, %2, %3 op_sel:[1,1] op_sel_hi:[1,0] neg_lo:[0,1]\n\tv_pk_mul_f32 %1, %4, %5 op_sel:[1,1] op_sel_hi:[1,0] neg_lo:[0,1]\n\t" \
    "v_pk_fma_f32 %0, %2, %3, %0 op_sel_hi:[0,1,1]\n\tv_pk_fma_f32 %1, %4, %5, %1 op_sel_hi:[0,1,1]" \
    : "=&v"(o1), "=&v"(o2) : "v"(a1), "v"(b1), "v"(a2), "v"(b2)); \
    asm("v_cvt_pk_f16_f32 %0, %2, %3\n\tv_cvt_pk_f16_f32 %1, %4, %5" : "=&v"(p1), "=&v"(p2) : "v"((o1).x), "v"((o1).y), "v"((o2).x), "v"((o2).y)); } while (0)
#define HM2(p1, a1, p2, a2, b) asm( \
    "v_pk_mul_f16 %0, %2, %4 op_sel_hi:[0,1]\n\tv_pk_mul_f16 %1, %3, %4 op_sel_hi:[0,1]\n\t" \
    "v_pk_fma_f16 %0, %2, %4, %0 op_sel:[1,1,0] op_sel_hi:[1,0,1] neg_lo:[0,1,0]\n\tv_pk_fma_f16 %1, %3, %4, %1 op_sel:[1,1,0] op_sel_hi:[1,0,1] neg_lo:[0,1,0]" \
    : "=&v"(p1), "=&v"(p2) : "v"(a1), "v"(a2), "v"(b))
DEVI f32x2 cexp_p(float a2, float f, float p) {
    float m = __builtin_amdgcn_exp2f(a2 * p);
    float r = __builtin_amdgcn_fractf(f * p);
    return f32x2{m * __builtin_amdgcn_cosf(r), m * __builtin_amdgcn_sinf(r)};
}
DEVI u32x4 mask_frag(unsigned bits, int g, unsigned val) {
    unsigned by = bits >> (8 * g);
    u32x4 r;
#pragma unroll
    for (int i = 0; i < 4; ++i) { unsigned t = (by >> (2 * i)) & 3u; r[i] = ((t | (t << 15)) & 0x10001u) * val; }
    return r;
}
struct Tab2 { unsigned v[8][64][4]; };
constexpr Tab2 make_tab2() {
    Tab2 t{};
    for (int l = 0; l < 64; ++l) {
        const int r = l & 15, g = l >> 4, j = l & 31, h = l >> 5;
        const unsigned bits[6] = {7u << (r + 11), 31u << (r + 6), 7u << (r + 3), 127u << (r + 5), 511u << (r + 4), 3u << (r + 12)};
        for (int m = 0; m < 6; ++m)
            for (int i = 0; i < 4; ++i) {
                const unsigned q = (bits[m] >> (8 * g + 2 * i)) & 3u, val = m < 5 ? 0x3f80u : 0xffffu;
                t.v[m][l][i] = (q & 1 ? val : 0u) | (q & 2 ? val << 16 : 0u);
            }
        for (int c = 0; c < 4; ++c) {
            const int v2 = 79 - (32 + (j >> 1)) - c + 4, v3 = 127 - (64 + 32 * h + (j >> 1)) - c + 4;
            t.v[6][l][c] = PT_OFF + (j & 1) * 1280 + c * 320 + (v2 >> 2) * 16 + h * 8 + (v2 & 3) * 2 - 64;
            t.v[7][l][c] = PT_OFF + PC_OFF + c * 320 + (v3 >> 2) * 16 + (j & 1) * 8 + (v3 & 3) * 2 - 64;
        }
    }
    return t;
}
__device__ const Tab2 g_tab2 = make_tab2();
DEVI float wave_max(float v) {
    v = fmaxf(v, __int_as_float(__builtin_amdgcn_update_dpp(0, __float_as_int(v), 0xB1, 0xf, 0xf, true)));
    v = fmaxf(v, __int_as_float(__builtin_amdgcn_update_dpp(0, __float_as_int(v), 0x4E, 0xf, 0xf, true)));
    v = fmaxf(v, __int_as_float(__builtin_amdgcn_update_dpp(0, __float_as_int(v), 0x124, 0xf, 0xf, true)));
    v = fmaxf(v, __int_as_float(__builtin_amdgcn_update_dpp(0, __float_as_int(v), 0x128, 0xf, 0xf, true)));
    const int i = __float_as_int(v);
    const int a = max(__builtin_amdgcn_readlane(i, 0), __builtin_amdgcn_readlane(i, 16)), b = max(__builtin_amdgcn_readlane(i, 32), __builtin_amdgcn_readlane(i, 48));
    return __int_as_float(max(a, b));
}

DEVI void ssm_role(const Params& P, lptr smem, int xcd, int li, int gid, int cg2, u64& tag, rsrc_t wsr) {
    const int gl = gid / 11, gi = gid - gl * 11, ch0 = gl * 64 + (gi < 10 ? gi * 6 : 58);
    const int tid = threadIdx.x, lane = tid & 63, w = __builtin_amdgcn_readfirstlane(tid >> 6);
    const int ch = w >> 1, half = (w ^ (w >> 2)) & 1, d = ch0 + ch;
    const bool own = gi < 10 || ch >= 2;
    const int lo16 = lane & 15, g = lane >> 4;
    STAMP_DECL; STAMP(1);
    __builtin_amdgcn_s_setprio(3);
    if (half) {
        const char* tp = (const char*)&g_tab2;
        SET_M0(TT_OFF + ch * 8192);
        glds16o<0>(tp, (unsigned)(lane * 16)); glds16o<1024>(tp, (unsigned)(lane * 16)); glds16o<2048>(tp, (unsigned)(lane * 16)); glds16o<3072>(tp, (unsigned)(lane * 16));
        SET_M0(TT_OFF + ch * 8192 + 4096);
        tp += 4096;
        glds16o<0>(tp, (unsigned)(lane * 16)); glds16o<1024>(tp, (unsigned)(lane * 16)); glds16o<2048>(tp, (unsigned)(lane * 16)); glds16o<3072>(tp, (unsigned)(lane * 16));
    }
    const unsigned po = (unsigned)(d * 256 + lane * 4);
    unsigned r_la, r_ai, r_cr, r_ci, r_dt, r_ds, junk = 0, junk2 = 0;
    asm volatile("global_load_dword %0, %6, %8\n\tglobal_load_dword %1, %6, %9\n\tglobal_load_dword %2, %6, %10\n\tglobal_load_dword %3, %6, %11\n\t"
                 "global_load_ushort %4, %7, %12\n\tglobal_load_ushort %5, %7, %13"
                 : "=&v"(r_cr), "=&v"(r_ci), "=&v"(r_la), "=&v"(r_ai), "=&v"(r_dt), "=&v"(r_ds)
                 : "v"(po), "v"(d * 2), "s"(P.cre), "s"(P.cim), "s"(P.logA), "s"(P.aim), "s"(P.logdt), "s"(P.dskip) : "memory");
#define TOUCH(dst, addr) asm volatile("global_load_dword %0, %1, off" : "+v"(dst) : "v"(addr) : "memory")
    const bool tch = w < NTW && li < 12;
    const char* tga = group_addr(P, xcd, tch ? w * 12 + li : 0, lane);
    RAW_BARRIER();
    if (tch && w < TW2) TOUCH(junk, tga);
    SCHED();
    const int tt = (tid + (li & 7) * 64) & 511;
    u32x3 xr[4] = {};
    const unsigned xoff = (unsigned)(tt * 4608 + ch0 * 2);
    const unsigned boff = own ? WS_B + (unsigned)(d * 4096 + half * 2048 + lane * 16) : WS_END;
#define GATHER(i) do { SCHED(); if (w < 8) asm volatile("global_load_dwordx3 %0, %1, %2 offset:%3" : "+v"(xr[i]) : "v"(xoff), "s"(P.x), "i"((i) * 1152) : "memory"); SCHED(); } while (0)
#define PRIO(i) do { SCHED(); __builtin_amdgcn_s_setprio(i); SCHED(); } while (0)
    SCHED();
    lptr xt = smem + XT_OFF + ch * XT_BYTES, tb = smem + TT_OFF + ch * 4 * TC, pt = smem + PT_OFF + ch * 4096;
    const int j32 = lane & 31, h = lane >> 5;
    if (!half) { lds_w32(smem + FL + 32, 0u); lds_w32(smem + FL + 36 + ch * 4, 0u); }
    int arow;
    {
        const u64 map0 = 0x0a09080803020100ull, map1 = 0x0c0b0a0a07060504ull, map2 = 0x1413110e12100f0dull;
        const u64 mm = j32 < 8 ? map0 : (j32 < 16 ? map1 : map2);
        arow = (int)((mm >> (8 * (j32 & 7))) & 0xff);
    }
    lptr ap = smem + arow * SP + w * 256 + h * 16, bp = smem + j32 * SP + w * 256 + h * 16;
    const int cpy = (3 - lo16) & 3;
    lptr xf7 = xt + (XPAD - 16 + 16 * lo16 + 8 * g) * 2 - 64 * 7;
    lptr tf = tb + cpy * TC + 2 * (527 - lo16 + 8 * g - cpy) - 64 * 16;
    lptr xc = smem + (w ^ 1) * 256 + lane * 4, xb = smem + XT_OFF + (XPAD + tt * 4) * 2;
    asm volatile("" : "+v"(ap), "+v"(bp), "+v"(xf7), "+v"(tf), "+v"(xc), "+v"(xb));
    if (tch && w < TW2) WAIT_VM(1); else WAIT_VM(0);
    asm volatile("" : "+v"(r_la), "+v"(r_ai), "+v"(r_cr), "+v"(r_ci), "+v"(r_dt), "+v"(r_ds));
    if (tch && w >= TW2) TOUCH(junk, tga);
    const float dt = rbf(__expf(bf2f(r_dt)));
    const float cm = wave_max(fmaxf(fmaxf(fabsf(bf2f(r_cr)), fabsf(bfhi(r_cr))), fmaxf(fabsf(bf2f(r_ci)), fabsf(bfhi(r_ci)))));
    unsigned ce = __float_as_uint(cm) >> 23; ce = ce < 16u ? 16u : (ce > 240u ? 240u : ce);
    const float cs = __uint_as_float((263u - ce) << 23), ics = __uint_as_float((ce - 9u) << 23);
    f32x16 kacc;
#pragma unroll
    for (int i = 0; i < 16; ++i) kacc[i] = 0.f;
    STAMP(2);
    {
        const unsigned sh = half * 16;
        const float la = bf2f(r_la >> sh), ai = bf2f(r_ai >> sh), cr = bf2f(r_cr >> sh) * cs, ci = bf2f(r_ci >> sh) * cs;
        const float a2 = (-rbf(__expf(la)) * dt) * 1.44269504089f, f = (ai * dt) * 0.15915494309f;
#define PUT(row, v) W_ADDTID(v, ((row) & 15) * SP)
        const f32x2 w1 = cexp_p(a2, -f, 1.f), W = cexp_p(a2, -f, 256.f);
        f32x2 u[8], v[4], c[9], e4, e8, t2;
        unsigned U[8], V[4], p, q;
        u[1] = cexp_p(a2, f, 8.f); v[1] = cexp_p(a2, f, 64.f);
        c[0] = f32x2{cr, -ci};
        U[1] = pk_f16(u[1]); V[1] = pk_f16(v[1]);
        GATHER(0);
        SET_M0(w * 256);
        PUT(0, pk_f16(c[0]));
        CM2(c[1], p, c[0], w1, u[2], U[2], u[1], u[1]); PUT(1, p);
        CM2(c[2], p, c[1], w1, u[3], U[3], u[2], u[1]); PUT(2, p);
        CM2(c[3], p, c[2], w1, u[4], U[4], u[2], u[2]); PUT(3, p);
        CM2(c[4], p, c[3], w1, u[5], U[5], u[4], u[1]); PUT(4, p);
        CM2(c[5], p, c[4], w1, u[6], U[6], u[4], u[2]); PUT(5, p);
        CM2(c[6], p, c[5], w1, u[7], U[7], u[4], u[3]); PUT(6, p);
        CM2(c[7], p, c[6], w1, v[2], V[2], v[1], v[1]); PUT(7, p);
        CM2(c[8], p, c[7], w1, v[3], V[3], v[2], v[1]);
        PRIO(2);
        GATHER(1);
        CM2(e8, p, c[8], W, e4, q, c[4], W); PUT(12, p); PUT(10, q);
        CM2(c[8], p, e8, W, c[4], q, e4, W); PUT(14, p); PUT(13, q);
        CM2(t2, p, c[0], W, e4, q, c[4], W); PUT(8, p); PUT(15, q);
        CM2(t2, p, c[2], W, e4, q, c[6], W); PUT(9, p); PUT(11, q);
        SET_M0(w * 256 + 16 * SP);
        CM2(e8, p, c[8], W, t2, q, W, W); PUT(17, p);
        CM2(e4, p, c[8], t2, c[0], q, e8, t2); PUT(16, p); PUT(18, q);
        CM2(c[8], p, e4, t2, e8, q, c[0], t2); PUT(19, p); PUT(20, q);
        PRIO(1);
        GATHER(2);
        u32x4 af[8];
#pragma unroll
        for (int s = 0; s < 8; ++s) af[s] = lds_r128(ap + 32 * s);
        CFENCE();
        PUT(16, V[2]); PUT(24, V[3]);
#pragma unroll
        for (int b = 1; b < 8; ++b) { HM2(p, V[2], q, V[3], U[b]); PUT(16 + b, p); PUT(24 + b, q); }
        SET_M0(w * 256);
        PRIO(0);
        GATHER(3);
        PUT(0, 0x3c00u); PUT(8, V[1]);
#pragma unroll
        for (int b = 1; b < 8; b += 2) { HM2(p, U[b], q, U[b == 7 ? 7 : b + 1], V[1]); PUT(8 + b, p); if (b < 7) PUT(9 + b, q); PUT(b, U[b]); if (b < 7) PUT(b + 1, U[b + 1]); }
#undef PUT
        u32x4 bfr[8];
#pragma unroll
        for (int s = 0; s < 8; ++s) bfr[s] = lds_r128(bp + 32 * s);
        SCHED();
        SCHED();
#pragma unroll
        for (int s = 0; s < 8; ++s)
            kacc = __builtin_amdgcn_mfma_f32_32x32x16_f16(__builtin_bit_cast(f16x8, af[s]), __builtin_bit_cast(f16x8, bfr[s]), kacc, 0, 0, 0);
    }
    STAMP(3);
    const float ks = 2.f * dt * ics;
#pragma unroll
    for (int i = 0; i < 12; ++i) kacc[i] *= ks;
    if (half) {
#pragma unroll
        for (int i = 0; i < 8; ++i) W_ADDTID(kacc[i], i * SP);
    } else {
#pragma unroll
        for (int i = 0; i < 4; ++i) W_ADDTID(kacc[8 + i], i * SP);
    }
    if (w >= 8 && li < 12) {
        const int n = (11 - w) * 12 + li, l = n / 9, c = n - l * 9;
        TOUCH(junk2, P.wo + (unsigned)((c * 64 + lane) * 1152 + (8 - l) * 128));
    }
    u32x4 ct[8];
#pragma unroll
    for (int i = 0; i < 8; ++i) ct[i] = lds_r128(smem + TT_OFF + ch * 8192 + i * 1024 + lane * 16);
    PIN_TAG();
    STAMP(4);
    BARRIER();
    STAMP(5);
    if (tid == 704) tag_post(P, tag);
    auto cleared = [&]() {
        if (w < 4) {
            WAIT_VM(0);
            if (lane == 0 && __hip_atomic_fetch_add((LAS unsigned*)(smem + FL + 32), 1u, __ATOMIC_RELAXED, __HIP_MEMORY_SCOPE_WORKGROUP) == 3u)
                __hip_atomic_store((GAS u64*)(P.ws + WS_BFL + gid * 128), tag, RLX_AGENT);
        }
    };
    auto x_in = [&]() {
        if (w < 8) {
            WAIT_VM(0); SCHED();
            asm volatile("" : "+v"(xr[0]), "+v"(xr[1]), "+v"(xr[2]), "+v"(xr[3]), "+v"(junk) :: "memory");
#pragma unroll
            for (int k = 0; k < 3; ++k) {
                u32x2 lo = {__builtin_amdgcn_perm(xr[1][k], xr[0][k], 0x05040100u), __builtin_amdgcn_perm(xr[3][k], xr[2][k], 0x05040100u)};
                u32x2 hi = {__builtin_amdgcn_perm(xr[1][k], xr[0][k], 0x07060302u), __builtin_amdgcn_perm(xr[3][k], xr[2][k], 0x07060302u)};
                lds_w64(xb + (2 * k) * XT_BYTES, lo); lds_w64(xb + (2 * k + 1) * XT_BYTES, hi);
            }
        }
        if (w < 4) {
#pragma unroll
            for (int i = 0; i < 6; ++i)
                if (i >= 2 || gi < 10) __builtin_amdgcn_raw_buffer_store_b128(u32x4{EMPTY, EMPTY, EMPTY, EMPTY}, wsr, (unsigned)(tid * 16), WS_B + (ch0 + i) * 4096, 16);
        }
        BARRIER();
        if (w >= 8) wo_issue<WOA>(P, cg2, w - 8, 4, lane);
    };
    auto TF = [&](int off) { u32x2 a = lds_r64(tf + off), b = lds_r64(tf + off + 8); return u32x4{a.x, a.y, b.x, b.y}; };
    f32x4 acc[8];
#pragma unroll
    for (int i = 0; i < 8; ++i) acc[i] = f32x4{0.f, 0.f, 0.f, 0.f};
    lptr axr;
    if (!half) {
        __builtin_amdgcn_s_setprio(2);
        lds_w64(xt + lane * 8, u32x2{0u, 0u});
#pragma unroll
        for (int i = 0; i < 8; ++i) kacc[i] += lds_rf(xc + i * SP);
        unsigned dd[6];
        dd[0] = pk_bf16(kacc[7], kacc[6]); dd[1] = pk_bf16(kacc[5], kacc[4]); dd[2] = pk_bf16(kacc[3], kacc[2]);
        dd[3] = pk_bf16(kacc[1], kacc[0] + (lane == 0 ? bf2f(r_ds) : 0.f));
        dd[4] = __builtin_amdgcn_update_dpp(0, dd[0], 0x138, 0xf, 0xf, true);
        dd[5] = __builtin_amdgcn_update_dpp(0, dd[1], 0x138, 0xf, 0xf, true);
        unsigned mm[5];
#pragma unroll
        for (int i = 0; i < 5; ++i) mm[i] = __builtin_amdgcn_alignbit(dd[i + 1], dd[i], 16);
        lptr tw = tb + 2 * (536 - 8 * lane);
        lds_w128(tw, u32x4{dd[0], dd[1], dd[2], dd[3]}); lds_w128(tw + TC, u32x4{mm[0], mm[1], mm[2], mm[3]});
        lds_w128(tw + 2 * TC, u32x4{dd[1], dd[2], dd[3], dd[4]}); lds_w128(tw + 3 * TC, u32x4{mm[1], mm[2], mm[3], mm[4]});
        {
            const unsigned n0 = __builtin_amdgcn_readlane(dd[0], 63), n1 = __builtin_amdgcn_readlane(dd[1], 63);
            const unsigned kk = (n0 & 0xffffu) * 0x10001u, kh = n0 << 16, m4 = (n0 >> 16) | (n1 << 16);
            const int c = lane & 3, bl = lane >> 2;
            u32x4 e;
            e.x = 0u; e.y = lane == 14 ? kh : (lane == 15 ? kk : 0u); e.z = lane == 12 ? kh : (bl == 3 ? kk : 0u);
            e.w = lane == 14 ? n0 : (lane == 15 ? m4 : (bl == 3 ? kk : 0u));
            if (bl < 6) lds_w128(tb + c * TC + bl * 16 + (bl >> 2) * 1024, e);
        }
        x_in();
        u32x4 X[8];
#pragma unroll
        for (int i = 0; i < 8; ++i) X[i] = lds_r128(xf7 + 64 * 6 + 512 * i);
        u32x4 k0 = TF(1024 - 64), k8 = TF(512 - 64);
#pragma unroll
        for (int i = 0; i < 8; ++i) asm volatile("" : "+v"(acc[i]));
        __builtin_amdgcn_s_setprio(0);
        STAMP(6);
#pragma unroll
        for (int c = 1; c < 8; ++c) {
            u32x4 k0n, k8n;
            if (c < 7) { k0n = TF(1024 - 64 * (c + 1)); k8n = TF(512 - 64 * (c + 1)); }
#pragma unroll
            for (int i = 0; i < 8; ++i) {
                SCHED();
                mfma16i(acc[i], k0, X[i]);
                if (i < 7) mfma16i(acc[i + 1], k8, X[i]);
                SCHED();
                if (c < 7) X[i] = lds_r128(xf7 + 64 * (6 - c) + 512 * i);
            }
            k0 = k0n; k8 = k8n;
            if (c == 5) { SCHED(); cleared(); SCHED(); }
        }
        MFMA_DONE();
        if (w < 4) wo_issue<0x1ff & ~WOA>(P, cg2, w >> 1, 2, lane);
        flag_wait(smem, 36 + ch * 4, 1);
#pragma unroll
        for (int i = 0; i < 4; ++i) lds_w128(xt + i * 1024 + lane * 16, __builtin_bit_cast(u32x4, acc[4 + i]));
        axr = pt + lane * 16;
    } else {
        u32x4 X[8];
#pragma unroll
        for (int i = 0; i < 4; ++i) if (i < 3 || lane < 48) lds_w128(pt + i * 1024 + lane * 16, u32x4{0u, 0u, 0u, 0u});
        CFENCE();
        const unsigned p89 = pk_bf16(kacc[8] + lds_rf(xc), kacc[9] + lds_rf(xc + SP)), pab = pk_bf16(kacc[10] + lds_rf(xc + 2 * SP), kacc[11] + lds_rf(xc + 3 * SP));
#pragma unroll
        for (int c = 0; c < 4; ++c) {
            lptr q2 = smem + (ct[6][c] + ch * 4096), q3 = smem + (ct[7][c] + ch * 4096);
            lds_w16(q2 + 64, p89); lds_w16(q2, p89 >> 16);
            lds_w16(q3 + 64, pab); lds_w16(q3, pab >> 16);
        }
        CFENCE();
        const u32x4 FX = ct[5] & (((unsigned)__builtin_amdgcn_readlane(p89, 63) >> 16) * 0x10001u);
        lptr pf2 = pt + cpy * 320 + ((79 - 64 - lo16 + 4 * g - cpy + 4) >> 2) * 16;
        x_in();
#pragma unroll
        for (int i = 0; i < 8; ++i) X[i] = lds_r128(xf7 + 64 * 7 + 512 * i);
        u32x4 TA[3], TB[3], TCc[4];
#pragma unroll
        for (int i = 0; i < 3; ++i) { TA[i] = lds_r128(pf2 + 64 * (2 - i)); TB[i] = lds_r128(pf2 + 1280 + 64 * (2 - i)); }
#pragma unroll
        for (int i = 0; i < 4; ++i) TCc[i] = lds_r128(pf2 + PC_OFF + 64 * (3 - i));
        STAMP(6);
        const f32x4 z4 = {0.f, 0.f, 0.f, 0.f};
#pragma unroll
        for (int st = 0; st < 6; ++st) {
            const u32x4 Xc = lds_r128(xf7 + 64 * 7 + 512 * st - 16);
            const u32x4 pa = pack8(mfma16(X[st], ct[0], z4), mfma16(X[st], ct[1], z4));
            const u32x4 pb = pack8(mfma16(X[st], ct[2], z4), mfma16(Xc, ct[1], z4));
#pragma unroll
            for (int dl = 2; dl <= 4; ++dl) if (st + dl < 8) {
                acc[st + dl] = mfma16(pa, TA[dl - 2], acc[st + dl]);
                acc[st + dl] = mfma16(pb, TB[dl - 2], acc[st + dl]);
            }
            if (st < 4) {
                const u32x4 pc = pack8(mfma16(X[st], ct[3], z4), mfma16(Xc, ct[4], z4));
#pragma unroll
                for (int dl = 4; dl <= 7; ++dl) if (st + dl < 8) acc[st + dl] = mfma16(pc, TCc[dl - 4], acc[st + dl]);
            }
        }
        SCHED(); lds_w32(smem + FL + 36 + ch * 4, 1u); cleared(); SCHED();
#pragma unroll
        for (int i = 4; i < 8; ++i) acc[i] = mfma16(FX, X[i - 4], acc[i]);
        const u32x4 k0 = TF(1024), k8 = TF(512), k16 = TF(0);
#pragma unroll
        for (int i = 0; i < 8; ++i) acc[i] = mfma16(k0, X[i], acc[i]);
#pragma unroll
        for (int i = 1; i < 8; ++i) acc[i] = mfma16(k8, X[i - 1], acc[i]);
#pragma unroll
        for (int i = 2; i < 8; ++i) acc[i] = mfma16(k16, X[i - 2], acc[i]);
        MFMA_DONE();
#pragma unroll
        for (int i = 0; i < 4; ++i) { lds_w128(pt + i * 1024 + lane * 16, __builtin_bit_cast(u32x4, acc[i])); acc[i] = acc[4 + i]; }
        axr = xt + lane * 16;
    }
    STAMP(7);
    if (w == 0 && lane < 8) lds_w32(smem + FL + lane * 4, 0u);
    WAIT_VM(0); SCHED();
    asm volatile("" :: "v"(junk2));
    BARRIER();
    STAMP(8);
#pragma unroll
    for (int i = 0; i < 4; ++i) acc[i] += __builtin_bit_cast(f32x4, lds_r128(axr + i * 1024));
#pragma unroll
    for (int i = 0; i < 2; ++i) {
        const f32x4 ya = acc[2 * i], yb = acc[2 * i + 1];
        const auto sx = __builtin_amdgcn_permlane16_swap(pk_bf16(ya[0], ya[1]), pk_bf16(yb[0], yb[1]), false, false);
        const auto sy = __builtin_amdgcn_permlane16_swap(pk_bf16(ya[2], ya[3]), pk_bf16(yb[2], yb[3]), false, false);
        st_sc1(wsr, boff + i * 1024, not_empty(u32x4{sx[0], sy[0], sx[1], sy[1]}));
    }
    BARRIER();
    STAMP(9);
    if (w < 2) STAMP_OUT(2 + 2 * w, blockIdx.x + 256 * w);
}

constexpr int SLOT = 34816, XCH = 16384, KVT_OFF = 151296, KVP_OFF = 27648;
DEVI constexpr int slot_off(int s) { return (s & 3) == 0 ? SLOT : ((s & 3) == 1 ? 0 : (s & 3) * SLOT); }
DEVI constexpr int wq_off(int t) { return t == 0 ? 0 : 49152 + t * 27648; }

constexpr int quad_src(int quad) {
    const int pp = quad >= 192 ? 1 : 0, qi = quad - 192 * pp;
    const bool isz = qi >= 180;
    const int el = isz ? 0 : qi / 5, qd = qi - el * 5, hi = el >= 18 ? 1 : 0;
    const int dql = isz ? (qi < 189 ? qi - 180 : 0) : qd + 4 * hi;
    const int et = isz ? 0 : (el < 4 ? 0 : (el < 20 ? 1 : 2)), col = isz ? 0 : (el < 4 ? 12 + el : (el < 20 ? el - 4 : el - 20));
    const int tile = dql == 8 ? 5 : (et == 0 ? (dql >> 2) : (et == 1 ? 2 + (dql >> 2) : 4));
    return KVP_OFF + pp * 6144 + tile * 1024 + ((dql == 8 ? et : (dql & 3)) * 16 + col) * 4;
}
constexpr int quad_dst(int quad, int hf) {
    const int pp = quad >= 192 ? 1 : 0, qi = quad - 192 * pp;
    const bool isz = qi >= 180;
    const int el = isz ? 0 : qi / 5, qd = qi - el * 5, hi = el >= 18 ? 1 : 0;
    const int dql = isz ? (qi < 189 ? qi - 180 : 0) : qd + 4 * hi;
    const int dq = 9 * pp + dql, ks = dq >> 3, e = 36 * pp + el, ee = e >> 4;
    const int fr = ee == 0 ? 0 : (ee < 3 ? 2 * ee - 1 + ks : 2 * ee - 2 + ks);
    const int hz = 2 * pp + ((dql > 4 || (dql == 4 && hf)) ? 1 : 0);
    const bool dead = isz ? qi >= 189 : ((!hi && qd == 4 && hf) || (hi && qd == 0 && !hf));
    return dead ? LDS_DUMMY : KVT_OFF + (isz ? (9 + ks) * 1024 + (dq & 3) * 256 + hz * 16 : fr * 1024 + ((dq & 3) * 16 + (e & 15)) * 16) + ((dq >> 2) & 1) * 8 + hf * 4;
}
struct Tab { int v[2048]; };
constexpr Tab make_tab() {
    Tab t{};
    for (int c = 0; c < 192; ++c) {
        const int l = c & 63, q = 2 * ((c & 192) + (l >> 5) * 16 + (l & 15)) + ((l >> 4) & 1);
        t.v[4 * c] = quad_src(2 * c); t.v[4 * c + 1] = quad_src(2 * c + 1);
        t.v[4 * c + 2] = (quad_dst(q, 0) - KVT_OFF) | ((quad_dst(q, 1) - KVT_OFF) << 16); t.v[4 * c + 3] = (quad_dst(q + 64, 0) - KVT_OFF) | ((quad_dst(q + 64, 1) - KVT_OFF) << 16);
    }
    for (int q = 0; q < 576; ++q) {
        const int rho = q / 9, pc = q % 9, k = (rho & 32) + 8 * ((rho >> 3) & 3) + 4 * (rho & 1) + ((rho >> 1) & 3), m = q >> 6;
        t.v[768 + q] = k * 3456 + pc * 16 - (m & 1) * 1024;
        t.v[1344 + q] = k * 1152 + pc * 16 - (m & 3) * 1024;
    }
    return t;
}
__device__ const Tab g_tab = make_tab();
constexpr int IDX = P2_B, GW_OFF = 139264;

#define GET_TAG() do { const u32x2 t_ = lds_r64(smem + LDS_TAG); tag = (u64)(unsigned)__builtin_amdgcn_readfirstlane(t_.x) | ((u64)(unsigned)__builtin_amdgcn_readfirstlane(t_.y) << 32); } while (0)
DEVI void gemm_kv(const Params& P, lptr smem, int rb, int hg, int kw, int cg2, u64& tag, rsrc_t wsr) {
    const int lane = threadIdx.x & 63, lo16 = lane & 15, g = lane >> 4;
    const int p = kw & 1, ct = (kw < 3 ? kw : 0) * 64 + lane;
    STAMP_DECL; STAMP(1);
    __builtin_amdgcn_s_setprio(2);
    const int xrow = ((kw >> 1) * 64 + lo16) * 128, sw = (g ^ ((lo16 >> 1) & 7)) << 4;
    lptr xa[2] = {smem + xrow + sw, smem + xrow + (sw ^ 64)};
    const int wrow = (4 * g + (lo16 >> 2)) * 288;
    lptr wa = smem + XCH + wrow + (9 * p + (lane & 3)) * 8, wa2 = smem + XCH + wrow + (9 * p + ((lane & 3) == 3 ? 18 : 8)) * 8;
    lptr flp = smem + FL, ixp = smem + IDX + ct * 16, irp = smem + IDX + 3072 + 2304 + lane * 4;
    unsigned one = 1u;
    u32x4 sel[2];
    sel[0] = mask_frag(lo16 < 8 ? 1u << lo16 : 0u, 0, 0x3f80u); sel[1] = mask_frag(lo16 >= 8 ? 1u << (lo16 - 8) : 0u, 0, 0x3f80u);
    unsigned sto = WS_PART + (unsigned)((hg * 16 + rb) * 3072 + ct * 16);
    unsigned off = WS_PART + (unsigned)((hg * 16 + g) * 3072 + kw * 1024 + lo16 * 16);
    asm volatile("" : "+v"(flp), "+v"(one), "+v"(ixp), "+v"(irp), "+v"(sel[0]), "+v"(sel[1]), "+v"(sto), "+v"(off));
    const f32x4 z4 = {0.f, 0.f, 0.f, 0.f};
    f32x4 acc[4][5];
#pragma unroll
    for (int i = 0; i < 4; ++i)
#pragma unroll
        for (int j = 0; j < 5; ++j) { const float e = j < 2 ? 1e-6f : (j == 2 && lo16 < 12 ? 1e-6f : 0.f); acc[i][j] = f32x4{e, e, e, e}; asm volatile("" : "+v"(acc[i][j])); }
    auto rel = [&](int s) { return (s & 2) ? slot_off(s) - 2 * SLOT : slot_off(s); };
    auto rebase = [&](int dlt) {
        xa[0] += dlt; xa[1] += dlt; wa += dlt; wa2 += dlt;
        asm volatile("" : "+v"(xa[0]), "+v"(xa[1]), "+v"(wa), "+v"(wa2));
    };
    constexpr int NB = 6;
    u32x2 wl[NB], wh[NB];
    u32x4 xf[2][4];
    auto rdw = [&](int f, int t) {
        const int n = f / 5, j = f % 5;
        const int o = rel(n >> 1) + (n & 1) * 9216 + t * 4608;
        const u32x2 v = j == 2 ? lds_tr(wa2 + o) : lds_tr(wa + o + (j == 0 ? 0 : (j == 1 ? 32 : (j == 3 ? 152 : 184))));
        if (t) wh[f % NB] = v; else wl[f % NB] = v;
    };
    auto rdx = [&](int n, int r) { xf[n & 1][r] = lds_r128(xa[n & 1] + rel(n >> 1) + r * 2048); };
    rebase(0);
    BARRIER();
    STAMP(2);
    rdw(0, 0); rdw(0, 1);
#pragma unroll
    for (int r = 0; r < 4; ++r) rdx(0, r);
#pragma unroll
    for (int f = 1; f < NB - 1; ++f) { rdw(f, 0); rdw(f, 1); }
    auto kstep = [&](const int n) __attribute__((always_inline)) {
        const int s = n >> 1;
#pragma unroll
        for (int m = 0; m < 20; ++m) {
            const int j = m >> 2, r = m & 3, f = 5 * n + j, fn = f + NB - 1;
            SCHED();
            const u32x4 wv = {wl[f % NB].x, wl[f % NB].y, wh[f % NB].x, wh[f % NB].y};
            mfma16i(acc[r][j], xf[n & 1][r], wv);
            SCHED();
            if (r < 2 && fn < 90) rdw(fn, r);
            if (r == 2 && j < 4 && n < 17) rdx(n + 1, j);
            if (m == 15 && n < 17) WAIT_LGKM(12);
            if (m == 18 && !(n & 1)) {
                if (s < 8 && (s & 1)) rebase((s & 2) ? -2 * SLOT : 2 * SLOT);
                if (s == 8) WAIT_LGKM0();
                RAW_BARRIER();
                if (s == 0) STAMP(3);
                if (s == 4) STAMP(4);
            }
            if (m == 19 && n < 17) WAIT_LGKM(2);
        }
    };
    kstep(0); kstep(1); kstep(2); kstep(3); kstep(4); kstep(5); kstep(6); kstep(7); kstep(8);
    kstep(9); kstep(10); kstep(11); kstep(12); kstep(13); kstep(14); kstep(15); kstep(16); kstep(17);
    __builtin_amdgcn_s_setprio(3);
    const u32x4 ix = lds_r128(ixp);
    unsigned oo[9];
#pragma unroll
    for (int i = 0; i < 9; ++i) oo[i] = *(LAS unsigned*)(irp + i * 256);
    MFMA_DONE();
    STAMP(5);
    {
        f32x4 r[6], t0 = z4, t1 = z4, t2 = z4;
#pragma unroll
        for (int i = 0; i < 5; ++i) r[i] = z4;
#pragma unroll
        for (int u = 0; u < 2; ++u) {
            u32x4 pk[3], vb[3];
#pragma unroll
            for (int j = 0; j < 3; ++j) pk[j] = pack8(phi4(acc[2 * u][j]), phi4(acc[2 * u + 1][j]));
            vb[0] = pack8(acc[2 * u][2], acc[2 * u + 1][2]);
            if (lo16 < 12) vb[0] = u32x4{0x3f803f80u, 0x3f803f80u, 0x3f803f80u, 0x3f803f80u};
            vb[1] = pack8(acc[2 * u][3], acc[2 * u + 1][3]); vb[2] = pack8(acc[2 * u][4], acc[2 * u + 1][4]);
            t0 = mfma16(pk[2], vb[0], t0); t1 = mfma16(pk[2], vb[1], t1); t2 = mfma16(pk[2], vb[2], t2);
            r[0] = mfma16(pk[0], vb[0], r[0]); r[1] = mfma16(pk[1], vb[0], r[1]); r[2] = mfma16(pk[0], vb[1], r[2]);
            r[3] = mfma16(pk[1], vb[1], r[3]); r[4] = mfma16(pk[1], vb[2], r[4]);
        }
        r[5] = g == 0 ? t0 : (g == 1 ? t1 : t2);
        MFMA_DONE();
        SET_M0(KVP_OFF + kw * 6144);
#pragma unroll
        for (int t = 5; t >= 0; --t) {
            W_ADDTID(r[t][0], t * 1024); W_ADDTID(r[t][1], t * 1024 + 256);
            W_ADDTID(r[t][2], t * 1024 + 512); W_ADDTID(r[t][3], t * 1024 + 768);
        }
        FLAG_ADD(F_KVP);
    }
    STAMP(6);
    if (kw < 3) {
        lptr dst[4] = {smem + KVT_OFF + (ix.z & 0xffffu), smem + KVT_OFF + (ix.z >> 16), smem + KVT_OFF + (ix.w & 0xffffu), smem + KVT_OFF + (ix.w >> 16)};
        asm volatile("" : "+v"(dst[0]), "+v"(dst[1]), "+v"(dst[2]), "+v"(dst[3]));
        flag_wait<0>(smem, F_KVP, 4);
        STAMP(7);
        float pa[2][2][4];
#pragma unroll
        for (int i = 0; i < 2; ++i)
#pragma unroll
            for (int r = 0; r < 2; ++r)
#pragma unroll
                for (int k = 0; k < 4; ++k) pa[i][r][k] = lds_rf(smem + ix[i] + r * 12288 + k * 256);
        SCHED();
        u32x4 pv;
#pragma unroll
        for (int i = 0; i < 2; ++i)
#pragma unroll
            for (int k = 0; k < 2; ++k)
                pv[2 * i + k] = pk_bf16(pa[i][0][2 * k] + pa[i][1][2 * k], pa[i][0][2 * k + 1] + pa[i][1][2 * k + 1]);
        st_sc1(wsr, sto, not_empty(pv));
        SCHED();
        FLAG_ADD(F_KVR);
        STAMP(8);
        WAIT_VM(0);
        STAMP(9);
        f32x4 gs[4][2];
        auto group = [&](int i, const u32x4 (&a)[4]) {
            gs[i][0] = mfma16(sel[1], a[1], mfma16(sel[0], a[0], z4));
            gs[i][1] = mfma16(sel[1], a[3], mfma16(sel[0], a[2], z4));
        };
        {
            u32x4 a[4][4];
#pragma unroll
            for (int i = 0; i < 4; ++i)
#pragma unroll
                for (int t = 0; t < 4; ++t) a[i][t] = __builtin_amdgcn_raw_buffer_load_b128(wsr, off + t * 256, ((i + rb) & 3) * 12288, 16);
            SCHED();
            FLAG_ADD(F_SW);
#pragma unroll
            for (int i = 0; i < 4; ++i) group(i, a[i]);
        }
#pragma unroll
        for (int i = 0; i < 4; ++i) {
#pragma unroll 1
            for (unsigned spins = 0; spins < SPIN_CAP; ++spins) {
                if (!__any(__builtin_isunordered(gs[i][0][0], gs[i][1][0]))) break;
                if (spins) __builtin_amdgcn_s_sleep(RSLEEP);
                CFENCE();
                u32x4 a[4];
#pragma unroll
                for (int t = 0; t < 4; ++t) a[t] = __builtin_amdgcn_raw_buffer_load_b128(wsr, off + t * 256, ((i + rb) & 3) * 12288, 16);
                group(i, a);
                if (spins >= 32 && __all(max(max(umax4(a[0]), umax4(a[1])), max(umax4(a[2]), umax4(a[3]))) != EMPTY)) break;
            }
        }
        f32x4 sum[2];
        sum[0] = (gs[0][0] + gs[1][0]) + (gs[2][0] + gs[3][0]); sum[1] = (gs[0][1] + gs[1][1]) + (gs[2][1] + gs[3][1]);
#pragma unroll
        for (int i = 0; i < 4; ++i) lds_w32(dst[i], pk_bf16(sum[i >> 1][2 * (i & 1)], sum[i >> 1][2 * (i & 1) + 1]));
        CFENCE();
        FLAG_ADD(F_D);
        STAMP(10);
    } else {
        auto wo_chunks = [&](int c0, int c1) {
#pragma unroll 1
            for (int c = c0; c < c1; ++c) {
                const char* ws = P.wo + cg2 * 144 + c * (64 * 1152);
                const unsigned l = c < 8 ? c * 9216 : WO8;
                SET_M0(l); glds16o<0>(ws, oo[0]); glds16o<1024>(ws, oo[1]); glds16o<2048>(ws, oo[2]); glds16o<3072>(ws, oo[3]);
                SET_M0(l + 4096); glds16o<0>(ws, oo[4]); glds16o<1024>(ws, oo[5]); glds16o<2048>(ws, oo[6]); glds16o<3072>(ws, oo[7]);
                SET_M0(l + 8192); glds16o<0>(ws, oo[8]);
            }
        };
        flag_wait<1>(smem, F_KVR, 3);
        wo_chunks(3, 6);
        flag_wait<1>(smem, F_Q0, 8);
        if (WOSW) flag_wait<1>(smem, F_SW, 3);
        wo_chunks(0, 3);
        WAIT_VM(27);
        FLAG_ADD(F_D);
        WAIT_VM(0);
        FLAG_ADD(F_W0);
    }
    GET_TAG();
    __builtin_amdgcn_s_setprio(0);
    if (kw == 0) STAMP_OUT(1, blockIdx.x);
}

DEVI void gemm_q(const Params& P, lptr smem, int rb, int hg, int t8, int cg2, u64& tag, rsrc_t wsr) {
    const int lane = threadIdx.x & 63, lw = t8 & 3;
    const bool ld = t8 < 4;
    STAMP_DECL; STAMP(1);
    __builtin_amdgcn_s_setprio(3);
    unsigned xo[4], wo_[5], qo[3];
    const int rx = hg & 3, rw = rb & 3;
    unsigned mx[4], mw[4];
    const unsigned xbase = (unsigned)((rb * 128 + 8 * lw + (lane >> 3)) * 1152 + (((lane & 7) ^ ((lane >> 4) + 4 * (lw & 1))) << 4));
#pragma unroll
    for (int i = 0; i < 4; ++i) { mx[i] = (((i + rx) & 3) * 4 + lw) * 1024; mw[i] = XCH + (((i + rw) & 3) * 4 + lw) * 1024; xo[i] = xbase + ((i + rx) & 3) * 36864; }
    const unsigned lb = lw * 4096;
    auto issue_x = [&](int s, int i0 = 0, int i1 = 4) {
#pragma unroll
        for (int i = i0; i < i1; ++i) glds16(P.x + s * 128, xo[i], slot_off(s) + mx[i]);
    };
    auto issue_w = [&](int s, int i0 = 0, int i1 = 4) {
        const char* ws = P.wqkv + s * (64 * 3456);
        if (i1 == 4 && (lw >> 1) == (s & 1)) glds16(ws, wo_[4], (unsigned)(slot_off(s) + XCH + 16384 + (lw & 1) * 1024));
#pragma unroll
        for (int i = i0; i < i1; ++i) glds16(ws, wo_[i], slot_off(s) + mw[i]);
    };
    auto issue_q = [&](int t, int i) {
        const char* ws = P.wqkv + hg * 144 + (3 * t + i) * (64 * 3456);
        if (lw == ((i + t) & 3)) glds16(ws, qo[2], (unsigned)(wq_off(t) + i * 9216 + 8192));
        SET_M0(wq_off(t) + i * 9216 + (lb >> 1));
        glds16o<0>(ws, qo[0]); glds16o<1024>(ws, qo[1]);
    };
    auto w_off = [&](int i) {
        const unsigned q = 64 * (i < 4 ? ((i + rw) & 3) * 4 + lw : 16 + (lw & 1)) + lane, rho = (q * 3641u) >> 16, pc = q - rho * 18;
        const unsigned k = (rho & 35u) + 8 * ((rho >> 2) & 3) + 4 * ((rho >> 4) & 1);
        wo_[i] = k * 3456 + (1152 + hg * 144) + pc * 16 + (pc < 9 ? 0u : 1008u);
    };
    if (ld) {
        issue_x(0, 0, 1); issue_x(1, 0, 1);
        SCHED();
        w_off(0);
        issue_w(0, 0, 1); issue_w(1, 0, 1);
        SCHED();
#pragma unroll
        for (int i = 1; i < 5; ++i) w_off(i);
        if (lw == 1) { glds16(P.gw, (unsigned)(lane * 16), GW_OFF); if (lane < 8) glds16(P.gw + 1024, (unsigned)(lane * 16), GW_OFF + 1024); }
        issue_x(0, 1, 4); issue_w(0, 1, 4); issue_w(1, 1, 4); issue_x(1, 1, 4);
        SET_M0(IDX + (lb >> 1));
        glds16o<0>((const char*)&g_tab + (lb >> 1), (unsigned)(lane * 16)); glds16o<1024>((const char*)&g_tab + (lb >> 1), (unsigned)(lane * 16));
    } else {
        if (lw == 0 && lane < 16) lds_w32(smem + FL + lane * 4, 0u);
#pragma unroll
        for (int i = 0; i < 3; ++i) lds_w128(smem + KVT_OFF + i * 4096 + lw * 1024 + lane * 16, u32x4{0u, 0u, 0u, 0u});
    }
    const int lo16 = lane & 15, g = lane >> 4;
    const int sw = (g ^ ((lo16 >> 1) & 7)) << 4;
    lptr xa[2] = {smem + (16 * t8 + lo16) * 128 + sw, smem + (16 * t8 + lo16) * 128 + (sw ^ 64)}, xb[2] = {xa[0] + 2 * SLOT, xa[1] + 2 * SLOT};
    lptr flp = smem + FL, iqp = smem + IDX + 3072 + lane * 4, gwp = smem + GW_OFF + g * 16;
    const int qrow = (2 * (4 * g + (lo16 >> 2))) * 144;
    lptr qw = smem + qrow + (lane & 3) * 8, qw4 = smem + qrow + (16 + (lane & 1)) * 8 + (lane & 2) * 2304;
    lptr qwh = qw + wq_off(1), qw4h = qw4 + wq_off(1), kfp = smem + KVT_OFF + lane * 16, kzp = smem + KVT_OFF + 9216 + (lane & 51) * 16;
    unsigned one = 1u, ob0 = WS_O + (unsigned)(((rb * 8 + t8) * 72 + hg * 9 + 2 * (g & 1) + (g >> 1)) * 256 + lo16 * 16);
    asm volatile("" : "+v"(flp), "+v"(one), "+v"(iqp), "+v"(gwp), "+v"(xa[0]), "+v"(xa[1]), "+v"(xb[0]), "+v"(xb[1]));
    asm volatile("" : "+v"(qw), "+v"(qw4), "+v"(qwh), "+v"(qw4h), "+v"(kfp), "+v"(kzp), "+v"(ob0));
    asm volatile(".subsection 2\n.Lkf_code_end:\n\t.fill 256, 4, 0xbf800000\n\t.subsection 0");
    const f32x4 z4 = {0.f, 0.f, 0.f, 0.f};
    f32x4 gacc = z4, qa[6];
#pragma unroll
    for (int j = 0; j < 6; ++j) { const float e = j < 5 ? 1e-6f : 0.f; qa[j] = f32x4{e, e, e, e}; asm volatile("" : "+v"(qa[j])); }
    u32x4 xk[18];
    u64 pf = 0;
    WAIT_VM(8); BARRIER();
    if (ld) { issue_w(2); issue_x(2); if (LA3) { issue_w(3); issue_x(3); } }
#pragma unroll
    for (int s = 0; s < 9; ++s) {
#pragma unroll
        for (int k = 0; k < 2; ++k) xk[2 * s + k] = lds_r128(((s & 2) ? xb[k] - 2 * SLOT : xa[k]) + slot_off(s));
        if (t8 == hg) {
            gacc = mfma16(lds_r128(gwp + s * 128), xk[2 * s], gacc);
            gacc = mfma16(lds_r128(gwp + s * 128 + 64), xk[2 * s + 1], gacc);
        }
        if (t8 == 4) {
            if (s == 1) { tag = *(const __attribute__((address_space(4))) u64*)(P.ws + WS_CTL); PIN_TAG(); if (lane == 0) lds_w64(smem + LDS_TAG, u32x2{(unsigned)tag, (unsigned)(tag >> 32)}); }
            if (s == 4) asm volatile("global_load_dwordx2 %0, %1, %2 sc1" : "=v"(pf) : "v"((lane < 5 ? lane : 0) * 128), "s"(P.ws + WS_PFL) : "memory");
            if (s == 7) {
                asm volatile("s_waitcnt vmcnt(0)" : "+v"(pf) :: "memory");
                if (!__all(pf == tag)) poll_flags(P, WS_PFL, 5, tag, lane);
            }
        }
        if (LA3) { if (s < 6) WAIT_VM(17); else if (s == 6) WAIT_VM(14); else WAIT_VM(12); }
        else if (s < 7) WAIT_VM(8); else WAIT_VM(6);
        BARRIER();
        if (ld) {
            const int sn = s + 3 + LA3;
            if (sn < 9) { issue_w(sn); issue_x(sn); } else if (sn < 12) { issue_q(sn - 9, 0); issue_q(sn - 9, 1); issue_q(sn - 9, 2); }
            if (s == 3) { qo[0] = *(LAS unsigned*)(iqp + lw * 512); qo[1] = *(LAS unsigned*)(iqp + lw * 512 + 256); qo[2] = *(LAS unsigned*)(iqp + 2048); CFENCE(); }
        }
    }
    __builtin_amdgcn_s_setprio(0);
    STAMP(5);
    if (!ld) {
        const unsigned co = WS_O + (unsigned)(((rb * 8 + 2 * lw) * 72 + hg * 9) * 256 + lane * 16);
#pragma unroll
        for (int i = 0; i < 5; ++i)
            if (i < 4 || lane < 32) st_sc1(wsr, i < 4 ? co + (i >> 1) * 18432 + (i & 1) * 1024 : co + 2048 + (lane >> 4) * (18432 - 256), u32x4{EMPTY, EMPTY, EMPTY, EMPTY});
    }
    if (t8 >= 5) {
        unsigned oo[9];
#pragma unroll
        for (int i = 0; i < 9; ++i) oo[i] = *(LAS unsigned*)(iqp + 2304 + i * 256);
        const char* ws = P.wo + cg2 * 144 + (t8 + 1) * (64 * 1152);
        const unsigned l = t8 < 7 ? (t8 + 1) * 9216 : WO8;
        SET_M0(l); glds16o<0>(ws, oo[0]); glds16o<1024>(ws, oo[1]); glds16o<2048>(ws, oo[2]); glds16o<3072>(ws, oo[3]);
        SET_M0(l + 4096); glds16o<0>(ws, oo[4]); glds16o<1024>(ws, oo[5]); glds16o<2048>(ws, oo[6]); glds16o<3072>(ws, oo[7]);
        SET_M0(l + 8192); glds16o<0>(ws, oo[8]);
    }
    GET_TAG();
    if (t8 == 4 && lane == 0) tag_post(P, tag);
    if (t8 == hg) {
        const float gate = rbf(__builtin_amdgcn_rcpf(1.f + __expf(-rbf(gacc[0]))));
        if (lane < 16) __hip_atomic_store((GAS float*)(P.ws + WS_GATE) + (rb * 128 + hg * 16 + lane), gate, RLX_AGENT);
    }
    auto peek = [&](int f) { return __hip_atomic_load((LAS unsigned*)(flp + f), __ATOMIC_RELAXED, __HIP_MEMORY_SCOPE_WORKGROUP); };
    auto rdq = [&](int ks, int j) {
        const int o = (ks < 6 ? ks : ks - 6) * 4608;
        return j < 4 ? lds_trfrag((ks < 6 ? qw : qwh) + o + 32 * j, 144) : lds_trfrag((ks < 6 ? qw4 : qw4h) + o, 144);
    };
    u32x4 wq[2][4], w4[2];
#pragma unroll
    for (int i = 0; i < 2; ++i) {
#pragma unroll
        for (int j = 0; j < 4; ++j) wq[i][j] = rdq(i, j);
        if (i == 0) w4[0] = rdq(0, 4);
    }
    unsigned seenq = 0;
#pragma unroll
    for (int ks = 0; ks < 18; ++ks) {
        if (ks == 3 && ld) { SCHED(); WAIT_VM(6); FLAG_ADD(F_LANDED); }
        if (ks == 4) { SCHED(); FLAG_ADD(F_Q0); seenq = peek(F_LANDED); NEED(seenq, F_LANDED, 4); }
        if (ks == 8) { SCHED(); WAIT_VM(0); FLAG_ADD(F_C1); }
        if (ks == 10) { SCHED(); seenq = peek(F_C1); NEED(seenq, F_C1, 8); }
        if (ks == QH) { SCHED(); seenq = peek(F_KVR); NEED(seenq, F_KVR, 3); STAMP(6); }
        if (ks == 16) { SCHED(); FLAG_ADD(F_QD); }
#pragma unroll
        for (int j = 0; j < 5; ++j) {
            SCHED();
            if (j == 0 && ks < 17) { if (ks & 1) WAIT_LGKM(10); else WAIT_LGKM(8); }
            if (j < 4) mfma16i(qa[j], wq[ks & 1][j], xk[ks]); else mfma16i(qa[4 + (ks & 1)], w4[(ks >> 1) & 1], xk[ks]);
            SCHED();
            if (ks + 2 < 18) { if (j < 4) wq[ks & 1][j] = rdq(ks + 2, j); else if (!(ks & 1)) w4[((ks >> 1) + 1) & 1] = rdq(ks + 2, 4); }
        }
    }
    __builtin_amdgcn_s_setprio(2);
    MFMA_DONE();
#pragma unroll
    for (int i = 0; i < 4; ++i) {
        const auto sw2 = __builtin_amdgcn_permlane32_swap(__float_as_uint(qa[5][i]), __float_as_uint(qa[5][i]), false, false);
        qa[4][i] += __uint_as_float(sw2[1]);
    }
    u32x4 b0 = pack8(phi4(qa[0]), phi4(qa[1])), b1 = pack8(phi4(qa[2]), phi4(qa[3])), b2 = pack8(phi4(qa[4]), z4);
    asm volatile("" : "+v"(b0), "+v"(b1), "+v"(b2));
    STAMP(7);
    flag_wait<1>(smem, F_D, 4);
        if (t8 == 7 && lane == 0) __hip_atomic_store((GAS u64*)(P.ws + WS_HINT + (rb * 8 + hg) * 128), tag, RLX_AGENT);
    STAMP(8);
    {
        u32x4 kf[12];
#pragma unroll
        for (int i = 0; i < 12; ++i) { kf[i] = lds_r128(i < 3 ? kzp + i * 1024 : kfp + (i - 3) * 1024); SCHED(); }
        SCHED();
        const f32x4 den = mfma16(kf[2], b2, mfma16(kf[1], b1, mfma16(kf[0], b0, f32x4{1e-6f, 1e-6f, 1e-6f, 1e-6f})));
        f32x4 o[5];
        o[0] = mfma16(kf[3], b0, z4);
        o[1] = mfma16(kf[5], b1, mfma16(kf[4], b0, z4));
        o[2] = mfma16(kf[7], b1, mfma16(kf[6], b0, z4));
        o[3] = mfma16(kf[9], b2, mfma16(kf[8], b1, z4));
        o[4] = mfma16(kf[11], b2, mfma16(kf[10], b1, z4));
        f32x4 r;
#pragma unroll
        for (int i = 0; i < 4; ++i) r[i] = __builtin_amdgcn_rcpf(den[i]);
        const float r1a = g == 0 ? r[0] : r[1], r2 = g == 0 ? r[1] : r[2], r3a = g <= 1 ? r[2] : r[3], r3b = g == 0 ? r[2] : r[3];
        u32x2 ob[5];
        ob[0] = u32x2{pk_bf16(o[0][0] * r[0], o[0][1] * r[0]), pk_bf16(o[0][2] * r[0], o[0][3] * r[0])};
        ob[1] = u32x2{pk_bf16(o[1][0] * r1a, o[1][1] * r1a), pk_bf16(o[1][2] * r[1], o[1][3] * r[1])};
        ob[2] = u32x2{pk_bf16(o[2][0] * r2, o[2][1] * r2), pk_bf16(o[2][2] * r2, o[2][3] * r2)};
        ob[3] = u32x2{pk_bf16(o[3][0] * r3a, o[3][1] * r3a), pk_bf16(o[3][2] * r3b, o[3][3] * r3b)};
        ob[4] = u32x2{pk_bf16(o[4][0] * r[3], o[4][1] * r[3]), pk_bf16(o[4][2] * r[3], o[4][3] * r[3])};
        SCHED();
        SCHED();
#pragma unroll
        for (int t = 0; t < 3; ++t) {
            const u32x2 a = ob[2 * t], b = ob[t < 2 ? 2 * t + 1 : 4];
            const auto sx = __builtin_amdgcn_permlane16_swap(a.x, b.x, false, false), sy = __builtin_amdgcn_permlane16_swap(a.y, b.y, false, false);
            if (t < 2 || g == 0) st_sc1(wsr, ob0 + 1024 * t, not_empty(u32x4{sx[0], sy[0], sx[1], sy[1]}));
        }
    }
    __builtin_amdgcn_s_setprio(0);
    STAMP(9);
    if (t8 == 0) STAMP_OUT(5, blockIdx.x + 256);
    if (t8 == 7) STAMP_OUT(7, blockIdx.x + 768);
}

DEVI void phase2(const Params& P, lptr smem, int rt64, int cg, bool gemm, u64 tag, rsrc_t wsr) {
    const int lane = threadIdx.x & 63, w0 = __builtin_amdgcn_readfirstlane(threadIdx.x >> 6);
    const int w = gemm ? (w0 + 8) % 12 : w0, tid = threadIdx.x, bt = tid - 192;
    const int lo16 = lane & 15, g = lane >> 4;
    const int rt = w & 3, kt = w >> 2;
    STAMP_DECL; STAMP(1);
    constexpr int pb = P2_B;
    const int pa = gemm ? P2_ACC : 82944;
    if (gemm && w < 4) flag_wait(smem, F_W0, 1);
    STAMP(2);
    const int qrow = (2 * (4 * g + (lo16 >> 2))) * 144;
    lptr qw = smem + qrow + (lane & 3) * 8, qw4 = smem + qrow + (16 + (lane & 1)) * 8;
    int wofs[3], oofs[3];
#pragma unroll
    for (int u = 0; u < 3; ++u) {
        const int c = kt * 3 + (u + cg) % 3;
        wofs[u] = (c < 8 || !gemm) ? c * 9216 : WO8; oofs[u] = c * 2048;
    }
    auto rdo = [&](int i, int j) { return j < 4 ? lds_trfrag(qw + wofs[i >> 1] + (i & 1) * 4608 + 32 * j, 144) : lds_trfrag(qw4 + wofs[i >> 1] + (i & 1) * 4608, 144); };
    const unsigned ooff = WS_O + (unsigned)((rt64 * 4 + rt) * 18432 + lane * 16);
    auto ldo = [&](int i) { return __builtin_amdgcn_raw_buffer_load_b128(wsr, ooff + (i & 1) * 1024, oofs[i >> 1], 16); };
    u32x4 wf[6][5];
#pragma unroll
    for (int i = 0; i < 6; ++i)
#pragma unroll
        for (int j = 0; j < 5; ++j) wf[i][j] = rdo(i, j);
    const int r0 = tid < 576 ? tid / 9 : 0, c0 = tid - r0 * 9;
    unsigned boff = WS_B + (unsigned)((cg * 72 + (bt >> 3)) * 4096 + (rt64 >> 3) * 1024 + (((bt >> 2) & 1) * 32 + ((rt64 >> 2) & 1) * 16 + (rt64 & 3) * 4 + (bt & 3)) * 16);
    lptr bp = smem + pb + ((bt & 3) * 2 + ((bt >> 2) & 1)) * 1168 + (bt >> 3) * 2;
    unsigned goff = WS_GATE + (unsigned)((rt64 * 64 + r0) * 4);
    lptr ap = smem + pa + kt * (64 * P2_AP) + (rt * 16 + lo16) * P2_AP + (g & 1) * 144 + (g >> 1) * 16;
    lptr fa = smem + pa + r0 * P2_AP + c0 * 16, fb = smem + pb + (tid + (r0 >> 3)) * 16;
    unsigned fo = (unsigned)((rt64 * 64 + r0) * 1152 + cg * 144 + c0 * 16);
    asm volatile("" : "+v"(boff), "+v"(goff), "+v"(bp), "+v"(ap), "+v"(fa), "+v"(fb), "+v"(fo));
    STAMP(3);
    if (w == 3) {
        const int a0 = cg * 72, g0 = (a0 >> 6) * 11 + (a0 & 63) / 6, a1 = a0 + 71, g1 = (a1 >> 6) * 11 + (a1 & 63) / 6;
        poll_addr<OSLEEP>(P, lane < 8 ? WS_HINT + ((rt64 >> 1) * 8 + lane) * 128 : WS_BFL + min(g0 + lane - 8, g1) * 128, tag);
        if (gemm) flag_wait<1>(smem, F_QD, 8);
        flag_set(F_H, 1);
    } else flag_wait<1>(smem, F_H, 1);
    STAMP(4);
    u32x4 bv; unsigned gv;
    auto b_put = [&]() {
#pragma unroll
        for (int q = 0; q < 4; ++q) { lds_w16(bp + (2 * q) * 144, bv[q]); lds_w16(bp + (2 * q + 1) * 144, bv[q] >> 16); }
    };
    bool b_todo = false;
    if (w0 >= 3) bv = ld_sc1(wsr, boff);
    f32x4 acc[5];
    {
        u32x4 of[6];
#pragma unroll
        for (int i = 0; i < 6; ++i) of[i] = ldo(i);
        gv = __builtin_amdgcn_raw_buffer_load_b32(wsr, goff, 0, 16);
        SCHED();
        if (w0 >= 3) { if (__all(umax4(bv) != EMPTY)) b_put(); else b_todo = true; }
        SCHED();
#pragma unroll
        for (int j = 0; j < 5; ++j) {
            acc[j] = f32x4{0.f, 0.f, 0.f, 0.f};
#pragma unroll
            for (int i = 0; i < 6; ++i) {
                acc[j] = mfma16(wf[i][j], of[i], acc[j]);
                if (i == 3 && j > 0) { SCHED(); lds_w128(ap + (j - 1) * 32, __builtin_bit_cast(u32x4, acc[j - 1])); SCHED(); }
            }
        }
        if (g < 2) lds_w128(ap + 128, __builtin_bit_cast(u32x4, acc[4]));
    }
    if (!__all(acc[4][0] == acc[4][0])) {
#pragma unroll
        for (int j = 0; j < 5; ++j) acc[j] = f32x4{0.f, 0.f, 0.f, 0.f};
        u32x4 of[6];
#pragma unroll 1
        for (unsigned spins = 0; spins < SPIN_CAP; ++spins) {
            CFENCE();
            unsigned m = 0;
#pragma unroll
            for (int i = 0; i < 6; ++i) of[i] = ldo(i);
#pragma unroll
            for (int i = 0; i < 6; ++i) m = max(m, umax4(of[i]));
            if (__all(m != EMPTY)) break;
        }
#pragma unroll
        for (int i = 0; i < 6; ++i)
#pragma unroll
            for (int j = 0; j < 5; ++j) acc[j] = mfma16(rdo(i, j), of[i], acc[j]);
#pragma unroll
        for (int j = 0; j < 5; ++j) if (j < 4 || g < 2) lds_w128(ap + j * 32, __builtin_bit_cast(u32x4, acc[j]));
    }
    if (b_todo) {
#pragma unroll 1
        for (unsigned spins = 0; spins < SPIN_CAP; ++spins) {
            CFENCE();
            bv = ld_sc1(wsr, boff);
            if (__all(umax4(bv) != EMPTY)) break;
            __builtin_amdgcn_s_sleep(2);
        }
        b_put();
    }
    const float gt = __uint_as_float(gv);
    float og = rbf(1.f - gt);
    asm volatile("" : "+v"(og));
    STAMP(5);
    BARRIER();
    STAMP(6);
    if (w0 < 9) {
        lptr a = fa;
        const f32x4 s0 = __builtin_bit_cast(f32x4, lds_r128(a)) + __builtin_bit_cast(f32x4, lds_r128(a + 64 * P2_AP)) + __builtin_bit_cast(f32x4, lds_r128(a + 128 * P2_AP));
        const f32x4 s1 = __builtin_bit_cast(f32x4, lds_r128(a + 144)) + __builtin_bit_cast(f32x4, lds_r128(a + 64 * P2_AP + 144)) + __builtin_bit_cast(f32x4, lds_r128(a + 128 * P2_AP + 144));
        const u32x4 bb = lds_r128(fb);
        u32x4 o;
#pragma unroll
        for (int i = 0; i < 4; ++i) {
            const float a0 = i < 2 ? s0[2 * i] : s1[2 * i - 4], a1 = i < 2 ? s0[2 * i + 1] : s1[2 * i - 3];
            o[i] = pk_bf16(gt * a0 + og * bf2f(bb[i]), gt * a1 + og * bfhi(bb[i]));
        }
        __builtin_amdgcn_raw_buffer_store_b128(o, __builtin_amdgcn_make_buffer_rsrc(P.out, 0, SEQ * DM * 2, 0x00020000), fo, 0, OUTAUX);
    }
    STAMP(7);
    if (w == 4) STAMP_OUT(blockIdx.x < 128 ? 3 : 6, blockIdx.x + 512);
}

DEVI void helper_role(const Params& P, lptr smem, int xcd, int li, u64& tag, rsrc_t wsr) {
    const int tid = threadIdx.x, lane = tid & 63, w = __builtin_amdgcn_readfirstlane(tid >> 6);
    STAMP_DECL; STAMP(1);
    if (li >= 13) {
        if (HD) __builtin_amdgcn_s_sleep(HD);
#pragma unroll 1
        for (int n = 12 * NTW + (li - 13) + 3 * w; n < 171; n += 36) touch_group(P, xcd, n, lane);
    }
    if (li == 12) {
#pragma unroll
        for (int i = 0; i < 7; ++i) {
            const int u = (i * 5 + xcd - 3) * 12 + w;
            if (u < 384) st_sc1(wsr, WS_PART + (unsigned)(u * 1024 + lane * 16), u32x4{EMPTY, EMPTY, EMPTY, EMPTY});
        }
        WAIT_VM(0);
    }
    PIN_TAG();
    BARRIER();
    if (tid == 0) {
        if (li == 12) __hip_atomic_store((GAS u64*)(P.ws + WS_PFL + (xcd - 3) * 128), tag, RLX_AGENT);
        tag_post(P, tag);
    }
    if (li >= 13) {
#pragma unroll 1
        for (int n = 171 + (li - 13) + 3 * w; n < 216; n += 36) touch_group(P, xcd, n, lane);
    }
    STAMP(2);
    if (w == 11) {
        STAMP_OUT(8, blockIdx.x);
        if (blockIdx.x == 255) {
            const GAS u64* f = (const GAS u64*)(P.ws + WS_ARR) + lane;
#pragma unroll 1
            for (unsigned spins = 0; spins < SPIN_CAP; ++spins) {
                const bool ok = __hip_atomic_load(f, RLX_AGENT) == tag && __hip_atomic_load(f + 64, RLX_AGENT) == tag &&
                                __hip_atomic_load(f + 128, RLX_AGENT) == tag && __hip_atomic_load(f + 192, RLX_AGENT) == tag;
                if (__all(ok)) break;
                __builtin_amdgcn_s_sleep(8);
            }
            if (lane == 0) __hip_atomic_store((GAS u64*)(P.ws + WS_CTL), tag, RLX_AGENT);
        }
    }
}

__global__ __launch_bounds__(768, 3) void k_fused(Params P) {
    __shared__ __attribute__((aligned(16))) char smem_[LDS_BYTES];
    lptr smem = (lptr)smem_;
    const int b = blockIdx.x, xcd = b & 7, idx = b >> 3;
    const int lane = threadIdx.x & 63, w = __builtin_amdgcn_readfirstlane(threadIdx.x >> 6);
    const rsrc_t wsr = __builtin_amdgcn_make_buffer_rsrc(P.ws, 0, (int)WS_END, 0x00020000);
    const int rt64 = xcd * 4 + (idx & 3), cg2 = idx >> 2;
    if (idx >= CPF && w < 6) {
        const char* pc; unsigned len;
        asm volatile("s_getpc_b64 %0\n.Lkf_code_begin:\n\ts_mov_b32 %1, .Lkf_code_end-.Lkf_code_begin" : "=s"(pc), "=s"(len));
        touch(pc, min((unsigned)(threadIdx.x * 128), len));
    }
    u64 tag;
    if (__builtin_expect(idx < 16, 1)) {
        const int rbl = idx & 3, hgl = idx >> 2, rb = (xcd >> 1) * 4 + rbl, hg = (xcd & 1) * 4 + hgl;
        if (__builtin_expect((w & 12) != 4, 1)) gemm_q(P, smem, rb, hg, w < 4 ? w : w - 4, cg2, tag, wsr);
        else gemm_kv(P, smem, rb, hg, w - 4, cg2, tag, wsr);
    } else {
        tag = *(const __attribute__((address_space(4))) u64*)(P.ws + WS_CTL);
        const int li = idx - 16, gid = li < 11 ? xcd * 11 + li : 88 + (li - 11) * 8 + xcd;
        if (gid < 99) ssm_role(P, smem, xcd, li, gid, cg2, tag, wsr);
        else {
            helper_role(P, smem, xcd, li, tag, wsr);
            if (w < 9) { wo_issue<0x1ff>(P, cg2, w, 9, lane); WAIT_VM(0); }
            if (w == 10 && lane < 16) lds_w32(smem + FL + lane * 4, 0u);
            BARRIER();
        }
    }
    phase2(P, smem, rt64, cg2, idx < 16, tag, wsr);
}

extern "C" void kernel_launch(void* const* d_in, const int* in_sizes, int n_in, void* d_out, int out_size, void* d_ws, size_t ws_size,
                              hipStream_t stream) {
    Params P;
    P.x = (const char*)d_in[0]; P.wqkv = (const char*)d_in[1]; P.wo = (const char*)d_in[2];
    P.logA = (const char*)d_in[4]; P.aim = (const char*)d_in[5]; P.cre = (const char*)d_in[6]; P.cim = (const char*)d_in[7];
    P.logdt = (const char*)d_in[8]; P.dskip = (const char*)d_in[9]; P.gw = (const char*)d_in[10];
    P.out = (char*)d_out; P.ws = (char*)d_ws;
    k_fused<<<256, 768, 0, stream>>>(P);
}
```

```cpp
#include <hip/hip_runtime.h>
#include <stdint.h>

#define GAS __attribute__((address_space(1)))
#define LAS __attribute__((address_space(3)))
#define DEVI __device__ __forceinline__
typedef unsigned short u16;
typedef unsigned long long u64;
typedef __attribute__((ext_vector_type(2))) unsigned u32x2;
typedef __attribute__((ext_vector_type(3))) unsigned u32x3;
typedef __attribute__((ext_vector_type(4))) unsigned u32x4;
typedef __attribute__((ext_vector_type(4))) short s16x4;
typedef __attribute__((ext_vector_type(2))) float f32x2;
typedef __attribute__((ext_vector_type(4))) float f32x4;
typedef __attribute__((ext_vector_type(16))) float f32x16;
typedef __attribute__((ext_vector_type(2))) __bf16 bf16x2;
typedef __attribute__((ext_vector_type(8))) __bf16 bf16x8;
typedef __attribute__((ext_vector_type(2))) _Float16 f16x2;
typedef __attribute__((ext_vector_type(8))) _Float16 f16x8;
typedef LAS char* lptr;

#define PREF 1
#define ROT 0
#define FSLEEP 2
#define NSLEEP "3"
#define LDRIFT 0
#define PSLEEP 6
#define CSLEEP 1
#define OSLEEP 2
#define RSLEEP 3
#define HOLD 1
#define GROT ((w + li) & 3)
#define NTW 8
#define OUTAUX 17
#define TEARLY 1
#define HDELAY 0
#define CPF 16
#define LA3 0
#define TD1 0
#define TD2 0
#define TW2 3
#define HD 0
#define QH 4
#define WOA 0x00f
#define WOT 48
#define WOSW 0
#define WPRE 3
constexpr int SEQ = 2048, DM = 576;

constexpr unsigned WS_CTL = 0;
constexpr unsigned WS_ARR = 256;
constexpr unsigned WS_BFL = 4096;
constexpr unsigned WS_HINT = 20480;
constexpr unsigned WS_GATE = 36864;
constexpr unsigned WS_PFL = 49152;
constexpr unsigned WS_PART = 65536;
constexpr unsigned WS_O = WS_PART + 8 * 16 * 3072;
constexpr unsigned WS_B = WS_O + SEQ * DM * 2;
constexpr unsigned WS_END = WS_B + SEQ * DM * 2;
constexpr unsigned EMPTY = 0xffffffffu;

constexpr size_t WS_STAMP = 16u << 20;
#define STAMP_DECL
#define STAMP(i)
#define CSTAMP(i)
#define STAMP_OUT(role, slot)

struct Params {
    const char* x; const char* wqkv; const char* wo; const char* logA; const char* aim; const char* cre; const char* cim;
    const char* logdt; const char* dskip; const char* gw; char* out; char* ws;
};

constexpr int LDS_BYTES = 163840, FL = 163584, LDS_TAG = FL + 64, LDS_DUMMY = FL + 128, LDS_JUNK = 161280;
enum { F_SW = 0, F_LANDED = 4, F_W1 = 20, F_B = 28, F_H = 24, F_KVP = 36, F_KVR = 40, F_Q0 = 44, F_QD = 48, F_D = 52, F_C1 = 56, F_W0 = 60 };
constexpr int WO8 = 132096, P2_ACC = 73728, P2_AP = 304, P2_B = 141312, P2_G = 150528;

DEVI float bf2f(unsigned h) { return __uint_as_float(h << 16); }
DEVI float bfhi(unsigned h) { return __uint_as_float(h & 0xffff0000u); }
DEVI unsigned pk_bf16(float a, float b) { return __builtin_bit_cast(unsigned, __builtin_convertvector(f32x2{a, b}, bf16x2)); }
DEVI unsigned pk_f16(f32x2 v) { return __builtin_bit_cast(unsigned, __builtin_convertvector(v, f16x2)); }
DEVI float rbf(float a) { return bf2f(pk_bf16(a, a)); }
DEVI f32x4 mfma16(u32x4 a, u32x4 b, f32x4 c) {
    return __builtin_amdgcn_mfma_f32_16x16x32_bf16(__builtin_bit_cast(bf16x8, a), __builtin_bit_cast(bf16x8, b), c, 0, 0, 0);
}
DEVI void mfma16i(f32x4& c, u32x4 a, u32x4 b) { asm volatile("v_mfma_f32_16x16x32_bf16 %0, %1, %2, %0" : "+v"(c) : "v"(a), "v"(b)); }
#define MFMA_DONE() do { __builtin_amdgcn_sched_barrier(0); asm volatile("s_nop 7\n\ts_nop 1" ::: "memory"); __builtin_amdgcn_sched_barrier(0); } while (0)
DEVI u32x4 lds_r128(lptr p) { return *(LAS u32x4*)p; }
DEVI u32x2 lds_r64(lptr p) { return *(LAS u32x2*)p; }
DEVI float lds_rf(lptr p) { return *(LAS float*)p; }
DEVI void lds_w128(lptr p, u32x4 v) { *(LAS u32x4*)p = v; }
DEVI void lds_w64(lptr p, u32x2 v) { *(LAS u32x2*)p = v; }
DEVI void lds_w32(lptr p, unsigned v) { *(LAS unsigned*)p = v; }
DEVI void lds_w16(lptr p, unsigned v) { *(LAS u16*)p = (u16)v; }
DEVI u32x2 lds_tr(lptr p) { return __builtin_bit_cast(u32x2, __builtin_amdgcn_ds_read_tr16_b64_v4i16((LAS s16x4*)p)); }
DEVI u32x4 lds_trfrag(lptr p, int off2) { u32x2 a = lds_tr(p), b = lds_tr(p + off2); return u32x4{a.x, a.y, b.x, b.y}; }
template <int OFF> DEVI void glds16o(const char* sbase, unsigned voff) {
    asm volatile("global_load_lds_dwordx4 %0, %1 offset:%2" :: "v"(voff), "s"(sbase), "i"(OFF) : "memory");
}
DEVI void glds16(const char* sbase, unsigned voff, unsigned l) {
    asm volatile("s_mov_b32 m0, %2\n\ts_nop 0\n\tglobal_load_lds_dwordx4 %0, %1" :: "v"(voff), "s"(sbase), "s"(l) : "memory");
}
#define SET_M0(v) asm volatile("s_mov_b32 m0, %0\n\ts_nop 0" :: "s"(v) : "memory")
#define W_ADDTID(v, off) asm volatile("ds_write_addtid_b32 %0 offset:%1" :: "v"(v), "i"(off) : "memory")
#define CFENCE() asm volatile("" ::: "memory")
#define SCHED() __builtin_amdgcn_sched_barrier(0)
#define WAIT_VM(n) asm volatile("s_waitcnt vmcnt(" #n ")" ::: "memory")
#define WAIT_LGKM(n) __builtin_amdgcn_s_waitcnt(0xC07F | ((n) << 8))
#define WAIT_LGKM0() asm volatile("s_waitcnt lgkmcnt(0)" ::: "memory")
#define BARRIER() do { SCHED(); asm volatile("s_waitcnt lgkmcnt(0)\n\ts_barrier" ::: "memory"); SCHED(); } while (0)
#define RAW_BARRIER() do { SCHED(); asm volatile("s_barrier" ::: "memory"); SCHED(); } while (0)
#define RLX_AGENT __ATOMIC_RELAXED, __HIP_MEMORY_SCOPE_AGENT
constexpr unsigned SPIN_CAP = 1u << 16;
typedef __amdgpu_buffer_rsrc_t rsrc_t;
DEVI u32x4 ld_sc1(rsrc_t r, unsigned off) { return __builtin_amdgcn_raw_buffer_load_b128(r, off, 0, 16); }
DEVI void st_sc1(rsrc_t r, unsigned off, u32x4 v) { __builtin_amdgcn_raw_buffer_store_b128(v, r, off, 0, 16); }

DEVI u32x4 pack8(f32x4 a, f32x4 b) { return u32x4{pk_bf16(a[0], a[1]), pk_bf16(a[2], a[3]), pk_bf16(b[0], b[1]), pk_bf16(b[2], b[3])}; }
DEVI float phi(float t) {
    const float e = __builtin_amdgcn_fmed3f(__builtin_amdgcn_exp2f(t * 1.44269504089f), 0.f, 1.f);
    return __int_as_float(max(__float_as_int(t), 0x358637bd)) + e;
}
DEVI f32x4 phi4(f32x4 v) {
    f32x2 a, b;
    const f32x2 c = {1.44269504089f, 1.44269504089f};
    asm("v_pk_mul_f32 %0, %2, %4\n\tv_pk_mul_f32 %1, %3, %4" : "=&v"(a), "=&v"(b) : "v"(f32x2{v[0], v[1]}), "v"(f32x2{v[2], v[3]}), "s"(c));
    f32x4 r;
#pragma unroll
    for (int i = 0; i < 4; ++i)
        r[i] = __int_as_float(max(__float_as_int(v[i]), 0x358637bd)) + __builtin_amdgcn_fmed3f(__builtin_amdgcn_exp2f(i < 2 ? a[i] : b[i - 2]), 0.f, 1.f);
    return r;
}
DEVI unsigned umax4(u32x4 a) { return max(max(a.x, a.y), max(a.z, a.w)); }
DEVI u32x4 not_empty(u32x4 a) { return a; }
DEVI void tag_post(const Params& P, u64 tag) { __hip_atomic_store((GAS u64*)(P.ws + WS_ARR) + blockIdx.x, tag, RLX_AGENT); }
#define PIN_TAG() do { asm volatile("" : "+s"(tag)); tag += 1ull; } while (0)
template <int SL = PSLEEP>
DEVI void poll_addr(const Params& P, unsigned off, u64 tag) {
    const GAS u64* f = (const GAS u64*)(P.ws + off);
#pragma unroll 1
    for (unsigned spins = 0; spins < SPIN_CAP; ++spins) {
        if (__all(__hip_atomic_load(f, RLX_AGENT) == tag)) break;
        __builtin_amdgcn_s_sleep(SL);
    }
}
template <int SL = PSLEEP>
DEVI void poll_flags(const Params& P, unsigned off, int n, u64 tag, int lane) { poll_addr<SL>(P, off + (lane < n ? lane : 0) * 128, tag); }
DEVI unsigned flag_get(lptr smem, int f) { return __builtin_amdgcn_readfirstlane(__hip_atomic_load((LAS unsigned*)(smem + FL + f), __ATOMIC_RELAXED, __HIP_MEMORY_SCOPE_WORKGROUP)); }
template <int SL = FSLEEP>
DEVI void flag_wait(lptr smem, int f, unsigned v) {
#pragma unroll 1
    for (unsigned spins = 0; spins < (SPIN_CAP << 4); ++spins) { if (flag_get(smem, f) >= v) break; if (SL) __builtin_amdgcn_s_sleep(SL); }
    CFENCE();
}
DEVI void flag_add(int f) { asm volatile("s_mov_b64 exec, 1\n\tds_add_u32 %0, %1\n\ts_mov_b64 exec, -1" :: "v"(FL + f), "v"(1u) : "memory"); }
#define FLAG_ADD(f) asm volatile("s_mov_b64 exec, 1\n\tds_add_u32 %0, %1 offset:%2\n\ts_mov_b64 exec, -1" :: "v"(flp), "v"(one), "i"(f) : "memory")
DEVI void flag_set(int f, unsigned v) { asm volatile("s_mov_b64 exec, 1\n\tds_write_b32 %0, %1\n\ts_mov_b64 exec, -1" :: "v"(FL + f), "v"(v) : "memory"); }
#define NEEDP(seen, ptr, f, v) do { unsigned c_; asm volatile( \
        "v_cmp_gt_u32 vcc, %4, %1\n\ts_cbranch_vccnz 0f\n1:\n\t.subsection 1\n" \
        "0:\n\ts_mov_b32 %0, 0x100000\n2:\n\ts_sleep " NSLEEP "\n\tds_read_b32 %1, %2 offset:%3\n\ts_sub_u32 %0, %0, 1\n\ts_waitcnt lgkmcnt(0)\n\ts_cbranch_scc1 1b\n\t" \
        "v_cmp_gt_u32 vcc, %4, %1\n\ts_cbranch_vccnz 2b\n\ts_branch 1b\n\t.subsection 0" \
        : "=&s"(c_), "+v"(seen) : "v"(ptr), "i"(f), "i"(v) : "memory", "scc", "vcc"); } while (0)
#define NEED(seen, f, v) NEEDP(seen, flp, f, v)
DEVI void touch(const char* sbase, unsigned voff) {
    asm volatile("s_mov_b32 m0, %2\n\ts_nop 0\n\tglobal_load_lds_dword %0, %1" :: "v"(voff), "s"(sbase), "s"(LDS_JUNK) : "memory");
}
DEVI const char* group_addr(const Params& P, int xcd, int n, int lane) {
    if (n < 126) {
        const int c0 = n / 18, r = n - c0 * 18, c = c0 + 2, l = r - 8;
        return r < 8 ? P.x + (unsigned)(((xcd >> 1) * 512 + r * 64 + lane) * 1152 + c * 128)
                     : P.wqkv + (unsigned)((c * 64 + lane) * 3456 + (l < 5 ? 1152 : 2304 - 640) + (xcd & 1) * 512 + l * 128);
    }
    const int c = (n - 126) / 5, l = n - 126 - c * 5;
    return P.wqkv + (unsigned)((c * 64 + lane) * 3456 + (xcd & 1) * 512 + l * 128);
}
DEVI void touch_group(const Params& P, int xcd, int n, int lane) {
    if (n < 126) {
        const int c0 = n / 18, r = n - c0 * 18, c = c0 + 2;
        if (r < 8) touch(P.x, (unsigned)(((xcd >> 1) * 512 + r * 64 + lane) * 1152 + c * 128));
        else { const int l = r - 8; touch(P.wqkv, (unsigned)((c * 64 + lane) * 3456 + (l < 5 ? 1152 : 2304 - 640) + (xcd & 1) * 512 + l * 128)); }
    } else if (n < 171) {
        const int c = (n - 126) / 5, l = n - 126 - c * 5;
        touch(P.wqkv, (unsigned)((c * 64 + lane) * 3456 + (xcd & 1) * 512 + l * 128));
    } else {
        const int l = (n - 171) / 9, c = n - 171 - l * 9;
        touch(P.wo, (unsigned)((c * 64 + lane) * 1152 + l * 128));
    }
}
template <unsigned CH>
DEVI void wo_issue(const Params& P, int cg, int first, int stride, int lane) {
#pragma unroll 1
    for (int m = first; m < 9; m += stride) {
        const int q = 64 * m + lane, rho = q / 9, pc = q - rho * 9;
        const int k = (rho & 32) + 8 * ((rho >> 3) & 3) + 4 * (rho & 1) + ((rho >> 1) & 3);
        const unsigned qo = (unsigned)(k * 1152 + cg * 144 + pc * 16);
#pragma unroll
        for (int c = 0; c < 9; ++c) if ((CH >> c) & 1) glds16(P.wo + c * (64 * 1152), qo, (unsigned)(c * 9216 + m * 1024));
    }
}

constexpr int SP = 3088;
constexpr int XT_OFF = 135936, XT_BYTES = 4608, XPAD = 256;
constexpr int TT_OFF = 107520, TC = 1184, PT_OFF = 82944, PC_OFF = 2560;

#define CM2(o1, p1, a1, b1, o2, p2, a2, b2) do { asm( \
    "v_pk_mul_f32 %0, %2, %3 op_sel:[1,1] op_sel_hi:[1,0] neg_lo:[0,1]\n\tv_pk_mul_f32 %1, %4, %5 op_sel:[1,1] op_sel_hi:[1,0] neg_lo:[0,1]\n\t" \
    "v_pk_fma_f32 %0, %2, %3, %0 op_sel_hi:[0,1,1]\n\tv_pk_fma_f32 %1, %4, %5, %1 op_sel_hi:[0,1,1]" \
    : "=&v"(o1), "=&v"(o2) : "v"(a1), "v"(b1), "v"(a2), "v"(b2)); \
    asm("v_cvt_pk_f16_f32 %0, %2, %3\n\tv_cvt_pk_f16_f32 %1, %4, %5" : "=&v"(p1), "=&v"(p2) : "v"((o1).x), "v"((o1).y), "v"((o2).x), "v"((o2).y)); } while (0)
#define HM2(p1, a1, p2, a2, b) asm( \
    "v_pk_mul_f16 %0, %2, %4 op_sel_hi:[0,1]\n\tv_pk_mul_f16 %1, %3, %4 op_sel_hi:[0,1]\n\t" \
    "v_pk_fma_f16 %0, %2, %4, %0 op_sel:[1,1,0] op_sel_hi:[1,0,1] neg_lo:[0,1,0]\n\tv_pk_fma_f16 %1, %3, %4, %1 op_sel:[1,1,0] op_sel_hi:[1,0,1] neg_lo:[0,1,0]" \
    : "=&v"(p1), "=&v"(p2) : "v"(a1), "v"(a2), "v"(b))
DEVI f32x2 cexp_p(float a2, float f, float p) {
    float m = __builtin_amdgcn_exp2f(a2 * p);
    float r = __builtin_amdgcn_fractf(f * p);
    return f32x2{m * __builtin_amdgcn_cosf(r), m * __builtin_amdgcn_sinf(r)};
}
DEVI u32x4 mask_frag(unsigned bits, int g, unsigned val) {
    unsigned by = bits >> (8 * g);
    u32x4 r;
#pragma unroll
    for (int i = 0; i < 4; ++i) { unsigned t = (by >> (2 * i)) & 3u; r[i] = ((t | (t << 15)) & 0x10001u) * val; }
    return r;
}
struct Tab2 { unsigned v[8][64][4]; };
constexpr Tab2 make_tab2() {
    Tab2 t{};
    for (int l = 0; l < 64; ++l) {
        const int r = l & 15, g = l >> 4, j = l & 31, h = l >> 5;
        const unsigned bits[6] = {7u << (r + 11), 31u << (r + 6), 7u << (r + 3), 127u << (r + 5), 511u << (r + 4), 3u << (r + 12)};
        for (int m = 0; m < 6; ++m)
            for (int i = 0; i < 4; ++i) {
                const unsigned q = (bits[m] >> (8 * g + 2 * i)) & 3u, val = m < 5 ? 0x3f80u : 0xffffu;
                t.v[m][l][i] = (q & 1 ? val : 0u) | (q & 2 ? val << 16 : 0u);
            }
        for (int c = 0; c < 4; ++c) {
            const int v2 = 79 - (32 + (j >> 1)) - c + 4, v3 = 127 - (64 + 32 * h + (j >> 1)) - c + 4;
            t.v[6][l][c] = PT_OFF + (j & 1) * 1280 + c * 320 + (v2 >> 2) * 16 + h * 8 + (v2 & 3) * 2 - 64;
            t.v[7][l][c] = PT_OFF + PC_OFF + c * 320 + (v3 >> 2) * 16 + (j & 1) * 8 + (v3 & 3) * 2 - 64;
        }
    }
    return t;
}
__device__ const Tab2 g_tab2 = make_tab2();
DEVI float wave_max(float v) {
    v = fmaxf(v, __int_as_float(__builtin_amdgcn_update_dpp(0, __float_as_int(v), 0xB1, 0xf, 0xf, true)));
    v = fmaxf(v, __int_as_float(__builtin_amdgcn_update_dpp(0, __float_as_int(v), 0x4E, 0xf, 0xf, true)));
    v = fmaxf(v, __int_as_float(__builtin_amdgcn_update_dpp(0, __float_as_int(v), 0x124, 0xf, 0xf, true)));
    v = fmaxf(v, __int_as_float(__builtin_amdgcn_update_dpp(0, __float_as_int(v), 0x128, 0xf, 0xf, true)));
    const int i = __float_as_int(v);
    const int a = max(__builtin_amdgcn_readlane(i, 0), __builtin_amdgcn_readlane(i, 16)), b = max(__builtin_amdgcn_readlane(i, 32), __builtin_amdgcn_readlane(i, 48));
    return __int_as_float(max(a, b));
}

DEVI void ssm_role(const Params& P, lptr smem, int xcd, int li, int gid, int cg2, u64& tag, rsrc_t wsr) {
    const int gl = gid / 11, gi = gid - gl * 11, ch0 = gl * 64 + (gi < 10 ? gi * 6 : 58);
    const int tid = threadIdx.x, lane = tid & 63, w = __builtin_amdgcn_readfirstlane(tid >> 6);
    const int ch = w >> 1, half = (w ^ (w >> 2)) & 1, d = ch0 + ch;
    const bool own = gi < 10 || ch >= 2;
    const int lo16 = lane & 15, g = lane >> 4;
    STAMP_DECL; STAMP(1);
    __builtin_amdgcn_s_setprio(3);
    if (half) {
        const char* tp = (const char*)&g_tab2;
        SET_M0(TT_OFF + ch * 8192);
        glds16o<0>(tp, (unsigned)(lane * 16)); glds16o<1024>(tp, (unsigned)(lane * 16)); glds16o<2048>(tp, (unsigned)(lane * 16)); glds16o<3072>(tp, (unsigned)(lane * 16));
        SET_M0(TT_OFF + ch * 8192 + 4096);
        tp += 4096;
        glds16o<0>(tp, (unsigned)(lane * 16)); glds16o<1024>(tp, (unsigned)(lane * 16)); glds16o<2048>(tp, (unsigned)(lane * 16)); glds16o<3072>(tp, (unsigned)(lane * 16));
    }
    const unsigned po = (unsigned)(d * 256 + lane * 4);
    unsigned r_la, r_ai, r_cr, r_ci, r_dt, r_ds, junk = 0, junk2 = 0;
    asm volatile("global_load_dword %0, %6, %8\n\tglobal_load_dword %1, %6, %9\n\tglobal_load_dword %2, %6, %10\n\tglobal_load_dword %3, %6, %11\n\t"
                 "global_load_ushort %4, %7, %12\n\tglobal_load_ushort %5, %7, %13"
                 : "=&v"(r_cr), "=&v"(r_ci), "=&v"(r_la), "=&v"(r_ai), "=&v"(r_dt), "=&v"(r_ds)
                 : "v"(po), "v"(d * 2), "s"(P.cre), "s"(P.cim), "s"(P.logA), "s"(P.aim), "s"(P.logdt), "s"(P.dskip) : "memory");
#define TOUCH(dst, addr) asm volatile("global_load_dword %0, %1, off" : "+v"(dst) : "v"(addr) : "memory")
    const bool tch = w < NTW && li < 12;
    const char* tga = group_addr(P, xcd, tch ? w * 12 + li : 0, lane);
    RAW_BARRIER();
    if (tch && w < TW2) TOUCH(junk, tga);
    SCHED();
    const int tt = (tid + (li & 7) * 64) & 511;
    u32x3 xr[4] = {};
    const unsigned xoff = (unsigned)(tt * 4608 + ch0 * 2);
    const unsigned boff = own ? WS_B + (unsigned)(d * 4096 + half * 2048 + lane * 16) : WS_END;
#define GATHER(i) do { SCHED(); if (w < 8) asm volatile("global_load_dwordx3 %0, %1, %2 offset:%3" : "+v"(xr[i]) : "v"(xoff), "s"(P.x), "i"((i) * 1152) : "memory"); SCHED(); } while (0)
#define PRIO(i) do { SCHED(); __builtin_amdgcn_s_setprio(i); SCHED(); } while (0)
    SCHED();
    lptr xt = smem + XT_OFF + ch * XT_BYTES, tb = smem + TT_OFF + ch * 4 * TC, pt = smem + PT_OFF + ch * 4096;
    const int j32 = lane & 31, h = lane >> 5;
    if (!half) { lds_w32(smem + FL + 32, 0u); lds_w32(smem + FL + 36 + ch * 4, 0u); }
    int arow;
    {
        const u64 map0 = 0x0a09080803020100ull, map1 = 0x0c0b0a0a07060504ull, map2 = 0x1413110e12100f0dull;
        const u64 mm = j32 < 8 ? map0 : (j32 < 16 ? map1 : map2);
        arow = (int)((mm >> (8 * (j32 & 7))) & 0xff);
    }
    lptr ap = smem + arow * SP + w * 256 + h * 16, bp = smem + j32 * SP + w * 256 + h * 16;
    const int cpy = (3 - lo16) & 3;
    lptr xf7 = xt + (XPAD - 16 + 16 * lo16 + 8 * g) * 2 - 64 * 7;
    lptr tf = tb + cpy * TC + 2 * (527 - lo16 + 8 * g - cpy) - 64 * 16;
    lptr xc = smem + (w ^ 1) * 256 + lane * 4, xb = smem + XT_OFF + (XPAD + tt * 4) * 2;
    asm volatile("" : "+v"(ap), "+v"(bp), "+v"(xf7), "+v"(tf), "+v"(xc), "+v"(xb));
    if (tch && w < TW2) WAIT_VM(1); else WAIT_VM(0);
    asm volatile("" : "+v"(r_la), "+v"(r_ai), "+v"(r_cr), "+v"(r_ci), "+v"(r_dt), "+v"(r_ds));
    if (tch && w >= TW2) TOUCH(junk, tga);
    const float dt = rbf(__expf(bf2f(r_dt)));
    const float cm = wave_max(fmaxf(fmaxf(fabsf(bf2f(r_cr)), fabsf(bfhi(r_cr))), fmaxf(fabsf(bf2f(r_ci)), fabsf(bfhi(r_ci)))));
    unsigned ce = __float_as_uint(cm) >> 23; ce = ce < 16u ? 16u : (ce > 240u ? 240u : ce);
    const float cs = __uint_as_float((263u - ce) << 23), ics = __uint_as_float((ce - 9u) << 23);
    f32x16 kacc;
#pragma unroll
    for (int i = 0; i < 16; ++i) kacc[i] = 0.f;
    STAMP(2);
    {
        const unsigned sh = half * 16;
        const float la = bf2f(r_la >> sh), ai = bf2f(r_ai >> sh), cr = bf2f(r_cr >> sh) * cs, ci = bf2f(r_ci >> sh) * cs;
        const float a2 = (-rbf(__expf(la)) * dt) * 1.44269504089f, f = (ai * dt) * 0.15915494309f;
#define PUT(row, v) W_ADDTID(v, ((row) & 15) * SP)
        const f32x2 w1 = cexp_p(a2, -f, 1.f), W = cexp_p(a2, -f, 256.f);
        f32x2 u[8], v[4], c[9], e4, e8, t2;
        unsigned U[8], V[4], p, q;
        u[1] = cexp_p(a2, f, 8.f); v[1] = cexp_p(a2, f, 64.f);
        c[0] = f32x2{cr, -ci};
        U[1] = pk_f16(u[1]); V[1] = pk_f16(v[1]);
        GATHER(0);
        SET_M0(w * 256);
        PUT(0, pk_f16(c[0]));
        CM2(c[1], p, c[0], w1, u[2], U[2], u[1], u[1]); PUT(1, p);
        CM2(c[2], p, c[1], w1, u[3], U[3], u[2], u[1]); PUT(2, p);
        CM2(c[3], p, c[2], w1, u[4], U[4], u[2], u[2]); PUT(3, p);
        CM2(c[4], p, c[3], w1, u[5], U[5], u[4], u[1]); PUT(4, p);
        CM2(c[5], p, c[4], w1, u[6], U[6], u[4], u[2]); PUT(5, p);
        CM2(c[6], p, c[5], w1, u[7], U[7], u[4], u[3]); PUT(6, p);
        CM2(c[7], p, c[6], w1, v[2], V[2], v[1], v[1]); PUT(7, p);
        CM2(c[8], p, c[7], w1, v[3], V[3], v[2], v[1]);
        PRIO(2);
        GATHER(1);
        CM2(e8, p, c[8], W, e4, q, c[4], W); PUT(12, p); PUT(10, q);
        CM2(c[8], p, e8, W, c[4], q, e4, W); PUT(14, p); PUT(13, q);
        CM2(t2, p, c[0], W, e4, q, c[4], W); PUT(8, p); PUT(15, q);
        CM2(t2, p, c[2], W, e4, q, c[6], W); PUT(9, p); PUT(11, q);
        SET_M0(w * 256 + 16 * SP);
        CM2(e8, p, c[8], W, t2, q, W, W); PUT(17, p);
        CM2(e4, p, c[8], t2, c[0], q, e8, t2); PUT(16, p); PUT(18, q);
        CM2(c[8], p, e4, t2, e8, q, c[0], t2); PUT(19, p); PUT(20, q);
        PRIO(1);
        GATHER(2);
        u32x4 af[8];
#pragma unroll
        for (int s = 0; s < 8; ++s) af[s] = lds_r128(ap + 32 * s);
        CFENCE();
        PUT(16, V[2]); PUT(24, V[3]);
#pragma unroll
        for (int b = 1; b < 8; ++b) { HM2(p, V[2], q, V[3], U[b]); PUT(16 + b, p); PUT(24 + b, q); }
        SET_M0(w * 256);
        PRIO(0);
        GATHER(3);
        PUT(0, 0x3c00u); PUT(8, V[1]);
#pragma unroll
        for (int b = 1; b < 8; b += 2) { HM2(p, U[b], q, U[b == 7 ? 7 : b + 1], V[1]); PUT(8 + b, p); if (b < 7) PUT(9 + b, q); PUT(b, U[b]); if (b < 7) PUT(b + 1, U[b + 1]); }
#undef PUT
        u32x4 bfr[8];
#pragma unroll
        for (int s = 0; s < 8; ++s) bfr[s] = lds_r128(bp + 32 * s);
        SCHED();
        SCHED();
#pragma unroll
        for (int s = 0; s < 8; ++s)
            kacc = __builtin_amdgcn_mfma_f32_32x32x16_f16(__builtin_bit_cast(f16x8, af[s]), __builtin_bit_cast(f16x8, bfr[s]), kacc, 0, 0, 0);
    }
    STAMP(3);
    const float ks = 2.f * dt * ics;
#pragma unroll
    for (int i = 0; i < 12; ++i) kacc[i] *= ks;
    if (half) {
#pragma unroll
        for (int i = 0; i < 8; ++i) W_ADDTID(kacc[i], i * SP);
    } else {
#pragma unroll
        for (int i = 0; i < 4; ++i) W_ADDTID(kacc[8 + i], i * SP);
    }
    if (w >= 8 && li < 12) {
        const int n = (11 - w) * 12 + li, l = n / 9, c = n - l * 9;
        TOUCH(junk2, P.wo + (unsigned)((c * 64 + lane) * 1152 + (8 - l) * 128));
    }
    u32x4 ct[8];
#pragma unroll
    for (int i = 0; i < 8; ++i) ct[i] = lds_r128(smem + TT_OFF + ch * 8192 + i * 1024 + lane * 16);
    PIN_TAG();
    STAMP(4);
    BARRIER();
    STAMP(5);
    if (tid == 704) tag_post(P, tag);
    auto cleared = [&]() {
        if (w < 4) {
            WAIT_VM(0);
            if (lane == 0 && __hip_atomic_fetch_add((LAS unsigned*)(smem + FL + 32), 1u, __ATOMIC_RELAXED, __HIP_MEMORY_SCOPE_WORKGROUP) == 3u)
                __hip_atomic_store((GAS u64*)(P.ws + WS_BFL + gid * 128), tag, RLX_AGENT);
        }
    };
    auto x_in = [&]() {
        if (w < 8) {
            WAIT_VM(0); SCHED();
            asm volatile("" : "+v"(xr[0]), "+v"(xr[1]), "+v"(xr[2]), "+v"(xr[3]), "+v"(junk) :: "memory");
#pragma unroll
            for (int k = 0; k < 3; ++k) {
                u32x2 lo = {__builtin_amdgcn_perm(xr[1][k], xr[0][k], 0x05040100u), __builtin_amdgcn_perm(xr[3][k], xr[2][k], 0x05040100u)};
                u32x2 hi = {__builtin_amdgcn_perm(xr[1][k], xr[0][k], 0x07060302u), __builtin_amdgcn_perm(xr[3][k], xr[2][k], 0x07060302u)};
                lds_w64(xb + (2 * k) * XT_BYTES, lo); lds_w64(xb + (2 * k + 1) * XT_BYTES, hi);
            }
        }
        if (w < 4) {
#pragma unroll
            for (int i = 0; i < 6; ++i)
                if (i >= 2 || gi < 10) __builtin_amdgcn_raw_buffer_store_b128(u32x4{EMPTY, EMPTY, EMPTY, EMPTY}, wsr, (unsigned)(tid * 16), WS_B + (ch0 + i) * 4096, 16);
        }
        BARRIER();
        if (w >= 8) wo_issue<WOA>(P, cg2, w - 8, 4, lane);
    };
    auto TF = [&](int off) { u32x2 a = lds_r64(tf + off), b = lds_r64(tf + off + 8); return u32x4{a.x, a.y, b.x, b.y}; };
    f32x4 acc[8];
#pragma unroll
    for (int i = 0; i < 8; ++i) acc[i] = f32x4{0.f, 0.f, 0.f, 0.f};
    lptr axr;
    if (!half) {
        __builtin_amdgcn_s_setprio(2);
        lds_w64(xt + lane * 8, u32x2{0u, 0u});
#pragma unroll
        for (int i = 0; i < 8; ++i) kacc[i] += lds_rf(xc + i * SP);
        unsigned dd[6];
        dd[0] = pk_bf16(kacc[7], kacc[6]); dd[1] = pk_bf16(kacc[5], kacc[4]); dd[2] = pk_bf16(kacc[3], kacc[2]);
        dd[3] = pk_bf16(kacc[1], kacc[0] + (lane == 0 ? bf2f(r_ds) : 0.f));
        dd[4] = __builtin_amdgcn_update_dpp(0, dd[0], 0x138, 0xf, 0xf, true);
        dd[5] = __builtin_amdgcn_update_dpp(0, dd[1], 0x138, 0xf, 0xf, true);
        unsigned mm[5];
#pragma unroll
        for (int i = 0; i < 5; ++i) mm[i] = __builtin_amdgcn_alignbit(dd[i + 1], dd[i], 16);
        lptr tw = tb + 2 * (536 - 8 * lane);
        lds_w128(tw, u32x4{dd[0], dd[1], dd[2], dd[3]}); lds_w128(tw + TC, u32x4{mm[0], mm[1], mm[2], mm[3]});
        lds_w128(tw + 2 * TC, u32x4{dd[1], dd[2], dd[3], dd[4]}); lds_w128(tw + 3 * TC, u32x4{mm[1], mm[2], mm[3], mm[4]});
        {
            const unsigned n0 = __builtin_amdgcn_readlane(dd[0], 63), n1 = __builtin_amdgcn_readlane(dd[1], 63);
            const unsigned kk = (n0 & 0xffffu) * 0x10001u, kh = n0 << 16, m4 = (n0 >> 16) | (n1 << 16);
            const int c = lane & 3, bl = lane >> 2;
            u32x4 e;
            e.x = 0u; e.y = lane == 14 ? kh : (lane == 15 ? kk : 0u); e.z = lane == 12 ? kh : (bl == 3 ? kk : 0u);
            e.w = lane == 14 ? n0 : (lane == 15 ? m4 : (bl == 3 ? kk : 0u));
            if (bl < 6) lds_w128(tb + c * TC + bl * 16 + (bl >> 2) * 1024, e);
        }
        x_in();
        u32x4 X[8];
#pragma unroll
        for (int i = 0; i < 8; ++i) X[i] = lds_r128(xf7 + 64 * 6 + 512 * i);
        u32x4 k0 = TF(1024 - 64), k8 = TF(512 - 64);
#pragma unroll
        for (int i = 0; i < 8; ++i) asm volatile("" : "+v"(acc[i]));
        __builtin_amdgcn_s_setprio(0);
        STAMP(6);
#pragma unroll
        for (int c = 1; c < 8; ++c) {
            u32x4 k0n, k8n;
            if (c < 7) { k0n = TF(1024 - 64 * (c + 1)); k8n = TF(512 - 64 * (c + 1)); }
#pragma unroll
            for (int i = 0; i < 8; ++i) {
                SCHED();
                mfma16i(acc[i], k0, X[i]);
                if (i < 7) mfma16i(acc[i + 1], k8, X[i]);
                SCHED();
                if (c < 7) X[i] = lds_r128(xf7 + 64 * (6 - c) + 512 * i);
            }
            k0 = k0n; k8 = k8n;
            if (c == 5) { SCHED(); cleared(); SCHED(); }
        }
        MFMA_DONE();
        if (w < 4) wo_issue<0x1ff & ~WOA>(P, cg2, w >> 1, 2, lane);
        flag_wait(smem, 36 + ch * 4, 1);
#pragma unroll
        for (int i = 0; i < 4; ++i) lds_w128(xt + i * 1024 + lane * 16, __builtin_bit_cast(u32x4, acc[4 + i]));
        axr = pt + lane * 16;
    } else {
        u32x4 X[8];
#pragma unroll
        for (int i = 0; i < 4; ++i) if (i < 3 || lane < 48) lds_w128(pt + i * 1024 + lane * 16, u32x4{0u, 0u, 0u, 0u});
        CFENCE();
        const unsigned p89 = pk_bf16(kacc[8] + lds_rf(xc), kacc[9] + lds_rf(xc + SP)), pab = pk_bf16(kacc[10] + lds_rf(xc + 2 * SP), kacc[11] + lds_rf(xc + 3 * SP));
#pragma unroll
        for (int c = 0; c < 4; ++c) {
            lptr q2 = smem + (ct[6][c] + ch * 4096), q3 = smem + (ct[7][c] + ch * 4096);
            lds_w16(q2 + 64, p89); lds_w16(q2, p89 >> 16);
            lds_w16(q3 + 64, pab); lds_w16(q3, pab >> 16);
        }
        CFENCE();
        const u32x4 FX = ct[5] & (((unsigned)__builtin_amdgcn_readlane(p89, 63) >> 16) * 0x10001u);
        lptr pf2 = pt + cpy * 320 + ((79 - 64 - lo16 + 4 * g - cpy + 4) >> 2) * 16;
        x_in();
#pragma unroll
        for (int i = 0; i < 8; ++i) X[i] = lds_r128(xf7 + 64 * 7 + 512 * i);
        u32x4 TA[3], TB[3], TCc[4];
#pragma unroll
        for (int i = 0; i < 3; ++i) { TA[i] = lds_r128(pf2 + 64 * (2 - i)); TB[i] = lds_r128(pf2 + 1280 + 64 * (2 - i)); }
#pragma unroll
        for (int i = 0; i < 4; ++i) TCc[i] = lds_r128(pf2 + PC_OFF + 64 * (3 - i));
        STAMP(6);
        const f32x4 z4 = {0.f, 0.f, 0.f, 0.f};
#pragma unroll
        for (int st = 0; st < 6; ++st) {
            const u32x4 Xc = lds_r128(xf7 + 64 * 7 + 512 * st - 16);
            const u32x4 pa = pack8(mfma16(X[st], ct[0], z4), mfma16(X[st], ct[1], z4));
            const u32x4 pb = pack8(mfma16(X[st], ct[2], z4), mfma16(Xc, ct[1], z4));
#pragma unroll
            for (int dl = 2; dl <= 4; ++dl) if (st + dl < 8) {
                acc[st + dl] = mfma16(pa, TA[dl - 2], acc[st + dl]);
                acc[st + dl] = mfma16(pb, TB[dl - 2], acc[st + dl]);
            }
            if (st < 4) {
                const u32x4 pc = pack8(mfma16(X[st], ct[3], z4), mfma16(Xc, ct[4], z4));
#pragma unroll
                for (int dl = 4; dl <= 7; ++dl) if (st + dl < 8) acc[st + dl] = mfma16(pc, TCc[dl - 4], acc[st + dl]);
            }
        }
        SCHED(); lds_w32(smem + FL + 36 + ch * 4, 1u); cleared(); SCHED();
#pragma unroll
        for (int i = 4; i < 8; ++i) acc[i] = mfma16(FX, X[i - 4], acc[i]);
        const u32x4 k0 = TF(1024), k8 = TF(512), k16 = TF(0);
#pragma unroll
        for (int i = 0; i < 8; ++i) acc[i] = mfma16(k0, X[i], acc[i]);
#pragma unroll
        for (int i = 1; i < 8; ++i) acc[i] = mfma16(k8, X[i - 1], acc[i]);
#pragma unroll
        for (int i = 2; i < 8; ++i) acc[i] = mfma16(k16, X[i - 2], acc[i]);
        MFMA_DONE();
#pragma unroll
        for (int i = 0; i < 4; ++i) { lds_w128(pt + i * 1024 + lane * 16, __builtin_bit_cast(u32x4, acc[i])); acc[i] = acc[4 + i]; }
        axr = xt + lane * 16;
    }
    STAMP(7);
    if (w == 0 && lane < 8) lds_w32(smem + FL + lane * 4, 0u);
    WAIT_VM(0); SCHED();
    asm volatile("" :: "v"(junk2));
    BARRIER();
    STAMP(8);
#pragma unroll
    for (int i = 0; i < 4; ++i) acc[i] += __builtin_bit_cast(f32x4, lds_r128(axr + i * 1024));
#pragma unroll
    for (int i = 0; i < 2; ++i) {
        const f32x4 ya = acc[2 * i], yb = acc[2 * i + 1];
        const auto sx = __builtin_amdgcn_permlane16_swap(pk_bf16(ya[0], ya[1]), pk_bf16(yb[0], yb[1]), false, false);
        const auto sy = __builtin_amdgcn_permlane16_swap(pk_bf16(ya[2], ya[3]), pk_bf16(yb[2], yb[3]), false, false);
        st_sc1(wsr, boff + i * 1024, not_empty(u32x4{sx[0], sy[0], sx[1], sy[1]}));
    }
    BARRIER();
    STAMP(9);
    if (w < 2) STAMP_OUT(2 + 2 * w, blockIdx.x + 256 * w);
}

constexpr int SLOT = 34816, XCH = 16384, KVT_OFF = 151296, KVP_OFF = 27648;
DEVI constexpr int slot_off(int s) { return (s & 3) == 0 ? SLOT : ((s & 3) == 1 ? 0 : (s & 3) * SLOT); }
DEVI constexpr int wq_off(int t) { return t == 0 ? 0 : 49152 + t * 27648; }

constexpr int quad_src(int quad) {
    const int pp = quad >= 192 ? 1 : 0, qi = quad - 192 * pp;
    const bool isz = qi >= 180;
    const int el = isz ? 0 : qi / 5, qd = qi - el * 5, hi = el >= 18 ? 1 : 0;
    const int dql = isz ? (qi < 189 ? qi - 180 : 0) : qd + 4 * hi;
    const int et = isz ? 0 : (el < 4 ? 0 : (el < 20 ? 1 : 2)), col = isz ? 0 : (el < 4 ? 12 + el : (el < 20 ? el - 4 : el - 20));
    const int tile = dql == 8 ? 5 : (et == 0 ? (dql >> 2) : (et == 1 ? 2 + (dql >> 2) : 4));
    return KVP_OFF + pp * 6144 + tile * 1024 + ((dql == 8 ? et : (dql & 3)) * 16 + col) * 4;
}
constexpr int quad_dst(int quad, int hf) {
    const int pp = quad >= 192 ? 1 : 0, qi = quad - 192 * pp;
    const bool isz = qi >= 180;
    const int el = isz ? 0 : qi / 5, qd = qi - el * 5, hi = el >= 18 ? 1 : 0;
    const int dql = isz ? (qi < 189 ? qi - 180 : 0) : qd + 4 * hi;
    const int dq = 9 * pp + dql, ks = dq >> 3, e = 36 * pp + el, ee = e >> 4;
    const int fr = ee == 0 ? 0 : (ee < 3 ? 2 * ee - 1 + ks : 2 * ee - 2 + ks);
    const int hz = 2 * pp + ((dql > 4 || (dql == 4 && hf)) ? 1 : 0);
    const bool dead = isz ? qi >= 189 : ((!hi && qd == 4 && hf) || (hi && qd == 0 && !hf));
    return dead ? LDS_DUMMY : KVT_OFF + (isz ? (9 + ks) * 1024 + (dq & 3) * 256 + hz * 16 : fr * 1024 + ((dq & 3) * 16 + (e & 15)) * 16) + ((dq >> 2) & 1) * 8 + hf * 4;
}
struct Tab { int v[2048]; };
constexpr Tab make_tab() {
    Tab t{};
    for (int c = 0; c < 192; ++c) {
        const int l = c & 63, q = 2 * ((c & 192) + (l >> 5) * 16 + (l & 15)) + ((l >> 4) & 1);
        t.v[4 * c] = quad_src(2 * c); t.v[4 * c + 1] = quad_src(2 * c + 1);
        t.v[4 * c + 2] = (quad_dst(q, 0) - KVT_OFF) | ((quad_dst(q, 1) - KVT_OFF) << 16); t.v[4 * c + 3] = (quad_dst(q + 64, 0) - KVT_OFF) | ((quad_dst(q + 64, 1) - KVT_OFF) << 16);
    }
    for (int q = 0; q < 576; ++q) {
        const int rho = q / 9, pc = q % 9, k = (rho & 32) + 8 * ((rho >> 3) & 3) + 4 * (rho & 1) + ((rho >> 1) & 3), m = q >> 6;
        t.v[768 + q] = k * 3456 + pc * 16 - (m & 1) * 1024;
        t.v[1344 + q] = k * 1152 + pc * 16 - (m & 3) * 1024;
    }
    return t;
}
__device__ const Tab g_tab = make_tab();
constexpr int IDX = P2_B, GW_OFF = 139264;

#define GET_TAG() do { const u32x2 t_ = lds_r64(smem + LDS_TAG); tag = (u64)(unsigned)__builtin_amdgcn_readfirstlane(t_.x) | ((u64)(unsigned)__builtin_amdgcn_readfirstlane(t_.y) << 32); } while (0)
DEVI void gemm_kv(const Params& P, lptr smem, int rb, int hg, int kw, int cg2, u64& tag, rsrc_t wsr) {
    const int lane = threadIdx.x & 63, lo16 = lane & 15, g = lane >> 4;
    const int p = kw & 1, ct = (kw < 3 ? kw : 0) * 64 + lane;
    STAMP_DECL; STAMP(1);
    __builtin_amdgcn_s_setprio(2);
    const int xrow = ((kw >> 1) * 64 + lo16) * 128, sw = (g ^ ((lo16 >> 1) & 7)) << 4;
    lptr xa[2] = {smem + xrow + sw, smem + xrow + (sw ^ 64)};
    const int wrow = (4 * g + (lo16 >> 2)) * 288;
    lptr wa = smem + XCH + wrow + (9 * p + (lane & 3)) * 8, wa2 = smem + XCH + wrow + (9 * p + ((lane & 3) == 3 ? 18 : 8)) * 8;
    lptr flp = smem + FL, ixp = smem + IDX + ct * 16, irp = smem + IDX + 3072 + 2304 + lane * 4;
    unsigned one = 1u;
    u32x4 sel[2];
    sel[0] = mask_frag(lo16 < 8 ? 1u << lo16 : 0u, 0, 0x3f80u); sel[1] = mask_frag(lo16 >= 8 ? 1u << (lo16 - 8) : 0u, 0, 0x3f80u);
    unsigned sto = WS_PART + (unsigned)((hg * 16 + rb) * 3072 + ct * 16);
    unsigned off = WS_PART + (unsigned)((hg * 16 + g) * 3072 + kw * 1024 + lo16 * 16);
    asm volatile("" : "+v"(flp), "+v"(one), "+v"(ixp), "+v"(irp), "+v"(sel[0]), "+v"(sel[1]), "+v"(sto), "+v"(off));
    const f32x4 z4 = {0.f, 0.f, 0.f, 0.f};
    f32x4 acc[4][5];
#pragma unroll
    for (int i = 0; i < 4; ++i)
#pragma unroll
        for (int j = 0; j < 5; ++j) { const float e = j < 2 ? 1e-6f : (j == 2 && lo16 < 12 ? 1e-6f : 0.f); acc[i][j] = f32x4{e, e, e, e}; asm volatile("" : "+v"(acc[i][j])); }
    auto rel = [&](int s) { return (s & 2) ? slot_off(s) - 2 * SLOT : slot_off(s); };
    auto rebase = [&](int dlt) {
        xa[0] += dlt; xa[1] += dlt; wa += dlt; wa2 += dlt;
        asm volatile("" : "+v"(xa[0]), "+v"(xa[1]), "+v"(wa), "+v"(wa2));
    };
    constexpr int NB = 6;
    u32x2 wl[NB], wh[NB];
    u32x4 xf[2][4];
    auto rdw = [&](int f, int t) {
        const int n = f / 5, j = f % 5;
        const int o = rel(n >> 1) + (n & 1) * 9216 + t * 4608;
        const u32x2 v = j == 2 ? lds_tr(wa2 + o) : lds_tr(wa + o + (j == 0 ? 0 : (j == 1 ? 32 : (j == 3 ? 152 : 184))));
        if (t) wh[f % NB] = v; else wl[f % NB] = v;
    };
    auto rdx = [&](int n, int r) { xf[n & 1][r] = lds_r128(xa[n & 1] + rel(n >> 1) + r * 2048); };
    rebase(0);
    BARRIER();
    STAMP(2);
    rdw(0, 0); rdw(0, 1);
#pragma unroll
    for (int r = 0; r < 4; ++r) rdx(0, r);
#pragma unroll
    for (int f = 1; f < NB - 1; ++f) { rdw(f, 0); rdw(f, 1); }
    auto kstep = [&](const int n) __attribute__((always_inline)) {
        const int s = n >> 1;
#pragma unroll
        for (int m = 0; m < 20; ++m) {
            const int j = m >> 2, r = m & 3, f = 5 * n + j, fn = f + NB - 1;
            SCHED();
            const u32x4 wv = {wl[f % NB].x, wl[f % NB].y, wh[f % NB].x, wh[f % NB].y};
            mfma16i(acc[r][j], xf[n & 1][r], wv);
            SCHED();
            if (r < 2 && fn < 90) rdw(fn, r);
            if (r == 2 && j < 4 && n < 17) rdx(n + 1, j);
            if (m == 15 && n < 17) WAIT_LGKM(12);
            if (m == 18 && !(n & 1)) {
                if (s < 8 && (s & 1)) rebase((s & 2) ? -2 * SLOT : 2 * SLOT);
                if (s == 8) WAIT_LGKM0();
                RAW_BARRIER();
                if (s == 0) STAMP(3);
                if (s == 4) STAMP(4);
            }
            if (m == 19 && n < 17) WAIT_LGKM(2);
        }
    };
    kstep(0); kstep(1); kstep(2); kstep(3); kstep(4); kstep(5); kstep(6); kstep(7); kstep(8);
    kstep(9); kstep(10); kstep(11); kstep(12); kstep(13); kstep(14); kstep(15); kstep(16); kstep(17);
    __builtin_amdgcn_s_setprio(3);
    const u32x4 ix = lds_r128(ixp);
    unsigned oo[9];
#pragma unroll
    for (int i = 0; i < 9; ++i) oo[i] = *(LAS unsigned*)(irp + i * 256);
    MFMA_DONE();
    STAMP(5);
    {
        f32x4 r[6], t0 = z4, t1 = z4, t2 = z4;
#pragma unroll
        for (int i = 0; i < 5; ++i) r[i] = z4;
#pragma unroll
        for (int u = 0; u < 2; ++u) {
            u32x4 pk[3], vb[3];
#pragma unroll
            for (int j = 0; j < 3; ++j) pk[j] = pack8(phi4(acc[2 * u][j]), phi4(acc[2 * u + 1][j]));
            vb[0] = pack8(acc[2 * u][2], acc[2 * u + 1][2]);
            if (lo16 < 12) vb[0] = u32x4{0x3f803f80u, 0x3f803f80u, 0x3f803f80u, 0x3f803f80u};
            vb[1] = pack8(acc[2 * u][3], acc[2 * u + 1][3]); vb[2] = pack8(acc[2 * u][4], acc[2 * u + 1][4]);
            t0 = mfma16(pk[2], vb[0], t0); t1 = mfma16(pk[2], vb[1], t1); t2 = mfma16(pk[2], vb[2], t2);
            r[0] = mfma16(pk[0], vb[0], r[0]); r[1] = mfma16(pk[1], vb[0], r[1]); r[2] = mfma16(pk[0], vb[1], r[2]);
            r[3] = mfma16(pk[1], vb[1], r[3]); r[4] = mfma16(pk[1], vb[2], r[4]);
        }
        r[5] = g == 0 ? t0 : (g == 1 ? t1 : t2);
        MFMA_DONE();
        SET_M0(KVP_OFF + kw * 6144);
#pragma unroll
        for (int t = 5; t >= 0; --t) {
            W_ADDTID(r[t][0], t * 1024); W_ADDTID(r[t][1], t * 1024 + 256);
            W_ADDTID(r[t][2], t * 1024 + 512); W_ADDTID(r[t][3], t * 1024 + 768);
        }
        FLAG_ADD(F_KVP);
    }
    STAMP(6);
    if (kw < 3) {
        lptr dst[4] = {smem + KVT_OFF + (ix.z & 0xffffu), smem + KVT_OFF + (ix.z >> 16), smem + KVT_OFF + (ix.w & 0xffffu), smem + KVT_OFF + (ix.w >> 16)};
        asm volatile("" : "+v"(dst[0]), "+v"(dst[1]), "+v"(dst[2]), "+v"(dst[3]));
        flag_wait<0>(smem, F_KVP, 4);
        STAMP(7);
        float pa[2][2][4];
#pragma unroll
        for (int i = 0; i < 2; ++i)
#pragma unroll
            for (int r = 0; r < 2; ++r)
#pragma unroll
                for (int k = 0; k < 4; ++k) pa[i][r][k] = lds_rf(smem + ix[i] + r * 12288 + k * 256);
        SCHED();
        u32x4 pv;
#pragma unroll
        for (int i = 0; i < 2; ++i)
#pragma unroll
            for (int k = 0; k < 2; ++k)
                pv[2 * i + k] = pk_bf16(pa[i][0][2 * k] + pa[i][1][2 * k], pa[i][0][2 * k + 1] + pa[i][1][2 * k + 1]);
        st_sc1(wsr, sto, not_empty(pv));
        SCHED();
        FLAG_ADD(F_KVR);
        STAMP(8);
        WAIT_VM(0);
        STAMP(9);
        f32x4 gs[4][2];
        auto group = [&](int i, const u32x4 (&a)[4]) {
            gs[i][0] = mfma16(sel[1], a[1], mfma16(sel[0], a[0], z4));
            gs[i][1] = mfma16(sel[1], a[3], mfma16(sel[0], a[2], z4));
        };
        {
            u32x4 a[4][4];
#pragma unroll
            for (int i = 0; i < 4; ++i)
#pragma unroll
                for (int t = 0; t < 4; ++t) a[i][t] = __builtin_amdgcn_raw_buffer_load_b128(wsr, off + t * 256, ((i + rb) & 3) * 12288, 16);
            SCHED();
            FLAG_ADD(F_SW);
#pragma unroll
            for (int i = 0; i < 4; ++i) group(i, a[i]);
        }
#pragma unroll
        for (int i = 0; i < 4; ++i) {
#pragma unroll 1
            for (unsigned spins = 0; spins < SPIN_CAP; ++spins) {
                if (!__any(__builtin_isunordered(gs[i][0][0], gs[i][1][0]))) break;
                if (spins) __builtin_amdgcn_s_sleep(RSLEEP);
                CFENCE();
                u32x4 a[4];
#pragma unroll
                for (int t = 0; t < 4; ++t) a[t] = __builtin_amdgcn_raw_buffer_load_b128(wsr, off + t * 256, ((i + rb) & 3) * 12288, 16);
                group(i, a);
                if (spins >= 32 && __all(max(max(umax4(a[0]), umax4(a[1])), max(umax4(a[2]), umax4(a[3]))) != EMPTY)) break;
            }
        }
        f32x4 sum[2];
        sum[0] = (gs[0][0] + gs[1][0]) + (gs[2][0] + gs[3][0]); sum[1] = (gs[0][1] + gs[1][1]) + (gs[2][1] + gs[3][1]);
#pragma unroll
        for (int i = 0; i < 4; ++i) lds_w32(dst[i], pk_bf16(sum[i >> 1][2 * (i & 1)], sum[i >> 1][2 * (i & 1) + 1]));
        CFENCE();
        FLAG_ADD(F_D);
        STAMP(10);
    } else {
        auto wo_chunks = [&](int c0, int c1) {
#pragma unroll 1
            for (int c = c0; c < c1; ++c) {
                const char* ws = P.wo + cg2 * 144 + c * (64 * 1152);
                const unsigned l = c < 8 ? c * 9216 : WO8;
                SET_M0(l); glds16o<0>(ws, oo[0]); glds16o<1024>(ws, oo[1]); glds16o<2048>(ws, oo[2]); glds16o<3072>(ws, oo[3]);
                SET_M0(l + 4096); glds16o<0>(ws, oo[4]); glds16o<1024>(ws, oo[5]); glds16o<2048>(ws, oo[6]); glds16o<3072>(ws, oo[7]);
                SET_M0(l + 8192); glds16o<0>(ws, oo[8]);
            }
        };
        flag_wait<1>(smem, F_KVR, 3);
        wo_chunks(3, 6);
        flag_wait<1>(smem, F_Q0, 8);
        if (WOSW) flag_wait<1>(smem, F_SW, 3);
        wo_chunks(0, 3);
        WAIT_VM(27);
        FLAG_ADD(F_D);
        WAIT_VM(0);
        FLAG_ADD(F_W0);
    }
    GET_TAG();
    __builtin_amdgcn_s_setprio(0);
    if (kw == 0) STAMP_OUT(1, blockIdx.x);
}

DEVI void gemm_q(const Params& P, lptr smem, int rb, int hg, int t8, int cg2, u64& tag, rsrc_t wsr) {
    const int lane = threadIdx.x & 63, lw = t8 & 3;
    const bool ld = t8 < 4;
    STAMP_DECL; STAMP(1);
    __builtin_amdgcn_s_setprio(3);
    unsigned xo[4], wo_[5], qo[3];
    const int rx = hg & 3, rw = rb & 3;
    unsigned mx[4], mw[4];
    const unsigned xbase = (unsigned)((rb * 128 + 8 * lw + (lane >> 3)) * 1152 + (((lane & 7) ^ ((lane >> 4) + 4 * (lw & 1))) << 4));
#pragma unroll
    for (int i = 0; i < 4; ++i) { mx[i] = (((i + rx) & 3) * 4 + lw) * 1024; mw[i] = XCH + (((i + rw) & 3) * 4 + lw) * 1024; xo[i] = xbase + ((i + rx) & 3) * 36864; }
    const unsigned lb = lw * 4096;
    auto issue_x = [&](int s, int i0 = 0, int i1 = 4) {
#pragma unroll
        for (int i = i0; i < i1; ++i) glds16(P.x + s * 128, xo[i], slot_off(s) + mx[i]);
    };
    auto issue_w = [&](int s, int i0 = 0, int i1 = 4) {
        const char* ws = P.wqkv + s * (64 * 3456);
        if (i1 == 4 && (lw >> 1) == (s & 1)) glds16(ws, wo_[4], (unsigned)(slot_off(s) + XCH + 16384 + (lw & 1) * 1024));
#pragma unroll
        for (int i = i0; i < i1; ++i) glds16(ws, wo_[i], slot_off(s) + mw[i]);
    };
    auto issue_q = [&](int t, int i) {
        const char* ws = P.wqkv + hg * 144 + (3 * t + i) * (64 * 3456);
        if (lw == ((i + t) & 3)) glds16(ws, qo[2], (unsigned)(wq_off(t) + i * 9216 + 8192));
        SET_M0(wq_off(t) + i * 9216 + (lb >> 1));
        glds16o<0>(ws, qo[0]); glds16o<1024>(ws, qo[1]);
    };
    auto w_off = [&](int i) {
        const unsigned q = 64 * (i < 4 ? ((i + rw) & 3) * 4 + lw : 16 + (lw & 1)) + lane, rho = (q * 3641u) >> 16, pc = q - rho * 18;
        const unsigned k = (rho & 35u) + 8 * ((rho >> 2) & 3) + 4 * ((rho >> 4) & 1);
        wo_[i] = k * 3456 + (1152 + hg * 144) + pc * 16 + (pc < 9 ? 0u : 1008u);
    };
    if (ld) {
        issue_x(0, 0, 1); issue_x(1, 0, 1);
        SCHED();
        w_off(0);
        issue_w(0, 0, 1); issue_w(1, 0, 1);
        SCHED();
#pragma unroll
        for (int i = 1; i < 5; ++i) w_off(i);
        if (lw == 1) { glds16(P.gw, (unsigned)(lane * 16), GW_OFF); if (lane < 8) glds16(P.gw + 1024, (unsigned)(lane * 16), GW_OFF + 1024); }
        issue_x(0, 1, 4); issue_w(0, 1, 4); issue_w(1, 1, 4); issue_x(1, 1, 4);
        SET_M0(IDX + (lb >> 1));
        glds16o<0>((const char*)&g_tab + (lb >> 1), (unsigned)(lane * 16)); glds16o<1024>((const char*)&g_tab + (lb >> 1), (unsigned)(lane * 16));
    } else {
        if (lw == 0 && lane < 16) lds_w32(smem + FL + lane * 4, 0u);
#pragma unroll
        for (int i = 0; i < 3; ++i) lds_w128(smem + KVT_OFF + i * 4096 + lw * 1024 + lane * 16, u32x4{0u, 0u, 0u, 0u});
    }
    const int lo16 = lane & 15, g = lane >> 4;
    const int sw = (g ^ ((lo16 >> 1) & 7)) << 4;
    lptr xa[2] = {smem + (16 * t8 + lo16) * 128 + sw, smem + (16 * t8 + lo16) * 128 + (sw ^ 64)}, xb[2] = {xa[0] + 2 * SLOT, xa[1] + 2 * SLOT};
    lptr flp = smem + FL, iqp = smem + IDX + 3072 + lane * 4, gwp = smem + GW_OFF + g * 16;
    const int qrow = (2 * (4 * g + (lo16 >> 2))) * 144;
    lptr qw = smem + qrow + (lane & 3) * 8, qw4 = smem + qrow + (16 + (lane & 1)) * 8 + (lane & 2) * 2304;
    lptr qwh = qw + wq_off(1), qw4h = qw4 + wq_off(1), kfp = smem + KVT_OFF + lane * 16, kzp = smem + KVT_OFF + 9216 + (lane & 51) * 16;
    unsigned one = 1u, ob0 = WS_O + (unsigned)(((rb * 8 + t8) * 72 + hg * 9 + 2 * (g & 1) + (g >> 1)) * 256 + lo16 * 16);
    asm volatile("" : "+v"(flp), "+v"(one), "+v"(iqp), "+v"(gwp), "+v"(xa[0]), "+v"(xa[1]), "+v"(xb[0]), "+v"(xb[1]));
    asm volatile("" : "+v"(qw), "+v"(qw4), "+v"(qwh), "+v"(qw4h), "+v"(kfp), "+v"(kzp), "+v"(ob0));
    asm volatile(".subsection 2\n.Lkf_code_end:\n\t.fill 256, 4, 0xbf800000\n\t.subsection 0");
    const f32x4 z4 = {0.f, 0.f, 0.f, 0.f};
    f32x4 gacc = z4, qa[6];
#pragma unroll
    for (int j = 0; j < 6; ++j) { const float e = j < 5 ? 1e-6f : 0.f; qa[j] = f32x4{e, e, e, e}; asm volatile("" : "+v"(qa[j])); }
    u32x4 xk[18];
    u64 pf = 0;
    WAIT_VM(8); BARRIER();
    if (ld) { issue_w(2); issue_x(2); if (LA3) { issue_w(3); issue_x(3); } }
#pragma unroll
    for (int s = 0; s < 9; ++s) {
#pragma unroll
        for (int k = 0; k < 2; ++k) xk[2 * s + k] = lds_r128(((s & 2) ? xb[k] - 2 * SLOT : xa[k]) + slot_off(s));
        if (t8 == hg) {
            gacc = mfma16(lds_r128(gwp + s * 128), xk[2 * s], gacc);
            gacc = mfma16(lds_r128(gwp + s * 128 + 64), xk[2 * s + 1], gacc);
        }
        if (t8 == 4) {
            if (s == 1) { tag = *(const __attribute__((address_space(4))) u64*)(P.ws + WS_CTL); PIN_TAG(); if (lane == 0) lds_w64(smem + LDS_TAG, u32x2{(unsigned)tag, (unsigned)(tag >> 32)}); }
            if (s == 4) asm volatile("global_load_dwordx2 %0, %1, %2 sc1" : "=v"(pf) : "v"((lane < 5 ? lane : 0) * 128), "s"(P.ws + WS_PFL) : "memory");
            if (s == 7) {
                asm volatile("s_waitcnt vmcnt(0)" : "+v"(pf) :: "memory");
                if (!__all(pf == tag)) poll_flags(P, WS_PFL, 5, tag, lane);
            }
        }
        if (LA3) { if (s < 6) WAIT_VM(17); else if (s == 6) WAIT_VM(14); else WAIT_VM(12); }
        else if (s < 7) WAIT_VM(8); else WAIT_VM(6);
        BARRIER();
        if (ld) {
            const int sn = s + 3 + LA3;
            if (sn < 9) { issue_w(sn); issue_x(sn); } else if (sn < 12) { issue_q(sn - 9, 0); issue_q(sn - 9, 1); issue_q(sn - 9, 2); }
            if (s == 3) { qo[0] = *(LAS unsigned*)(iqp + lw * 512); qo[1] = *(LAS unsigned*)(iqp + lw * 512 + 256); qo[2] = *(LAS unsigned*)(iqp + 2048); CFENCE(); }
        }
    }
    __builtin_amdgcn_s_setprio(0);
    STAMP(5);
    if (!ld) {
        const unsigned co = WS_O + (unsigned)(((rb * 8 + 2 * lw) * 72 + hg * 9) * 256 + lane * 16);
#pragma unroll
        for (int i = 0; i < 5; ++i)
            if (i < 4 || lane < 32) st_sc1(wsr, i < 4 ? co + (i >> 1) * 18432 + (i & 1) * 1024 : co + 2048 + (lane >> 4) * (18432 - 256), u32x4{EMPTY, EMPTY, EMPTY, EMPTY});
    }
    if (t8 >= 5) {
        unsigned oo[9];
#pragma unroll
        for (int i = 0; i < 9; ++i) oo[i] = *(LAS unsigned*)(iqp + 2304 + i * 256);
        const char* ws = P.wo + cg2 * 144 + (t8 + 1) * (64 * 1152);
        const unsigned l = t8 < 7 ? (t8 + 1) * 9216 : WO8;
        SET_M0(l); glds16o<0>(ws, oo[0]); glds16o<1024>(ws, oo[1]); glds16o<2048>(ws, oo[2]); glds16o<3072>(ws, oo[3]);
        SET_M0(l + 4096); glds16o<0>(ws, oo[4]); glds16o<1024>(ws, oo[5]); glds16o<2048>(ws, oo[6]); glds16o<3072>(ws, oo[7]);
        SET_M0(l + 8192); glds16o<0>(ws, oo[8]);
    }
    GET_TAG();
    if (t8 == 4 && lane == 0) tag_post(P, tag);
    if (t8 == hg) {
        const float gate = rbf(__builtin_amdgcn_rcpf(1.f + __expf(-rbf(gacc[0]))));
        if (lane < 16) __hip_atomic_store((GAS float*)(P.ws + WS_GATE) + (rb * 128 + hg * 16 + lane), gate, RLX_AGENT);
    }
    auto peek = [&](int f) { return __hip_atomic_load((LAS unsigned*)(flp + f), __ATOMIC_RELAXED, __HIP_MEMORY_SCOPE_WORKGROUP); };
    auto rdq = [&](int ks, int j) {
        const int o = (ks < 6 ? ks : ks - 6) * 4608;
        return j < 4 ? lds_trfrag((ks < 6 ? qw : qwh) + o + 32 * j, 144) : lds_trfrag((ks < 6 ? qw4 : qw4h) + o, 144);
    };
    u32x4 wq[2][4], w4[2];
#pragma unroll
    for (int i = 0; i < 2; ++i) {
#pragma unroll
        for (int j = 0; j < 4; ++j) wq[i][j] = rdq(i, j);
        if (i == 0) w4[0] = rdq(0, 4);
    }
    unsigned seenq = 0;
#pragma unroll
    for (int ks = 0; ks < 18; ++ks) {
        if (ks == 3 && ld) { SCHED(); WAIT_VM(6); FLAG_ADD(F_LANDED); }
        if (ks == 4) { SCHED(); FLAG_ADD(F_Q0); seenq = peek(F_LANDED); NEED(seenq, F_LANDED, 4); }
        if (ks == 8) { SCHED(); WAIT_VM(0); FLAG_ADD(F_C1); }
        if (ks == 10) { SCHED(); seenq = peek(F_C1); NEED(seenq, F_C1, 8); }
        if (ks == QH) { SCHED(); seenq = peek(F_KVR); NEED(seenq, F_KVR, 3); STAMP(6); }
        if (ks == 16) { SCHED(); FLAG_ADD(F_QD); }
#pragma unroll
        for (int j = 0; j < 5; ++j) {
            SCHED();
            if (j == 0 && ks < 17) { if (ks & 1) WAIT_LGKM(10); else WAIT_LGKM(8); }
            if (j < 4) mfma16i(qa[j], wq[ks & 1][j], xk[ks]); else mfma16i(qa[4 + (ks & 1)], w4[(ks >> 1) & 1], xk[ks]);
            SCHED();
            if (ks + 2 < 18) { if (j < 4) wq[ks & 1][j] = rdq(ks + 2, j); else if (!(ks & 1)) w4[((ks >> 1) + 1) & 1] = rdq(ks + 2, 4); }
        }
    }
    __builtin_amdgcn_s_setprio(2);
    MFMA_DONE();
#pragma unroll
    for (int i = 0; i < 4; ++i) {
        const auto sw2 = __builtin_amdgcn_permlane32_swap(__float_as_uint(qa[5][i]), __float_as_uint(qa[5][i]), false, false);
        qa[4][i] += __uint_as_float(sw2[1]);
    }
    u32x4 b0 = pack8(phi4(qa[0]), phi4(qa[1])), b1 = pack8(phi4(qa[2]), phi4(qa[3])), b2 = pack8(phi4(qa[4]), z4);
    asm volatile("" : "+v"(b0), "+v"(b1), "+v"(b2));
    STAMP(7);
    flag_wait<1>(smem, F_D, 4);
        if (t8 == 7 && lane == 0) __hip_atomic_store((GAS u64*)(P.ws + WS_HINT + (rb * 8 + hg) * 128), tag, RLX_AGENT);
    STAMP(8);
    {
        u32x4 kf[12];
#pragma unroll
        for (int i = 0; i < 12; ++i) { kf[i] = lds_r128(i < 3 ? kzp + i * 1024 : kfp + (i - 3) * 1024); SCHED(); }
        SCHED();
        const f32x4 den = mfma16(kf[2], b2, mfma16(kf[1], b1, mfma16(kf[0], b0, f32x4{1e-6f, 1e-6f, 1e-6f, 1e-6f})));
        f32x4 o[5];
        o[0] = mfma16(kf[3], b0, z4);
        o[1] = mfma16(kf[5], b1, mfma16(kf[4], b0, z4));
        o[2] = mfma16(kf[7], b1, mfma16(kf[6], b0, z4));
        o[3] = mfma16(kf[9], b2, mfma16(kf[8], b1, z4));
        o[4] = mfma16(kf[11], b2, mfma16(kf[10], b1, z4));
        f32x4 r;
#pragma unroll
        for (int i = 0; i < 4; ++i) r[i] = __builtin_amdgcn_rcpf(den[i]);
        const float r1a = g == 0 ? r[0] : r[1], r2 = g == 0 ? r[1] : r[2], r3a = g <= 1 ? r[2] : r[3], r3b = g == 0 ? r[2] : r[3];
        u32x2 ob[5];
        ob[0] = u32x2{pk_bf16(o[0][0] * r[0], o[0][1] * r[0]), pk_bf16(o[0][2] * r[0], o[0][3] * r[0])};
        ob[1] = u32x2{pk_bf16(o[1][0] * r1a, o[1][1] * r1a), pk_bf16(o[1][2] * r[1], o[1][3] * r[1])};
        ob[2] = u32x2{pk_bf16(o[2][0] * r2, o[2][1] * r2), pk_bf16(o[2][2] * r2, o[2][3] * r2)};
        ob[3] = u32x2{pk_bf16(o[3][0] * r3a, o[3][1] * r3a), pk_bf16(o[3][2] * r3b, o[3][3] * r3b)};
        ob[4] = u32x2{pk_bf16(o[4][0] * r[3], o[4][1] * r[3]), pk_bf16(o[4][2] * r[3], o[4][3] * r[3])};
        SCHED();
        SCHED();
#pragma unroll
        for (int t = 0; t < 3; ++t) {
            const u32x2 a = ob[2 * t], b = ob[t < 2 ? 2 * t + 1 : 4];
            const auto sx = __builtin_amdgcn_permlane16_swap(a.x, b.x, false, false), sy = __builtin_amdgcn_permlane16_swap(a.y, b.y, false, false);
            if (t < 2 || g == 0) st_sc1(wsr, ob0 + 1024 * t, not_empty(u32x4{sx[0], sy[0], sx[1], sy[1]}));
        }
    }
    __builtin_amdgcn_s_setprio(0);
    STAMP(9);
    if (t8 == 0) STAMP_OUT(5, blockIdx.x + 256);
    if (t8 == 7) STAMP_OUT(7, blockIdx.x + 768);
}

DEVI void phase2(const Params& P, lptr smem, int rt64, int cg, bool gemm, u64 tag, rsrc_t wsr) {
    const int lane = threadIdx.x & 63, w0 = __builtin_amdgcn_readfirstlane(threadIdx.x >> 6);
    const int w = gemm ? (w0 + 8) % 12 : w0, tid = threadIdx.x, bt = tid - 192;
    const int lo16 = lane & 15, g = lane >> 4;
    const int rt = w & 3, kt = w >> 2;
    STAMP_DECL; STAMP(1);
    constexpr int pb = P2_B;
    const int pa = gemm ? P2_ACC : 82944;
    if (gemm && w < 4) flag_wait(smem, F_W0, 1);
    STAMP(2);
    const int qrow = (2 * (4 * g + (lo16 >> 2))) * 144;
    lptr qw = smem + qrow + (lane & 3) * 8, qw4 = smem + qrow + (16 + (lane & 1)) * 8;
    int wofs[3], oofs[3];
#pragma unroll
    for (int u = 0; u < 3; ++u) {
        const int c = kt * 3 + (u + cg) % 3;
        wofs[u] = (c < 8 || !gemm) ? c * 9216 : WO8; oofs[u] = c * 2048;
    }
    auto rdo = [&](int i, int j) { return j < 4 ? lds_trfrag(qw + wofs[i >> 1] + (i & 1) * 4608 + 32 * j, 144) : lds_trfrag(qw4 + wofs[i >> 1] + (i & 1) * 4608, 144); };
    const unsigned ooff = WS_O + (unsigned)((rt64 * 4 + rt) * 18432 + lane * 16);
    auto ldo = [&](int i) { return __builtin_amdgcn_raw_buffer_load_b128(wsr, ooff + (i & 1) * 1024, oofs[i >> 1], 16); };
    u32x4 wf[6][5];
#pragma unroll
    for (int i = 0; i < 6; ++i)
#pragma unroll
        for (int j = 0; j < WPRE; ++j) wf[i][j] = rdo(i, j);
    const int r0 = tid < 576 ? tid / 9 : 0, c0 = tid - r0 * 9;
    unsigned boff = WS_B + (unsigned)((cg * 72 + (bt >> 3)) * 4096 + (rt64 >> 3) * 1024 + (((bt >> 2) & 1) * 32 + ((rt64 >> 2) & 1) * 16 + (rt64 & 3) * 4 + (bt & 3)) * 16);
    lptr bp = smem + pb + ((bt & 3) * 2 + ((bt >> 2) & 1)) * 1168 + (bt >> 3) * 2;
    unsigned goff = WS_GATE + (unsigned)((rt64 * 64 + r0) * 4);
    lptr ap = smem + pa + kt * (64 * P2_AP) + (rt * 16 + lo16) * P2_AP + (g & 1) * 144 + (g >> 1) * 16;
    lptr fa = smem + pa + r0 * P2_AP + c0 * 16, fb = smem + pb + (tid + (r0 >> 3)) * 16;
    unsigned fo = (unsigned)((rt64 * 64 + r0) * 1152 + cg * 144 + c0 * 16);
    asm volatile("" : "+v"(boff), "+v"(goff), "+v"(bp), "+v"(ap), "+v"(fa), "+v"(fb), "+v"(fo));
    STAMP(3);
    if (w == 3) {
        const int a0 = cg * 72, g0 = (a0 >> 6) * 11 + (a0 & 63) / 6, a1 = a0 + 71, g1 = (a1 >> 6) * 11 + (a1 & 63) / 6;
        poll_addr<OSLEEP>(P, lane < 8 ? WS_HINT + ((rt64 >> 1) * 8 + lane) * 128 : WS_BFL + min(g0 + lane - 8, g1) * 128, tag);
        if (gemm) flag_wait<1>(smem, F_QD, 8);
        flag_set(F_H, 1);
    } else flag_wait<1>(smem, F_H, 1);
    STAMP(4);
    u32x4 bv; unsigned gv;
    auto b_put = [&]() {
#pragma unroll
        for (int q = 0; q < 4; ++q) { lds_w16(bp + (2 * q) * 144, bv[q]); lds_w16(bp + (2 * q + 1) * 144, bv[q] >> 16); }
    };
    bool b_todo = false;
    if (w0 >= 3) bv = ld_sc1(wsr, boff);
    f32x4 acc[5];
    {
        u32x4 of[6];
#pragma unroll
        for (int i = 0; i < 6; ++i) of[i] = ldo(i);
        gv = __builtin_amdgcn_raw_buffer_load_b32(wsr, goff, 0, 16);
        SCHED();
#pragma unroll
        for (int j = WPRE; j < 5; ++j)
#pragma unroll
            for (int i = 0; i < 6; ++i) wf[i][j] = rdo(i, j);
        SCHED();
        if (w0 >= 3) { if (__all(umax4(bv) != EMPTY)) b_put(); else b_todo = true; }
        SCHED();
#pragma unroll
        for (int j = 0; j < 5; ++j) {
            acc[j] = f32x4{0.f, 0.f, 0.f, 0.f};
#pragma unroll
            for (int i = 0; i < 6; ++i) {
                acc[j] = mfma16(wf[i][j], of[i], acc[j]);
                if (i == 3 && j > 0) { SCHED(); lds_w128(ap + (j - 1) * 32, __builtin_bit_cast(u32x4, acc[j - 1])); SCHED(); }
            }
        }
        if (g < 2) lds_w128(ap + 128, __builtin_bit_cast(u32x4, acc[4]));
    }
    if (!__all(acc[4][0] == acc[4][0])) {
#pragma unroll
        for (int j = 0; j < 5; ++j) acc[j] = f32x4{0.f, 0.f, 0.f, 0.f};
        u32x4 of[6];
#pragma unroll 1
        for (unsigned spins = 0; spins < SPIN_CAP; ++spins) {
            CFENCE();
            unsigned m = 0;
#pragma unroll
            for (int i = 0; i < 6; ++i) of[i] = ldo(i);
#pragma unroll
            for (int i = 0; i < 6; ++i) m = max(m, umax4(of[i]));
            if (__all(m != EMPTY)) break;
        }
#pragma unroll
        for (int i = 0; i < 6; ++i)
#pragma unroll
            for (int j = 0; j < 5; ++j) acc[j] = mfma16(rdo(i, j), of[i], acc[j]);
#pragma unroll
        for (int j = 0; j < 5; ++j) if (j < 4 || g < 2) lds_w128(ap + j * 32, __builtin_bit_cast(u32x4, acc[j]));
    }
    if (b_todo) {
#pragma unroll 1
        for (unsigned spins = 0; spins < SPIN_CAP; ++spins) {
            CFENCE();
            bv = ld_sc1(wsr, boff);
            if (__all(umax4(bv) != EMPTY)) break;
            __builtin_amdgcn_s_sleep(2);
        }
        b_put();
    }
    const float gt = __uint_as_float(gv);
    float og = rbf(1.f - gt);
    asm volatile("" : "+v"(og));
    STAMP(5);
    BARRIER();
    STAMP(6);
    if (w0 < 9) {
        lptr a = fa;
        const f32x4 s0 = __builtin_bit_cast(f32x4, lds_r128(a)) + __builtin_bit_cast(f32x4, lds_r128(a + 64 * P2_AP)) + __builtin_bit_cast(f32x4, lds_r128(a + 128 * P2_AP));
        const f32x4 s1 = __builtin_bit_cast(f32x4, lds_r128(a + 144)) + __builtin_bit_cast(f32x4, lds_r128(a + 64 * P2_AP + 144)) + __builtin_bit_cast(f32x4, lds_r128(a + 128 * P2_AP + 144));
        const u32x4 bb = lds_r128(fb);
        u32x4 o;
#pragma unroll
        for (int i = 0; i < 4; ++i) {
            const float a0 = i < 2 ? s0[2 * i] : s1[2 * i - 4], a1 = i < 2 ? s0[2 * i + 1] : s1[2 * i - 3];
            o[i] = pk_bf16(gt * a0 + og * bf2f(bb[i]), gt * a1 + og * bfhi(bb[i]));
        }
        __builtin_amdgcn_raw_buffer_store_b128(o, __builtin_amdgcn_make_buffer_rsrc(P.out, 0, SEQ * DM * 2, 0x00020000), fo, 0, OUTAUX);
    }
    STAMP(7);
    if (w == 4) STAMP_OUT(blockIdx.x < 128 ? 3 : 6, blockIdx.x + 512);
}

DEVI void helper_role(const Params& P, lptr smem, int xcd, int li, u64& tag, rsrc_t wsr) {
    const int tid = threadIdx.x, lane = tid & 63, w = __builtin_amdgcn_readfirstlane(tid >> 6);
    STAMP_DECL; STAMP(1);
    if (li >= 13) {
        if (HD) __builtin_amdgcn_s_sleep(HD);
#pragma unroll 1
        for (int n = 12 * NTW + (li - 13) + 3 * w; n < 171; n += 36) touch_group(P, xcd, n, lane);
    }
    if (li == 12) {
#pragma unroll
        for (int i = 0; i < 7; ++i) {
            const int u = (i * 5 + xcd - 3) * 12 + w;
            if (u < 384) st_sc1(wsr, WS_PART + (unsigned)(u * 1024 + lane * 16), u32x4{EMPTY, EMPTY, EMPTY, EMPTY});
        }
        WAIT_VM(0);
    }
    PIN_TAG();
    BARRIER();
    if (tid == 0) {
        if (li == 12) __hip_atomic_store((GAS u64*)(P.ws + WS_PFL + (xcd - 3) * 128), tag, RLX_AGENT);
        tag_post(P, tag);
    }
    if (li >= 13) {
#pragma unroll 1
        for (int n = 171 + (li - 13) + 3 * w; n < 216; n += 36) touch_group(P, xcd, n, lane);
    }
    STAMP(2);
    if (w == 11) {
        STAMP_OUT(8, blockIdx.x);
        if (blockIdx.x == 255) {
            const GAS u64* f = (const GAS u64*)(P.ws + WS_ARR) + lane;
#pragma unroll 1
            for (unsigned spins = 0; spins < SPIN_CAP; ++spins) {
                const bool ok = __hip_atomic_load(f, RLX_AGENT) == tag && __hip_atomic_load(f + 64, RLX_AGENT) == tag &&
                                __hip_atomic_load(f + 128, RLX_AGENT) == tag && __hip_atomic_load(f + 192, RLX_AGENT) == tag;
                if (__all(ok)) break;
                __builtin_amdgcn_s_sleep(8);
            }
            if (lane == 0) __hip_atomic_store((GAS u64*)(P.ws + WS_CTL), tag, RLX_AGENT);
        }
    }
}

__global__ __launch_bounds__(768, 3) void k_fused(Params P) {
    __shared__ __attribute__((aligned(16))) char smem_[LDS_BYTES];
    lptr smem = (lptr)smem_;
    const int b = blockIdx.x, xcd = b & 7, idx = b >> 3;
    const int lane = threadIdx.x & 63, w = __builtin_amdgcn_readfirstlane(threadIdx.x >> 6);
    const rsrc_t wsr = __builtin_amdgcn_make_buffer_rsrc(P.ws, 0, (int)WS_END, 0x00020000);
    const int rt64 = xcd * 4 + (idx & 3), cg2 = idx >> 2;
    if (idx >= CPF && w < 6) {
        const char* pc; unsigned len;
        asm volatile("s_getpc_b64 %0\n.Lkf_code_begin:\n\ts_mov_b32 %1, .Lkf_code_end-.Lkf_code_begin" : "=s"(pc), "=s"(len));
        touch(pc, min((unsigned)(threadIdx.x * 128), len));
    }
    u64 tag;
    if (__builtin_expect(idx < 16, 1)) {
        const int rbl = idx & 3, hgl = idx >> 2, rb = (xcd >> 1) * 4 + rbl, hg = (xcd & 1) * 4 + hgl;
        if (__builtin_expect((w & 12) != 4, 1)) gemm_q(P, smem, rb, hg, w < 4 ? w : w - 4, cg2, tag, wsr);
        else gemm_kv(P, smem, rb, hg, w - 4, cg2, tag, wsr);
    } else {
        tag = *(const __attribute__((address_space(4))) u64*)(P.ws + WS_CTL);
        const int li = idx - 16, gid = li < 11 ? xcd * 11 + li : 88 + (li - 11) * 8 + xcd;
        if (gid < 99) ssm_role(P, smem, xcd, li, gid, cg2, tag, wsr);
        else {
            helper_role(P, smem, xcd, li, tag, wsr);
            if (w < 9) { wo_issue<0x1ff>(P, cg2, w, 9, lane); WAIT_VM(0); }
            if (w == 10 && lane < 16) lds_w32(smem + FL + lane * 4, 0u);
            BARRIER();
        }
    }
    phase2(P, smem, rt64, cg2, idx < 16, tag, wsr);
}

extern "C" void kernel_launch(void* const* d_in, const int* in_sizes, int n_in, void* d_out, int out_size, void* d_ws, size_t ws_size,
                              hipStream_t stream) {
    Params P;
    P.x = (const char*)d_in[0]; P.wqkv = (const char*)d_in[1]; P.wo = (const char*)d_in[2];
    P.logA = (const char*)d_in[4]; P.aim = (const char*)d_in[5]; P.cre = (const char*)d_in[6]; P.cim = (const char*)d_in[7];
    P.logdt = (const char*)d_in[8]; P.dskip = (const char*)d_in[9]; P.gw = (const char*)d_in[10];
    P.out = (char*)d_out; P.ws = (char*)d_ws;
    k_fused<<<256, 768, 0, stream>>>(P);
}
```

```cpp
#include <hip/hip_runtime.h>
#include <stdint.h>

#define GAS __attribute__((address_space(1)))
#define LAS __attribute__((address_space(3)))
#define DEVI __device__ __forceinline__
typedef unsigned short u16;
typedef unsigned long long u64;
typedef __attribute__((ext_vector_type(2))) unsigned u32x2;
typedef __attribute__((ext_vector_type(3))) unsigned u32x3;
typedef __attribute__((ext_vector_type(4))) unsigned u32x4;
typedef __attribute__((ext_vector_type(4))) short s16x4;
typedef __attribute__((ext_vector_type(2))) float f32x2;
typedef __attribute__((ext_vector_type(4))) float f32x4;
typedef __attribute__((ext_vector_type(16))) float f32x16;
typedef __attribute__((ext_vector_type(2))) __bf16 bf16x2;
typedef __attribute__((ext_vector_type(8))) __bf16 bf16x8;
typedef __attribute__((ext_vector_type(2))) _Float16 f16x2;
typedef __attribute__((ext_vector_type(8))) _Float16 f16x8;
typedef LAS char* lptr;

#define PREF 1
#define ROT 0
#define FSLEEP 2
#define NSLEEP "3"
#define LDRIFT 0
#define PSLEEP 6
#define CSLEEP 1
#define OSLEEP 2
#define RSLEEP 3
#define HOLD 1
#define GROT ((w + li) & 3)
#define NTW 8
#define OUTAUX 17
#define TEARLY 1
#define HDELAY 0
#define CPF 16
#define LA3 0
#define TD1 0
#define TD2 0
#define TW2 3
#define HD 0
#define QH 4
#define WOA 0x00f
#define WOT 48
#define WOSW 0
constexpr int SEQ = 2048, DM = 576;

constexpr unsigned WS_CTL = 0;
constexpr unsigned WS_ARR = 256;
constexpr unsigned WS_BFL = 4096;
constexpr unsigned WS_HINT = 20480;
constexpr unsigned WS_GATE = 36864;
constexpr unsigned WS_PFL = 49152;
constexpr unsigned WS_PART = 65536;
constexpr unsigned WS_O = WS_PART + 8 * 16 * 3072;
constexpr unsigned WS_B = WS_O + SEQ * DM * 2;
constexpr unsigned WS_END = WS_B + SEQ * DM * 2;
constexpr unsigned EMPTY = 0xffffffffu;

constexpr size_t WS_STAMP = 16u << 20;
#define STAMP_DECL
#define STAMP(i)
#define CSTAMP(i)
#define STAMP_OUT(role, slot)

struct Params {
    const char* x; const char* wqkv; const char* wo; const char* logA; const char* aim; const char* cre; const char* cim;
    const char* logdt; const char* dskip; const char* gw; char* out; char* ws;
};

constexpr int LDS_BYTES = 163840, FL = 163584, LDS_TAG = FL + 64, LDS_DUMMY = FL + 128, LDS_JUNK = 161280;
enum { F_SW = 0, F_LANDED = 4, F_W1 = 20, F_B = 28, F_H = 24, F_KVP = 36, F_KVR = 40, F_Q0 = 44, F_QD = 48, F_D = 52, F_C1 = 56, F_W0 = 60 };
constexpr int WO8 = 132096, P2_ACC = 73728, P2_AP = 304, P2_B = 141312, P2_G = 150528;

DEVI float bf2f(unsigned h) { return __uint_as_float(h << 16); }
DEVI float bfhi(unsigned h) { return __uint_as_float(h & 0xffff0000u); }
DEVI unsigned pk_bf16(float a, float b) { return __builtin_bit_cast(unsigned, __builtin_convertvector(f32x2{a, b}, bf16x2)); }
DEVI unsigned pk_f16(f32x2 v) { return __builtin_bit_cast(unsigned, __builtin_convertvector(v, f16x2)); }
DEVI float rbf(float a) { return bf2f(pk_bf16(a, a)); }
DEVI f32x4 mfma16(u32x4 a, u32x4 b, f32x4 c) {
    return __builtin_amdgcn_mfma_f32_16x16x32_bf16(__builtin_bit_cast(bf16x8, a), __builtin_bit_cast(bf16x8, b), c, 0, 0, 0);
}
DEVI void mfma16i(f32x4& c, u32x4 a, u32x4 b) { asm volatile("v_mfma_f32_16x16x32_bf16 %0, %1, %2, %0" : "+v"(c) : "v"(a), "v"(b)); }
#define MFMA_DONE() do { __builtin_amdgcn_sched_barrier(0); asm volatile("s_nop 7\n\ts_nop 1" ::: "memory"); __builtin_amdgcn_sched_barrier(0); } while (0)
DEVI u32x4 lds_r128(lptr p) { return *(LAS u32x4*)p; }
DEVI u32x2 lds_r64(lptr p) { return *(LAS u32x2*)p; }
DEVI float lds_rf(lptr p) { return *(LAS float*)p; }
DEVI void lds_w128(lptr p, u32x4 v) { *(LAS u32x4*)p = v; }
DEVI void lds_w64(lptr p, u32x2 v) { *(LAS u32x2*)p = v; }
DEVI void lds_w32(lptr p, unsigned v) { *(LAS unsigned*)p = v; }
DEVI void lds_w16(lptr p, unsigned v) { *(LAS u16*)p = (u16)v; }
DEVI u32x2 lds_tr(lptr p) { return __builtin_bit_cast(u32x2, __builtin_amdgcn_ds_read_tr16_b64_v4i16((LAS s16x4*)p)); }
DEVI u32x4 lds_trfrag(lptr p, int off2) { u32x2 a = lds_tr(p), b = lds_tr(p + off2); return u32x4{a.x, a.y, b.x, b.y}; }
template <int OFF> DEVI void glds16o(const char* sbase, unsigned voff) {
    asm volatile("global_load_lds_dwordx4 %0, %1 offset:%2" :: "v"(voff), "s"(sbase), "i"(OFF) : "memory");
}
DEVI void glds16(const char* sbase, unsigned voff, unsigned l) {
    asm volatile("s_mov_b32 m0, %2\n\ts_nop 0\n\tglobal_load_lds_dwordx4 %0, %1" :: "v"(voff), "s"(sbase), "s"(l) : "memory");
}
#define SET_M0(v) asm volatile("s_mov_b32 m0, %0\n\ts_nop 0" :: "s"(v) : "memory")
#define W_ADDTID(v, off) asm volatile("ds_write_addtid_b32 %0 offset:%1" :: "v"(v), "i"(off) : "memory")
#define CFENCE() asm volatile("" ::: "memory")
#define SCHED() __builtin_amdgcn_sched_barrier(0)
#define WAIT_VM(n) asm volatile("s_waitcnt vmcnt(" #n ")" ::: "memory")
#define WAIT_LGKM(n) __builtin_amdgcn_s_waitcnt(0xC07F | ((n) << 8))
#define WAIT_LGKM0() asm volatile("s_waitcnt lgkmcnt(0)" ::: "memory")
#define BARRIER() do { SCHED(); asm volatile("s_waitcnt lgkmcnt(0)\n\ts_barrier" ::: "memory"); SCHED(); } while (0)
#define RAW_BARRIER() do { SCHED(); asm volatile("s_barrier" ::: "memory"); SCHED(); } while (0)
#define RLX_AGENT __ATOMIC_RELAXED, __HIP_MEMORY_SCOPE_AGENT
constexpr unsigned SPIN_CAP = 1u << 16;
typedef __amdgpu_buffer_rsrc_t rsrc_t;
DEVI u32x4 ld_sc1(rsrc_t r, unsigned off) { return __builtin_amdgcn_raw_buffer_load_b128(r, off, 0, 16); }
DEVI void st_sc1(rsrc_t r, unsigned off, u32x4 v) { __builtin_amdgcn_raw_buffer_store_b128(v, r, off, 0, 16); }

DEVI u32x4 pack8(f32x4 a, f32x4 b) { return u32x4{pk_bf16(a[0], a[1]), pk_bf16(a[2], a[3]), pk_bf16(b[0], b[1]), pk_bf16(b[2], b[3])}; }
DEVI float phi(float t) {
    const float e = __builtin_amdgcn_fmed3f(__builtin_amdgcn_exp2f(t * 1.44269504089f), 0.f, 1.f);
    return __int_as_float(max(__float_as_int(t), 0x358637bd)) + e;
}
DEVI f32x4 phi4(f32x4 v) {
    f32x2 a, b;
    const f32x2 c = {1.44269504089f, 1.44269504089f};
    asm("v_pk_mul_f32 %0, %2, %4\n\tv_pk_mul_f32 %1, %3, %4" : "=&v"(a), "=&v"(b) : "v"(f32x2{v[0], v[1]}), "v"(f32x2{v[2], v[3]}), "s"(c));
    f32x4 r;
#pragma unroll
    for (int i = 0; i < 4; ++i)
        r[i] = __int_as_float(max(__float_as_int(v[i]), 0x358637bd)) + __builtin_amdgcn_fmed3f(__builtin_amdgcn_exp2f(i < 2 ? a[i] : b[i - 2]), 0.f, 1.f);
    return r;
}
DEVI unsigned umax4(u32x4 a) { return max(max(a.x, a.y), max(a.z, a.w)); }
DEVI u32x4 not_empty(u32x4 a) { return a; }
DEVI void tag_post(const Params& P, u64 tag) { __hip_atomic_store((GAS u64*)(P.ws + WS_ARR) + blockIdx.x, tag, RLX_AGENT); }
#define PIN_TAG() do { asm volatile("" : "+s"(tag)); tag += 1ull; } while (0)
template <int SL = PSLEEP>
DEVI void poll_addr(const Params& P, unsigned off, u64 tag) {
    const GAS u64* f = (const GAS u64*)(P.ws + off);
#pragma unroll 1
    for (unsigned spins = 0; spins < SPIN_CAP; ++spins) {
        if (__all(__hip_atomic_load(f, RLX_AGENT) == tag)) break;
        __builtin_amdgcn_s_sleep(SL);
    }
}
template <int SL = PSLEEP>
DEVI void poll_flags(const Params& P, unsigned off, int n, u64 tag, int lane) { poll_addr<SL>(P, off + (lane < n ? lane : 0) * 128, tag); }
DEVI unsigned flag_get(lptr smem, int f) { return __builtin_amdgcn_readfirstlane(__hip_atomic_load((LAS unsigned*)(smem + FL + f), __ATOMIC_RELAXED, __HIP_MEMORY_SCOPE_WORKGROUP)); }
template <int SL = FSLEEP>
DEVI void flag_wait(lptr smem, int f, unsigned v) {
#pragma unroll 1
    for (unsigned spins = 0; spins < (SPIN_CAP << 4); ++spins) { if (flag_get(smem, f) >= v) break; if (SL) __builtin_amdgcn_s_sleep(SL); }
    CFENCE();
}
DEVI void flag_add(int f) { asm volatile("s_mov_b64 exec, 1\n\tds_add_u32 %0, %1\n\ts_mov_b64 exec, -1" :: "v"(FL + f), "v"(1u) : "memory"); }
#define FLAG_ADD(f) asm volatile("s_mov_b64 exec, 1\n\tds_add_u32 %0, %1 offset:%2\n\ts_mov_b64 exec, -1" :: "v"(flp), "v"(one), "i"(f) : "memory")
DEVI void flag_set(int f, unsigned v) { asm volatile("s_mov_b64 exec, 1\n\tds_write_b32 %0, %1\n\ts_mov_b64 exec, -1" :: "v"(FL + f), "v"(v) : "memory"); }
#define NEEDP(seen, ptr, f, v) do { unsigned c_; asm volatile( \
        "v_cmp_gt_u32 vcc, %4, %1\n\ts_cbranch_vccnz 0f\n1:\n\t.subsection 1\n" \
        "0:\n\ts_mov_b32 %0, 0x100000\n2:\n\ts_sleep " NSLEEP "\n\tds_read_b32 %1, %2 offset:%3\n\ts_sub_u32 %0, %0, 1\n\ts_waitcnt lgkmcnt(0)\n\ts_cbranch_scc1 1b\n\t" \
        "v_cmp_gt_u32 vcc, %4, %1\n\ts_cbranch_vccnz 2b\n\ts_branch 1b\n\t.subsection 0" \
        : "=&s"(c_), "+v"(seen) : "v"(ptr), "i"(f), "i"(v) : "memory", "scc", "vcc"); } while (0)
#define NEED(seen, f, v) NEEDP(seen, flp, f, v)
DEVI void touch(const char* sbase, unsigned voff) {
    asm volatile("s_mov_b32 m0, %2\n\ts_nop 0\n\tglobal_load_lds_dword %0, %1" :: "v"(voff), "s"(sbase), "s"(LDS_JUNK) : "memory");
}
DEVI const char* group_addr(const Params& P, int xcd, int n, int lane) {
    if (n < 126) {
        const int c0 = n / 18, r = n - c0 * 18, c = c0 + 2, l = r - 8;
        return r < 8 ? P.x + (unsigned)(((xcd >> 1) * 512 + r * 64 + lane) * 1152 + c * 128)
                     : P.wqkv + (unsigned)((c * 64 + lane) * 3456 + (l < 5 ? 1152 : 2304 - 640) + (xcd & 1) * 512 + l * 128);
    }
    const int c = (n - 126) / 5, l = n - 126 - c * 5;
    return P.wqkv + (unsigned)((c * 64 + lane) * 3456 + (xcd & 1) * 512 + l * 128);
}
DEVI void touch_group(const Params& P, int xcd, int n, int lane) {
    if (n < 126) {
        const int c0 = n / 18, r = n - c0 * 18, c = c0 + 2;
        if (r < 8) touch(P.x, (unsigned)(((xcd >> 1) * 512 + r * 64 + lane) * 1152 + c * 128));
        else { const int l = r - 8; touch(P.wqkv, (unsigned)((c * 64 + lane) * 3456 + (l < 5 ? 1152 : 2304 - 640) + (xcd & 1) * 512 + l * 128)); }
    } else if (n < 171) {
        const int c = (n - 126) / 5, l = n - 126 - c * 5;
        touch(P.wqkv, (unsigned)((c * 64 + lane) * 3456 + (xcd & 1) * 512 + l * 128));
    } else {
        const int l = (n - 171) / 9, c = n - 171 - l * 9;
        touch(P.wo, (unsigned)((c * 64 + lane) * 1152 + l * 128));
    }
}
template <unsigned CH>
DEVI void wo_issue(const Params& P, int cg, int first, int stride, int lane) {
#pragma unroll 1
    for (int m = first; m < 9; m += stride) {
        const int q = 64 * m + lane, rho = q / 9, pc = q - rho * 9;
        const int k = (rho & 32) + 8 * ((rho >> 3) & 3) + 4 * (rho & 1) + ((rho >> 1) & 3);
        const unsigned qo = (unsigned)(k * 1152 + cg * 144 + pc * 16);
#pragma unroll
        for (int c = 0; c < 9; ++c) if ((CH >> c) & 1) glds16(P.wo + c * (64 * 1152), qo, (unsigned)(c * 9216 + m * 1024));
    }
}

constexpr int SP = 3088;
constexpr int XT_OFF = 135936, XT_BYTES = 4608, XPAD = 256;
constexpr int TT_OFF = 107520, TC = 1184, PT_OFF = 82944, PC_OFF = 2560;

#define CM2(o1, p1, a1, b1, o2, p2, a2, b2) do { asm( \
    "v_pk_mul_f32 %0, %2, %3 op_sel:[1,1] op_sel_hi:[1,0] neg_lo:[0,1]\n\tv_pk_mul_f32 %1, %4, %5 op_sel:[1,1] op_sel_hi:[1,0] neg_lo:[0,1]\n\t" \
    "v_pk_fma_f32 %0, %2, %3, %0 op_sel_hi:[0,1,1]\n\tv_pk_fma_f32 %1, %4, %5, %1 op_sel_hi:[0,1,1]" \
    : "=&v"(o1), "=&v"(o2) : "v"(a1), "v"(b1), "v"(a2), "v"(b2)); \
    asm("v_cvt_pk_f16_f32 %0, %2, %3\n\tv_cvt_pk_f16_f32 %1, %4, %5" : "=&v"(p1), "=&v"(p2) : "v"((o1).x), "v"((o1).y), "v"((o2).x), "v"((o2).y)); } while (0)
#define HM2(p1, a1, p2, a2, b) asm( \
    "v_pk_mul_f16 %0, %2, %4 op_sel_hi:[0,1]\n\tv_pk_mul_f16 %1, %3, %4 op_sel_hi:[0,1]\n\t" \
    "v_pk_fma_f16 %0, %2, %4, %0 op_sel:[1,1,0] op_sel_hi:[1,0,1] neg_lo:[0,1,0]\n\tv_pk_fma_f16 %1, %3, %4, %1 op_sel:[1,1,0] op_sel_hi:[1,0,1] neg_lo:[0,1,0]" \
    : "=&v"(p1), "=&v"(p2) : "v"(a1), "v"(a2), "v"(b))
DEVI f32x2 cexp_p(float a2, float f, float p) {
    float m = __builtin_amdgcn_exp2f(a2 * p);
    float r = __builtin_amdgcn_fractf(f * p);
    return f32x2{m * __builtin_amdgcn_cosf(r), m * __builtin_amdgcn_sinf(r)};
}
DEVI u32x4 mask_frag(unsigned bits, int g, unsigned val) {
    unsigned by = bits >> (8 * g);
    u32x4 r;
#pragma unroll
    for (int i = 0; i < 4; ++i) { unsigned t = (by >> (2 * i)) & 3u; r[i] = ((t | (t << 15)) & 0x10001u) * val; }
    return r;
}
struct Tab2 { unsigned v[8][64][4]; };
constexpr Tab2 make_tab2() {
    Tab2 t{};
    for (int l = 0; l < 64; ++l) {
        const int r = l & 15, g = l >> 4, j = l & 31, h = l >> 5;
        const unsigned bits[6] = {7u << (r + 11), 31u << (r + 6), 7u << (r + 3), 127u << (r + 5), 511u << (r + 4), 3u << (r + 12)};
        for (int m = 0; m < 6; ++m)
            for (int i = 0; i < 4; ++i) {
                const unsigned q = (bits[m] >> (8 * g + 2 * i)) & 3u, val = m < 5 ? 0x3f80u : 0xffffu;
                t.v[m][l][i] = (q & 1 ? val : 0u) | (q & 2 ? val << 16 : 0u);
            }
        for (int c = 0; c < 4; ++c) {
            const int v2 = 79 - (32 + (j >> 1)) - c + 4, v3 = 127 - (64 + 32 * h + (j >> 1)) - c + 4;
            t.v[6][l][c] = PT_OFF + (j & 1) * 1280 + c * 320 + (v2 >> 2) * 16 + h * 8 + (v2 & 3) * 2 - 64;
            t.v[7][l][c] = PT_OFF + PC_OFF + c * 320 + (v3 >> 2) * 16 + (j & 1) * 8 + (v3 & 3) * 2 - 64;
        }
    }
    return t;
}
__device__ const Tab2 g_tab2 = make_tab2();
DEVI float wave_max(float v) {
    v = fmaxf(v, __int_as_float(__builtin_amdgcn_update_dpp(0, __float_as_int(v), 0xB1, 0xf, 0xf, true)));
    v = fmaxf(v, __int_as_float(__builtin_amdgcn_update_dpp(0, __float_as_int(v), 0x4E, 0xf, 0xf, true)));
    v = fmaxf(v, __int_as_float(__builtin_amdgcn_update_dpp(0, __float_as_int(v), 0x124, 0xf, 0xf, true)));
    v = fmaxf(v, __int_as_float(__builtin_amdgcn_update_dpp(0, __float_as_int(v), 0x128, 0xf, 0xf, true)));
    const int i = __float_as_int(v);
    const int a = max(__builtin_amdgcn_readlane(i, 0), __builtin_amdgcn_readlane(i, 16)), b = max(__builtin_amdgcn_readlane(i, 32), __builtin_amdgcn_readlane(i, 48));
    return __int_as_float(max(a, b));
}

DEVI void ssm_role(const Params& P, lptr smem, int xcd, int li, int gid, int cg2, u64& tag, rsrc_t wsr) {
    const int gl = gid / 11, gi = gid - gl * 11, ch0 = gl * 64 + (gi < 10 ? gi * 6 : 58);
    const int tid = threadIdx.x, lane = tid & 63, w = __builtin_amdgcn_readfirstlane(tid >> 6);
    const int ch = w >> 1, half = (w ^ (w >> 2)) & 1, d = ch0 + ch;
    const bool own = gi < 10 || ch >= 2;
    const int lo16 = lane & 15, g = lane >> 4;
    STAMP_DECL; STAMP(1);
    __builtin_amdgcn_s_setprio(3);
    if (half) {
        const char* tp = (const char*)&g_tab2;
        SET_M0(TT_OFF + ch * 8192);
        glds16o<0>(tp, (unsigned)(lane * 16)); glds16o<1024>(tp, (unsigned)(lane * 16)); glds16o<2048>(tp, (unsigned)(lane * 16)); glds16o<3072>(tp, (unsigned)(lane * 16));
        SET_M0(TT_OFF + ch * 8192 + 4096);
        tp += 4096;
        glds16o<0>(tp, (unsigned)(lane * 16)); glds16o<1024>(tp, (unsigned)(lane * 16)); glds16o<2048>(tp, (unsigned)(lane * 16)); glds16o<3072>(tp, (unsigned)(lane * 16));
    }
    const unsigned po = (unsigned)(d * 256 + lane * 4);
    unsigned r_la, r_ai, r_cr, r_ci, r_dt, r_ds, junk = 0, junk2 = 0;
    asm volatile("global_load_dword %0, %6, %8\n\tglobal_load_dword %1, %6, %9\n\tglobal_load_dword %2, %6, %10\n\tglobal_load_dword %3, %6, %11\n\t"
                 "global_load_ushort %4, %7, %12\n\tglobal_load_ushort %5, %7, %13"
                 : "=&v"(r_cr), "=&v"(r_ci), "=&v"(r_la), "=&v"(r_ai), "=&v"(r_dt), "=&v"(r_ds)
                 : "v"(po), "v"(d * 2), "s"(P.cre), "s"(P.cim), "s"(P.logA), "s"(P.aim), "s"(P.logdt), "s"(P.dskip) : "memory");
#define TOUCH(dst, addr) asm volatile("global_load_dword %0, %1, off" : "+v"(dst) : "v"(addr) : "memory")
    const bool tch = w < NTW && li < 12;
    const char* tga = group_addr(P, xcd, tch ? w * 12 + li : 0, lane);
    RAW_BARRIER();
    if (tch && w < TW2) TOUCH(junk, tga);
    SCHED();
    const int tt = (tid + (li & 7) * 64) & 511;
    u32x3 xr[4] = {};
    const unsigned xoff = (unsigned)(tt * 4608 + ch0 * 2);
    const unsigned boff = own ? WS_B + (unsigned)(d * 4096 + half * 2048 + lane * 16) : WS_END;
#define GATHER(i) do { SCHED(); if (w < 8) asm volatile("global_load_dwordx3 %0, %1, %2 offset:%3" : "+v"(xr[i]) : "v"(xoff), "s"(P.x), "i"((i) * 1152) : "memory"); SCHED(); } while (0)
#define PRIO(i) do { SCHED(); __builtin_amdgcn_s_setprio(i); SCHED(); } while (0)
    SCHED();
    lptr xt = smem + XT_OFF + ch * XT_BYTES, tb = smem + TT_OFF + ch * 4 * TC, pt = smem + PT_OFF + ch * 4096;
    const int j32 = lane & 31, h = lane >> 5;
    if (!half) { lds_w32(smem + FL + 32, 0u); lds_w32(smem + FL + 36 + ch * 4, 0u); }
    int arow;
    {
        const u64 map0 = 0x0a09080803020100ull, map1 = 0x0c0b0a0a07060504ull, map2 = 0x1413110e12100f0dull;
        const u64 mm = j32 < 8 ? map0 : (j32 < 16 ? map1 : map2);
        arow = (int)((mm >> (8 * (j32 & 7))) & 0xff);
    }
    lptr ap = smem + arow * SP + w * 256 + h * 16, bp = smem + j32 * SP + w * 256 + h * 16;
    const int cpy = (3 - lo16) & 3;
    lptr xf7 = xt + (XPAD - 16 + 16 * lo16 + 8 * g) * 2 - 64 * 7;
    lptr tf = tb + cpy * TC + 2 * (527 - lo16 + 8 * g - cpy) - 64 * 16;
    lptr xc = smem + (w ^ 1) * 256 + lane * 4, xb = smem + XT_OFF + (XPAD + tt * 4) * 2;
    asm volatile("" : "+v"(ap), "+v"(bp), "+v"(xf7), "+v"(tf), "+v"(xc), "+v"(xb));
    if (tch && w < TW2) WAIT_VM(1); else WAIT_VM(0);
    asm volatile("" : "+v"(r_la), "+v"(r_ai), "+v"(r_cr), "+v"(r_ci), "+v"(r_dt), "+v"(r_ds));
    if (tch && w >= TW2) TOUCH(junk, tga);
    const float dt = rbf(__expf(bf2f(r_dt)));
    const float cm = wave_max(fmaxf(fmaxf(fabsf(bf2f(r_cr)), fabsf(bfhi(r_cr))), fmaxf(fabsf(bf2f(r_ci)), fabsf(bfhi(r_ci)))));
    unsigned ce = __float_as_uint(cm) >> 23; ce = ce < 16u ? 16u : (ce > 240u ? 240u : ce);
    const float cs = __uint_as_float((263u - ce) << 23), ics = __uint_as_float((ce - 9u) << 23);
    f32x16 kacc;
#pragma unroll
    for (int i = 0; i < 16; ++i) kacc[i] = 0.f;
    STAMP(2);
    {
        const unsigned sh = half * 16;
        const float la = bf2f(r_la >> sh), ai = bf2f(r_ai >> sh), cr = bf2f(r_cr >> sh) * cs, ci = bf2f(r_ci >> sh) * cs;
        const float a2 = (-rbf(__expf(la)) * dt) * 1.44269504089f, f = (ai * dt) * 0.15915494309f;
#define PUT(row, v) W_ADDTID(v, ((row) & 15) * SP)
        const f32x2 w1 = cexp_p(a2, -f, 1.f), W = cexp_p(a2, -f, 256.f);
        f32x2 u[8], v[4], c[9], e4, e8, t2;
        unsigned U[8], V[4], p, q;
        u[1] = cexp_p(a2, f, 8.f); v[1] = cexp_p(a2, f, 64.f);
        c[0] = f32x2{cr, -ci};
        U[1] = pk_f16(u[1]); V[1] = pk_f16(v[1]);
        GATHER(0);
        SET_M0(w * 256);
        PUT(0, pk_f16(c[0]));
        CM2(c[1], p, c[0], w1, u[2], U[2], u[1], u[1]); PUT(1, p);
        CM2(c[2], p, c[1], w1, u[3], U[3], u[2], u[1]); PUT(2, p);
        CM2(c[3], p, c[2], w1, u[4], U[4], u[2], u[2]); PUT(3, p);
        CM2(c[4], p, c[3], w1, u[5], U[5], u[4], u[1]); PUT(4, p);
        CM2(c[5], p, c[4], w1, u[6], U[6], u[4], u[2]); PUT(5, p);
        CM2(c[6], p, c[5], w1, u[7], U[7], u[4], u[3]); PUT(6, p);
        CM2(c[7], p, c[6], w1, v[2], V[2], v[1], v[1]); PUT(7, p);
        CM2(c[8], p, c[7], w1, v[3], V[3], v[2], v[1]);
        PRIO(2);
        GATHER(1);
        CM2(e8, p, c[8], W, e4, q, c[4], W); PUT(12, p); PUT(10, q);
        CM2(c[8], p, e8, W, c[4], q, e4, W); PUT(14, p); PUT(13, q);
        CM2(t2, p, c[0], W, e4, q, c[4], W); PUT(8, p); PUT(15, q);
        CM2(t2, p, c[2], W, e4, q, c[6], W); PUT(9, p); PUT(11, q);
        SET_M0(w * 256 + 16 * SP);
        CM2(e8, p, c[8], W, t2, q, W, W); PUT(17, p);
        CM2(e4, p, c[8], t2, c[0], q, e8, t2); PUT(16, p); PUT(18, q);
        CM2(c[8], p, e4, t2, e8, q, c[0], t2); PUT(19, p); PUT(20, q);
        PRIO(1);
        GATHER(2);
        u32x4 af[8];
#pragma unroll
        for (int s = 0; s < 8; ++s) af[s] = lds_r128(ap + 32 * s);
        CFENCE();
        PUT(16, V[2]); PUT(24, V[3]);
#pragma unroll
        for (int b = 1; b < 8; ++b) { HM2(p, V[2], q, V[3], U[b]); PUT(16 + b, p); PUT(24 + b, q); }
        SET_M0(w * 256);
        PRIO(0);
        GATHER(3);
        PUT(0, 0x3c00u); PUT(8, V[1]);
#pragma unroll
        for (int b = 1; b < 8; b += 2) { HM2(p, U[b], q, U[b == 7 ? 7 : b + 1], V[1]); PUT(8 + b, p); if (b < 7) PUT(9 + b, q); PUT(b, U[b]); if (b < 7) PUT(b + 1, U[b + 1]); }
#undef PUT
        u32x4 bfr[8];
#pragma unroll
        for (int s = 0; s < 8; ++s) bfr[s] = lds_r128(bp + 32 * s);
        SCHED();
        SCHED();
#pragma unroll
        for (int s = 0; s < 8; ++s)
            kacc = __builtin_amdgcn_mfma_f32_32x32x16_f16(__builtin_bit_cast(f16x8, af[s]), __builtin_bit_cast(f16x8, bfr[s]), kacc, 0, 0, 0);
    }
    STAMP(3);
    const float ks = 2.f * dt * ics;
#pragma unroll
    for (int i = 0; i < 12; ++i) kacc[i] *= ks;
    if (half) {
#pragma unroll
        for (int i = 0; i < 8; ++i) W_ADDTID(kacc[i], i * SP);
    } else {
#pragma unroll
        for (int i = 0; i < 4; ++i) W_ADDTID(kacc[8 + i], i * SP);
    }
    if (w >= 8 && li < 12) {
        const int n = (11 - w) * 12 + li, l = n / 9, c = n - l * 9;
        TOUCH(junk2, P.wo + (unsigned)((c * 64 + lane) * 1152 + (8 - l) * 128));
    }
    u32x4 ct[8];
#pragma unroll
    for (int i = 0; i < 8; ++i) ct[i] = lds_r128(smem + TT_OFF + ch * 8192 + i * 1024 + lane * 16);
    PIN_TAG();
    STAMP(4);
    BARRIER();
    STAMP(5);
    if (tid == 704) tag_post(P, tag);
    auto cleared = [&]() {
        if (w < 4) {
            WAIT_VM(0);
            if (lane == 0 && __hip_atomic_fetch_add((LAS unsigned*)(smem + FL + 32), 1u, __ATOMIC_RELAXED, __HIP_MEMORY_SCOPE_WORKGROUP) == 3u)
                __hip_atomic_store((GAS u64*)(P.ws + WS_BFL + gid * 128), tag, RLX_AGENT);
        }
    };
    auto x_in = [&]() {
        if (w < 8) {
            WAIT_VM(0); SCHED();
            asm volatile("" : "+v"(xr[0]), "+v"(xr[1]), "+v"(xr[2]), "+v"(xr[3]), "+v"(junk) :: "memory");
#pragma unroll
            for (int k = 0; k < 3; ++k) {
                u32x2 lo = {__builtin_amdgcn_perm(xr[1][k], xr[0][k], 0x05040100u), __builtin_amdgcn_perm(xr[3][k], xr[2][k], 0x05040100u)};
                u32x2 hi = {__builtin_amdgcn_perm(xr[1][k], xr[0][k], 0x07060302u), __builtin_amdgcn_perm(xr[3][k], xr[2][k], 0x07060302u)};
                lds_w64(xb + (2 * k) * XT_BYTES, lo); lds_w64(xb + (2 * k + 1) * XT_BYTES, hi);
            }
        }
        if (w < 4) {
#pragma unroll
            for (int i = 0; i < 6; ++i)
                if (i >= 2 || gi < 10) __builtin_amdgcn_raw_buffer_store_b128(u32x4{EMPTY, EMPTY, EMPTY, EMPTY}, wsr, (unsigned)(tid * 16), WS_B + (ch0 + i) * 4096, 16);
        }
        BARRIER();
        if (w >= 8) wo_issue<WOA>(P, cg2, w - 8, 4, lane);
    };
    auto TF = [&](int off) { u32x2 a = lds_r64(tf + off), b = lds_r64(tf + off + 8); return u32x4{a.x, a.y, b.x, b.y}; };
    f32x4 acc[8];
#pragma unroll
    for (int i = 0; i < 8; ++i) acc[i] = f32x4{0.f, 0.f, 0.f, 0.f};
    lptr axr;
    if (!half) {
        __builtin_amdgcn_s_setprio(2);
        lds_w64(xt + lane * 8, u32x2{0u, 0u});
#pragma unroll
        for (int i = 0; i < 8; ++i) kacc[i] += lds_rf(xc + i * SP);
        unsigned dd[6];
        dd[0] = pk_bf16(kacc[7], kacc[6]); dd[1] = pk_bf16(kacc[5], kacc[4]); dd[2] = pk_bf16(kacc[3], kacc[2]);
        dd[3] = pk_bf16(kacc[1], kacc[0] + (lane == 0 ? bf2f(r_ds) : 0.f));
        dd[4] = __builtin_amdgcn_update_dpp(0, dd[0], 0x138, 0xf, 0xf, true);
        dd[5] = __builtin_amdgcn_update_dpp(0, dd[1], 0x138, 0xf, 0xf, true);
        unsigned mm[5];
#pragma unroll
        for (int i = 0; i < 5; ++i) mm[i] = __builtin_amdgcn_alignbit(dd[i + 1], dd[i], 16);
        lptr tw = tb + 2 * (536 - 8 * lane);
        lds_w128(tw, u32x4{dd[0], dd[1], dd[2], dd[3]}); lds_w128(tw + TC, u32x4{mm[0], mm[1], mm[2], mm[3]});
        lds_w128(tw + 2 * TC, u32x4{dd[1], dd[2], dd[3], dd[4]}); lds_w128(tw + 3 * TC, u32x4{mm[1], mm[2], mm[3], mm[4]});
        {
            const unsigned n0 = __builtin_amdgcn_readlane(dd[0], 63), n1 = __builtin_amdgcn_readlane(dd[1], 63);
            const unsigned kk = (n0 & 0xffffu) * 0x10001u, kh = n0 << 16, m4 = (n0 >> 16) | (n1 << 16);
            const int c = lane & 3, bl = lane >> 2;
            u32x4 e;
            e.x = 0u; e.y = lane == 14 ? kh : (lane == 15 ? kk : 0u); e.z = lane == 12 ? kh : (bl == 3 ? kk : 0u);
            e.w = lane == 14 ? n0 : (lane == 15 ? m4 : (bl == 3 ? kk : 0u));
            if (bl < 6) lds_w128(tb + c * TC + bl * 16 + (bl >> 2) * 1024, e);
        }
        x_in();
        u32x4 X[8];
#pragma unroll
        for (int i = 0; i < 8; ++i) X[i] = lds_r128(xf7 + 64 * 6 + 512 * i);
        u32x4 k0 = TF(1024 - 64), k8 = TF(512 - 64);
#pragma unroll
        for (int i = 0; i < 8; ++i) asm volatile("" : "+v"(acc[i]));
        __builtin_amdgcn_s_setprio(0);
        STAMP(6);
#pragma unroll
        for (int c = 1; c < 8; ++c) {
            u32x4 k0n, k8n;
            if (c < 7) { k0n = TF(1024 - 64 * (c + 1)); k8n = TF(512 - 64 * (c + 1)); }
#pragma unroll
            for (int i = 0; i < 8; ++i) {
                SCHED();
                mfma16i(acc[i], k0, X[i]);
                if (i < 7) mfma16i(acc[i + 1], k8, X[i]);
                SCHED();
                if (c < 7) X[i] = lds_r128(xf7 + 64 * (6 - c) + 512 * i);
            }
            k0 = k0n; k8 = k8n;
            if (c == 5) { SCHED(); cleared(); SCHED(); }
        }
        MFMA_DONE();
        if (w < 4) wo_issue<0x1ff & ~WOA>(P, cg2, w >> 1, 2, lane);
        flag_wait(smem, 36 + ch * 4, 1);
#pragma unroll
        for (int i = 0; i < 4; ++i) lds_w128(xt + i * 1024 + lane * 16, __builtin_bit_cast(u32x4, acc[4 + i]));
        axr = pt + lane * 16;
    } else {
        u32x4 X[8];
#pragma unroll
        for (int i = 0; i < 4; ++i) if (i < 3 || lane < 48) lds_w128(pt + i * 1024 + lane * 16, u32x4{0u, 0u, 0u, 0u});
        CFENCE();
        const unsigned p89 = pk_bf16(kacc[8] + lds_rf(xc), kacc[9] + lds_rf(xc + SP)), pab = pk_bf16(kacc[10] + lds_rf(xc + 2 * SP), kacc[11] + lds_rf(xc + 3 * SP));
#pragma unroll
        for (int c = 0; c < 4; ++c) {
            lptr q2 = smem + (ct[6][c] + ch * 4096), q3 = smem + (ct[7][c] + ch * 4096);
            lds_w16(q2 + 64, p89); lds_w16(q2, p89 >> 16);
            lds_w16(q3 + 64, pab); lds_w16(q3, pab >> 16);
        }
        CFENCE();
        const u32x4 FX = ct[5] & (((unsigned)__builtin_amdgcn_readlane(p89, 63) >> 16) * 0x10001u);
        lptr pf2 = pt + cpy * 320 + ((79 - 64 - lo16 + 4 * g - cpy + 4) >> 2) * 16;
        x_in();
#pragma unroll
        for (int i = 0; i < 8; ++i) X[i] = lds_r128(xf7 + 64 * 7 + 512 * i);
        u32x4 TA[3], TB[3], TCc[4];
#pragma unroll
        for (int i = 0; i < 3; ++i) { TA[i] = lds_r128(pf2 + 64 * (2 - i)); TB[i] = lds_r128(pf2 + 1280 + 64 * (2 - i)); }
#pragma unroll
        for (int i = 0; i < 4; ++i) TCc[i] = lds_r128(pf2 + PC_OFF + 64 * (3 - i));
        STAMP(6);
        const f32x4 z4 = {0.f, 0.f, 0.f, 0.f};
#pragma unroll
        for (int st = 0; st < 6; ++st) {
            const u32x4 Xc = lds_r128(xf7 + 64 * 7 + 512 * st - 16);
            const u32x4 pa = pack8(mfma16(X[st], ct[0], z4), mfma16(X[st], ct[1], z4));
            const u32x4 pb = pack8(mfma16(X[st], ct[2], z4), mfma16(Xc, ct[1], z4));
#pragma unroll
            for (int dl = 2; dl <= 4; ++dl) if (st + dl < 8) {
                acc[st + dl] = mfma16(pa, TA[dl - 2], acc[st + dl]);
                acc[st + dl] = mfma16(pb, TB[dl - 2], acc[st + dl]);
            }
            if (st < 4) {
                const u32x4 pc = pack8(mfma16(X[st], ct[3], z4), mfma16(Xc, ct[4], z4));
#pragma unroll
                for (int dl = 4; dl <= 7; ++dl) if (st + dl < 8) acc[st + dl] = mfma16(pc, TCc[dl - 4], acc[st + dl]);
            }
        }
        SCHED(); lds_w32(smem + FL + 36 + ch * 4, 1u); cleared(); SCHED();
#pragma unroll
        for (int i = 4; i < 8; ++i) acc[i] = mfma16(FX, X[i - 4], acc[i]);
        const u32x4 k0 = TF(1024), k8 = TF(512), k16 = TF(0);
#pragma unroll
        for (int i = 0; i < 8; ++i) acc[i] = mfma16(k0, X[i], acc[i]);
#pragma unroll
        for (int i = 1; i < 8; ++i) acc[i] = mfma16(k8, X[i - 1], acc[i]);
#pragma unroll
        for (int i = 2; i < 8; ++i) acc[i] = mfma16(k16, X[i - 2], acc[i]);
        MFMA_DONE();
#pragma unroll
        for (int i = 0; i < 4; ++i) { lds_w128(pt + i * 1024 + lane * 16, __builtin_bit_cast(u32x4, acc[i])); acc[i] = acc[4 + i]; }
        axr = xt + lane * 16;
    }
    STAMP(7);
    if (w == 0 && lane < 8) lds_w32(smem + FL + lane * 4, 0u);
    WAIT_VM(0); SCHED();
    asm volatile("" :: "v"(junk2));
    BARRIER();
    STAMP(8);
#pragma unroll
    for (int i = 0; i < 4; ++i) acc[i] += __builtin_bit_cast(f32x4, lds_r128(axr + i * 1024));
#pragma unroll
    for (int i = 0; i < 2; ++i) {
        const f32x4 ya = acc[2 * i], yb = acc[2 * i + 1];
        const auto sx = __builtin_amdgcn_permlane16_swap(pk_bf16(ya[0], ya[1]), pk_bf16(yb[0], yb[1]), false, false);
        const auto sy = __builtin_amdgcn_permlane16_swap(pk_bf16(ya[2], ya[3]), pk_bf16(yb[2], yb[3]), false, false);
        st_sc1(wsr, boff + i * 1024, not_empty(u32x4{sx[0], sy[0], sx[1], sy[1]}));
    }
    BARRIER();
    STAMP(9);
    if (w < 2) STAMP_OUT(2 + 2 * w, blockIdx.x + 256 * w);
}

constexpr int SLOT = 34816, XCH = 16384, KVT_OFF = 151296, KVP_OFF = 27648;
DEVI constexpr int slot_off(int s) { return (s & 3) == 0 ? SLOT : ((s & 3) == 1 ? 0 : (s & 3) * SLOT); }
DEVI constexpr int wq_off(int t) { return t == 0 ? 0 : 49152 + t * 27648; }

constexpr int quad_src(int quad) {
    const int pp = quad >= 192 ? 1 : 0, qi = quad - 192 * pp;
    const bool isz = qi >= 180;
    const int el = isz ? 0 : qi / 5, qd = qi - el * 5, hi = el >= 18 ? 1 : 0;
    const int dql = isz ? (qi < 189 ? qi - 180 : 0) : qd + 4 * hi;
    const int et = isz ? 0 : (el < 4 ? 0 : (el < 20 ? 1 : 2)), col = isz ? 0 : (el < 4 ? 12 + el : (el < 20 ? el - 4 : el - 20));
    const int tile = dql == 8 ? 5 : (et == 0 ? (dql >> 2) : (et == 1 ? 2 + (dql >> 2) : 4));
    return KVP_OFF + pp * 6144 + tile * 1024 + ((dql == 8 ? et : (dql & 3)) * 16 + col) * 4;
}
constexpr int quad_dst(int quad, int hf) {
    const int pp = quad >= 192 ? 1 : 0, qi = quad - 192 * pp;
    const bool isz = qi >= 180;
    const int el = isz ? 0 : qi / 5, qd = qi - el * 5, hi = el >= 18 ? 1 : 0;
    const int dql = isz ? (qi < 189 ? qi - 180 : 0) : qd + 4 * hi;
    const int dq = 9 * pp + dql, ks = dq >> 3, e = 36 * pp + el, ee = e >> 4;
    const int fr = ee == 0 ? 0 : (ee < 3 ? 2 * ee - 1 + ks : 2 * ee - 2 + ks);
    const int hz = 2 * pp + ((dql > 4 || (dql == 4 && hf)) ? 1 : 0);
    const bool dead = isz ? qi >= 189 : ((!hi && qd == 4 && hf) || (hi && qd == 0 && !hf));
    return dead ? LDS_DUMMY : KVT_OFF + (isz ? (9 + ks) * 1024 + (dq & 3) * 256 + hz * 16 : fr * 1024 + ((dq & 3) * 16 + (e & 15)) * 16) + ((dq >> 2) & 1) * 8 + hf * 4;
}
struct Tab { int v[2048]; };
constexpr Tab make_tab() {
    Tab t{};
    for (int c = 0; c < 192; ++c) {
        const int l = c & 63, q = 2 * ((c & 192) + (l >> 5) * 16 + (l & 15)) + ((l >> 4) & 1);
        t.v[4 * c] = quad_src(2 * c); t.v[4 * c + 1] = quad_src(2 * c + 1);
        t.v[4 * c + 2] = (quad_dst(q, 0) - KVT_OFF) | ((quad_dst(q, 1) - KVT_OFF) << 16); t.v[4 * c + 3] = (quad_dst(q + 64, 0) - KVT_OFF) | ((quad_dst(q + 64, 1) - KVT_OFF) << 16);
    }
    for (int q = 0; q < 576; ++q) {
        const int rho = q / 9, pc = q % 9, k = (rho & 32) + 8 * ((rho >> 3) & 3) + 4 * (rho & 1) + ((rho >> 1) & 3), m = q >> 6;
        t.v[768 + q] = k * 3456 + pc * 16 - (m & 1) * 1024;
        t.v[1344 + q] = k * 1152 + pc * 16 - (m & 3) * 1024;
    }
    return t;
}
__device__ const Tab g_tab = make_tab();
constexpr int IDX = P2_B, GW_OFF = 139264;

#define GET_TAG() do { const u32x2 t_ = lds_r64(smem + LDS_TAG); tag = (u64)(unsigned)__builtin_amdgcn_readfirstlane(t_.x) | ((u64)(unsigned)__builtin_amdgcn_readfirstlane(t_.y) << 32); } while (0)
DEVI void gemm_kv(const Params& P, lptr smem, int rb, int hg, int kw, int cg2, u64& tag, rsrc_t wsr) {
    const int lane = threadIdx.x & 63, lo16 = lane & 15, g = lane >> 4;
    const int p = kw & 1, ct = (kw < 3 ? kw : 0) * 64 + lane;
    STAMP_DECL; STAMP(1);
    __builtin_amdgcn_s_setprio(2);
    const int xrow = ((kw >> 1) * 64 + lo16) * 128, sw = (g ^ ((lo16 >> 1) & 7)) << 4;
    lptr xa[2] = {smem + xrow + sw, smem + xrow + (sw ^ 64)};
    const int wrow = (4 * g + (lo16 >> 2)) * 288;
    lptr wa = smem + XCH + wrow + (9 * p + (lane & 3)) * 8, wa2 = smem + XCH + wrow + (9 * p + ((lane & 3) == 3 ? 18 : 8)) * 8;
    lptr flp = smem + FL, ixp = smem + IDX + ct * 16, irp = smem + IDX + 3072 + 2304 + lane * 4;
    unsigned one = 1u;
    u32x4 sel[2];
    sel[0] = mask_frag(lo16 < 8 ? 1u << lo16 : 0u, 0, 0x3f80u); sel[1] = mask_frag(lo16 >= 8 ? 1u << (lo16 - 8) : 0u, 0, 0x3f80u);
    unsigned sto = WS_PART + (unsigned)((hg * 16 + rb) * 3072 + ct * 16);
    unsigned off = WS_PART + (unsigned)((hg * 16 + g) * 3072 + kw * 1024 + lo16 * 16);
    asm volatile("" : "+v"(flp), "+v"(one), "+v"(ixp), "+v"(irp), "+v"(sel[0]), "+v"(sel[1]), "+v"(sto), "+v"(off));
    const f32x4 z4 = {0.f, 0.f, 0.f, 0.f};
    f32x4 acc[4][5];
#pragma unroll
    for (int i = 0; i < 4; ++i)
#pragma unroll
        for (int j = 0; j < 5; ++j) { const float e = j < 2 ? 1e-6f : (j == 2 && lo16 < 12 ? 1e-6f : 0.f); acc[i][j] = f32x4{e, e, e, e}; asm volatile("" : "+v"(acc[i][j])); }
    auto rel = [&](int s) { return (s & 2) ? slot_off(s) - 2 * SLOT : slot_off(s); };
    auto rebase = [&](int dlt) {
        xa[0] += dlt; xa[1] += dlt; wa += dlt; wa2 += dlt;
        asm volatile("" : "+v"(xa[0]), "+v"(xa[1]), "+v"(wa), "+v"(wa2));
    };
    constexpr int NB = 6;
    u32x2 wl[NB], wh[NB];
    u32x4 xf[2][4];
    auto rdw = [&](int f, int t) {
        const int n = f / 5, j = f % 5;
        const int o = rel(n >> 1) + (n & 1) * 9216 + t * 4608;
        const u32x2 v = j == 2 ? lds_tr(wa2 + o) : lds_tr(wa + o + (j == 0 ? 0 : (j == 1 ? 32 : (j == 3 ? 152 : 184))));
        if (t) wh[f % NB] = v; else wl[f % NB] = v;
    };
    auto rdx = [&](int n, int r) { xf[n & 1][r] = lds_r128(xa[n & 1] + rel(n >> 1) + r * 2048); };
    rebase(0);
    BARRIER();
    STAMP(2);
    rdw(0, 0); rdw(0, 1);
#pragma unroll
    for (int r = 0; r < 4; ++r) rdx(0, r);
#pragma unroll
    for (int f = 1; f < NB - 1; ++f) { rdw(f, 0); rdw(f, 1); }
    auto kstep = [&](const int n) __attribute__((always_inline)) {
        const int s = n >> 1;
#pragma unroll
        for (int m = 0; m < 20; ++m) {
            const int j = m >> 2, r = m & 3, f = 5 * n + j, fn = f + NB - 1;
            SCHED();
            const u32x4 wv = {wl[f % NB].x, wl[f % NB].y, wh[f % NB].x, wh[f % NB].y};
            mfma16i(acc[r][j], xf[n & 1][r], wv);
            SCHED();
            if (r < 2 && fn < 90) rdw(fn, r);
            if (r == 2 && j < 4 && n < 17) rdx(n + 1, j);
            if (m == 15 && n < 17) WAIT_LGKM(12);
            if (m == 18 && !(n & 1)) {
                if (s < 8 && (s & 1)) rebase((s & 2) ? -2 * SLOT : 2 * SLOT);
                if (s == 8) WAIT_LGKM0();
                RAW_BARRIER();
                if (s == 0) STAMP(3);
                if (s == 4) STAMP(4);
            }
            if (m == 19 && n < 17) WAIT_LGKM(2);
        }
    };
    kstep(0); kstep(1); kstep(2); kstep(3); kstep(4); kstep(5); kstep(6); kstep(7); kstep(8);
    kstep(9); kstep(10); kstep(11); kstep(12); kstep(13); kstep(14); kstep(15); kstep(16); kstep(17);
    __builtin_amdgcn_s_setprio(3);
    const u32x4 ix = lds_r128(ixp);
    unsigned oo[9];
#pragma unroll
    for (int i = 0; i < 9; ++i) oo[i] = *(LAS unsigned*)(irp + i * 256);
    MFMA_DONE();
    STAMP(5);
    {
        f32x4 r[6], t0 = z4, t1 = z4, t2 = z4;
#pragma unroll
        for (int i = 0; i < 5; ++i) r[i] = z4;
#pragma unroll
        for (int u = 0; u < 2; ++u) {
            u32x4 pk[3], vb[3];
#pragma unroll
            for (int j = 0; j < 3; ++j) pk[j] = pack8(phi4(acc[2 * u][j]), phi4(acc[2 * u + 1][j]));
            vb[0] = pack8(acc[2 * u][2], acc[2 * u + 1][2]);
            if (lo16 < 12) vb[0] = u32x4{0x3f803f80u, 0x3f803f80u, 0x3f803f80u, 0x3f803f80u};
            vb[1] = pack8(acc[2 * u][3], acc[2 * u + 1][3]); vb[2] = pack8(acc[2 * u][4], acc[2 * u + 1][4]);
            t0 = mfma16(pk[2], vb[0], t0); t1 = mfma16(pk[2], vb[1], t1); t2 = mfma16(pk[2], vb[2], t2);
            r[0] = mfma16(pk[0], vb[0], r[0]); r[1] = mfma16(pk[1], vb[0], r[1]); r[2] = mfma16(pk[0], vb[1], r[2]);
            r[3] = mfma16(pk[1], vb[1], r[3]); r[4] = mfma16(pk[1], vb[2], r[4]);
        }
        r[5] = g == 0 ? t0 : (g == 1 ? t1 : t2);
        MFMA_DONE();
        SET_M0(KVP_OFF + kw * 6144);
#pragma unroll
        for (int t = 5; t >= 0; --t) {
            W_ADDTID(r[t][0], t * 1024); W_ADDTID(r[t][1], t * 1024 + 256);
            W_ADDTID(r[t][2], t * 1024 + 512); W_ADDTID(r[t][3], t * 1024 + 768);
        }
        FLAG_ADD(F_KVP);
    }
    STAMP(6);
    if (kw < 3) {
        lptr dst[4] = {smem + KVT_OFF + (ix.z & 0xffffu), smem + KVT_OFF + (ix.z >> 16), smem + KVT_OFF + (ix.w & 0xffffu), smem + KVT_OFF + (ix.w >> 16)};
        asm volatile("" : "+v"(dst[0]), "+v"(dst[1]), "+v"(dst[2]), "+v"(dst[3]));
        flag_wait<0>(smem, F_KVP, 4);
        STAMP(7);
        float pa[2][2][4];
#pragma unroll
        for (int i = 0; i < 2; ++i)
#pragma unroll
            for (int r = 0; r < 2; ++r)
#pragma unroll
                for (int k = 0; k < 4; ++k) pa[i][r][k] = lds_rf(smem + ix[i] + r * 12288 + k * 256);
        SCHED();
        u32x4 pv;
#pragma unroll
        for (int i = 0; i < 2; ++i)
#pragma unroll
            for (int k = 0; k < 2; ++k)
                pv[2 * i + k] = pk_bf16(pa[i][0][2 * k] + pa[i][1][2 * k], pa[i][0][2 * k + 1] + pa[i][1][2 * k + 1]);
        st_sc1(wsr, sto, not_empty(pv));
        SCHED();
        FLAG_ADD(F_KVR);
        STAMP(8);
        WAIT_VM(0);
        STAMP(9);
        f32x4 gs[4][2];
        auto group = [&](int i, const u32x4 (&a)[4]) {
            gs[i][0] = mfma16(sel[1], a[1], mfma16(sel[0], a[0], z4));
            gs[i][1] = mfma16(sel[1], a[3], mfma16(sel[0], a[2], z4));
        };
        {
            u32x4 a[4][4];
#pragma unroll
            for (int i = 0; i < 4; ++i)
#pragma unroll
                for (int t = 0; t < 4; ++t) a[i][t] = __builtin_amdgcn_raw_buffer_load_b128(wsr, off + t * 256, ((i + rb) & 3) * 12288, 16);
            SCHED();
            FLAG_ADD(F_SW);
#pragma unroll
            for (int i = 0; i < 4; ++i) group(i, a[i]);
        }
#pragma unroll
        for (int i = 0; i < 4; ++i) {
#pragma unroll 1
            for (unsigned spins = 0; spins < SPIN_CAP; ++spins) {
                if (!__any(__builtin_isunordered(gs[i][0][0], gs[i][1][0]))) break;
                if (spins) __builtin_amdgcn_s_sleep(RSLEEP);
                CFENCE();
                u32x4 a[4];
#pragma unroll
                for (int t = 0; t < 4; ++t) a[t] = __builtin_amdgcn_raw_buffer_load_b128(wsr, off + t * 256, ((i + rb) & 3) * 12288, 16);
                group(i, a);
                if (spins >= 32 && __all(max(max(umax4(a[0]), umax4(a[1])), max(umax4(a[2]), umax4(a[3]))) != EMPTY)) break;
            }
        }
        f32x4 sum[2];
        sum[0] = (gs[0][0] + gs[1][0]) + (gs[2][0] + gs[3][0]); sum[1] = (gs[0][1] + gs[1][1]) + (gs[2][1] + gs[3][1]);
#pragma unroll
        for (int i = 0; i < 4; ++i) lds_w32(dst[i], pk_bf16(sum[i >> 1][2 * (i & 1)], sum[i >> 1][2 * (i & 1) + 1]));
        CFENCE();
        FLAG_ADD(F_D);
        STAMP(10);
    } else {
        auto wo_chunks = [&](int c0, int c1) {
#pragma unroll 1
            for (int c = c0; c < c1; ++c) {
                const char* ws = P.wo + cg2 * 144 + c * (64 * 1152);
                const unsigned l = c < 8 ? c * 9216 : WO8;
                SET_M0(l); glds16o<0>(ws, oo[0]); glds16o<1024>(ws, oo[1]); glds16o<2048>(ws, oo[2]); glds16o<3072>(ws, oo[3]);
                SET_M0(l + 4096); glds16o<0>(ws, oo[4]); glds16o<1024>(ws, oo[5]); glds16o<2048>(ws, oo[6]); glds16o<3072>(ws, oo[7]);
                SET_M0(l + 8192); glds16o<0>(ws, oo[8]);
            }
        };
        flag_wait<1>(smem, F_KVR, 3);
        wo_chunks(3, 6);
        flag_wait<1>(smem, F_Q0, 8);
        if (WOSW) flag_wait<1>(smem, F_SW, 3);
        wo_chunks(0, 3);
        WAIT_VM(27);
        FLAG_ADD(F_D);
        WAIT_VM(0);
        FLAG_ADD(F_W0);
    }
    GET_TAG();
    __builtin_amdgcn_s_setprio(0);
    if (kw == 0) STAMP_OUT(1, blockIdx.x);
}

DEVI void gemm_q(const Params& P, lptr smem, int rb, int hg, int t8, int cg2, u64& tag, rsrc_t wsr) {
    const int lane = threadIdx.x & 63, lw = t8 & 3;
    const bool ld = t8 < 4;
    STAMP_DECL; STAMP(1);
    __builtin_amdgcn_s_setprio(3);
    unsigned xo[4], wo_[5], qo[3];
    const int rx = hg & 3, rw = rb & 3;
    unsigned mx[4], mw[4];
    const unsigned xbase = (unsigned)((rb * 128 + 8 * lw + (lane >> 3)) * 1152 + (((lane & 7) ^ ((lane >> 4) + 4 * (lw & 1))) << 4));
#pragma unroll
    for (int i = 0; i < 4; ++i) { mx[i] = (((i + rx) & 3) * 4 + lw) * 1024; mw[i] = XCH + (((i + rw) & 3) * 4 + lw) * 1024; xo[i] = xbase + ((i + rx) & 3) * 36864; }
    const unsigned lb = lw * 4096;
    auto issue_x = [&](int s, int i0 = 0, int i1 = 4) {
#pragma unroll
        for (int i = i0; i < i1; ++i) glds16(P.x + s * 128, xo[i], slot_off(s) + mx[i]);
    };
    auto issue_w = [&](int s, int i0 = 0, int i1 = 4) {
        const char* ws = P.wqkv + s * (64 * 3456);
        if (i1 == 4 && (lw >> 1) == (s & 1)) glds16(ws, wo_[4], (unsigned)(slot_off(s) + XCH + 16384 + (lw & 1) * 1024));
#pragma unroll
        for (int i = i0; i < i1; ++i) glds16(ws, wo_[i], slot_off(s) + mw[i]);
    };
    auto issue_q = [&](int t, int i) {
        const char* ws = P.wqkv + hg * 144 + (3 * t + i) * (64 * 3456);
        if (lw == ((i + t) & 3)) glds16(ws, qo[2], (unsigned)(wq_off(t) + i * 9216 + 8192));
        SET_M0(wq_off(t) + i * 9216 + (lb >> 1));
        glds16o<0>(ws, qo[0]); glds16o<1024>(ws, qo[1]);
    };
    auto w_off = [&](int i) {
        const unsigned q = 64 * (i < 4 ? ((i + rw) & 3) * 4 + lw : 16 + (lw & 1)) + lane, rho = (q * 3641u) >> 16, pc = q - rho * 18;
        const unsigned k = (rho & 35u) + 8 * ((rho >> 2) & 3) + 4 * ((rho >> 4) & 1);
        wo_[i] = k * 3456 + (1152 + hg * 144) + pc * 16 + (pc < 9 ? 0u : 1008u);
    };
    if (ld) {
        issue_x(0, 0, 1); issue_x(1, 0, 1);
        SCHED();
        w_off(0);
        issue_w(0, 0, 1); issue_w(1, 0, 1);
        SCHED();
#pragma unroll
        for (int i = 1; i < 5; ++i) w_off(i);
        if (lw == 1) { glds16(P.gw, (unsigned)(lane * 16), GW_OFF); if (lane < 8) glds16(P.gw + 1024, (unsigned)(lane * 16), GW_OFF + 1024); }
        issue_x(0, 1, 4); issue_w(0, 1, 4); issue_w(1, 1, 4); issue_x(1, 1, 4);
        SET_M0(IDX + (lb >> 1));
        glds16o<0>((const char*)&g_tab + (lb >> 1), (unsigned)(lane * 16)); glds16o<1024>((const char*)&g_tab + (lb >> 1), (unsigned)(lane * 16));
    } else {
        if (lw == 0 && lane < 16) lds_w32(smem + FL + lane * 4, 0u);
#pragma unroll
        for (int i = 0; i < 3; ++i) lds_w128(smem + KVT_OFF + i * 4096 + lw * 1024 + lane * 16, u32x4{0u, 0u, 0u, 0u});
    }
    const int lo16 = lane & 15, g = lane >> 4;
    const int sw = (g ^ ((lo16 >> 1) & 7)) << 4;
    lptr xa[2] = {smem + (16 * t8 + lo16) * 128 + sw, smem + (16 * t8 + lo16) * 128 + (sw ^ 64)}, xb[2] = {xa[0] + 2 * SLOT, xa[1] + 2 * SLOT};
    lptr flp = smem + FL, iqp = smem + IDX + 3072 + lane * 4, gwp = smem + GW_OFF + g * 16;
    const int qrow = (2 * (4 * g + (lo16 >> 2))) * 144;
    lptr qw = smem + qrow + (lane & 3) * 8, qw4 = smem + qrow + (16 + (lane & 1)) * 8 + (lane & 2) * 2304;
    lptr qwh = qw + wq_off(1), qw4h = qw4 + wq_off(1), kfp = smem + KVT_OFF + lane * 16, kzp = smem + KVT_OFF + 9216 + (lane & 51) * 16;
    unsigned one = 1u, ob0 = WS_O + (unsigned)(((rb * 8 + t8) * 72 + hg * 9 + 2 * (g & 1) + (g >> 1)) * 256 + lo16 * 16);
    asm volatile("" : "+v"(flp), "+v"(one), "+v"(iqp), "+v"(gwp), "+v"(xa[0]), "+v"(xa[1]), "+v"(xb[0]), "+v"(xb[1]));
    asm volatile("" : "+v"(qw), "+v"(qw4), "+v"(qwh), "+v"(qw4h), "+v"(kfp), "+v"(kzp), "+v"(ob0));
    asm volatile(".subsection 2\n.Lkf_code_end:\n\t.fill 256, 4, 0xbf800000\n\t.subsection 0");
    const f32x4 z4 = {0.f, 0.f, 0.f, 0.f};
    f32x4 gacc = z4, qa[6];
#pragma unroll
    for (int j = 0; j < 6; ++j) { const float e = j < 5 ? 1e-6f : 0.f; qa[j] = f32x4{e, e, e, e}; asm volatile("" : "+v"(qa[j])); }
    u32x4 xk[18];
    u64 pf = 0;
    WAIT_VM(8); BARRIER();
    if (ld) { issue_w(2); issue_x(2); if (LA3) { issue_w(3); issue_x(3); } }
#pragma unroll
    for (int s = 0; s < 9; ++s) {
#pragma unroll
        for (int k = 0; k < 2; ++k) xk[2 * s + k] = lds_r128(((s & 2) ? xb[k] - 2 * SLOT : xa[k]) + slot_off(s));
        if (t8 == hg) {
            gacc = mfma16(lds_r128(gwp + s * 128), xk[2 * s], gacc);
            gacc = mfma16(lds_r128(gwp + s * 128 + 64), xk[2 * s + 1], gacc);
        }
        if (t8 == 4) {
            if (s == 1) { tag = *(const __attribute__((address_space(4))) u64*)(P.ws + WS_CTL); PIN_TAG(); if (lane == 0) lds_w64(smem + LDS_TAG, u32x2{(unsigned)tag, (unsigned)(tag >> 32)}); }
            if (s == 4) asm volatile("global_load_dwordx2 %0, %1, %2 sc1" : "=v"(pf) : "v"((lane < 5 ? lane : 0) * 128), "s"(P.ws + WS_PFL) : "memory");
            if (s == 7) {
                asm volatile("s_waitcnt vmcnt(0)" : "+v"(pf) :: "memory");
                if (!__all(pf == tag)) poll_flags(P, WS_PFL, 5, tag, lane);
            }
        }
        if (LA3) { if (s < 6) WAIT_VM(17); else if (s == 6) WAIT_VM(14); else WAIT_VM(12); }
        else if (s < 7) WAIT_VM(8); else WAIT_VM(6);
        BARRIER();
        if (ld) {
            const int sn = s + 3 + LA3;
            if (sn < 9) { issue_w(sn); issue_x(sn); } else if (sn < 12) { issue_q(sn - 9, 0); issue_q(sn - 9, 1); issue_q(sn - 9, 2); }
            if (s == 3) { qo[0] = *(LAS unsigned*)(iqp + lw * 512); qo[1] = *(LAS unsigned*)(iqp + lw * 512 + 256); qo[2] = *(LAS unsigned*)(iqp + 2048); CFENCE(); }
        }
    }
    __builtin_amdgcn_s_setprio(0);
    STAMP(5);
    if (!ld) {
        const unsigned co = WS_O + (unsigned)(((rb * 8 + 2 * lw) * 72 + hg * 9) * 256 + lane * 16);
#pragma unroll
        for (int i = 0; i < 5; ++i)
            if (i < 4 || lane < 32) st_sc1(wsr, i < 4 ? co + (i >> 1) * 18432 + (i & 1) * 1024 : co + 2048 + (lane >> 4) * (18432 - 256), u32x4{EMPTY, EMPTY, EMPTY, EMPTY});
    }
    if (t8 >= 5) {
        unsigned oo[9];
#pragma unroll
        for (int i = 0; i < 9; ++i) oo[i] = *(LAS unsigned*)(iqp + 2304 + i * 256);
        const char* ws = P.wo + cg2 * 144 + (t8 + 1) * (64 * 1152);
        const unsigned l = t8 < 7 ? (t8 + 1) * 9216 : WO8;
        SET_M0(l); glds16o<0>(ws, oo[0]); glds16o<1024>(ws, oo[1]); glds16o<2048>(ws, oo[2]); glds16o<3072>(ws, oo[3]);
        SET_M0(l + 4096); glds16o<0>(ws, oo[4]); glds16o<1024>(ws, oo[5]); glds16o<2048>(ws, oo[6]); glds16o<3072>(ws, oo[7]);
        SET_M0(l + 8192); glds16o<0>(ws, oo[8]);
    }
    GET_TAG();
    if (t8 == 4 && lane == 0) tag_post(P, tag);
    if (t8 == hg) {
        const float gate = rbf(__builtin_amdgcn_rcpf(1.f + __expf(-rbf(gacc[0]))));
        if (lane < 16) __hip_atomic_store((GAS float*)(P.ws + WS_GATE) + (rb * 128 + hg * 16 + lane), gate, RLX_AGENT);
    }
    auto peek = [&](int f) { return __hip_atomic_load((LAS unsigned*)(flp + f), __ATOMIC_RELAXED, __HIP_MEMORY_SCOPE_WORKGROUP); };
    auto rdq = [&](int ks, int j) {
        const int o = (ks < 6 ? ks : ks - 6) * 4608;
        return j < 4 ? lds_trfrag((ks < 6 ? qw : qwh) + o + 32 * j, 144) : lds_trfrag((ks < 6 ? qw4 : qw4h) + o, 144);
    };
    u32x4 wq[2][4], w4[2];
#pragma unroll
    for (int i = 0; i < 2; ++i) {
#pragma unroll
        for (int j = 0; j < 4; ++j) wq[i][j] = rdq(i, j);
        if (i == 0) w4[0] = rdq(0, 4);
    }
    unsigned seenq = 0;
#pragma unroll
    for (int ks = 0; ks < 18; ++ks) {
        if (ks == 3 && ld) { SCHED(); WAIT_VM(6); FLAG_ADD(F_LANDED); }
        if (ks == 4) { SCHED(); FLAG_ADD(F_Q0); seenq = peek(F_LANDED); NEED(seenq, F_LANDED, 4); }
        if (ks == 8) { SCHED(); WAIT_VM(0); FLAG_ADD(F_C1); }
        if (ks == 10) { SCHED(); seenq = peek(F_C1); NEED(seenq, F_C1, 8); }
        if (ks == QH) { SCHED(); seenq = peek(F_KVR); NEED(seenq, F_KVR, 3); STAMP(6); }
        if (ks == 16) { SCHED(); FLAG_ADD(F_QD); }
#pragma unroll
        for (int j = 0; j < 5; ++j) {
            SCHED();
            if (j == 0 && ks < 17) { if (ks & 1) WAIT_LGKM(10); else WAIT_LGKM(8); }
            if (j < 4) mfma16i(qa[j], wq[ks & 1][j], xk[ks]); else mfma16i(qa[4 + (ks & 1)], w4[(ks >> 1) & 1], xk[ks]);
            SCHED();
            if (ks + 2 < 18) { if (j < 4) wq[ks & 1][j] = rdq(ks + 2, j); else if (!(ks & 1)) w4[((ks >> 1) + 1) & 1] = rdq(ks + 2, 4); }
        }
    }
    __builtin_amdgcn_s_setprio(2);
    MFMA_DONE();
#pragma unroll
    for (int i = 0; i < 4; ++i) {
        const auto sw2 = __builtin_amdgcn_permlane32_swap(__float_as_uint(qa[5][i]), __float_as_uint(qa[5][i]), false, false);
        qa[4][i] += __uint_as_float(sw2[1]);
    }
    u32x4 b0 = pack8(phi4(qa[0]), phi4(qa[1])), b1 = pack8(phi4(qa[2]), phi4(qa[3])), b2 = pack8(phi4(qa[4]), z4);
    asm volatile("" : "+v"(b0), "+v"(b1), "+v"(b2));
    STAMP(7);
    flag_wait<1>(smem, F_D, 4);
        if (t8 == 7 && lane == 0) __hip_atomic_store((GAS u64*)(P.ws + WS_HINT + (rb * 8 + hg) * 128), tag, RLX_AGENT);
    STAMP(8);
    {
        u32x4 kf[12];
#pragma unroll
        for (int i = 0; i < 12; ++i) { kf[i] = lds_r128(i < 3 ? kzp + i * 1024 : kfp + (i - 3) * 1024); SCHED(); }
        SCHED();
        const f32x4 den = mfma16(kf[2], b2, mfma16(kf[1], b1, mfma16(kf[0], b0, f32x4{1e-6f, 1e-6f, 1e-6f, 1e-6f})));
        f32x4 o[5];
        o[0] = mfma16(kf[3], b0, z4);
        o[1] = mfma16(kf[5], b1, mfma16(kf[4], b0, z4));
        o[2] = mfma16(kf[7], b1, mfma16(kf[6], b0, z4));
        o[3] = mfma16(kf[9], b2, mfma16(kf[8], b1, z4));
        o[4] = mfma16(kf[11], b2, mfma16(kf[10], b1, z4));
        f32x4 r;
#pragma unroll
        for (int i = 0; i < 4; ++i) r[i] = __builtin_amdgcn_rcpf(den[i]);
        const float r1a = g == 0 ? r[0] : r[1], r2 = g == 0 ? r[1] : r[2], r3a = g <= 1 ? r[2] : r[3], r3b = g == 0 ? r[2] : r[3];
        u32x2 ob[5];
        ob[0] = u32x2{pk_bf16(o[0][0] * r[0], o[0][1] * r[0]), pk_bf16(o[0][2] * r[0], o[0][3] * r[0])};
        ob[1] = u32x2{pk_bf16(o[1][0] * r1a, o[1][1] * r1a), pk_bf16(o[1][2] * r[1], o[1][3] * r[1])};
        ob[2] = u32x2{pk_bf16(o[2][0] * r2, o[2][1] * r2), pk_bf16(o[2][2] * r2, o[2][3] * r2)};
        ob[3] = u32x2{pk_bf16(o[3][0] * r3a, o[3][1] * r3a), pk_bf16(o[3][2] * r3b, o[3][3] * r3b)};
        ob[4] = u32x2{pk_bf16(o[4][0] * r[3], o[4][1] * r[3]), pk_bf16(o[4][2] * r[3], o[4][3] * r[3])};
        SCHED();
        SCHED();
#pragma unroll
        for (int t = 0; t < 3; ++t) {
            const u32x2 a = ob[2 * t], b = ob[t < 2 ? 2 * t + 1 : 4];
            const auto sx = __builtin_amdgcn_permlane16_swap(a.x, b.x, false, false), sy = __builtin_amdgcn_permlane16_swap(a.y, b.y, false, false);
            if (t < 2 || g == 0) st_sc1(wsr, ob0 + 1024 * t, not_empty(u32x4{sx[0], sy[0], sx[1], sy[1]}));
        }
    }
    __builtin_amdgcn_s_setprio(0);
    STAMP(9);
    if (t8 == 0) STAMP_OUT(5, blockIdx.x + 256);
    if (t8 == 7) STAMP_OUT(7, blockIdx.x + 768);
}

DEVI void phase2(const Params& P, lptr smem, int rt64, int cg, bool gemm, u64 tag, rsrc_t wsr) {
    const int lane = threadIdx.x & 63, w0 = __builtin_amdgcn_readfirstlane(threadIdx.x >> 6);
    const int w = gemm ? (w0 + 8) % 12 : w0, tid = threadIdx.x, bt = tid - 192;
    const int lo16 = lane & 15, g = lane >> 4;
    const int rt = w & 3, kt = w >> 2;
    STAMP_DECL; STAMP(1);
    constexpr int pb = P2_B;
    const int pa = gemm ? P2_ACC : 82944;
    if (gemm && w < 4) flag_wait(smem, F_W0, 1);
    STAMP(2);
    const int qrow = (2 * (4 * g + (lo16 >> 2))) * 144;
    lptr qw = smem + qrow + (lane & 3) * 8, qw4 = smem + qrow + (16 + (lane & 1)) * 8;
    int wofs[3], oofs[3];
#pragma unroll
    for (int u = 0; u < 3; ++u) {
        const int c = kt * 3 + (u + cg) % 3;
        wofs[u] = (c < 8 || !gemm) ? c * 9216 : WO8; oofs[u] = c * 2048;
    }
    auto rdo = [&](int i, int j) { return j < 4 ? lds_trfrag(qw + wofs[i >> 1] + (i & 1) * 4608 + 32 * j, 144) : lds_trfrag(qw4 + wofs[i >> 1] + (i & 1) * 4608, 144); };
    const unsigned ooff = WS_O + (unsigned)((rt64 * 4 + rt) * 18432 + lane * 16);
    auto ldo = [&](int i) { return __builtin_amdgcn_raw_buffer_load_b128(wsr, ooff + (i & 1) * 1024, oofs[i >> 1], 16); };
    u32x4 wf[6][5];
#pragma unroll
    for (int i = 0; i < 6; ++i)
#pragma unroll
        for (int j = 0; j < 5; ++j) wf[i][j] = rdo(i, j);
    const int r0 = tid < 576 ? tid / 9 : 0, c0 = tid - r0 * 9;
    unsigned boff = WS_B + (unsigned)((cg * 72 + (bt >> 3)) * 4096 + (rt64 >> 3) * 1024 + (((bt >> 2) & 1) * 32 + ((rt64 >> 2) & 1) * 16 + (rt64 & 3) * 4 + (bt & 3)) * 16);
    lptr bp = smem + pb + ((bt & 3) * 2 + ((bt >> 2) & 1)) * 1168 + (bt >> 3) * 2;
    unsigned goff = WS_GATE + (unsigned)((rt64 * 64 + r0) * 4);
    lptr ap = smem + pa + kt * (64 * P2_AP) + (rt * 16 + lo16) * P2_AP + (g & 1) * 144 + (g >> 1) * 16;
    lptr fa = smem + pa + r0 * P2_AP + c0 * 16, fb = smem + pb + (tid + (r0 >> 3)) * 16;
    unsigned fo = (unsigned)((rt64 * 64 + r0) * 1152 + cg * 144 + c0 * 16);
    asm volatile("" : "+v"(boff), "+v"(goff), "+v"(bp), "+v"(ap), "+v"(fa), "+v"(fb), "+v"(fo));
    STAMP(3);
    if (w == 3) {
        const int a0 = cg * 72, g0 = (a0 >> 6) * 11 + (a0 & 63) / 6, a1 = a0 + 71, g1 = (a1 >> 6) * 11 + (a1 & 63) / 6;
        poll_addr<OSLEEP>(P, lane < 8 ? WS_HINT + ((rt64 >> 1) * 8 + lane) * 128 : WS_BFL + min(g0 + lane - 8, g1) * 128, tag);
        if (gemm) flag_wait<1>(smem, F_QD, 8);
        flag_set(F_H, 1);
    } else flag_wait<1>(smem, F_H, 1);
    STAMP(4);
    u32x4 bv; unsigned gv;
    auto b_put = [&]() {
#pragma unroll
        for (int q = 0; q < 4; ++q) { lds_w16(bp + (2 * q) * 144, bv[q]); lds_w16(bp + (2 * q + 1) * 144, bv[q] >> 16); }
    };
    bool b_todo = false;
    if (w0 >= 3) bv = ld_sc1(wsr, boff);
    f32x4 acc[5];
    {
        u32x4 of[6];
#pragma unroll
        for (int i = 0; i < 6; ++i) of[i] = ldo(i);
        gv = __builtin_amdgcn_raw_buffer_load_b32(wsr, goff, 0, 16);
        SCHED();
        if (w0 >= 3) { if (__all(umax4(bv) != EMPTY)) b_put(); else b_todo = true; }
        SCHED();
#pragma unroll
        for (int j = 0; j < 5; ++j) {
            acc[j] = f32x4{0.f, 0.f, 0.f, 0.f};
#pragma unroll
            for (int i = 0; i < 6; ++i) {
                acc[j] = mfma16(wf[i][j], of[i], acc[j]);
                if (i == 3 && j > 0) { SCHED(); lds_w128(ap + (j - 1) * 32, __builtin_bit_cast(u32x4, acc[j - 1])); SCHED(); }
            }
        }
        if (g < 2) lds_w128(ap + 128, __builtin_bit_cast(u32x4, acc[4]));
    }
    if (!__all(acc[4][0] == acc[4][0])) {
#pragma unroll
        for (int j = 0; j < 5; ++j) acc[j] = f32x4{0.f, 0.f, 0.f, 0.f};
        u32x4 of[6];
#pragma unroll 1
        for (unsigned spins = 0; spins < SPIN_CAP; ++spins) {
            CFENCE();
            unsigned m = 0;
#pragma unroll
            for (int i = 0; i < 6; ++i) of[i] = ldo(i);
#pragma unroll
            for (int i = 0; i < 6; ++i) m = max(m, umax4(of[i]));
            if (__all(m != EMPTY)) break;
        }
#pragma unroll
        for (int i = 0; i < 6; ++i)
#pragma unroll
            for (int j = 0; j < 5; ++j) acc[j] = mfma16(rdo(i, j), of[i], acc[j]);
#pragma unroll
        for (int j = 0; j < 5; ++j) if (j < 4 || g < 2) lds_w128(ap + j * 32, __builtin_bit_cast(u32x4, acc[j]));
    }
    if (b_todo) {
#pragma unroll 1
        for (unsigned spins = 0; spins < SPIN_CAP; ++spins) {
            CFENCE();
            bv = ld_sc1(wsr, boff);
            if (__all(umax4(bv) != EMPTY)) break;
            __builtin_amdgcn_s_sleep(2);
        }
        b_put();
    }
    const float gt = __uint_as_float(gv);
    float og = rbf(1.f - gt);
    asm volatile("" : "+v"(og));
    STAMP(5);
    BARRIER();
    STAMP(6);
    if (w0 < 9) {
        lptr a = fa;
        const f32x4 s0 = __builtin_bit_cast(f32x4, lds_r128(a)) + __builtin_bit_cast(f32x4, lds_r128(a + 64 * P2_AP)) + __builtin_bit_cast(f32x4, lds_r128(a + 128 * P2_AP));
        const f32x4 s1 = __builtin_bit_cast(f32x4, lds_r128(a + 144)) + __builtin_bit_cast(f32x4, lds_r128(a + 64 * P2_AP + 144)) + __builtin_bit_cast(f32x4, lds_r128(a + 128 * P2_AP + 144));
        const u32x4 bb = lds_r128(fb);
        u32x4 o;
#pragma unroll
        for (int i = 0; i < 4; ++i) {
            const float a0 = i < 2 ? s0[2 * i] : s1[2 * i - 4], a1 = i < 2 ? s0[2 * i + 1] : s1[2 * i - 3];
            o[i] = pk_bf16(gt * a0 + og * bf2f(bb[i]), gt * a1 + og * bfhi(bb[i]));
        }
        __builtin_amdgcn_raw_buffer_store_b128(o, __builtin_amdgcn_make_buffer_rsrc(P.out, 0, SEQ * DM * 2, 0x00020000), fo, 0, OUTAUX);
    }
    STAMP(7);
    if (w == 4) STAMP_OUT(blockIdx.x < 128 ? 3 : 6, blockIdx.x + 512);
}

DEVI void helper_role(const Params& P, lptr smem, int xcd, int li, u64& tag, rsrc_t wsr) {
    const int tid = threadIdx.x, lane = tid & 63, w = __builtin_amdgcn_readfirstlane(tid >> 6);
    STAMP_DECL; STAMP(1);
    if (li >= 13) {
        if (HD) __builtin_amdgcn_s_sleep(HD);
#pragma unroll 1
        for (int n = 12 * NTW + (li - 13) + 3 * w; n < 171; n += 36) touch_group(P, xcd, n, lane);
    }
    if (li == 12) {
#pragma unroll
        for (int i = 0; i < 7; ++i) {
            const int u = (i * 5 + xcd - 3) * 12 + w;
            if (u < 384) st_sc1(wsr, WS_PART + (unsigned)(u * 1024 + lane * 16), u32x4{EMPTY, EMPTY, EMPTY, EMPTY});
        }
        WAIT_VM(0);
    }
    PIN_TAG();
    BARRIER();
    if (tid == 0) {
        if (li == 12) __hip_atomic_store((GAS u64*)(P.ws + WS_PFL + (xcd - 3) * 128), tag, RLX_AGENT);
        tag_post(P, tag);
    }
    if (li >= 13) {
#pragma unroll 1
        for (int n = 171 + (li - 13) + 3 * w; n < 216; n += 36) touch_group(P, xcd, n, lane);
    }
    STAMP(2);
    if (w == 11) {
        STAMP_OUT(8, blockIdx.x);
        if (blockIdx.x == 255) {
            const GAS u64* f = (const GAS u64*)(P.ws + WS_ARR) + lane;
#pragma unroll 1
            for (unsigned spins = 0; spins < SPIN_CAP; ++spins) {
                const bool ok = __hip_atomic_load(f, RLX_AGENT) == tag && __hip_atomic_load(f + 64, RLX_AGENT) == tag &&
                                __hip_atomic_load(f + 128, RLX_AGENT) == tag && __hip_atomic_load(f + 192, RLX_AGENT) == tag;
                if (__all(ok)) break;
                __builtin_amdgcn_s_sleep(8);
            }
            if (lane == 0) __hip_atomic_store((GAS u64*)(P.ws + WS_CTL), tag, RLX_AGENT);
        }
    }
}

__global__ __launch_bounds__(768, 3) void k_fused(Params P) {
    __shared__ __attribute__((aligned(16))) char smem_[LDS_BYTES];
    lptr smem = (lptr)smem_;
    const int b = blockIdx.x, xcd = b & 7, idx = b >> 3;
    const int lane = threadIdx.x & 63, w = __builtin_amdgcn_readfirstlane(threadIdx.x >> 6);
    const rsrc_t wsr = __builtin_amdgcn_make_buffer_rsrc(P.ws, 0, (int)WS_END, 0x00020000);
    const int rt64 = xcd * 4 + (idx & 3), cg2 = idx >> 2;
    if (idx >= CPF && w < 6) {
        const char* pc; unsigned len;
        asm volatile("s_getpc_b64 %0\n.Lkf_code_begin:\n\ts_mov_b32 %1, .Lkf_code_end-.Lkf_code_begin" : "=s"(pc), "=s"(len));
        touch(pc, min((unsigned)(threadIdx.x * 128), len));
    }
    u64 tag;
    if (__builtin_expect(idx < 16, 1)) {
        const int rbl = idx & 3, hgl = idx >> 2, rb = (xcd >> 1) * 4 + rbl, hg = (xcd & 1) * 4 + hgl;
        if (__builtin_expect((w & 12) != 4, 1)) gemm_q(P, smem, rb, hg, w < 4 ? w : w - 4, cg2, tag, wsr);
        else gemm_kv(P, smem, rb, hg, w - 4, cg2, tag, wsr);
    } else {
        tag = *(const __attribute__((address_space(4))) u64*)(P.ws + WS_CTL);
        const int li = idx - 16, gid = li < 11 ? xcd * 11 + li : 88 + (li - 11) * 8 + xcd;
        if (gid < 99) ssm_role(P, smem, xcd, li, gid, cg2, tag, wsr);
        else {
            helper_role(P, smem, xcd, li, tag, wsr);
            if (w < 9) { wo_issue<0x1ff>(P, cg2, w, 9, lane); WAIT_VM(0); }
            if (w == 10 && lane < 16) lds_w32(smem + FL + lane * 4, 0u);
            BARRIER();
        }
    }
    phase2(P, smem, rt64, cg2, idx < 16, tag, wsr);
}

extern "C" void kernel_launch(void* const* d_in, const int* in_sizes, int n_in, void* d_out, int out_size, void* d_ws, size_t ws_size,
                              hipStream_t stream) {
    Params P;
    P.x = (const char*)d_in[0]; P.wqkv = (const char*)d_in[1]; P.wo = (const char*)d_in[2];
    P.logA = (const char*)d_in[4]; P.aim = (const char*)d_in[5]; P.cre = (const char*)d_in[6]; P.cim = (const char*)d_in[7];
    P.logdt = (const char*)d_in[8]; P.dskip = (const char*)d_in[9]; P.gw = (const char*)d_in[10];
    P.out = (char*)d_out; P.ws = (char*)d_ws;
    k_fused<<<256, 768, 0, stream>>>(P);
}
```
